```python
import torch

def _softmax(x, axis=-1):
    return torch.softmax(x, dim=axis)
def _gelu(x):
    return torch.nn.functional.gelu(x, approximate="tanh")
def _silu(x):
    return torch.nn.functional.silu(x)
def _sigmoid(x):
    return torch.sigmoid(x)
def _layernorm(x, axis=-1, eps=1e-5):
    m = x.mean(dim=axis, keepdim=True)
    v = x.var(dim=axis, keepdim=True, unbiased=False)
    return (x - m) / torch.sqrt(v + eps)
def _scatter_add(tgt, idx, src, axis=0):
    return tgt.clone().index_add_(axis, idx, src)
def _segment_sum(src, seg, n):
    out = torch.zeros((n,) + src.shape[1:], dtype=src.dtype)
    return out.index_add_(0, seg, src)
def _pad(x, pw):
    flat = tuple(v for pair in reversed(pw) for v in pair)
    return torch.nn.functional.pad(x, flat)

kernel_name = 'parallel_sum__linear_kernel_hedgehog__geglu_ffn__prompt'

def setup_inputs():

    _w = (lambda a: (lambda t: t.to(torch.bfloat16) if t.is_floating_point() else t)(torch.as_tensor(a)))
    def _A_setup():
        B, S, D, H = 1, 2048, 448, 16
        Dh = D // H
        return {
            "x": _w((lambda *s: torch.randn(*s, dtype=torch.bfloat16) * 0.05)(B, S, D)),
            "wqkv": _w((lambda *s: torch.randn(*s, dtype=torch.bfloat16) * 0.8)(D, 3 * D)),
            "w_phi": _w((lambda *s: torch.randn(*s, dtype=torch.bfloat16) * 0.8)(H, Dh, Dh) * (Dh ** -0.5)),
            "wo": _w((lambda *s: torch.randn(*s, dtype=torch.bfloat16) * 0.2)(D, D)),
        }
    _A_in = _A_setup()

    def _B_setup():
        B, S, D, F = 1, 2048, 448, 1792
        return {
            "x": _w((lambda *s: torch.randn(*s, dtype=torch.bfloat16) * 0.05)(B, S, D)),
            "w_gate": _w((lambda *s: torch.randn(*s, dtype=torch.bfloat16) * 0.1)(D, F)),
            "w_up": _w((lambda *s: torch.randn(*s, dtype=torch.bfloat16) * 0.1)(D, F)),
            "w_down": _w((lambda *s: torch.randn(*s, dtype=torch.bfloat16) * 0.1)(F, D)),
        }
    _B_in = _B_setup()

    return {
        "A_x": _A_in["x"],
        "A_wqkv": _A_in["wqkv"],
        "A_w_phi": _A_in["w_phi"],
        "A_wo": _A_in["wo"],
        "B_x": _B_in["x"],
        "B_w_gate": _B_in["w_gate"],
        "B_w_up": _B_in["w_up"],
        "B_w_down": _B_in["w_down"],
    }

def reference(A_x, A_wqkv, A_w_phi, A_wo, B_x, B_w_gate, B_w_up, B_w_down):
    def _A_ref(x, wqkv, w_phi, wo):
        B, S, D = x.shape
        H = 16; Dh = D // H
        qkv = (lambda x, *d: x.permute(*d))((x @ wqkv).reshape(B, S, 3, H, Dh), 2, 0, 3, 1, 4)
        q, k, v = qkv[0], qkv[1], qkv[2]
        pq = torch.einsum("bhsd,hdf->bhsf", q, w_phi)
        pk = torch.einsum("bhsd,hdf->bhsf", k, w_phi)
        phi_q = torch.concatenate([_softmax(pq, axis=-1), _softmax(-pq, axis=-1)], axis=-1)
        phi_k = torch.concatenate([_softmax(pk, axis=-1), _softmax(-pk, axis=-1)], axis=-1)
        kv = torch.einsum("bhsf,bhsd->bhfd", phi_k, v)
        z  = torch.einsum("bhsf->bhf", phi_k)
        num = torch.einsum("bhqf,bhfd->bhqd", phi_q, kv)
        den = torch.einsum("bhqf,bhf->bhq", phi_q, z)[..., None] + 1e-6
        o = num / den
        return (lambda x, *d: x.permute(*d))(o, 0, 2, 1, 3).reshape(B, S, D) @ wo

    def _B_ref(x, w_gate, w_up, w_down):
        return (_gelu(x @ w_gate) * (x @ w_up)) @ w_down

    x = A_x
    xn = _layernorm(x, axis=-1)
    return x + _A_ref(xn, A_wqkv, A_w_phi, A_wo) + _B_ref(xn, B_w_gate, B_w_up, B_w_down)
```

```cpp
#include <hip/hip_runtime.h>
#include <hip/hip_bf16.h>
typedef __hip_bfloat16 bf16;
#define S_ 2048
#define D_ 448
#define F_ 1792
#define H_ 16
#define DH 28
#define MIMIC 1
__device__ inline float rb(float x){
  return __bfloat162float(__float2bfloat16(x));
}
__device__ inline float rbb(float x){ return __bfloat162float(__float2bfloat16(x)); }
__global__ void k_ln(const bf16* x, float* xn){
  int r = blockIdx.x; int l = threadIdx.x;
  float v[7]; float s=0;
  for(int i=0;i<7;i++){ v[i]=__bfloat162float(x[r*D_+l+64*i]); s+=v[i]; }
  for(int o=32;o;o>>=1) s+=__shfl_xor(s,o);
  float mf = s/D_;
  float q=0; for(int i=0;i<7;i++){ float d=v[i]-mf; q+=d*d; }
  for(int o=32;o;o>>=1) q+=__shfl_xor(q,o);
  float m = rb(mf); float var = rb(q/D_);
  float den = rb(sqrtf(rb(var+1e-5f)));
  for(int i=0;i<7;i++) xn[r*D_+l+64*i] = rbb(rb(v[i]-m)/den);
}
__global__ void k_gemm(const float* A, const bf16* B, float* C, int M,int N,int K){
  int n = blockIdx.x*64+threadIdx.x; int m = blockIdx.y;
  if(n>=N) return;
  float acc=0; for(int k=0;k<K;k++) acc += A[m*K+k]*__bfloat162float(B[k*N+n]);
  C[m*N+n]=acc;
}
__global__ void k_phi(const float* qkv, const bf16* wphi, float* phi  ){
  int idx = blockIdx.x*256+threadIdx.x; if(idx>=2*H_*S_) return;
  int s = idx % S_; int h = (idx/S_)%H_; int w = idx/(S_*H_);
  float p[DH];
  #pragma unroll
  for(int f=0;f<DH;f++){ float a=0; for(int d=0;d<DH;d++) a+= rbb(qkv[s*3*D_+w*D_+h*DH+d])*__bfloat162float(wphi[(h*DH+d)*DH+f]); p[f]=rb(a);}
  float mx=-1e30f, mn=1e30f;
  #pragma unroll
  for(int f=0;f<DH;f++){ mx=fmaxf(mx,p[f]); mn=fminf(mn,p[f]); }
  float s1=0,s2=0;
  #pragma unroll
  for(int f=0;f<DH;f++){ s1+=expf(p[f]-mx); s2+=expf(mn-p[f]); }
  #pragma unroll
  for(int f=0;f<DH;f++){ phi[((size_t)(w*H_+h)*S_+s)*56+f]=rbb(expf(p[f]-mx)/s1); phi[((size_t)(w*H_+h)*S_+s)*56+28+f]=rbb(expf(mn-p[f])/s2);}
}
__global__ void k_kv(const float* qkv, const float* phi, float* kv  ){
  int h = blockIdx.x/56, f = blockIdx.x%56; int d = threadIdx.x;
  float a=0;
  for(int s=0;s<S_;s++){ float vv = d<28 ? rbb(qkv[s*3*D_+2*D_+h*DH+d]) : 1.f; a += phi[((size_t)(H_+h)*S_+s)*56+f]*vv; }
  kv[(h*56+f)*32+d]=rbb(a);
}
__global__ void k_o(const float* phi, const float* kv, float* o){
  int idx = blockIdx.x*256+threadIdx.x; if(idx>=S_*D_) return;
  int s = idx/D_, c = idx%D_, h=c/DH, d=c%DH;
  float num=0, den=0;
  for(int f=0;f<56;f++){ float p=phi[((size_t)h*S_+s)*56+f]; num+=p*kv[(h*56+f)*32+d]; den+=p*kv[(h*56+f)*32+28]; }
  o[idx]=rbb(rb(num)/rb(rb(den)+1e-6f));
}
__global__ void k_h(const float* g, const float* u, float* h){
  int idx = blockIdx.x*256+threadIdx.x; if(idx>=S_*F_) return;
  float x = rb(g[idx]); float t = tanhf(0.7978845608028654f*(x+0.044715f*x*x*x));
  float ge = rb(0.5f*x*(1.f+t));
  h[idx]=rbb(ge*rb(u[idx]));
}
__global__ void k_fin(const bf16* x, const float* a, const float* b, bf16* out){
  int idx = blockIdx.x*256+threadIdx.x; if(idx>=S_*D_) return;
  out[idx]=__float2bfloat16(rb(__bfloat162float(x[idx])+rb(a[idx]))+rb(b[idx]));
}
extern "C" void kernel_launch(void* const* d_in, const int* in_sizes, int n_in, void* d_out, int out_size, void* d_ws, size_t ws_size, hipStream_t stream){
  const bf16* x=(const bf16*)d_in[0]; const bf16* wqkv=(const bf16*)d_in[1]; const bf16* wphi=(const bf16*)d_in[2]; const bf16* wo=(const bf16*)d_in[3];
  const bf16* wg=(const bf16*)d_in[5]; const bf16* wu=(const bf16*)d_in[6]; const bf16* wd=(const bf16*)d_in[7];
  float* ws=(float*)d_ws;
  float* xn=ws; ws+=S_*D_;
  float* qkv=ws; ws+=S_*3*D_;
  float* g=ws; ws+=S_*F_;
  float* u=ws; ws+=S_*F_;
  float* h=ws; ws+=S_*F_;
  float* phi=ws; ws+=2*H_*S_*56;
  float* kv=ws; ws+=H_*56*32;
  float* o=ws; ws+=S_*D_;
  float* a=ws; ws+=S_*D_;
  float* b=ws; ws+=S_*D_;
  k_ln<<<S_,64,0,stream>>>(x,xn);
  k_gemm<<<dim3(21,S_),64,0,stream>>>(xn,wqkv,qkv,S_,3*D_,D_);
  k_gemm<<<dim3(28,S_),64,0,stream>>>(xn,wg,g,S_,F_,D_);
  k_gemm<<<dim3(28,S_),64,0,stream>>>(xn,wu,u,S_,F_,D_);
  k_phi<<<2*H_*S_/256,256,0,stream>>>(qkv,wphi,phi);
  k_kv<<<H_*56,32,0,stream>>>(qkv,phi,kv);
  k_o<<<S_*D_/256,256,0,stream>>>(phi,kv,o);
  k_h<<<S_*F_/256,256,0,stream>>>(g,u,h);
  k_gemm<<<dim3(7,S_),64,0,stream>>>(o,wo,a,S_,D_,D_);
  k_gemm<<<dim3(7,S_),64,0,stream>>>(h,wd,b,S_,D_,F_);
  k_fin<<<S_*D_/256,256,0,stream>>>(x,a,b,(bf16*)d_out);
}
```

```cpp
#include <hip/hip_runtime.h>
#include <hip/hip_bf16.h>
#include <utility>

#define DI __device__ __forceinline__
#define LAS __attribute__((address_space(3)))
typedef __attribute__((ext_vector_type(8))) short bf16x8;
typedef __attribute__((ext_vector_type(4))) short s16x4;
typedef __attribute__((ext_vector_type(2))) __bf16 bf2;
typedef __attribute__((ext_vector_type(2))) float f32x2;
typedef __attribute__((ext_vector_type(4))) float f32x4;
typedef __attribute__((ext_vector_type(16))) float f32x16;
typedef __attribute__((ext_vector_type(2))) unsigned u32x2;
typedef __attribute__((ext_vector_type(4))) unsigned u32x4;
typedef __amdgpu_buffer_rsrc_t rsrc_t;

constexpr unsigned XROW = 896, QROW = 2688, FROW = 3584;
constexpr unsigned FS = 128;
constexpr unsigned WS_EPOCH = 0;
constexpr unsigned WS_KVP = 131072;
constexpr unsigned WS_A2 = WS_KVP + 16 * 16 * 8192;
constexpr unsigned WS_ST = WS_A2 + 64 * 144 * 1024;
constexpr unsigned WS_FH = WS_ST + 262144;
constexpr unsigned WS_KVR = WS_FH + 2048 * FS;
constexpr unsigned WS_END = WS_KVR + 16 * 8192;
constexpr unsigned L_GU = 65536, L_KV = 131072;
constexpr unsigned L_TAIL = L_KV + 3 * 8192;
constexpr unsigned L_WPK = L_TAIL;
constexpr unsigned L_ST = L_TAIL + 2048;
constexpr unsigned L_CS = L_TAIL + 4096;
constexpr unsigned L_PHI = L_GU + 32768;
constexpr unsigned L_V = L_KV + 16384;
constexpr unsigned L_FREE = 32768;

#define MFMA(a, b, c) __builtin_amdgcn_mfma_f32_32x32x16_bf16((a), (b), (c), 0, 0, 0)
#define MFMA16(a, b, c) __builtin_amdgcn_mfma_f32_16x16x32_bf16((a), (b), (c), 0, 0, 0)
#define AI __attribute__((always_inline))
#define BAR() __builtin_amdgcn_s_barrier()
#define WAITV(n) asm volatile("s_waitcnt vmcnt(" #n ")" ::: "memory")
#define WAITL0() asm volatile("s_waitcnt lgkmcnt(0)" ::: "memory")
#define SB() __builtin_amdgcn_sched_barrier(0)
#define MEMFENCE() asm volatile("" ::: "memory")

template <int N> DI void waitv() { asm volatile("s_waitcnt vmcnt(%c0)" :: "i"(N) : "memory"); }

#define INSZ(x) (x)
DI rsrc_t mkrs(const void* p, unsigned bytes) { return __builtin_amdgcn_make_buffer_rsrc((void*)p, (short)0, (int)bytes, 0x00020000); }
DI void dma(unsigned lds, unsigned limm, unsigned voff, rsrc_t rs, unsigned soff, unsigned simm) {
  unsigned t;
  asm volatile("s_add_u32 m0, %1, %c5\n\ts_add_u32 %0, %4, %c6\n\tbuffer_load_dwordx4 %2, %3, %0 offen lds"
               : "=&s"(t) : "s"(lds), "v"(voff), "s"(rs), "s"(soff), "i"(limm), "i"(simm) : "memory", "scc");
}
DI s16x4 trd(unsigned a) { return __builtin_amdgcn_ds_read_tr16_b64_v4i16((LAS s16x4*)(size_t)a); }
DI bf16x8 tr2(unsigned a, unsigned off0, unsigned off1) {
  LAS char* p = (LAS char*)(size_t)a;
  s16x4 lo = __builtin_amdgcn_ds_read_tr16_b64_v4i16((LAS s16x4*)(p + off0));
  s16x4 hi = __builtin_amdgcn_ds_read_tr16_b64_v4i16((LAS s16x4*)(p + off1));
  return __builtin_shufflevector(lo, hi, 0, 1, 2, 3, 4, 5, 6, 7);
}
template <class T> DI T ldsr(unsigned a, unsigned off) { return *(LAS T*)((LAS char*)(size_t)a + off); }
template <class T> DI void ldsw(unsigned a, unsigned off, T v) { *(LAS T*)((LAS char*)(size_t)a + off) = v; }
DI unsigned pk2(float a, float b) { f32x2 f = {a, b}; return __builtin_bit_cast(unsigned, __builtin_convertvector(f, bf2)); }
DI float rbf(float a) { return __uint_as_float(pk2(a, a) << 16); }
DI float xsum(float v) { auto r = __builtin_amdgcn_permlane32_swap(__float_as_uint(v), __float_as_uint(v), false, false); return __uint_as_float(r[0]) + __uint_as_float(r[1]); }
DI float xmax(float v) { auto r = __builtin_amdgcn_permlane32_swap(__float_as_uint(v), __float_as_uint(v), false, false); return fmaxf(__uint_as_float(r[0]), __uint_as_float(r[1])); }
DI float xmin(float v) { auto r = __builtin_amdgcn_permlane32_swap(__float_as_uint(v), __float_as_uint(v), false, false); return fminf(__uint_as_float(r[0]), __uint_as_float(r[1])); }
DI float xhi(float v) { auto r = __builtin_amdgcn_permlane32_swap(__float_as_uint(v), __float_as_uint(v), false, false); return __uint_as_float(r[1]); }
DI void st16(u32x4 v, rsrc_t rs, unsigned voff, unsigned soff, unsigned simm) {
  unsigned t;
  asm volatile("s_add_u32 %0, %4, %c5\n\tbuffer_store_dwordx4 %1, %2, %3, %0 offen sc1\n\ts_nop 1" : "=&s"(t) : "v"(v), "v"(voff), "s"(rs), "s"(soff), "i"(simm) : "memory", "scc");
}
DI void stflag(unsigned v, rsrc_t rs, unsigned soff) {
  unsigned voff = (__builtin_amdgcn_mbcnt_hi(~0u, __builtin_amdgcn_mbcnt_lo(~0u, 0u)) == 0u) ? 0u : 0x80000000u;
  __builtin_amdgcn_raw_buffer_store_b32(v, rs, voff, soff, 16);
}
#define SLEEPN 2
DI u32x4 ld16(rsrc_t rs, unsigned voff, unsigned soff, unsigned simm) {
  u32x4 r; unsigned t;
  asm volatile("s_add_u32 %1, %4, %c5\n\tbuffer_load_dwordx4 %0, %2, %3, %1 offen sc1" : "=v"(r), "=&s"(t) : "v"(voff), "s"(rs), "s"(soff), "i"(simm) : "memory", "scc");
  return r;
}
template <unsigned STEP> DI void ld8w(u32x4 (&p)[8], rsrc_t rs, unsigned voff, unsigned soff) {
  unsigned t;
#define L1(k) "buffer_load_dwordx4 %" #k ", %9, %10, %8 offen sc1\n\ts_add_u32 %8, %8, %c12\n\t"
  asm volatile("s_mov_b32 %8, %11\n\t" L1(0) L1(1) L1(2) L1(3) L1(4) L1(5) L1(6) L1(7) "s_waitcnt vmcnt(0)"
               : "=&v"(p[0]), "=&v"(p[1]), "=&v"(p[2]), "=&v"(p[3]), "=&v"(p[4]), "=&v"(p[5]), "=&v"(p[6]), "=&v"(p[7]), "=&s"(t)
               : "v"(voff), "s"(rs), "s"(soff), "i"(STEP) : "memory", "scc");
#undef L1
}
DI bool tagged(const u32x4 (&p)[8], unsigned tag, unsigned mask) {
  unsigned chk = 0;
#pragma unroll
  for (int i = 0; i < 8; i++)
#pragma unroll
    for (int k = 0; k < 4; k++) chk |= p[i][k] ^ tag;
  return __all((chk & mask) == 0u);
}
template <unsigned STEP> DI void waittag(u32x4 (&p)[8], rsrc_t rs, unsigned voff, unsigned soff, unsigned tag, unsigned mask) {
  asm volatile("s_waitcnt vmcnt(0)" : "+v"(p[0]), "+v"(p[1]), "+v"(p[2]), "+v"(p[3]), "+v"(p[4]), "+v"(p[5]), "+v"(p[6]), "+v"(p[7]) :: "memory");
#pragma nounroll
  for (int it = 0; it < (1 << 16) && !tagged(p, tag, mask); ++it) { __builtin_amdgcn_s_sleep(SLEEPN); ld8w<STEP>(p, rs, voff, soff); }
}
DI void poll(rsrc_t rs, unsigned voff, unsigned soff, unsigned E, unsigned need) {
  voff = need ? voff : 0x80000000u;
#pragma nounroll
  for (int it = 0; it < (1 << 16); ++it) {
    unsigned v;
    asm volatile("buffer_load_dword %0, %1, %2, %3 offen sc1\n\ts_waitcnt vmcnt(0)" : "=v"(v) : "v"(voff), "s"(rs), "s"(soff) : "memory");
    bool ok = (need == 0u) || ((v - E - need) <= (3u - need));
    if (__all(ok)) break;
    __builtin_amdgcn_s_sleep(SLEEPN);
  }
}

template <bool B> struct BoolC { static constexpr bool value = B; };
template <int I> struct IntC { static constexpr int value = I; };
template <int... I, class F> DI void sfor_impl(std::integer_sequence<int, I...>, F&& f) { (f(IntC<I>{}), ...); }
template <int N, class F> DI void sfor(F&& f) { sfor_impl(std::make_integer_sequence<int, N>{}, f); }
#define SFOR(n, N) sfor<N>([&](auto n##_) __attribute__((always_inline)) { constexpr int n = decltype(n##_)::value;
#define SEND });
struct RowC { unsigned negm2; float r, K1, K2, Ri; };

DI void fixmean(f32x16& acc, unsigned csaddr, unsigned off, unsigned negm2, bool hh) {
  f32x4 p = ldsr<f32x4>(csaddr, off);
  float cs = (p[0] + p[1]) + (p[2] + p[3]);
  float hi = rbf(cs), r1 = cs - hi, mid = rbf(r1), lo = r1 - mid;
  u32x4 w = {hh ? 0u : pk2(hi, mid), hh ? 0u : pk2(lo, 0.f), 0u, 0u};
  u32x4 xe = {negm2, negm2, 0u, 0u};
  acc = MFMA(__builtin_bit_cast(bf16x8, w), __builtin_bit_cast(bf16x8, xe), acc);
}
DI u32x4 epiF(const f32x16& a, const RowC& c) {
  float h[8];
#pragma unroll
  for (int i = 0; i < 8; i++) {
    float g = a[i], t = g * g, p = __builtin_fmaf(t, c.K2, c.K1), e = __builtin_amdgcn_exp2f(g * p);
    float s = __builtin_amdgcn_rcpf(__builtin_fmaf(e, c.Ri, c.Ri));
    h[i] = (g * a[i + 8]) * s;
  }
  u32x4 r = {pk2(h[0], h[1]), pk2(h[2], h[3]), pk2(h[4], h[5]), pk2(h[6], h[7])};
  return r;
}
DI f32x16 featproj(const f32x16& a, float r, unsigned wpaddr, unsigned off, bool hh) {
  u32x4 q0, q1;
#pragma unroll
  for (int i = 0; i < 4; i++) { q0[i] = pk2(a[2 * i] * r, a[2 * i + 1] * r); q1[i] = pk2(a[8 + 2 * i] * r, a[9 + 2 * i] * r); }
  q1[2] = hh ? 0u : q1[2]; q1[3] = hh ? 0u : q1[3];
  bf16x8 w0 = tr2(wpaddr, off, off + 8 * 56), w1 = tr2(wpaddr, off + 16 * 56, off + 24 * 56);
  f32x16 z;
#pragma unroll
  for (int i = 0; i < 16; i++) z[i] = 0.f;
  z = MFMA(w0, __builtin_bit_cast(bf16x8, q0), z);
  z = MFMA(w1, __builtin_bit_cast(bf16x8, q1), z);
  return z;
}
DI f32x16 dualsm(f32x16 p, bool hh) {
#pragma unroll
  for (int i = 12; i < 16; i++) p[i] = hh ? p[11] : p[i];
  float mx = p[0], mn = p[0];
#pragma unroll
  for (int i = 1; i < 16; i++) { mx = fmaxf(mx, p[i]); mn = fminf(mn, p[i]); }
  mx = xmax(mx); mn = xmin(mn);
  const float L = 1.4426950408889634f;
  float bx = -mx * L, bn = mn * L, ep[16], en[16], sp = 0.f, sn = 0.f;
#pragma unroll
  for (int i = 0; i < 16; i++) {
    ep[i] = __builtin_amdgcn_exp2f(__builtin_fmaf(p[i], L, bx));
    en[i] = __builtin_amdgcn_exp2f(__builtin_fmaf(p[i], -L, bn));
    if (i >= 12) { ep[i] = hh ? 0.f : ep[i]; en[i] = hh ? 0.f : en[i]; }
    sp += ep[i]; sn += en[i];
  }
  float ip = __builtin_amdgcn_rcpf(xsum(sp)), in = __builtin_amdgcn_rcpf(xsum(sn));
  f32x16 o;
#pragma unroll
  for (int i = 0; i < 8; i++) {
    o[i] = __uint_as_float(pk2(ep[2 * i] * ip, ep[2 * i + 1] * ip));
    o[8 + i] = __uint_as_float(pk2(en[2 * i] * in, en[2 * i + 1] * in));
  }
  return o;
}
DI bf16x8 sub4(const f32x16& v, int k) { f32x4 t = {v[4 * k], v[4 * k + 1], v[4 * k + 2], v[4 * k + 3]}; return __builtin_bit_cast(bf16x8, t); }

DI constexpr unsigned lc1_base(int ch) { return ch == 6 ? L_KV : ((ch & 1) ? L_GU : 0u) + (ch < 2 ? 49152u : ch < 4 ? 0u : 16384u); }
DI unsigned vxor(unsigned& a, unsigned k) { asm volatile("" : "+v"(a)); return a ^ k; }

#define STAMP(i)
#define STAMPS_OUT()
#define CSTAMP(i)

constexpr int PD = 4, RING = 9;
constexpr int later_ops(int m, int n, int n0, int NS) {
  int cnt = 0; bool seen = false;
  for (int j = 0; j < PD && j < n; j++) { if (seen) cnt += 2; if (j == m) seen = true; }
  for (int st = 0; st < m; st++) {
    if (st + PD < n) { if (seen) cnt += 2; if (st + PD == m) seen = true; }
    if (n0 + st + RING < NS && seen) cnt += 2;
  }
  return cnt;
}
DI constexpr unsigned ring_area(int j) { return j == 0 ? L_FREE : j == 1 ? L_PHI : lc1_base(j - 2); }

__global__ __launch_bounds__(512, 2) void fused_kernel(const char* __restrict__ px, const char* __restrict__ pqkv, const char* __restrict__ pphi,
                                                       const char* __restrict__ pwo, const char* __restrict__ pwg, const char* __restrict__ pwu,
                                                       const char* __restrict__ pwd, char* __restrict__ pout, char* __restrict__ ws) {
  __shared__ __attribute__((aligned(16))) char smem[163840];
  const unsigned LB = (unsigned)(size_t)smem;
  const unsigned tid = threadIdx.x, lane = tid & 63;
  const unsigned w = __builtin_amdgcn_readfirstlane(tid >> 6), g = w >> 2, wr = w & 3;
  const unsigned b = blockIdx.x, xcd = b & 7, bi = b >> 3, grp = xcd >> 1, half = xcd & 1, rbl = bi >> 3, ci = bi & 7;
  const unsigned rb = 4 * grp + rbl, c = 7 * half + ci, head = 8 * half + ((ci + 3 - half) & 7);
  const bool attn = ci == 7;
  STAMP(0); CSTAMP(14);
  auto prog = [&](auto at_, auto gg_) AI {
  constexpr bool AT = decltype(at_)::value; constexpr int GG = decltype(gg_)::value;
  const rsrc_t rsX = mkrs(px, INSZ(2048 * XROW)), rsQ = mkrs(pqkv, INSZ(448 * QROW));
  constexpr unsigned wstr = AT ? QROW : FROW, lcstep = AT ? 224 : 128, dU = AT ? 128 : 0;
  const rsrc_t rsG = mkrs(AT ? pqkv : pwg, INSZ(448 * wstr)), rsU = mkrs(AT ? pqkv : pwu, INSZ(448 * wstr));
  const unsigned sG0 = AT ? half * 448 : c * 256, sX0 = rb * 128 * XROW;
  const unsigned r8 = lane >> 3, c8 = lane & 7, sa = (r8 >> 1) & 1;
  const unsigned vx0 = (16 * w + r8) * XROW + ((c8 ^ (r8 >> 1)) << 4), vx1 = (16 * w + 8 + r8) * XROW + ((c8 ^ (4 + (r8 >> 1))) << 4);
  unsigned cg = AT ? (c8 ^ (4 * sa)) : (c8 ^ (2 * sa)), cu = AT ? (c8 ^ (4 * sa)) : (c8 ^ (4 + 2 * sa));
  if (AT && cu >= 6) cu -= 2;
  const unsigned vG = (8 * w + r8) * wstr + (cg << 4), vU = (8 * w + r8) * wstr + (cu << 4);
  const unsigned lc = c8 ^ (4 * sa);
  const unsigned vKV = (8 * w + r8) * QROW + 896 * (1 + (lc >> 2)) + ((lc & 3) << 4) + ((56 * head) & ~15u);
  const unsigned lw1 = LB + w * 1024, lw2 = LB + w * 2048;
  const unsigned rot = (ci + 2 * rbl) % 7;
  auto cidx = [&](int st) AI -> unsigned { unsigned t = rot + st; return t >= 7 ? t - 7 : t; };
  auto issueA = [&](int st, int k) AI {
    const unsigned sl = st & 3, ch = cidx(st);
    if (k == 0) dma(lw2, sl * 16384, vx0, rsX, sX0 + ch * 128, 0);
    if (k == 1) dma(lw2, sl * 16384 + 1024, vx1, rsX, sX0 + ch * 128, 0);
    if (k == 2) dma(lw1, L_GU + sl * 16384, vG, rsG, sG0 + ch * 64 * wstr, 0);
    if (k == 3) dma(lw1, L_GU + sl * 16384 + 8192, vU, rsU, sG0 + ch * 64 * wstr, dU);
    if (k == 4) dma(lw1, L_KV + sl * 8192, vKV, rsQ, ch * 64 * QROW, 0);
  };
  auto issueL = [&](int hc) AI {
    const int st = hc >> 1; const unsigned ch = cidx(st);
    if (hc & 1) dma(lw1, lc1_base(st) + 8192, vU, rsU, sG0 + ch * 64 * wstr, dU + lcstep);
    else dma(lw1, lc1_base(st), vG, rsG, sG0 + ch * 64 * wstr, lcstep);
  };
  auto issueG = [&](int bar, int k) AI {
    if (bar == 0) issueA(2 + k / 5, k % 5);
    else if (bar <= 3) issueA(bar + 3, k);
    else if (k < 4) issueL(4 * (bar - 4) + k);
    else if (bar == 4) dma(LB + (w & 1) * 1024, L_WPK, lane * 16, mkrs(pphi, 16 * 1568), head * 1568 + (w & 1) * 1024, 0);
    else issueL(12 + (bar - 5));
  };
#pragma unroll
  for (int k = 0; k < 5; k++) issueA(0, k);
  const rsrc_t rsWS = mkrs(ws, WS_END);
  unsigned Ev;
  asm volatile("buffer_load_dword %0, off, %1, 0 sc1" : "=v"(Ev) : "s"(rsWS) : "memory");
#pragma unroll
  for (int k = 0; k < 5; k++) issueA(1, k);
  issueG(0, 0);

  const bool hh = lane >= 32;
  const unsigned hu = lane >> 5, blk = (lane >> 4) & 1, q4 = (lane >> 2) & 3, p4 = lane & 3, ta = (q4 >> 1) & 1, l31 = lane & 31;
  const unsigned perm = wr ^ (2 * GG);
  const unsigned rowoff = (8 * hu + q4) * 128 + (perm << 11);
  const unsigned pc = (4 * blk + p4) < 6 ? (4 * blk + p4) : 6;
  auto tileaddr = [&](unsigned t) AI -> unsigned {
    if constexpr (AT) { unsigned o = 56 * t + 8 * pc; return (o >> 7) * 8192 + ((((o >> 4) & 7) ^ (4 * ta)) << 4) + (o & 8) + rowoff; }
    else return blk * 8192 + (((2 * t + (p4 >> 1)) ^ (4 * blk + 2 * ta)) << 4) + 8 * (p4 & 1) + rowoff;
  };
  unsigned aA[3], aB[2];
  {
    unsigned o = (head & 1) * 8 + 8 * pc;
    unsigned ak = L_KV + (((o >> 4) ^ (4 * ta)) << 4) + (o & 8) + rowoff;
    aA[0] = LB + (GG ? L_GU + tileaddr(1) : ak);
    aA[1] = LB + (GG ? L_GU + tileaddr(2) : (ak ^ 64u));
    aA[2] = LB + L_GU + tileaddr(GG ? 3 : 0);
    aB[0] = LB + tileaddr(2 * GG); aB[1] = LB + tileaddr(2 * GG + 1);
  }
  const unsigned xrow = 32 * wr + l31;
  const unsigned aX = LB + xrow * 128 + (((2 * perm + hu) ^ ((xrow >> 1) & 7)) << 4);
  const unsigned selv = (((lane >> 4) ^ (lane >> 2)) & 1) ? 0u : 0x3f803f80u;
  const u32x4 sel4 = {selv, selv, selv, selv};
  const bf16x8 sel = __builtin_bit_cast(bf16x8, sel4);

  bf16x8 xf[28], wa[2][3], wb[2][2];
  f32x16 accA[3], accB[2];
  f32x4 csA[3], csB[2];
#pragma unroll
  for (int j = 0; j < 3; j++) { for (int i = 0; i < 16; i++) accA[j][i] = 0.f; for (int i = 0; i < 4; i++) csA[j][i] = 0.f; }
#pragma unroll
  for (int j = 0; j < 2; j++) { for (int i = 0; i < 16; i++) accB[j][i] = 0.f; for (int i = 0; i < 4; i++) csB[j][i] = 0.f; }
  float s1[2] = {0.f, 0.f}, s2[2] = {0.f, 0.f};

  const rsrc_t rsWD = mkrs(pwd, INSZ(1792 * XROW)), rsWO = mkrs(pwo, INSZ(448 * XROW));
  const unsigned vW2 = r8 * XROW + ((c8 ^ (4 * sa)) << 4) + ci * 128;
  constexpr int N1 = GG ? 8 : 6, NS = GG ? 16 : 20;
  auto ksb = [&](int i) AI -> unsigned { return GG ? 16 * wr : i < 12 ? wr : 8 * wr; };
  auto ksi = [&](int i) AI -> unsigned { return GG ? 8 * ((i >> 2) & 1) + (i & 3) + 4 * (i >> 3) : i < 12 ? 64 + 8 * (i % 6) + 4 * (i / 6) : 112 + (i - 12); };
  auto issueW2 = [&](int i, int hf) AI {
    if (GG == 0 && i >= 12) dma(lw2, ring_area(i % RING) + hf * 1024, vW2, rsWO, 112 * wr * XROW, (28 * ((i - 12) >> 1) + 16 * ((i - 12) & 1) + 8 * hf) * XROW);
    else dma(lw2, ring_area(i % RING) + hf * 1024, vW2, rsWD, ksb(i) * 16 * XROW, (ksi(i) * 16 + 8 * hf) * XROW);
  };

  const unsigned aCSw = LB + L_CS + l31 * 16 + wr * 4 + GG * 1536, aCSr = LB + L_CS + l31 * 16 + GG * 1536;
  auto readX = [&](int n) AI { xf[n] = ldsr<bf16x8>(aX ^ (unsigned)((n & 3) << 5), ((n >> 2) & 3) * 16384); };
  auto readA = [&](int n, int j) AI {
    const unsigned o = ((n >> 2) & 3) * ((GG == 0 && j < 2) ? 8192 : 16384);
    wa[n & 1][j] = tr2(aA[j] ^ (unsigned)((n & 3) << 11), o, o + 512);
  };
  auto readB = [&](int n, int j) AI {
    const unsigned base = lc1_base(n >> 2);
    wb[n & 1][j] = tr2(vxor(aB[j], ((n & 3) << 11) | (base & 0x30000u)), base & 0xffffu, (base & 0xffffu) + 512);
  };
  WAITV(7);
  BAR(); MEMFENCE();
  STAMP(1);
  readX(0); readA(0, 0); readA(0, 1); readA(0, 2);
  unsigned E = 0;
  SFOR(n, 28)
    SB();
    if constexpr (n == 27) {
#pragma unroll
      for (int j = 0; j < 3; j++) ldsw<float>(aCSw, j * 512, csA[j][0]);
      s1[0] = xsum(s1[0] + s1[1]); s2[0] = xsum(s2[0] + s2[1]);
      { f32x2 t = {s1[0], s2[0]}; ldsw<f32x2>(LB + L_ST + GG * 1024 + xrow * 8, 0, t); }
    }
    if constexpr ((n & 3) == 3) {
      if constexpr (n == 3) { asm volatile("s_waitcnt vmcnt(10)" : "+v"(Ev) :: "memory"); E = __builtin_amdgcn_readfirstlane(Ev); } else WAITV(10);
      WAITL0(); BAR(); MEMFENCE();
      if constexpr (n == 27) STAMP(2);
    }
    constexpr int bar = (n + 1) >> 2, ph = (n + 1) & 3;
    u32x4 u = __builtin_bit_cast(u32x4, xf[n]);
#pragma unroll
    for (int j = 0; j < 3; j++) {
      if constexpr (n < 27) { if (j == 0) readX(n + 1); readA(n + 1, j); }
      SB();
      accA[j] = MFMA(wa[n & 1][j], xf[n], accA[j]);
      const int slot = 3 * ph + j;
      if (bar == 0) issueG(0, slot - 2);
      else if (bar <= 6) { if (slot == 0) issueG(bar, 0); if (slot == 2) issueG(bar, 1); if (slot == 5) issueG(bar, 2); if (slot == 7) issueG(bar, 3); if (slot == 10) issueG(bar, 4); }
      else if (j < 2) {
        if constexpr (AT) dma(lw2, L_FREE + j * 1024, lane * 16, mkrs(pphi, 16 * 1568), half * 8 * 1568 + w * 2048, j * 1024);
        else issueW2(0, j);
      }
      if ((n & 3) == 0) csA[j] = MFMA16(sel, wa[n & 1][j], csA[j]);
      if ((n & 3) == 1 || (n & 3) == 2) {
        u32x4 v = __builtin_bit_cast(u32x4, xf[n - 1]);
#pragma unroll
        for (int i = (j == 0 ? 0 : j + 1); i < j + 2; i++) {
          float lo = __uint_as_float(v[i] << 16), hi = __uint_as_float(v[i] & 0xffff0000u);
          s1[0] += lo; s1[1] += hi; s2[0] = __builtin_fmaf(lo, lo, s2[0]); s2[1] = __builtin_fmaf(hi, hi, s2[1]);
        }
      }
      SB();
    }
  SEND

  RowC rc;
  {
    f32x2 o = ldsr<f32x2>(LB + L_ST + (GG ^ 1) * 1024 + xrow * 8, 0);
    float mean = (s1[0] + o[0]) * (1.f / 448.f), var = fmaxf((s2[0] + o[1]) * (1.f / 448.f) - mean * mean, 0.f);
    float den = rbf(__builtin_sqrtf(rbf(rbf(var) + 1e-5f)));
    rc.negm2 = pk2(-mean, -mean); rc.r = 1.f / den;
    const float k1 = -2.f * 1.4426950408889634f * 0.7978845608028654f;
    rc.K1 = k1 * rc.r; rc.K2 = k1 * 0.044715f * rc.r * rc.r * rc.r; rc.Ri = den * den;
  }
  const unsigned rt = 4 * rb + wr;
  const unsigned sA2 = WS_A2 + rt * 144 * 1024 + (AT ? half * 16384 : c * 8192), vL16 = lane * 16;
  const unsigned sFown = WS_FH + (rt * 32 + (AT ? 28 + 2 * half : 2 * c) + GG) * FS;
  const unsigned aWP = LB + (4 * hu + q4) * 56 + (16 * blk + 4 * p4) * 2;
  const unsigned T12 = (E + 1) & 0xfffu, T16 = (E + 1) & 0xffffu;

#pragma unroll
  for (int j = 0; j < 3; j++) fixmean(accA[j], aCSr, j * 512, rc.negm2, hh);
  if constexpr (GG == 0) {
    f32x16 pk = featproj(accA[0], rc.r, aWP + L_WPK, 0, hh);
    const unsigned aIW = LB + L_PHI + xrow * 64 + ((hu ^ ((xrow >> 1) & 7)) << 3);
    u32x2 vv[4];
#pragma unroll
    for (int k = 0; k < 4; k++) { vv[k][0] = pk2(accA[1][4 * k] * rc.r, accA[1][4 * k + 1] * rc.r); vv[k][1] = pk2(accA[1][4 * k + 2] * rc.r, accA[1][4 * k + 3] * rc.r); }
    vv[3][0] = hh ? 0x00003f80u : vv[3][0]; vv[3][1] = hh ? 0u : vv[3][1];
#pragma unroll
    for (int k = 0; k < 4; k++) ldsw<u32x2>(aIW ^ (unsigned)(k << 4), L_V - L_PHI, vv[k]);
    pk = dualsm(pk, hh);
#pragma unroll
    for (int k = 0; k < 4; k++) {
      f32x2 t0 = {pk[2 * k], pk[2 * k + 1]}, t1 = {pk[8 + 2 * k], pk[9 + 2 * k]};
      ldsw<f32x2>(aIW ^ (unsigned)(k << 4), 0, t0); ldsw<f32x2>(aIW ^ (unsigned)(k << 4), 8192, t1);
    }
  } else if constexpr (!AT) {
#pragma unroll
    for (int j = 0; j < 3; j++) st16(epiF(accA[j], rc), rsWS, vL16, sA2, (1 + j) * 1024);
  }
  STAMP(3);
  waitv<AT ? 0 : GG ? 5 : 2>(); WAITL0(); BAR(); MEMFENCE();
  if constexpr (GG == 0) {
    if constexpr (!AT) st16(epiF(accA[2], rc), rsWS, vL16, sA2, 0);
    else accA[2] = dualsm(featproj(accA[2], rc.r, aWP + L_FREE, 0, hh), hh);
  } else if (wr < 2) {
    f32x16 kv;
#pragma unroll
    for (int i = 0; i < 16; i++) kv[i] = 0.f;
    const unsigned r0 = 8 * hu + q4, u0 = 4 * blk + p4;
    const unsigned b0 = LB + L_PHI + r0 * 64 + ((u0 ^ (r0 >> 1)) << 3), b1 = LB + L_PHI + (r0 + 4) * 64 + ((u0 ^ ((r0 + 4) >> 1)) << 3);
    bf16x8 fa[8], fb[8];
#pragma unroll
    for (int s = 0; s < 8; s++) {
      fa[s] = __builtin_shufflevector(trd(b0 + wr * 8192 + s * 1024), trd(b1 + wr * 8192 + s * 1024), 0, 1, 2, 3, 4, 5, 6, 7);
      fb[s] = __builtin_shufflevector(trd(b0 + (L_V - L_PHI) + s * 1024), trd(b1 + (L_V - L_PHI) + s * 1024), 0, 1, 2, 3, 4, 5, 6, 7);
    }
#pragma unroll
    for (int s = 0; s < 8; s++) kv = MFMA(fa[s], fb[s], kv);
#pragma unroll
    for (int k = 0; k < 4; k++) {
      u32x4 t;
#pragma unroll
      for (int i = 0; i < 4; i++) t[i] = (__float_as_uint(kv[4 * k + i]) & ~0xfffu) | T12;
      st16(t, rsWS, vL16, ((head * 16 + rb) * 2 + wr) * 4096, WS_KVP + k * 1024);
    }
  }
  STAMP(4);

  const unsigned rt2 = 2 * (8 * grp + 4 * half + rbl);
  const unsigned sF = WS_FH + rt2 * 32 * FS;
  const unsigned fc = l31 >> 1, fg = l31 & 1;
  const bool nd1 = GG ? (fc >> 1) == wr : (fc >= 8 && fc < 14 && fg == (wr != 0));
  const bool nd2 = GG ? (fc >> 1) == wr : (fc >= 8 && fc < 14 && fg == (wr >> 1));
  unsigned fl1 = 0;
  u32x4 pv[8];
  const unsigned vKP = l31 * 16 + hu * 8192, sKP = (rb * 32 + half) * 4096 + wr * 1024 + GG * 512;
  readB(0, 0); readB(0, 1);
  SFOR(n, 28)
    SB();
    if constexpr (n == 27) {
#pragma unroll
      for (int j = 0; j < 2; j++) ldsw<float>(aCSw, j * 512, csB[j][0]);
    }
    if constexpr ((n & 3) == 3) {
      if constexpr (n == 11 && !AT) { WAITV(6); stflag(E + 1, rsWS, sFown); }
      WAITL0(); BAR(); MEMFENCE();
      if constexpr (n == 27) STAMP(5);
    }
    constexpr int bar = (n + 1) >> 2, ph = (n + 1) & 3;
#pragma unroll
    for (int j = 0; j < 2; j++) {
      if constexpr (n < 27) readB(n + 1, j);
      SB();
      accB[j] = MFMA(wb[n & 1][j], xf[n], accB[j]);
      if constexpr (!AT) {
        if (bar == 1 && j == 0) issueW2(1 + (ph >> 1), ph & 1);
        if (bar >= 2 && bar <= 6 && j == 0 && (ph & 1) == 0) issueW2(bar + 1, ph >> 1);
        if (n == 26 && j == 1) asm volatile("buffer_load_dword %0, %1, %2, %3 offen sc1" : "=v"(fl1) : "v"(nd1 ? lane * FS : 0x80000000u), "s"(rsWS), "s"(sF) : "memory");
      } else if (n >= 20 && n < 24) pv[2 * (n - 20) + j] = ld16(rsWS, vKP, sKP, WS_KVP + (2 * (n - 20) + j) * 16384);
      if ((n & 3) == 0) csB[j] = MFMA16(sel, wb[n & 1][j], csB[j]);
      SB();
    }
  SEND
#pragma unroll
  for (int j = 0; j < 2; j++) fixmean(accB[j], aCSr, j * 512, rc.negm2, hh);

  if constexpr (AT) {
    waittag<16384>(pv, rsWS, vKP, sKP + WS_KVP, T12, 0xfffu);
    STAMP(6);
    {
      u32x4 o;
#pragma unroll
      for (int k = 0; k < 4; k++) {
        float sum = 0.f;
#pragma unroll
        for (int i = 0; i < 8; i++) sum += __uint_as_float(pv[i][k] ^ T12);
        sum = xsum(sum);
        o[k] = (pk2(sum, sum) & 0xffff0000u) | T16;
      }
      st16(o, rsWS, hh ? 0x80000000u : l31 * 16, (rb * 2 + half) * 4096 + wr * 1024 + GG * 512, WS_KVR);
    }
    accB[0] = dualsm(featproj(accB[0], rc.r, aWP + L_FREE, (4 + 2 * GG) * 1568, hh), hh);
    accB[1] = dualsm(featproj(accB[1], rc.r, aWP + L_FREE, (5 + 2 * GG) * 1568, hh), hh);
    if constexpr (GG) {
      accA[0] = dualsm(featproj(accA[0], rc.r, aWP + L_FREE, 1 * 1568, hh), hh);
      accA[1] = dualsm(featproj(accA[1], rc.r, aWP + L_FREE, 2 * 1568, hh), hh);
    }
    const unsigned sKR = (8 * half + w) * 8192;
#pragma unroll
    for (int i = 0; i < 8; i++) pv[i] = ld16(rsWS, vL16, sKR, WS_KVR + i * 1024);
    if constexpr (GG) accA[2] = dualsm(featproj(accA[2], rc.r, aWP + L_FREE, 3 * 1568, hh), hh);
    STAMP(7);
    waittag<1024>(pv, rsWS, vL16, sKR + WS_KVR, T16, 0xffffu);
    STAMP(8);
#pragma unroll
    for (int i = 0; i < 4; i++) {
      u32x4 f = {__builtin_amdgcn_perm(pv[2 * i][1], pv[2 * i][0], 0x07060302u), __builtin_amdgcn_perm(pv[2 * i][3], pv[2 * i][2], 0x07060302u),
                 __builtin_amdgcn_perm(pv[2 * i + 1][1], pv[2 * i + 1][0], 0x07060302u), __builtin_amdgcn_perm(pv[2 * i + 1][3], pv[2 * i + 1][2], 0x07060302u)};
      ldsw<u32x4>(LB + w * 4096 + vL16, i * 1024, f);
    }
    WAITL0(); BAR(); MEMFENCE();
    auto ohead = [&](const f32x16& phi, unsigned lh) AI {
      f32x16 o;
#pragma unroll
      for (int i = 0; i < 16; i++) o[i] = 0.f;
#pragma unroll
      for (int i = 0; i < 4; i++) o = MFMA(ldsr<bf16x8>(LB + vL16, lh * 4096 + i * 1024), sub4(phi, i), o);
      const float inv = __builtin_amdgcn_rcpf(xhi(o[12]) + 1e-6f);
      u32x4 f0, f1;
#pragma unroll
      for (int i = 0; i < 4; i++) { f0[i] = pk2(o[2 * i] * inv, o[2 * i + 1] * inv); f1[i] = pk2(o[8 + 2 * i] * inv, o[9 + 2 * i] * inv); }
      f1[2] = hh ? 0u : f1[2]; f1[3] = hh ? 0u : f1[3];
      st16(f0, rsWS, vL16, sA2, (112 + 2 * lh) * 1024); st16(f1, rsWS, vL16, sA2, (113 + 2 * lh) * 1024);
    };
    ohead(accA[2], GG ? 3 : 0); ohead(accB[0], 4 + 2 * GG); ohead(accB[1], 5 + 2 * GG);
    if constexpr (GG) { ohead(accA[0], 1); ohead(accA[1], 2); }
    STAMP(9);
    WAITV(0);
    stflag(E + 1, rsWS, sFown);
    STAMP(10);
  } else {
    st16(epiF(accB[0], rc), rsWS, vL16, sA2, (4 + 2 * GG) * 1024);
    st16(epiF(accB[1], rc), rsWS, vL16, sA2, (5 + 2 * GG) * 1024);
    STAMP(6);
    const unsigned sA = WS_A2 + rt2 * 144 * 1024;
    const unsigned aW = lw2 + (4 * hu + q4) * 128 + ((((4 * blk + p4) >> 1) ^ (4 * ta)) << 4) + 8 * (p4 & 1);
    f32x16 acc[2][2];
#pragma unroll
    for (int i = 0; i < 4; i++)
#pragma unroll
      for (int k = 0; k < 16; k++) acc[i >> 1][i & 1][k] = 0.f;
    u32x4 af[NS][2];
    u32x2 xr[2];
    auto loadA = [&](int i) AI { af[i][0] = ld16(rsWS, vL16, sA + ksb(i) * 1024, ksi(i) * 1024); af[i][1] = ld16(rsWS, vL16, sA + ksb(i) * 1024, (144 + ksi(i)) * 1024); };
    auto stage = [&](auto n0_, auto n_, auto first_) AI {
      constexpr int n0 = decltype(n0_)::value, n = decltype(n_)::value; constexpr bool first = decltype(first_)::value;
#pragma unroll
      for (int j = 0; j < PD; j++) loadA(n0 + j);
      if constexpr (first) { waitv<2 * PD>(); stflag(E + 2, rsWS, sFown); issueW2(8, 0); issueW2(8, 1); STAMP(7); }
      SFOR(m, n)
        constexpr int i = n0 + m;
        constexpr int extra = (first && m < PD) ? 3 : 0;
        asm volatile("s_waitcnt vmcnt(%c2)" : "+v"(af[i][0]), "+v"(af[i][1]) : "i"(later_ops(m, n, n0, NS) + extra) : "memory");
        constexpr unsigned ra = ring_area(i % RING);
        const unsigned la = aW + (ra & 0x30000u);
        bf16x8 w0 = tr2(la, ra & 0xffffu, (ra & 0xffffu) + 1024), w1 = tr2(la ^ 64u, ra & 0xffffu, (ra & 0xffffu) + 1024);
        SB();
        if constexpr (m + PD < n) loadA(i + PD);
#pragma unroll
        for (int mm = 0; mm < 2; mm++) acc[0][mm] = MFMA(w0, __builtin_bit_cast(bf16x8, af[i][mm]), acc[0][mm]);
        if constexpr (i + RING < NS) { WAITL0(); SB(); issueW2(i + RING, 0); issueW2(i + RING, 1); }
#pragma unroll
        for (int mm = 0; mm < 2; mm++) acc[1][mm] = MFMA(w1, __builtin_bit_cast(bf16x8, af[i][mm]), acc[1][mm]);
        SB();
      SEND
    };
    asm volatile("s_waitcnt vmcnt(2)" : "+v"(fl1) :: "memory");
    if (!__all(!nd1 || (fl1 - E - 1u) <= 2u)) poll(rsWS, lane * FS, sF, E, nd1 ? 1u : 0u);
    stage(IntC<0>{}, IntC<N1>{}, BoolC<true>{});
    STAMP(8);
    const unsigned nt = wr >> 1, mt = wr & 1;
    const unsigned vO = (32 * mt + l31) * XROW + 8 * hu + 32 * GG, sO = rt2 * 32 * XROW + ci * 128 + nt * 64;
#pragma unroll
    for (int k = 0; k < 2; k++) asm volatile("buffer_load_dwordx2 %0, %1, %2, %3 offen offset:%c4" : "=v"(xr[k]) : "v"(vO), "s"(rsX), "s"(sO), "i"(16 * k) : "memory");
    poll(rsWS, lane * FS, sF, E, nd2 ? 2u : 0u);
    STAMP(9);
    stage(IntC<N1>{}, IntC<N1>{}, BoolC<false>{});
    STAMP(10);
    if constexpr (GG == 0) {
      poll(rsWS, lane * FS, sF, E, (fc == 14 + (wr >> 1)) ? 1u : 0u);
      STAMP(11);
      stage(IntC<12>{}, IntC<8>{}, BoolC<false>{});
    }
#pragma unroll
    for (int qd = 0; qd < 4; qd++)
#pragma unroll
      for (int k = 0; k < 4; k++) {
        const unsigned ra = ring_area(2 * qd + (k >> 1)) + (k & 1) * 1024;
        ldsw<bf16x8>(lw2 + vL16 + (ra & 0x30000u), ra & 0xffffu, sub4(acc[qd >> 1][qd & 1], k));
      }
    WAITL0(); BAR(); MEMFENCE();
    STAMP(12);
    const unsigned j2 = 2 * wr + GG;
    const unsigned ra = j2 == 0 ? L_FREE : j2 == 1 ? L_PHI : ((j2 & 1) ? L_GU : 0u) + (j2 < 4 ? 49152u : j2 < 6 ? 0u : 16384u);
    asm volatile("" : "+v"(xr[0]), "+v"(xr[1]));
    const rsrc_t rsO = mkrs(pout, 2048 * XROW);
#pragma unroll
    for (int k = 0; k < 2; k++) {
      f32x4 sum = {__uint_as_float(xr[k][0] << 16), __uint_as_float(xr[k][0] & 0xffff0000u), __uint_as_float(xr[k][1] << 16), __uint_as_float(xr[k][1] & 0xffff0000u)};
#pragma unroll
      for (int v = 0; v < 8; v++) sum += ldsr<f32x4>(LB + ra + vL16, v * 2048 + k * 1024);
      u32x2 o = {pk2(sum[0], sum[1]), pk2(sum[2], sum[3])};
      __builtin_amdgcn_raw_buffer_store_b64(o, rsO, vO + 16 * k, sO, 0);
    }
    if (b == 0 && w == 0) stflag(E + 4, rsWS, WS_EPOCH);
  }
  STAMPS_OUT();
  };
  if (!attn && g) prog(BoolC<false>{}, IntC<1>{});
  else if (!attn) prog(BoolC<false>{}, IntC<0>{});
  else if (g) prog(BoolC<true>{}, IntC<1>{});
  else prog(BoolC<true>{}, IntC<0>{});
}

extern "C" void kernel_launch(void* const* d_in, const int* in_sizes, int n_in, void* d_out, int out_size, void* d_ws, size_t ws_size,
                              hipStream_t stream) {
  fused_kernel<<<256, 512, 0, stream>>>((const char*)d_in[0], (const char*)d_in[1], (const char*)d_in[2], (const char*)d_in[3], (const char*)d_in[5],
                                        (const char*)d_in[6], (const char*)d_in[7], (char*)d_out, (char*)d_ws);
}
```

```cpp
#include <hip/hip_runtime.h>
#include <hip/hip_bf16.h>
#include <utility>

#define DI __device__ __forceinline__
#define LAS __attribute__((address_space(3)))
typedef __attribute__((ext_vector_type(8))) short bf16x8;
typedef __attribute__((ext_vector_type(4))) short s16x4;
typedef __attribute__((ext_vector_type(2))) __bf16 bf2;
typedef __attribute__((ext_vector_type(2))) float f32x2;
typedef __attribute__((ext_vector_type(4))) float f32x4;
typedef __attribute__((ext_vector_type(16))) float f32x16;
typedef __attribute__((ext_vector_type(2))) unsigned u32x2;
typedef __attribute__((ext_vector_type(4))) unsigned u32x4;
typedef __amdgpu_buffer_rsrc_t rsrc_t;
#define F2U(x) __builtin_bit_cast(unsigned, (float)(x))
#define U2F(x) __builtin_bit_cast(float, (unsigned)(x))
#define ALL(x) (__builtin_amdgcn_ballot_w64(!(x)) == 0ull)
#define ANY(x) (__builtin_amdgcn_ballot_w64(x) != 0ull)
#define UMAX(a, b) __builtin_elementwise_max((unsigned)(a), (unsigned)(b))

constexpr unsigned XROW = 896, QROW = 2688, FROW = 3584;
constexpr unsigned FS = 128;
constexpr unsigned WS_EPOCH = 0;
constexpr unsigned WS_GR = 4096;
constexpr unsigned WS_KVP = 131072;
constexpr unsigned WS_A2 = WS_KVP + 16 * 16 * 8192;
constexpr unsigned WS_ST = WS_A2 + 64 * 144 * 1024;
constexpr unsigned WS_FH = WS_ST + 262144;
constexpr unsigned WS_KVR = WS_FH + 2048 * FS;
constexpr unsigned WS_END = WS_KVR + 16 * 8192;
constexpr unsigned L_GU = 65536, L_KV = 131072;
constexpr unsigned L_TAIL = L_KV + 3 * 8192;
constexpr unsigned L_WPK = L_TAIL;
constexpr unsigned L_ST = L_TAIL + 2048;
constexpr unsigned L_CS = L_TAIL + 4096;
constexpr unsigned L_PHI = L_GU + 32768;
constexpr unsigned L_V = L_KV + 16384;
constexpr unsigned L_FREE = 32768;

#define MFMA(a, b, c) __builtin_amdgcn_mfma_f32_32x32x16_bf16((a), (b), (c), 0, 0, 0)
#define MFMA16(a, b, c) __builtin_amdgcn_mfma_f32_16x16x32_bf16((a), (b), (c), 0, 0, 0)
#define AI __attribute__((always_inline))
#define BAR() __builtin_amdgcn_s_barrier()
#define WAITV(n) asm volatile("s_waitcnt vmcnt(" #n ")" ::: "memory")
#define WAITL0() asm volatile("s_waitcnt lgkmcnt(0)" ::: "memory")
#define SB() __builtin_amdgcn_sched_barrier(0)
#define MEMFENCE() asm volatile("" ::: "memory")

template <int N> DI void waitv() { asm volatile("s_waitcnt vmcnt(%c0)" :: "i"(N) : "memory"); }

#define INSZ(x) (x)
DI rsrc_t mkrs(const void* p, unsigned bytes) { return __builtin_amdgcn_make_buffer_rsrc((void*)p, (short)0, (int)bytes, 0x00020000); }
DI void dma(unsigned lds, unsigned limm, unsigned voff, rsrc_t rs, unsigned soff, unsigned simm) {
  unsigned t;
  asm volatile("s_add_u32 m0, %1, %c5\n\ts_add_u32 %0, %4, %c6\n\tbuffer_load_dwordx4 %2, %3, %0 offen lds"
               : "=&s"(t) : "s"(lds), "v"(voff), "s"(rs), "s"(soff), "i"(limm), "i"(simm) : "memory", "scc");
}
DI s16x4 trd(unsigned a) { return __builtin_amdgcn_ds_read_tr16_b64_v4i16((LAS s16x4*)(size_t)a); }
DI bf16x8 tr2(unsigned a, unsigned off0, unsigned off1) {
  LAS char* p = (LAS char*)(size_t)a;
  s16x4 lo = __builtin_amdgcn_ds_read_tr16_b64_v4i16((LAS s16x4*)(p + off0));
  s16x4 hi = __builtin_amdgcn_ds_read_tr16_b64_v4i16((LAS s16x4*)(p + off1));
  return __builtin_shufflevector(lo, hi, 0, 1, 2, 3, 4, 5, 6, 7);
}
template <class T> DI T ldsr(unsigned a, unsigned off) { return *(LAS T*)((LAS char*)(size_t)a + off); }
template <class T> DI void ldsw(unsigned a, unsigned off, T v) { *(LAS T*)((LAS char*)(size_t)a + off) = v; }
DI unsigned pk2(float a, float b) { f32x2 f = {a, b}; return __builtin_bit_cast(unsigned, __builtin_convertvector(f, bf2)); }
DI float rbf(float a) { return U2F(pk2(a, a) << 16); }
DI float dot2(unsigned a, unsigned b, float c) { return __builtin_amdgcn_fdot2_f32_bf16(__builtin_bit_cast(bf2, a), __builtin_bit_cast(bf2, b), c, false); }
DI float xsum(float v) { auto r = __builtin_amdgcn_permlane32_swap(F2U(v), F2U(v), false, false); return U2F(r[0]) + U2F(r[1]); }
DI float xmax(float v) { auto r = __builtin_amdgcn_permlane32_swap(F2U(v), F2U(v), false, false); return __builtin_fmaxf(U2F(r[0]), U2F(r[1])); }
DI float xmin(float v) { auto r = __builtin_amdgcn_permlane32_swap(F2U(v), F2U(v), false, false); return __builtin_fminf(U2F(r[0]), U2F(r[1])); }
DI float xhi(float v) { auto r = __builtin_amdgcn_permlane32_swap(F2U(v), F2U(v), false, false); return U2F(r[1]); }
DI void st16(u32x4 v, rsrc_t rs, unsigned voff, unsigned soff, unsigned simm) {
  unsigned t;
  asm volatile("s_add_u32 %0, %4, %c5\n\tbuffer_store_dwordx4 %1, %2, %3, %0 offen sc1\n\ts_nop 1" : "=&s"(t) : "v"(v), "v"(voff), "s"(rs), "s"(soff), "i"(simm) : "memory", "scc");
}
DI void stflag(unsigned v, rsrc_t rs, unsigned soff) {
  unsigned voff = (__builtin_amdgcn_mbcnt_hi(~0u, __builtin_amdgcn_mbcnt_lo(~0u, 0u)) == 0u) ? 0u : 0x80000000u;
  __builtin_amdgcn_raw_buffer_store_b32(v, rs, voff, soff, 16);
}
#define SLEEPR 4
#define SLEEPN 2
DI u32x4 ld16(rsrc_t rs, unsigned voff, unsigned soff, unsigned simm) {
  u32x4 r; unsigned t;
  asm volatile("s_add_u32 %1, %4, %c5\n\tbuffer_load_dwordx4 %0, %2, %3, %1 offen sc1" : "=v"(r), "=&s"(t) : "v"(voff), "s"(rs), "s"(soff), "i"(simm) : "memory", "scc");
  return r;
}
template <unsigned STEP> DI void ld8w(u32x4 (&p)[8], rsrc_t rs, unsigned voff, unsigned soff) {
  unsigned t;
#define L1(k) "buffer_load_dwordx4 %" #k ", %9, %10, %8 offen sc1\n\ts_add_u32 %8, %8, %c12\n\t"
  asm volatile("s_mov_b32 %8, %11\n\t" L1(0) L1(1) L1(2) L1(3) L1(4) L1(5) L1(6) L1(7) "s_waitcnt vmcnt(0)"
               : "=&v"(p[0]), "=&v"(p[1]), "=&v"(p[2]), "=&v"(p[3]), "=&v"(p[4]), "=&v"(p[5]), "=&v"(p[6]), "=&v"(p[7]), "=&s"(t)
               : "v"(voff), "s"(rs), "s"(soff), "i"(STEP) : "memory", "scc");
#undef L1
}
DI bool tagged(const u32x4 (&p)[8], unsigned tag, unsigned mask) {
  unsigned chk = 0;
#pragma unroll
  for (int i = 0; i < 8; i++)
#pragma unroll
    for (int k = 0; k < 4; k++) chk |= p[i][k] ^ tag;
  return ALL((chk & mask) == 0u);
}
template <unsigned STEP> DI void waittag(u32x4 (&p)[8], rsrc_t rs, unsigned voff, unsigned soff, unsigned tag, unsigned mask) {
  asm volatile("s_waitcnt vmcnt(0)" : "+v"(p[0]), "+v"(p[1]), "+v"(p[2]), "+v"(p[3]), "+v"(p[4]), "+v"(p[5]), "+v"(p[6]), "+v"(p[7]) :: "memory");
#pragma nounroll
  for (int it = 0; it < (1 << 16) && !tagged(p, tag, mask); ++it) { __builtin_amdgcn_s_sleep(SLEEPN); ld8w<STEP>(p, rs, voff, soff); }
}
#define RL(a, b, so) "buffer_load_dwordx4 %" #a ", %[v], %[r], %" #so " offen sc1\n\ts_add_u32 %[t], %" #so ", 147456\n\tbuffer_load_dwordx4 %" #b ", %[v], %[r], %[t] offen sc1\n\t"
template <int N> DI void reld(u32x4& a0, u32x4& b0, u32x4& a1, u32x4& b1, u32x4& a2, u32x4& b2, u32x4& a3, u32x4& b3, rsrc_t rs, unsigned voff,
                              unsigned s0, unsigned s1, unsigned s2, unsigned s3) {
  unsigned t;
  if constexpr (N == 4) asm volatile(RL(0, 1, 9) RL(2, 3, 10) RL(4, 5, 11) RL(6, 7, 12) "s_waitcnt vmcnt(0)"
      : "+v"(a0), "+v"(b0), "+v"(a1), "+v"(b1), "+v"(a2), "+v"(b2), "+v"(a3), "+v"(b3), [t] "=&s"(t) : "s"(s0), "s"(s1), "s"(s2), "s"(s3), [v] "v"(voff), [r] "s"(rs) : "memory", "scc");
  if constexpr (N == 3) asm volatile(RL(0, 1, 7) RL(2, 3, 8) RL(4, 5, 9) "s_waitcnt vmcnt(0)"
      : "+v"(a0), "+v"(b0), "+v"(a1), "+v"(b1), "+v"(a2), "+v"(b2), [t] "=&s"(t) : "s"(s0), "s"(s1), "s"(s2), [v] "v"(voff), [r] "s"(rs) : "memory", "scc");
  if constexpr (N == 2) asm volatile(RL(0, 1, 5) RL(2, 3, 6) "s_waitcnt vmcnt(0)"
      : "+v"(a0), "+v"(b0), "+v"(a1), "+v"(b1), [t] "=&s"(t) : "s"(s0), "s"(s1), [v] "v"(voff), [r] "s"(rs) : "memory", "scc");
  if constexpr (N == 1) asm volatile(RL(0, 1, 3) "s_waitcnt vmcnt(0)"
      : "+v"(a0), "+v"(b0), [t] "=&s"(t) : "s"(s0), [v] "v"(voff), [r] "s"(rs) : "memory", "scc");
}
DI void poll(rsrc_t rs, unsigned voff, unsigned soff, unsigned E, unsigned need) {
  voff = need ? voff : 0x80000000u;
#pragma nounroll
  for (int it = 0; it < (1 << 16); ++it) {
    unsigned v;
    asm volatile("buffer_load_dword %0, %1, %2, %3 offen sc1\n\ts_waitcnt vmcnt(0)" : "=v"(v) : "v"(voff), "s"(rs), "s"(soff) : "memory");
    bool ok = (need == 0u) || ((v - E - need) <= (3u - need));
    if (ALL(ok)) break;
    __builtin_amdgcn_s_sleep(SLEEPN);
  }
}

template <bool B> struct BoolC { static constexpr bool value = B; };
template <int I> struct IntC { static constexpr int value = I; };
template <int... I, class F> DI void sfor_impl(std::integer_sequence<int, I...>, F&& f) { (f(IntC<I>{}), ...); }
template <int N, class F> DI void sfor(F&& f) { sfor_impl(std::make_integer_sequence<int, N>{}, f); }
#define SFOR(n, N) sfor<N>([&](auto n##_) AI { constexpr int n = decltype(n##_)::value;
#define SEND });
struct RowC { float r, K1, K2, Ri, Lr; };

DI unsigned hilo(float v) { float hi = rbf(v); return pk2(hi, v - hi); }
DI float max3(float a, float b, float c) { return __builtin_fmaxf(__builtin_fmaxf(a, b), c); }
DI float min3(float a, float b, float c) { return __builtin_fminf(__builtin_fminf(a, b), c); }
DI unsigned pk2s(float a, float b) { unsigned r; asm("v_cvt_pk_bf16_f32 %0, %1, %2" : "=v"(r) : "v"(a), "v"(b)); return r; }
DI f32x2 sub2(const f32x16& v, int k) { f32x2 t = {v[2 * k], v[2 * k + 1]}; return t; }
DI f32x2 exp2v(f32x2 v) { f32x2 r = {__builtin_amdgcn_exp2f(v[0]), __builtin_amdgcn_exp2f(v[1])}; return r; }
DI f32x2 fma2(f32x2 a, float b, float c) { f32x2 bb = {b, b}, cc = {c, c}; return __builtin_elementwise_fma(a, bb, cc); }
DI u32x4 epiP(const f32x16& a, const RowC& c) {
  u32x4 r;
#pragma unroll
  for (int i = 0; i < 4; i++) {
    const f32x2 g = sub2(a, i), d = fma2(exp2v(g * fma2(g * g, c.K2, c.K1)), c.Ri, c.Ri);
    const f32x2 s = {__builtin_amdgcn_rcpf(d[0]), __builtin_amdgcn_rcpf(d[1])}, h = (g * sub2(a, 4 + i)) * s;
    r[i] = pk2(h[0], h[1]);
  }
  return r;
}
struct EpiS { float x0, x1, m0, m1; u32x4 r; };
template <int P, int PH> DI void epiSlice(const f32x16& a, const RowC& c, EpiS& s) {
  const float g0 = a[2 * P], g1 = a[2 * P + 1];
  if constexpr (PH == 0) { s.x0 = g0 * __builtin_fmaf(g0 * g0, c.K2, c.K1); s.x1 = g1 * __builtin_fmaf(g1 * g1, c.K2, c.K1); }
  if constexpr (PH == 1) { s.x0 = __builtin_amdgcn_exp2f(s.x0); s.x1 = __builtin_amdgcn_exp2f(s.x1); s.m0 = g0 * a[8 + 2 * P]; s.m1 = g1 * a[9 + 2 * P]; }
  if constexpr (PH == 2) { s.x0 = __builtin_amdgcn_rcpf(__builtin_fmaf(s.x0, c.Ri, c.Ri)); s.x1 = __builtin_amdgcn_rcpf(__builtin_fmaf(s.x1, c.Ri, c.Ri)); }
  if constexpr (PH == 3) s.r[P] = pk2s(s.m0 * s.x0, s.m1 * s.x1);
}
DI f32x16 featproj(const f32x16& a, unsigned wpaddr, unsigned off, bool hh) {
  u32x4 q0, q1;
#pragma unroll
  for (int i = 0; i < 4; i++) { q0[i] = pk2(a[2 * i], a[2 * i + 1]); q1[i] = pk2(a[8 + 2 * i], a[9 + 2 * i]); }
  q1[2] = hh ? 0u : q1[2]; q1[3] = hh ? 0u : q1[3];
  bf16x8 w0 = tr2(wpaddr, off, off + 8 * 56), w1 = tr2(wpaddr, off + 16 * 56, off + 24 * 56);
  f32x16 z;
#pragma unroll
  for (int i = 0; i < 16; i++) z[i] = 0.f;
  z = MFMA(w0, __builtin_bit_cast(bf16x8, q0), z);
  z = MFMA(w1, __builtin_bit_cast(bf16x8, q1), z);
  return z;
}
DI float max16(const f32x16& p) { float m = max3(p[0], p[1], p[2]); _Pragma("unroll") for (int i = 3; i < 15; i += 2) m = max3(m, p[i], p[i + 1]); return xmax(__builtin_fmaxf(m, p[15])); }
DI float min16(const f32x16& p) { float m = min3(p[0], p[1], p[2]); _Pragma("unroll") for (int i = 3; i < 15; i += 2) m = min3(m, p[i], p[i + 1]); return xmin(__builtin_fminf(m, p[15])); }
DI f32x16 dualexpP(const f32x16& p, float Lr) {
  const float bx = -max16(p) * Lr, bn = min16(p) * Lr;
  f32x16 o;
#pragma unroll
  for (int i = 0; i < 8; i++) {
    const f32x2 ep = exp2v(fma2(sub2(p, i), Lr, bx)), en = exp2v(fma2(sub2(p, i), -Lr, bn));
    o[i] = U2F(pk2(ep[0], ep[1])); o[8 + i] = U2F(pk2(en[0], en[1]));
  }
  return o;
}
struct SmS { f32x16 p, e; float en[16], bx, bn, sp, sn, tp, tn; };
DI bf16x8 sub4(const f32x16& v, int k) { f32x4 t = {v[4 * k], v[4 * k + 1], v[4 * k + 2], v[4 * k + 3]}; return __builtin_bit_cast(bf16x8, t); }

DI constexpr unsigned lc1_base(int ch) { return ch == 6 ? L_KV : ((ch & 1) ? L_GU : 0u) + (ch < 2 ? 49152u : ch < 4 ? 0u : 16384u); }
template <int CTRL, int RM> DI float dppadd(float v) { return v + U2F(__builtin_amdgcn_update_dpp(0u, F2U(v), CTRL, RM, 0xf, false)); }
DI float wavesum63(float v) {
  v = dppadd<0x111, 0xf>(v); v = dppadd<0x112, 0xf>(v); v = dppadd<0x114, 0xf>(v); v = dppadd<0x118, 0xf>(v);
  v = dppadd<0x142, 0xa>(v); return dppadd<0x143, 0xc>(v);
}

#define STAMP(i)
#define STAMPS_OUT()
#define CSTAMP(i)

constexpr int PD = 4, RING = 9;
constexpr int later_ops(int m, int n, int n0, int NS) {
  int cnt = 0; bool seen = false;
  for (int j = 0; j < PD && j < n; j++) { if (seen) cnt += 2; if (j == m) seen = true; }
  for (int st = 0; st < m; st++) {
    if (st + PD < n) { if (seen) cnt += 2; if (st + PD == m) seen = true; }
    if (n0 + st + RING < NS && seen) cnt += 2;
  }
  return cnt;
}
DI constexpr unsigned ring_area(int j) { return j == 0 ? L_FREE : j <= 5 ? lc1_base(j - 1) : j == 6 ? L_PHI : lc1_base(j - 2); }
constexpr int cumG1(int s) { return s < 4 ? 0 : s < 22 ? (s - 4) / 2 : (9 + (s - 22) * 5 / 4 < 48 ? 9 + (s - 22) * 5 / 4 : 48); }

#define CODE_BYTES 65536
__global__ __launch_bounds__(512, 2) void fused_kernel(const char* px_, const char* pqkv_, const char* pwg_, const char* pwu_, char* ws_, const char* pphi_,
                                                       const char* pwd_, const char* pwo_, char* pout_) {
  typedef __attribute__((ext_vector_type(8))) unsigned long long u64x8;
  u64x8 ka; unsigned long long kb;
  asm volatile("s_load_dwordx16 %0, %2, 0\n\ts_load_dwordx2 %1, %2, 64" : "=&s"(ka), "=&s"(kb) : "s"(__builtin_amdgcn_kernarg_segment_ptr()));
  __shared__ __attribute__((aligned(16))) char smem[163840];
  const unsigned LB = (unsigned)(size_t)smem;
  const unsigned tid = __builtin_amdgcn_workitem_id_x(), lane = tid & 63;
  const unsigned w = __builtin_amdgcn_readfirstlane(tid >> 6), g = w >> 2, wr = w & 3;
  const unsigned b = __builtin_amdgcn_workgroup_id_x(), xcd = b & 7, bi = b >> 3, grp = xcd >> 1, half = xcd & 1, rbl = bi & 3, cp = bi >> 2, ci = cp - 1;
  const unsigned rb = 4 * grp + rbl, c = 7 * half + ci;
  const unsigned head = 8 * half + (((half ? 0x76425310u : 0x75316420u) >> (4 * cp)) & 7);
  const bool attn = cp == 0;
  STAMP(0); CSTAMP(14);
  const unsigned wstr = attn ? QROW : FROW;
  const unsigned sG0 = half * (attn ? 448 : 1792) + (attn ? 0 : ci * 256), sX0 = rb * 128 * XROW;
  const unsigned r8 = lane >> 3, c8 = lane & 7, sa = (r8 >> 1) & 1, lc = c8 ^ (4 * sa);
  unsigned vx0 = (16 * w + r8) * XROW + ((c8 ^ (r8 >> 1)) << 4), vx1 = (vx0 ^ 64u) + 8 * XROW;
  const unsigned cg = attn ? lc : (c8 ^ (2 * sa)), cu = attn ? (lc >= 6 ? lc - 2 : lc) : (cg ^ 4);
  unsigned vG = (8 * w + r8) * wstr + (cg << 4), vU = (8 * w + r8) * wstr + (cu << 4);
  unsigned vKV = (8 * w + r8) * QROW + 896 * (1 + (lc >> 2)) + ((lc & 3) << 4) + ((56 * head) & ~15u);
  const unsigned lw1 = LB + w * 1024, lw2 = LB + w * 2048;
#define ROT (((0x24924u >> (2 * (cp + rbl))) & 3) * 2)
  const unsigned rot = ROT;
  const unsigned srow = 128 * rb + 64 * half + 8 * cp + w;
  unsigned vrow = lane < 56 ? lane * 16 : 0x80000000u;
  asm volatile("s_waitcnt lgkmcnt(0)" : "+s"(ka), "+s"(kb), "+v"(vx0), "+v"(vx1), "+v"(vG), "+v"(vU), "+v"(vKV), "+v"(vrow));
  const char *px = (const char*)ka[0], *pqkv = (const char*)ka[1], *pwg = (const char*)ka[2], *pwu = (const char*)ka[3], *pphi = (const char*)ka[5],
             *pwd = (const char*)ka[6], *pwo = (const char*)ka[7];
  char *ws = (char*)ka[4], *pout = (char*)kb;
  const rsrc_t rsX = mkrs(px, INSZ(2048 * XROW)), rsQ = mkrs(pqkv, INSZ(448 * QROW));
  const rsrc_t rsG = mkrs(attn ? pqkv : pwg, INSZ(448 * wstr)), rsU = mkrs(attn ? pqkv + 128 : pwu, INSZ(448 * wstr - (attn ? 128 : 0)));
  auto cidx = [&](int st) AI -> unsigned { unsigned t = rot + st; return t >= 7 ? t - 7 : t; };
  auto issueA = [&](int st, int k) AI {
    const unsigned sl = st & 3, ch = cidx(st);
    if (k == 0) dma(lw2, sl * 16384, vx0, rsX, sX0 + ch * 128, 0);
    if (k == 1) dma(lw2, sl * 16384 + 1024, vx1, rsX, sX0 + ch * 128, 0);
    if (k == 2) dma(lw1, L_GU + sl * 16384, vG, rsG, sG0 + ch * 64 * wstr, 0);
    if (k == 3) dma(lw1, L_GU + sl * 16384 + 8192, vU, rsU, sG0 + ch * 64 * wstr, 0);
    if (k == 4) dma(lw1, L_KV + sl * 8192, vKV, rsQ, ch * 64 * QROW, 0);
  };
  const rsrc_t rsWS = mkrs(ws, WS_END);
  unsigned Ev, cpf; u32x4 xrw;
  asm volatile("buffer_load_dwordx4 %0, %1, %2, %3 offen" : "=v"(xrw) : "v"(vrow), "s"(rsX), "s"(srow * XROW) : "memory");
  asm volatile("buffer_load_dword %0, off, %1, 0 sc1" : "=v"(Ev) : "s"(rsWS) : "memory");
#pragma unroll
  for (int k = 0; k < 5; k++) issueA(0, k);
  {
    unsigned long long pc; asm volatile("s_getpc_b64 %0" : "=s"(pc));
    const unsigned vo = w == 0 && lane < 24 ? (bi * 24 + lane) * 128 : 0x80000000u;
    asm volatile("buffer_load_dword %0, %1, %2, 0 offen" : "=v"(cpf) : "v"(vo), "s"(mkrs((const void*)pc, CODE_BYTES)) : "memory");
  }
#pragma unroll
  for (int k = 0; k < 5; k++) issueA(1, k);
  issueA(2, 0);
  asm volatile("s_waitcnt vmcnt(12)" : "+v"(xrw), "+v"(Ev) :: "memory");
  const unsigned E = __builtin_amdgcn_readfirstlane(Ev);
  {
    float s1 = 0.f, s2 = 0.f;
#pragma unroll
    for (int i = 0; i < 4; i++) { s1 = dot2(xrw[i], 0x3f803f80u, s1); s2 = dot2(xrw[i], xrw[i], s2); }
    s1 = wavesum63(s1); s2 = wavesum63(s2);
    const float mean = s1 * (1.f / 448.f), var = __builtin_fmaxf(s2 * (1.f / 448.f) - mean * mean, 0.f);
    u32x2 gr = {pk2(-mean, __builtin_amdgcn_sqrtf(rbf(rbf(var) + 1e-5f))), E + 1};
    __builtin_amdgcn_raw_buffer_store_b64(gr, rsWS, lane == 63 ? 0u : 0x80000000u, WS_GR + srow * 8, 16);
  }

  auto prog = [&](auto at_, auto gg_) AI {
  constexpr bool AT = decltype(at_)::value; constexpr int GG = decltype(gg_)::value;
  constexpr unsigned lcstep = AT ? 224 : 128;
  auto issueL = [&](int hc) AI {
    const int st = hc >> 1; const unsigned ch = cidx(st);
    if (hc & 1) dma(lw1, lc1_base(st) + 8192, vU, rsU, sG0 + ch * 64 * wstr, lcstep);
    else dma(lw1, lc1_base(st), vG, rsG, sG0 + ch * 64 * wstr, lcstep);
  };
  auto issueG = [&](int bar, int k) AI {
    if (bar == 0) issueA(2 + k / 5, k % 5);
    else if (bar <= 3) issueA(bar + 3, k);
    else if (k < 4) issueL(4 * (bar - 4) + k);
    else if (bar == 4) dma(LB + (w & 1) * 1024, L_WPK, lane * 16, mkrs(pphi, 16 * 1568), head * 1568 + (w & 1) * 1024, 0);
    else issueL(12 + (bar - 5));
  };

  const bool hh = lane >= 32;
  const unsigned hu = lane >> 5, blk = (lane >> 4) & 1, q4 = (lane >> 2) & 3, p4 = lane & 3, ta = (q4 >> 1) & 1, l31 = lane & 31;
  const unsigned perm = wr ^ (2 * GG);
  const unsigned rowoff = (8 * hu + q4) * 128 + (perm << 11);
  const unsigned pc = (4 * blk + p4) < 6 ? (4 * blk + p4) : 6;
  auto tileaddr = [&](unsigned t) AI -> unsigned {
    if constexpr (AT) { unsigned o = 56 * t + 8 * pc; return (o >> 7) * 8192 + ((((o >> 4) & 7) ^ (4 * ta)) << 4) + (o & 8) + rowoff; }
    else return blk * 8192 + (((2 * t + (p4 >> 1)) ^ (4 * blk + 2 * ta)) << 4) + 8 * (p4 & 1) + rowoff;
  };
  unsigned aA[3], aB[2];
  {
    unsigned o = (head & 1) * 8 + 8 * pc;
    unsigned ak = L_KV + (((o >> 4) ^ (4 * ta)) << 4) + (o & 8) + rowoff;
    aA[0] = LB + (GG ? L_GU + tileaddr(1) : ak);
    aA[1] = LB + (GG ? L_GU + tileaddr(2) : (ak ^ 64u));
    aA[2] = LB + L_GU + tileaddr(GG ? 3 : 0);
    aB[0] = LB + tileaddr(2 * GG); aB[1] = LB + tileaddr(2 * GG + 1);
  }
  const unsigned xrow = 32 * wr + l31;
  const unsigned aX = LB + xrow * 128 + (((2 * perm + hu) ^ ((xrow >> 1) & 7)) << 4);
  const unsigned selv = (((lane >> 4) ^ (lane >> 2)) & 1) ? 0u : 0x3f803f80u;
  const u32x4 sel4 = {selv, selv, selv, selv};
  const bf16x8 sel = __builtin_bit_cast(bf16x8, sel4);

  bf16x8 xf[28], wa[2][3], wb[3][2];
  f32x16 accA[3], accB[2];
  f32x4 csA[3], csB[2];
#pragma unroll
  for (int j = 0; j < 3; j++) { for (int i = 0; i < 16; i++) accA[j][i] = 0.f; for (int i = 0; i < 4; i++) csA[j][i] = 0.f; }
#pragma unroll
  for (int j = 0; j < 2; j++) { for (int i = 0; i < 16; i++) accB[j][i] = 0.f; for (int i = 0; i < 4; i++) csB[j][i] = 0.f; }
  u32x2 grn;

  const rsrc_t rsWD = mkrs(pwd, INSZ(1792 * XROW)), rsWO = mkrs(pwo, INSZ(448 * XROW));
  const unsigned vW2 = r8 * XROW + (lc << 4) + ci * 128;
  constexpr int NS = GG ? 16 : 20;
#define RHO ((rbl + cp) & 3)
  const unsigned rho = RHO;
  auto t_of = [](int i) constexpr { return (0x76543021u >> (4 * (i >> 1))) & 7; };
  auto ksb = [&](int i) AI -> unsigned {
    if (GG) return 16 * wr + ((t_of(i) & 3) ^ rho);
    if (i < 12) { const unsigned cc = i % 6 + rho + (rho >> 1); return wr + 8 * (cc >= 6 ? cc - 6 : cc); }
    return 8 * wr + 2 * (((i - 12) >> 1) ^ rho);
  };
  auto ksi = [&](int i) AI -> unsigned { return GG ? 8 * (i & 1) + (t_of(i) & 4) : i < 12 ? 64 + 4 * (i / 6) : 112 + (i & 1); };
  auto issueW2 = [&](int i, int hf) AI {
    if (GG == 0 && i >= 12) dma(lw2, ring_area(i % RING) + hf * 1024, vW2, rsWO, ksb(i) * 14 * XROW, (16 * (i & 1) + 8 * hf) * XROW);
    else dma(lw2, ring_area(i % RING) + hf * 1024, vW2, rsWD, ksb(i) * 16 * XROW, (ksi(i) * 16 + 8 * hf) * XROW);
  };

  const unsigned aCSw = LB + L_CS + l31 * 16 + wr * 4 + GG * 2048, aCSr = LB + L_CS + l31 * 16 + GG * 2048;
  auto readX = [&](int n) AI { xf[n] = ldsr<bf16x8>(aX ^ (unsigned)((n & 3) << 5), ((n >> 2) & 3) * 16384); };
  auto readA = [&](int n, int j) AI {
    const unsigned o = ((n >> 2) & 3) * ((GG == 0 && j < 2) ? 8192 : 16384);
    wa[n & 1][j] = tr2(aA[j] ^ (unsigned)((n & 3) << 11), o, o + 512);
  };
  auto readB = [&](int n, int j) AI {
    const unsigned base = lc1_base(n >> 2);
    if ((n & 3) == 0) asm volatile("" : "+v"(aB[j]));
    wb[n % 3][j] = tr2(aB[j] ^ (((n & 3) << 11) | (base & 0x30000u)), base & 0xffffu, (base & 0xffffu) + 512);
  };
  WAITV(8);
  BAR(); MEMFENCE();
  STAMP(1);
  readX(0); readA(0, 0); readA(0, 1); readA(0, 2);
  SFOR(n, 28)
    SB();
    if constexpr (n == 27) {
#pragma unroll
      for (int j = 0; j < 3; j++) ldsw<unsigned>(aCSw, j * 512, hilo(csA[j][0]));
    }
    if constexpr ((n & 3) == 3) {
      if constexpr (n == 3) asm volatile("s_waitcnt vmcnt(11)" : "+v"(cpf) :: "memory");
      else if constexpr (n == 27) asm volatile("s_waitcnt vmcnt(5)" : "+v"(grn) :: "memory");
      else if constexpr (n == 23) WAITV(11); else WAITV(10);
      { WAITL0(); BAR(); MEMFENCE(); }
      if constexpr (n == 27) STAMP(2);
    }
    constexpr int bar = (n + 1) >> 2, ph = (n + 1) & 3;
#pragma unroll
    for (int j = 0; j < 3; j++) {
      if constexpr (n < 27) { if (j == 0) readX(n + 1); readA(n + 1, j); }
      else if (j < 2) { readB(j, 0); readB(j, 1); }
      SB();
      accA[j] = MFMA(wa[n & 1][j], xf[n], accA[j]);
      const int slot = 3 * ph + j;
      if (bar == 0) issueG(0, slot - 2);
      else if (bar <= 6) { if (slot == 0) issueG(bar, 0); if (slot == 2) issueG(bar, 1); if (slot == 5) issueG(bar, 2); if (slot == 7) issueG(bar, 3); if (slot == 10) issueG(bar, 4); }
      else if (j < 2) {
        if constexpr (AT) dma(lw2, L_FREE + j * 1024, lane * 16, mkrs(pphi, 16 * 1568), half * 8 * 1568 + w * 2048, j * 1024);
        else issueW2(0, j);
      }
      if ((n & 3) == 0) csA[j] = MFMA16(sel, wa[n & 1][j], csA[j]);
      if (n == 22 && j == 2) asm volatile("buffer_load_dwordx2 %0, %1, %2, %3 offen sc1" : "=v"(grn) : "v"(xrow * 8), "s"(rsWS), "s"(WS_GR + rb * 1024) : "memory");
      SB();
    }
  SEND

  RowC rc; bf16x8 xe; unsigned denb;
  auto rowconst = [&]() AI {
#pragma nounroll
    for (int it = 0; it < (1 << 16) && !ALL(grn[1] == E + 1); ++it) {
      __builtin_amdgcn_s_sleep(SLEEPN);
      asm volatile("buffer_load_dwordx2 %0, %1, %2, %3 offen sc1\n\ts_waitcnt vmcnt(0)" : "=v"(grn) : "v"(xrow * 8), "s"(rsWS), "s"(WS_GR + rb * 1024) : "memory");
    }
    const float den = U2F(grn[0] & 0xffff0000u);
    const unsigned nm = hh ? 0u : __builtin_amdgcn_perm(grn[0], grn[0], 0x01000100u);
    u32x4 t = {nm, nm, nm, nm}; xe = __builtin_bit_cast(bf16x8, t);
    denb = grn[0] >> 16;
    rc.r = __builtin_amdgcn_rcpf(den);
    const float k1 = -2.f * 1.4426950408889634f * 0.7978845608028654f;
    rc.K1 = k1 * rc.r; rc.K2 = (k1 * 0.044715f) * rc.r * (rc.r * rc.r); rc.Ri = den * den; rc.Lr = 1.4426950408889634f * rc.r;
  };
  auto fixmean = [&](f32x16& acc, int slot) AI { acc = MFMA(ldsr<bf16x8>(aCSr, slot * 512), xe, acc); };
  const unsigned rt = 4 * rb + wr;
  const unsigned sA2 = WS_A2 + rt * 144 * 1024 + (AT ? half * 16384 : c * 8192), vL16 = lane * 16;
  const unsigned sFown = WS_FH + (rt * 32 + (AT ? 28 + 2 * half : 2 * c) + GG) * FS;
  const unsigned aWP = LB + (4 * hu + q4) * 56 + 8 * pc;
  const unsigned T12 = (E + 1) & 0xfffu, T16 = (E + 1) & 0xffffu;
  const unsigned rt2 = 2 * (8 * grp + 4 * half + rbl);
  const unsigned sF = WS_FH + rt2 * 32 * FS;
  const unsigned fc = l31 >> 1;
  const bool nd1 = GG ? (fc >> 1) == wr : (fc >= 8 && fc < 14) || fc == 14 + (wr >> 1);
  const u32x4 sent = {~0u, ~0u, ~0u, ~0u};
  auto clr = [&](int ks) AI { st16(sent, rsWS, vL16, sA2, ks * 1024); };

  const unsigned aIW = LB + L_PHI + xrow * 64 + ((hu ^ ((xrow >> 1) & 7)) << 3);
  SmS sm;
  auto kstep = [&](auto s_) AI {
    constexpr int s = decltype(s_)::value;
    if constexpr (s == 0) sm.p = featproj(accA[0], aWP + L_WPK, 0, hh);
    if constexpr (s == 1 || s == 2) {
#pragma unroll
      for (int k = 2 * s - 2; k < 2 * s; k++) {
        u32x2 vv = {pk2(accA[1][4 * k], accA[1][4 * k + 1]), pk2(accA[1][4 * k + 2], accA[1][4 * k + 3])};
        if (k == 3) vv[0] = hh ? denb : vv[0];
        ldsw<u32x2>(aIW ^ (unsigned)(k << 4), L_V - L_PHI, vv);
      }
    }
    if constexpr (s == 3) sm.bx = -max16(sm.p) * rc.Lr;
    if constexpr (s == 4) sm.bn = min16(sm.p) * rc.Lr;
    if constexpr (s >= 5 && s < 13) {
      constexpr int i = 2 * (s - 5);
      sm.e[i] = __builtin_amdgcn_exp2f(__builtin_fmaf(sm.p[i], rc.Lr, sm.bx)); sm.e[i + 1] = __builtin_amdgcn_exp2f(__builtin_fmaf(sm.p[i + 1], rc.Lr, sm.bx));
      sm.en[i] = __builtin_amdgcn_exp2f(__builtin_fmaf(sm.p[i], -rc.Lr, sm.bn)); sm.en[i + 1] = __builtin_amdgcn_exp2f(__builtin_fmaf(sm.p[i + 1], -rc.Lr, sm.bn));
      if constexpr (i == 0) { sm.sp = sm.e[0] + sm.e[1]; sm.sn = sm.en[0] + sm.en[1]; }
      else if constexpr (i < 12) { sm.sp = (sm.sp + sm.e[i]) + sm.e[i + 1]; sm.sn = (sm.sn + sm.en[i]) + sm.en[i + 1]; }
      else if constexpr (i == 12) { sm.tp = sm.e[12] + sm.e[13]; sm.tn = sm.en[12] + sm.en[13]; }
      else { sm.tp = (sm.tp + sm.e[14]) + sm.e[15]; sm.tn = (sm.tn + sm.en[14]) + sm.en[15]; }
    }
    if constexpr (s == 13) {
      sm.sp = __builtin_amdgcn_rcpf(xsum(sm.sp + (hh ? 0.f : sm.tp))) * rc.r; sm.sn = __builtin_amdgcn_rcpf(xsum(sm.sn + (hh ? 0.f : sm.tn))) * rc.r;
    }
    if constexpr (s >= 14 && s < 18) {
      constexpr int k = s - 14;
      u32x2 t0 = {pk2s(sm.e[4 * k] * sm.sp, sm.e[4 * k + 1] * sm.sp), pk2s(sm.e[4 * k + 2] * sm.sp, sm.e[4 * k + 3] * sm.sp)};
      u32x2 t1 = {pk2s(sm.en[4 * k] * sm.sn, sm.en[4 * k + 1] * sm.sn), pk2s(sm.en[4 * k + 2] * sm.sn, sm.en[4 * k + 3] * sm.sn)};
      ldsw<u32x2>(aIW ^ (unsigned)(k << 4), 0, t0); ldsw<u32x2>(aIW ^ (unsigned)(k << 4), 8192, t1);
    }
  };
  auto qstep = [&](auto s_, f32x16& a, unsigned lh) AI {
    constexpr int s = decltype(s_)::value;
    if constexpr (s == 0) sm.p = featproj(a, aWP + L_FREE, lh * 1568, hh);
    if constexpr (s == 3) sm.bx = -max16(sm.p) * rc.Lr;
    if constexpr (s == 4) sm.bn = min16(sm.p) * rc.Lr;
    if constexpr (s >= 5 && s < 21) {
      constexpr int i = (s - 5) >> 1;
      float t0, t1;
      if constexpr ((s - 5) & 1) { t0 = __builtin_fmaf(sm.p[2 * i], -rc.Lr, sm.bn); t1 = __builtin_fmaf(sm.p[2 * i + 1], -rc.Lr, sm.bn); }
      else { t0 = __builtin_fmaf(sm.p[2 * i], rc.Lr, sm.bx); t1 = __builtin_fmaf(sm.p[2 * i + 1], rc.Lr, sm.bx); }
      asm volatile("" : "+v"(t0));
      a[8 * ((s - 5) & 1) + i] = U2F(pk2(__builtin_amdgcn_exp2f(t0), __builtin_amdgcn_exp2f(t1)));
      if constexpr (s == 20) asm volatile("" : "+v"(a));
    }
  };
  auto kvpart = [&]() AI {
    if (wr < 2) {
      f32x16 kv;
#pragma unroll
      for (int i = 0; i < 16; i++) kv[i] = 0.f;
      const unsigned r0 = 8 * hu + q4, u0 = 4 * blk + p4;
      const unsigned b0 = LB + L_PHI + r0 * 64 + ((u0 ^ (r0 >> 1)) << 3), b1 = LB + L_PHI + (r0 + 4) * 64 + ((u0 ^ ((r0 + 4) >> 1)) << 3);
#pragma unroll
      for (int h2 = 0; h2 < 2; h2++) {
        bf16x8 fa[4], fb[4];
#pragma unroll
        for (int s = 0; s < 4; s++) {
          const unsigned o = (4 * h2 + s) * 1024;
          fa[s] = __builtin_shufflevector(trd(b0 + wr * 8192 + o), trd(b1 + wr * 8192 + o), 0, 1, 2, 3, 4, 5, 6, 7);
          fb[s] = __builtin_shufflevector(trd(b0 + (L_V - L_PHI) + o), trd(b1 + (L_V - L_PHI) + o), 0, 1, 2, 3, 4, 5, 6, 7);
        }
#pragma unroll
        for (int s = 0; s < 4; s++) kv = MFMA(fa[s], fb[s], kv);
      }
#pragma unroll
      for (int k = 0; k < 4; k++) {
        u32x4 t;
#pragma unroll
        for (int i = 0; i < 4; i++) t[i] = (F2U(kv[4 * k + i]) & ~0xfffu) | T12;
        st16(t, rsWS, vL16, ((head * 16 + rb) * 2 + wr) * 4096, WS_KVP + k * 1024);
      }
    }
  };
  auto qexp2 = [&](f32x16& a0, unsigned l0, f32x16& a1, unsigned l1) AI {
    const f32x16 p0 = featproj(a0, aWP + L_FREE, l0 * 1568, hh), p1 = featproj(a1, aWP + L_FREE, l1 * 1568, hh);
    a0 = dualexpP(p0, rc.Lr); a1 = dualexpP(p1, rc.Lr); asm volatile("" : "+v"(a0), "+v"(a1));
  };

  unsigned fl1 = 0;
  EpiS es;
  u32x4 pv[8];
  const unsigned vKP = l31 * 16 + hu * 8192, sKP = (rb * 32 + half) * 4096 + wr * 1024 + GG * 512;
  if constexpr (GG == 0) __builtin_amdgcn_s_setprio(1);
  SFOR(n, 28)
    if constexpr (n == 27) { ldsw<unsigned>(aCSw, 3 * 512, hilo(csB[0][0])); ldsw<unsigned>(aCSw, 0, hilo(csB[1][0])); }
    if constexpr (n == 11) waitv<AT ? 0 : 4>();
    if constexpr (AT && n == 7) waitv<GG ? 10 : 6>();
    if constexpr ((n & 3) == 3 && n != 15) {
      if constexpr (n == 27 || n == 11) WAITL0(); else asm volatile("s_waitcnt lgkmcnt(4)" ::: "memory");
      BAR(); MEMFENCE();
    }
    if constexpr (n == 11) stflag(E + 1, rsWS, sFown);
    if constexpr (n == 11) { STAMP(3); if constexpr (GG == 0) { kvpart(); __builtin_amdgcn_s_setprio(0); } else __builtin_amdgcn_s_setprio(1); STAMP(4); }
    SFOR(j, 2)
      constexpr int q = 2 * n + j;
      if constexpr (n + 2 < 28) readB(n + 2, j);
      SB();
      accB[j] = MFMA(wb[n % 3][j], xf[n], accB[j]);
      if constexpr ((n & 3) == 0) csB[j] = MFMA16(sel, wb[n % 3][j], csB[j]);
      if constexpr (q == 0) rowconst();
      if constexpr (q == 1) { fixmean(accA[0], 0); fixmean(accA[1], 1); }
      if constexpr (q == 2) fixmean(accA[2], 2);
      if constexpr (GG == 0 && q >= 4 && q < 22) kstep(IntC<q - 4>{});
      if constexpr (AT) {
        if constexpr (GG == 0 && q < 6) clr(q < 2 ? 112 + q : 118 + q);
        if constexpr (GG == 1 && q < 10) clr(q < 6 ? 114 + q : 118 + q);
        if constexpr (GG == 0 && q >= 33 && q < 54) qstep(IntC<q - 33>{}, accA[2], 0);
        if constexpr (GG == 1 && q >= 14 && q < 35) qstep(IntC<q - 14>{}, accA[0], 1);
        if constexpr (GG == 1 && q >= 35) qstep(IntC<q - 35>{}, accA[1], 2);
      } else {
        if constexpr (GG == 0 && q < 3) clr(q == 0 ? 0 : 3 + q);
        if constexpr (GG == 1 && q < 5) clr(q < 3 ? 1 + q : 3 + q);
        if constexpr (GG == 0 && q >= 36 && q < 52) { epiSlice<((q - 36) >> 2) & 3, q & 3>(accA[2], rc, es); if constexpr (q == 51) st16(es.r, rsWS, vL16, sA2, 0); }
        if constexpr (GG == 1) {
          SFOR(u, cumG1(q + 1) - cumG1(q))
            constexpr int e = cumG1(q) + u;
            epiSlice<(e >> 2) & 3, e & 3>(accA[e >> 4], rc, es);
            if constexpr ((e & 15) == 15) st16(es.r, rsWS, vL16, sA2, (1 + (e >> 4)) * 1024);
          SEND
        }
        if constexpr (j == 0 && (n == 3 || n == 5)) issueW2(1, (n - 3) >> 1);
        if constexpr (j == 0 && (n == 8 || n == 10)) issueW2(2, (n - 8) >> 1);
        if constexpr (j == 0 && (n == 12 || n == 14)) issueW2(3, (n - 12) >> 1);
        if constexpr (q >= 38 && q < 44) issueW2(4 + ((q - 38) >> 1), q & 1);
        if constexpr (j == 0 && (n == 23 || n == 24)) issueW2(7, n - 23);
        if constexpr (q == 37) asm volatile("buffer_load_dword %0, %1, %2, %3 offen sc1" : "=v"(fl1) : "v"(nd1 ? lane * FS : 0x80000000u), "s"(rsWS), "s"(sF) : "memory");
        if constexpr (n == 27) issueW2(8, j);
      }
      SB();
    SEND
  SEND
  __builtin_amdgcn_s_setprio(0);
  fixmean(accB[0], 3); fixmean(accB[1], 0);

  if constexpr (AT) {
#pragma unroll
    for (int i = 0; i < 8; i++) pv[i] = ld16(rsWS, vKP, sKP, WS_KVP + i * 16384);
    if constexpr (GG) { accA[2] = dualexpP(featproj(accA[2], aWP + L_FREE, 3 * 1568, hh), rc.Lr); asm volatile("" : "+v"(accA[2])); }
    STAMP(5);
    waittag<16384>(pv, rsWS, vKP, sKP + WS_KVP, T12, 0xfffu);
    STAMP(6);
    {
      u32x4 o;
#pragma unroll
      for (int k = 0; k < 4; k++) {
        float sum = 0.f;
#pragma unroll
        for (int i = 0; i < 8; i++) sum += U2F(pv[i][k] ^ T12);
        sum = xsum(sum);
        sum = l31 == 29 ? 1.f : sum;
        if (GG == 1) sum = wr == 3 ? 0.f : sum;
        o[k] = (pk2(sum, sum) & 0xffff0000u) | T16;
      }
      st16(o, rsWS, hh ? 0x80000000u : l31 * 16, (rb * 2 + half) * 4096 + wr * 1024 + GG * 512, WS_KVR);
    }
    qexp2(accB[0], 4 + 2 * GG, accB[1], 5 + 2 * GG);
    const unsigned sKR = (8 * half + w) * 8192;
#pragma unroll
    for (int i = 0; i < 8; i++) pv[i] = ld16(rsWS, vL16, sKR, WS_KVR + i * 1024);
    STAMP(7);
    waittag<1024>(pv, rsWS, vL16, sKR + WS_KVR, T16, 0xffffu);
    STAMP(8);
#pragma unroll
    for (int i = 0; i < 4; i++) {
      u32x4 f = {__builtin_amdgcn_perm(pv[2 * i][1], pv[2 * i][0], 0x07060302u), __builtin_amdgcn_perm(pv[2 * i][3], pv[2 * i][2], 0x07060302u),
                 __builtin_amdgcn_perm(pv[2 * i + 1][1], pv[2 * i + 1][0], 0x07060302u), __builtin_amdgcn_perm(pv[2 * i + 1][3], pv[2 * i + 1][2], 0x07060302u)};
      ldsw<u32x4>(LB + w * 4096 + vL16, i * 1024, f);
    }
    WAITL0(); BAR(); MEMFENCE();
    auto ohead = [&](const f32x16& phi, unsigned lh) AI {
      f32x16 op, on;
#pragma unroll
      for (int i = 0; i < 16; i++) { op[i] = 0.f; on[i] = 0.f; }
#pragma unroll
      for (int i = 0; i < 2; i++) {
        op = MFMA(ldsr<bf16x8>(LB + vL16, lh * 4096 + i * 1024), sub4(phi, i), op);
        on = MFMA(ldsr<bf16x8>(LB + vL16, lh * 4096 + (2 + i) * 1024), sub4(phi, 2 + i), on);
      }
      const float ia = __builtin_amdgcn_rcpf(op[13]), ib = __builtin_amdgcn_rcpf(on[13]);
      const float id = __builtin_amdgcn_rcpf(__builtin_fmaf(op[12], ia, __builtin_fmaf(on[12], ib, 1e-6f)));
      const float ca = xhi(ia * id), cb = xhi(ib * id);
      const f32x16 o = op * ca + on * cb;
      u32x4 f0, f1;
#pragma unroll
      for (int i = 0; i < 4; i++) { f0[i] = pk2(o[2 * i], o[2 * i + 1]); f1[i] = pk2(o[8 + 2 * i], o[9 + 2 * i]); }
      f1[2] = hh ? 0u : f1[2]; f1[3] = hh ? 0u : f1[3];
      st16(f0, rsWS, vL16, sA2, (112 + 2 * lh) * 1024); st16(f1, rsWS, vL16, sA2, (113 + 2 * lh) * 1024);
    };
    ohead(accA[2], GG ? 3 : 0); ohead(accB[0], 4 + 2 * GG); ohead(accB[1], 5 + 2 * GG);
    if constexpr (GG) { ohead(accA[0], 1); ohead(accA[1], 2); }
    STAMP(9);
  } else {
    STAMP(5);
    const unsigned sA = WS_A2 + rt2 * 144 * 1024;
    unsigned ln = vL16; asm volatile("" : "+v"(ln));
    const unsigned aW = lw2 + (ln & 0x200) + ((ln & 0xc0) << 1) + ((((ln >> 3) & 0x20) | ((ln >> 1) & 0x10)) ^ ((ln >> 1) & 0x40)) + ((ln >> 1) & 8);
    f32x16 acc[2][2];
#pragma unroll
    for (int i = 0; i < 4; i++)
#pragma unroll
      for (int k = 0; k < 16; k++) acc[i >> 1][i & 1][k] = 0.f;
    u32x4 af[NS][2];
    u32x2 xr[2];
    auto loadA = [&](int i) AI { af[i][0] = ld16(rsWS, vL16, sA + ksb(i) * 1024, ksi(i) * 1024); af[i][1] = ld16(rsWS, vL16, sA + ksb(i) * 1024, (144 + ksi(i)) * 1024); };
    const unsigned nt = wr >> 1, mt = wr & 1;
    const unsigned vO = (32 * mt + ((ln >> 4) & 31)) * XROW + ((ln >> 6) & 8) + 32 * GG, sO = rt2 * 32 * XROW + ci * 128 + nt * 64;
    st16(epiP(accB[0], rc), rsWS, vL16, sA2, (4 + 2 * GG) * 1024);
    st16(epiP(accB[1], rc), rsWS, vL16, sA2, (5 + 2 * GG) * 1024);
    STAMP(6);
    asm volatile("s_waitcnt vmcnt(%c1)" : "+v"(fl1) : "i"(GG ? 14 : 13) : "memory");
    if (!ALL(!nd1 || fl1 == E + 1u)) poll(rsWS, lane * FS, sF, E, nd1 ? 1u : 0u);
#pragma unroll
    for (int j = 0; j < PD; j++) loadA(j);
#pragma unroll
    for (int k = 0; k < 2; k++) asm volatile("buffer_load_dwordx2 %0, %1, %2, %3 offen offset:%c4" : "=v"(xr[k]) : "v"(vO), "s"(rsX), "s"(sO), "i"(16 * k) : "memory");
    STAMP(7);
    SFOR(i, NS)
      asm volatile("s_waitcnt vmcnt(%c2)" : "+v"(af[i][0]), "+v"(af[i][1]) : "i"(later_ops(i, NS, 0, NS) + (i < PD ? 2 : 0)) : "memory");
      constexpr unsigned ra = ring_area(i % RING);
      const unsigned la = aW + (ra & 0x30000u);
      bf16x8 w0 = tr2(la, ra & 0xffffu, (ra & 0xffffu) + 1024), w1 = tr2(la ^ 64u, ra & 0xffffu, (ra & 0xffffu) + 1024);
      auto stale = [&]() AI {
        unsigned m = UMAX(UMAX(UMAX(af[i][0][0], af[i][0][1]), UMAX(af[i][0][2], af[i][0][3])), UMAX(UMAX(af[i][1][0], af[i][1][1]), UMAX(af[i][1][2], af[i][1][3])));
        return ANY(m == ~0u);
      };
      if (__builtin_expect(stale(), 0)) {
        constexpr int i1 = i + 1 < NS ? i + 1 : i, i2 = i + 2 < NS ? i + 2 : i1, i3 = i + 3 < NS ? i + 3 : i2;
        const unsigned o0 = sA + (ksb(i) + ksi(i)) * 1024;
        int it = 0;
#pragma nounroll
        do { __builtin_amdgcn_s_sleep(SLEEPR); reld<1>(af[i][0], af[i][1], af[i][0], af[i][1], af[i][0], af[i][1], af[i][0], af[i][1], rsWS, vL16, o0, o0, o0, o0); } while (++it < (1 << 14) && stale());
        if constexpr (i + 1 < NS) {
          __builtin_amdgcn_s_sleep(0);
          reld<(NS - 1 - i < 3 ? NS - 1 - i : 3)>(af[i1][0], af[i1][1], af[i2][0], af[i2][1], af[i3][0], af[i3][1], af[i3][0], af[i3][1], rsWS, vL16,
                sA + (ksb(i1) + ksi(i1)) * 1024, sA + (ksb(i2) + ksi(i2)) * 1024, sA + (ksb(i3) + ksi(i3)) * 1024, o0);
        }
      }
      if constexpr (i == 6) STAMP(8);
      if constexpr (i == 12) STAMP(9);
      SB();
      if constexpr (i + PD < NS) loadA(i + PD);
#pragma unroll
      for (int mm = 0; mm < 2; mm++) acc[0][mm] = MFMA(w0, __builtin_bit_cast(bf16x8, af[i][mm]), acc[0][mm]);
      if constexpr (i + RING < NS) { WAITL0(); SB(); issueW2(i + RING, 0); issueW2(i + RING, 1); }
#pragma unroll
      for (int mm = 0; mm < 2; mm++) acc[1][mm] = MFMA(w1, __builtin_bit_cast(bf16x8, af[i][mm]), acc[1][mm]);
      SB();
    SEND
    STAMP(10);
#pragma unroll
    for (int qd = 0; qd < 4; qd++)
#pragma unroll
      for (int k = 0; k < 4; k++) {
        const unsigned ra = ring_area(2 * qd + (k >> 1)) + (k & 1) * 1024;
        ldsw<bf16x8>(lw2 + vL16 + (ra & 0x30000u), ra & 0xffffu, sub4(acc[qd >> 1][qd & 1], k));
      }
    WAITL0(); BAR(); MEMFENCE();
    STAMP(12);
    const unsigned j2 = 2 * wr + GG;
    const unsigned ra = ((0x56140732u >> (4 * j2)) & 7) * 16384;
    asm volatile("" : "+v"(xr[0]), "+v"(xr[1]));
    const rsrc_t rsO = mkrs(pout, 2048 * XROW);
#pragma unroll
    for (int k = 0; k < 2; k++) {
      f32x4 sum = {U2F(xr[k][0] << 16), U2F(xr[k][0] & 0xffff0000u), U2F(xr[k][1] << 16), U2F(xr[k][1] & 0xffff0000u)};
#pragma unroll
      for (int v = 0; v < 8; v++) sum += ldsr<f32x4>(LB + ra + vL16, v * 2048 + k * 1024);
      u32x2 o = {pk2(sum[0], sum[1]), pk2(sum[2], sum[3])};
      __builtin_amdgcn_raw_buffer_store_b64(o, rsO, vO + 16 * k, sO, 0);
    }
    if (b == 32 && w == 0) stflag(E + 4, rsWS, WS_EPOCH);
  }
  STAMPS_OUT();
  };
  if (!attn && g) prog(BoolC<false>{}, IntC<1>{});
  else if (!attn) prog(BoolC<false>{}, IntC<0>{});
  else if (g) prog(BoolC<true>{}, IntC<1>{});
  else prog(BoolC<true>{}, IntC<0>{});
}

extern "C" void kernel_launch(void* const* d_in, const int* in_sizes, int n_in, void* d_out, int out_size, void* d_ws, size_t ws_size,
                              hipStream_t stream) {
  fused_kernel<<<256, 512, 0, stream>>>((const char*)d_in[0], (const char*)d_in[1], (const char*)d_in[5], (const char*)d_in[6], (char*)d_ws,
                                        (const char*)d_in[2], (const char*)d_in[7], (const char*)d_in[3], (char*)d_out);
}
```

```cpp
#define WK 0
#include <hip/hip_runtime.h>
#include <hip/hip_bf16.h>
#include <utility>

#define DI __device__ __forceinline__
#define LAS __attribute__((address_space(3)))
typedef __attribute__((ext_vector_type(8))) short bf16x8;
typedef __attribute__((ext_vector_type(4))) short s16x4;
typedef __attribute__((ext_vector_type(2))) __bf16 bf2;
typedef __attribute__((ext_vector_type(2))) float f32x2;
typedef __attribute__((ext_vector_type(4))) float f32x4;
typedef __attribute__((ext_vector_type(16))) float f32x16;
typedef __attribute__((ext_vector_type(2))) unsigned u32x2;
typedef __attribute__((ext_vector_type(4))) unsigned u32x4;
typedef __amdgpu_buffer_rsrc_t rsrc_t;
#define F2U(x) __builtin_bit_cast(unsigned, (float)(x))
#define U2F(x) __builtin_bit_cast(float, (unsigned)(x))
#define ALL(x) (__builtin_amdgcn_ballot_w64(!(x)) == 0ull)
#define ANY(x) (__builtin_amdgcn_ballot_w64(x) != 0ull)
#define UMAX(a, b) __builtin_elementwise_max((unsigned)(a), (unsigned)(b))

constexpr unsigned XROW = 896, QROW = 2688, FROW = 3584;
constexpr unsigned FS = 128;
constexpr unsigned WS_EPOCH = 0;
constexpr unsigned WS_GR = 4096;
constexpr unsigned WS_KVP = 131072;
constexpr unsigned WS_A2 = WS_KVP + 16 * 16 * 8192;
constexpr unsigned WS_ST = WS_A2 + 64 * 144 * 1024;
constexpr unsigned WS_FH = WS_ST + 262144;
constexpr unsigned WS_KVR = WS_FH + 2048 * FS;
constexpr unsigned WS_END = WS_KVR + 16 * 8192;
constexpr unsigned L_GU = 65536, L_KV = 131072;
constexpr unsigned L_TAIL = L_KV + 3 * 8192;
constexpr unsigned L_WPK = L_TAIL;
constexpr unsigned L_ST = L_TAIL + 2048;
constexpr unsigned L_CS = L_TAIL + 4096;
constexpr unsigned L_PHI = L_GU + 32768;
constexpr unsigned L_V = L_KV + 16384;
constexpr unsigned L_FREE = 32768;

#define MFMA(a, b, c) __builtin_amdgcn_mfma_f32_32x32x16_bf16((a), (b), (c), 0, 0, 0)
#define MFMA16(a, b, c) __builtin_amdgcn_mfma_f32_16x16x32_bf16((a), (b), (c), 0, 0, 0)
#define AI __attribute__((always_inline))
#define BAR() __builtin_amdgcn_s_barrier()
#define WAITV(n) asm volatile("s_waitcnt vmcnt(" #n ")" ::: "memory")
#define WAITL0() asm volatile("s_waitcnt lgkmcnt(0)" ::: "memory")
#define SB() __builtin_amdgcn_sched_barrier(0)
#define MEMFENCE() asm volatile("" ::: "memory")

template <int N> DI void waitv() { asm volatile("s_waitcnt vmcnt(%c0)" :: "i"(N) : "memory"); }

#define INSZ(x) (x)
DI rsrc_t mkrs(const void* p, unsigned bytes) { return __builtin_amdgcn_make_buffer_rsrc((void*)p, (short)0, (int)bytes, 0x00020000); }
DI void dma(unsigned lds, unsigned limm, unsigned voff, rsrc_t rs, unsigned soff, unsigned simm) {
  unsigned t;
  asm volatile("s_add_u32 m0, %1, %c5\n\ts_add_u32 %0, %4, %c6\n\tbuffer_load_dwordx4 %2, %3, %0 offen lds"
               : "=&s"(t) : "s"(lds), "v"(voff), "s"(rs), "s"(soff), "i"(limm), "i"(simm) : "memory", "scc");
}
DI s16x4 trd(unsigned a) { return __builtin_amdgcn_ds_read_tr16_b64_v4i16((LAS s16x4*)(size_t)a); }
DI bf16x8 tr2(unsigned a, unsigned off0, unsigned off1) {
  LAS char* p = (LAS char*)(size_t)a;
  s16x4 lo = __builtin_amdgcn_ds_read_tr16_b64_v4i16((LAS s16x4*)(p + off0));
  s16x4 hi = __builtin_amdgcn_ds_read_tr16_b64_v4i16((LAS s16x4*)(p + off1));
  return __builtin_shufflevector(lo, hi, 0, 1, 2, 3, 4, 5, 6, 7);
}
template <class T> DI T ldsr(unsigned a, unsigned off) { return *(LAS T*)((LAS char*)(size_t)a + off); }
template <class T> DI void ldsw(unsigned a, unsigned off, T v) { *(LAS T*)((LAS char*)(size_t)a + off) = v; }
DI unsigned pk2(float a, float b) { f32x2 f = {a, b}; return __builtin_bit_cast(unsigned, __builtin_convertvector(f, bf2)); }
DI float rbf(float a) { return U2F(pk2(a, a) << 16); }
DI float dot2(unsigned a, unsigned b, float c) { return __builtin_amdgcn_fdot2_f32_bf16(__builtin_bit_cast(bf2, a), __builtin_bit_cast(bf2, b), c, false); }
DI float xsum(float v) { auto r = __builtin_amdgcn_permlane32_swap(F2U(v), F2U(v), false, false); return U2F(r[0]) + U2F(r[1]); }
DI float xmax(float v) { auto r = __builtin_amdgcn_permlane32_swap(F2U(v), F2U(v), false, false); return __builtin_fmaxf(U2F(r[0]), U2F(r[1])); }
DI float xmin(float v) { auto r = __builtin_amdgcn_permlane32_swap(F2U(v), F2U(v), false, false); return __builtin_fminf(U2F(r[0]), U2F(r[1])); }
DI float xhi(float v) { auto r = __builtin_amdgcn_permlane32_swap(F2U(v), F2U(v), false, false); return U2F(r[1]); }
DI void st16(u32x4 v, rsrc_t rs, unsigned voff, unsigned soff, unsigned simm) {
  unsigned t;
  asm volatile("s_add_u32 %0, %4, %c5\n\tbuffer_store_dwordx4 %1, %2, %3, %0 offen sc1\n\ts_nop 1" : "=&s"(t) : "v"(v), "v"(voff), "s"(rs), "s"(soff), "i"(simm) : "memory", "scc");
}
DI void stflag(unsigned v, rsrc_t rs, unsigned soff) {
  unsigned voff = (__builtin_amdgcn_mbcnt_hi(~0u, __builtin_amdgcn_mbcnt_lo(~0u, 0u)) == 0u) ? 0u : 0x80000000u;
  __builtin_amdgcn_raw_buffer_store_b32(v, rs, voff, soff, 16);
}
#define SLEEPR 4
#define SLEEPN 2
DI u32x4 ld16(rsrc_t rs, unsigned voff, unsigned soff, unsigned simm) {
  u32x4 r; unsigned t;
  asm volatile("s_add_u32 %1, %4, %c5\n\tbuffer_load_dwordx4 %0, %2, %3, %1 offen sc1" : "=v"(r), "=&s"(t) : "v"(voff), "s"(rs), "s"(soff), "i"(simm) : "memory", "scc");
  return r;
}
template <unsigned STEP> DI void ld8w(u32x4 (&p)[8], rsrc_t rs, unsigned voff, unsigned soff) {
  unsigned t;
#define L1(k) "buffer_load_dwordx4 %" #k ", %9, %10, %8 offen sc1\n\ts_add_u32 %8, %8, %c12\n\t"
  asm volatile("s_mov_b32 %8, %11\n\t" L1(0) L1(1) L1(2) L1(3) L1(4) L1(5) L1(6) L1(7) "s_waitcnt vmcnt(0)"
               : "=&v"(p[0]), "=&v"(p[1]), "=&v"(p[2]), "=&v"(p[3]), "=&v"(p[4]), "=&v"(p[5]), "=&v"(p[6]), "=&v"(p[7]), "=&s"(t)
               : "v"(voff), "s"(rs), "s"(soff), "i"(STEP) : "memory", "scc");
#undef L1
}
DI bool tagged(const u32x4 (&p)[8], unsigned tag, unsigned mask) {
  unsigned chk = 0;
#pragma unroll
  for (int i = 0; i < 8; i++)
#pragma unroll
    for (int k = 0; k < 4; k++) chk |= p[i][k] ^ tag;
  return ALL((chk & mask) == 0u);
}
template <unsigned STEP> DI void waittag(u32x4 (&p)[8], rsrc_t rs, unsigned voff, unsigned soff, unsigned tag, unsigned mask) {
  asm volatile("s_waitcnt vmcnt(0)" : "+v"(p[0]), "+v"(p[1]), "+v"(p[2]), "+v"(p[3]), "+v"(p[4]), "+v"(p[5]), "+v"(p[6]), "+v"(p[7]) :: "memory");
#pragma nounroll
  for (int it = 0; it < (1 << 16) && !tagged(p, tag, mask); ++it) { __builtin_amdgcn_s_sleep(SLEEPN); ld8w<STEP>(p, rs, voff, soff); }
}
#define RL(a, b, so) "buffer_load_dwordx4 %" #a ", %[v], %[r], %" #so " offen sc1\n\ts_add_u32 %[t], %" #so ", 147456\n\tbuffer_load_dwordx4 %" #b ", %[v], %[r], %[t] offen sc1\n\t"
template <int N> DI void reld(u32x4& a0, u32x4& b0, u32x4& a1, u32x4& b1, u32x4& a2, u32x4& b2, u32x4& a3, u32x4& b3, rsrc_t rs, unsigned voff,
                              unsigned s0, unsigned s1, unsigned s2, unsigned s3) {
  unsigned t;
  if constexpr (N == 4) asm volatile(RL(0, 1, 9) RL(2, 3, 10) RL(4, 5, 11) RL(6, 7, 12) "s_waitcnt vmcnt(0)"
      : "+v"(a0), "+v"(b0), "+v"(a1), "+v"(b1), "+v"(a2), "+v"(b2), "+v"(a3), "+v"(b3), [t] "=&s"(t) : "s"(s0), "s"(s1), "s"(s2), "s"(s3), [v] "v"(voff), [r] "s"(rs) : "memory", "scc");
  if constexpr (N == 3) asm volatile(RL(0, 1, 7) RL(2, 3, 8) RL(4, 5, 9) "s_waitcnt vmcnt(0)"
      : "+v"(a0), "+v"(b0), "+v"(a1), "+v"(b1), "+v"(a2), "+v"(b2), [t] "=&s"(t) : "s"(s0), "s"(s1), "s"(s2), [v] "v"(voff), [r] "s"(rs) : "memory", "scc");
  if constexpr (N == 2) asm volatile(RL(0, 1, 5) RL(2, 3, 6) "s_waitcnt vmcnt(0)"
      : "+v"(a0), "+v"(b0), "+v"(a1), "+v"(b1), [t] "=&s"(t) : "s"(s0), "s"(s1), [v] "v"(voff), [r] "s"(rs) : "memory", "scc");
  if constexpr (N == 1) asm volatile(RL(0, 1, 3) "s_waitcnt vmcnt(0)"
      : "+v"(a0), "+v"(b0), [t] "=&s"(t) : "s"(s0), [v] "v"(voff), [r] "s"(rs) : "memory", "scc");
}
DI void poll(rsrc_t rs, unsigned voff, unsigned soff, unsigned E, unsigned need) {
  voff = need ? voff : 0x80000000u;
#pragma nounroll
  for (int it = 0; it < (1 << 16); ++it) {
    unsigned v;
    asm volatile("buffer_load_dword %0, %1, %2, %3 offen sc1\n\ts_waitcnt vmcnt(0)" : "=v"(v) : "v"(voff), "s"(rs), "s"(soff) : "memory");
    bool ok = (need == 0u) || ((v - E - need) <= (3u - need));
    if (ALL(ok)) break;
    __builtin_amdgcn_s_sleep(SLEEPN);
  }
}

template <bool B> struct BoolC { static constexpr bool value = B; };
template <int I> struct IntC { static constexpr int value = I; };
template <int... I, class F> DI void sfor_impl(std::integer_sequence<int, I...>, F&& f) { (f(IntC<I>{}), ...); }
template <int N, class F> DI void sfor(F&& f) { sfor_impl(std::make_integer_sequence<int, N>{}, f); }
#define SFOR(n, N) sfor<N>([&](auto n##_) AI { constexpr int n = decltype(n##_)::value;
#define SEND });
struct RowC { float r, K1, K2, Ri, Lr; };

DI unsigned hilo(float v) { float hi = rbf(v); return pk2(hi, v - hi); }
DI float max3(float a, float b, float c) { return __builtin_fmaxf(__builtin_fmaxf(a, b), c); }
DI float min3(float a, float b, float c) { return __builtin_fminf(__builtin_fminf(a, b), c); }
DI unsigned pk2s(float a, float b) { unsigned r; asm("v_cvt_pk_bf16_f32 %0, %1, %2" : "=v"(r) : "v"(a), "v"(b)); return r; }
DI f32x2 sub2(const f32x16& v, int k) { f32x2 t = {v[2 * k], v[2 * k + 1]}; return t; }
DI f32x2 exp2v(f32x2 v) { f32x2 r = {__builtin_amdgcn_exp2f(v[0]), __builtin_amdgcn_exp2f(v[1])}; return r; }
DI f32x2 fma2(f32x2 a, float b, float c) { f32x2 bb = {b, b}, cc = {c, c}; return __builtin_elementwise_fma(a, bb, cc); }
DI u32x4 epiP(const f32x16& a, const RowC& c) {
  u32x4 r;
#pragma unroll
  for (int i = 0; i < 4; i++) {
    const f32x2 g = sub2(a, i), d = fma2(exp2v(g * fma2(g * g, c.K2, c.K1)), c.Ri, c.Ri);
    const f32x2 s = {__builtin_amdgcn_rcpf(d[0]), __builtin_amdgcn_rcpf(d[1])}, h = (g * sub2(a, 4 + i)) * s;
    r[i] = pk2(h[0], h[1]);
  }
  return r;
}
struct EpiS { float x0, x1, m0, m1; u32x4 r; };
template <int P, int PH> DI void epiSlice(const f32x16& a, const RowC& c, EpiS& s) {
  const float g0 = a[2 * P], g1 = a[2 * P + 1];
  if constexpr (PH == 0) { s.x0 = g0 * __builtin_fmaf(g0 * g0, c.K2, c.K1); s.x1 = g1 * __builtin_fmaf(g1 * g1, c.K2, c.K1); }
  if constexpr (PH == 1) { s.x0 = __builtin_amdgcn_exp2f(s.x0); s.x1 = __builtin_amdgcn_exp2f(s.x1); s.m0 = g0 * a[8 + 2 * P]; s.m1 = g1 * a[9 + 2 * P]; }
  if constexpr (PH == 2) { s.x0 = __builtin_amdgcn_rcpf(__builtin_fmaf(s.x0, c.Ri, c.Ri)); s.x1 = __builtin_amdgcn_rcpf(__builtin_fmaf(s.x1, c.Ri, c.Ri)); }
  if constexpr (PH == 3) s.r[P] = pk2s(s.m0 * s.x0, s.m1 * s.x1);
}
DI f32x16 featproj(const f32x16& a, unsigned wpaddr, unsigned off, bool hh) {
  u32x4 q0, q1;
#pragma unroll
  for (int i = 0; i < 4; i++) { q0[i] = pk2(a[2 * i], a[2 * i + 1]); q1[i] = pk2(a[8 + 2 * i], a[9 + 2 * i]); }
  q1[2] = hh ? 0u : q1[2]; q1[3] = hh ? 0u : q1[3];
  bf16x8 w0 = tr2(wpaddr, off, off + 8 * 56), w1 = tr2(wpaddr, off + 16 * 56, off + 24 * 56);
  f32x16 z;
#pragma unroll
  for (int i = 0; i < 16; i++) z[i] = 0.f;
  z = MFMA(w0, __builtin_bit_cast(bf16x8, q0), z);
  z = MFMA(w1, __builtin_bit_cast(bf16x8, q1), z);
  return z;
}
DI float max16(const f32x16& p) { float m = max3(p[0], p[1], p[2]); _Pragma("unroll") for (int i = 3; i < 15; i += 2) m = max3(m, p[i], p[i + 1]); return xmax(__builtin_fmaxf(m, p[15])); }
DI float min16(const f32x16& p) { float m = min3(p[0], p[1], p[2]); _Pragma("unroll") for (int i = 3; i < 15; i += 2) m = min3(m, p[i], p[i + 1]); return xmin(__builtin_fminf(m, p[15])); }
DI f32x16 dualexpP(const f32x16& p, float Lr) {
  const float bx = -max16(p) * Lr, bn = min16(p) * Lr;
  f32x16 o;
#pragma unroll
  for (int i = 0; i < 8; i++) {
    const f32x2 ep = exp2v(fma2(sub2(p, i), Lr, bx)), en = exp2v(fma2(sub2(p, i), -Lr, bn));
    o[i] = U2F(pk2(ep[0], ep[1])); o[8 + i] = U2F(pk2(en[0], en[1]));
  }
  return o;
}
struct SmS { f32x16 p, e; float en[16], bx, bn, sp, sn, tp, tn; };
DI bf16x8 sub4(const f32x16& v, int k) { f32x4 t = {v[4 * k], v[4 * k + 1], v[4 * k + 2], v[4 * k + 3]}; return __builtin_bit_cast(bf16x8, t); }

DI constexpr unsigned lc1_base(int ch) { return ch == 6 ? L_KV : ((ch & 1) ? L_GU : 0u) + (ch < 2 ? 49152u : ch < 4 ? 0u : 16384u); }
template <int CTRL, int RM> DI float dppadd(float v) { return v + U2F(__builtin_amdgcn_update_dpp(0u, F2U(v), CTRL, RM, 0xf, false)); }
DI float wavesum63(float v) {
  v = dppadd<0x111, 0xf>(v); v = dppadd<0x112, 0xf>(v); v = dppadd<0x114, 0xf>(v); v = dppadd<0x118, 0xf>(v);
  v = dppadd<0x142, 0xa>(v); return dppadd<0x143, 0xc>(v);
}

#define STAMP(i)
#define STAMPS_OUT()
#define CSTAMP(i)

constexpr int PD = 4, RING = 9;
constexpr int later_ops(int m, int n, int n0, int NS) {
  int cnt = 0; bool seen = false;
  for (int j = 0; j < PD && j < n; j++) { if (seen) cnt += 2; if (j == m) seen = true; }
  for (int st = 0; st < m; st++) {
    if (st + PD < n) { if (seen) cnt += 2; if (st + PD == m) seen = true; }
    if (n0 + st + RING < NS && seen) cnt += 2;
  }
  return cnt;
}
DI constexpr unsigned ring_area(int j) { return j == 0 ? L_FREE : j <= 5 ? lc1_base(j - 1) : j == 6 ? L_PHI : lc1_base(j - 2); }
constexpr int cumG1(int s) { return s < 4 ? 0 : s < 22 ? (s - 4) / 2 : (9 + (s - 22) * 5 / 4 < 48 ? 9 + (s - 22) * 5 / 4 : 48); }

#define CODE_BYTES 65536
__global__ __launch_bounds__(512, 2) void fused_kernel(const char* px_, const char* pqkv_, const char* pwg_, const char* pwu_, char* ws_, const char* pphi_,
                                                       const char* pwd_, const char* pwo_, char* pout_) {
  typedef __attribute__((ext_vector_type(8))) unsigned long long u64x8;
  u64x8 ka; unsigned long long kb;
  asm volatile("s_load_dwordx16 %0, %2, 0\n\ts_load_dwordx2 %1, %2, 64" : "=&s"(ka), "=&s"(kb) : "s"(__builtin_amdgcn_kernarg_segment_ptr()));
  __shared__ __attribute__((aligned(16))) char smem[163840];
  const unsigned LB = (unsigned)(size_t)smem;
  const unsigned tid = __builtin_amdgcn_workitem_id_x(), lane = tid & 63;
  const unsigned w = __builtin_amdgcn_readfirstlane(tid >> 6), g = w >> 2, wr = w & 3;
  const unsigned b = __builtin_amdgcn_workgroup_id_x(), xcd = b & 7, bi = b >> 3, grp = xcd >> 1, half = xcd & 1, rbl = bi & 3, cp = bi >> 2, ci = cp - 1;
  const unsigned rb = 4 * grp + rbl, c = 7 * half + ci;
  const unsigned head = 8 * half + (((half ? 0x76425310u : 0x75316420u) >> (4 * cp)) & 7);
  const bool attn = cp == 0;
  STAMP(0); CSTAMP(14);
  const unsigned wstr = attn ? QROW : FROW;
  const unsigned sG0 = half * (attn ? 448 : 1792) + (attn ? 0 : ci * 256), sX0 = rb * 128 * XROW;
  const unsigned r8 = lane >> 3, c8 = lane & 7, sa = (r8 >> 1) & 1, lc = c8 ^ (4 * sa);
  unsigned vx0 = (16 * w + r8) * XROW + ((c8 ^ (r8 >> 1)) << 4), vx1 = (vx0 ^ 64u) + 8 * XROW;
  const unsigned cg = attn ? lc : (c8 ^ (2 * sa)), cu = attn ? (lc >= 6 ? lc - 2 : lc) : (cg ^ 4);
  unsigned vG = (8 * w + r8) * wstr + (cg << 4), vU = (8 * w + r8) * wstr + (cu << 4);
  unsigned vKV = (8 * w + r8) * QROW + 896 * (1 + (lc >> 2)) + ((lc & 3) << 4) + ((56 * head) & ~15u);
  const unsigned lw1 = LB + w * 1024, lw2 = LB + w * 2048;
#define ROT (((0x24924u >> (2 * (cp + rbl))) & 3) * 2)
  const unsigned rot = ROT;
  const unsigned srow = 128 * rb + 64 * half + 8 * cp + w;
  unsigned vrow = lane < 56 ? lane * 16 : 0x80000000u;
  asm volatile("s_waitcnt lgkmcnt(0)" : "+s"(ka), "+s"(kb), "+v"(vx0), "+v"(vx1), "+v"(vG), "+v"(vU), "+v"(vKV), "+v"(vrow));
  const char *px = (const char*)ka[0], *pqkv = (const char*)ka[1], *pwg = (const char*)ka[2], *pwu = (const char*)ka[3], *pphi = (const char*)ka[5],
             *pwd = (const char*)ka[6], *pwo = (const char*)ka[7];
  char *ws = (char*)ka[4], *pout = (char*)kb;
  const rsrc_t rsX = mkrs(px, INSZ(2048 * XROW)), rsQ = mkrs(pqkv, INSZ(448 * QROW));
  const rsrc_t rsG = mkrs(attn ? pqkv : pwg, INSZ(448 * wstr)), rsU = mkrs(attn ? pqkv + 128 : pwu, INSZ(448 * wstr - (attn ? 128 : 0)));
  auto cidx = [&](int st) AI -> unsigned { unsigned t = rot + st; return t >= 7 ? t - 7 : t; };
  auto issueA = [&](int st, int k) AI {
    const unsigned sl = st & 3, ch = cidx(st);
    if (k == 0) dma(lw2, sl * 16384, vx0, rsX, sX0 + ch * 128, 0);
    if (k == 1) dma(lw2, sl * 16384 + 1024, vx1, rsX, sX0 + ch * 128, 0);
    if (k == 2) dma(lw1, L_GU + sl * 16384, vG, rsG, sG0 + ch * 64 * wstr, 0);
    if (k == 3) dma(lw1, L_GU + sl * 16384 + 8192, vU, rsU, sG0 + ch * 64 * wstr, 0);
    if (k == 4) dma(lw1, L_KV + sl * 8192, vKV, rsQ, ch * 64 * QROW, 0);
  };
  const rsrc_t rsWS = mkrs(ws, WS_END);
  unsigned Ev, cpf; u32x4 xrw;
  asm volatile("buffer_load_dwordx4 %0, %1, %2, %3 offen" : "=v"(xrw) : "v"(vrow), "s"(rsX), "s"(srow * XROW) : "memory");
  asm volatile("buffer_load_dword %0, off, %1, 0 sc1" : "=v"(Ev) : "s"(rsWS) : "memory");
#pragma unroll
  for (int k = 0; k < 5; k++) issueA(0, k);
  {
    unsigned long long pc; asm volatile("s_getpc_b64 %0" : "=s"(pc));
    const unsigned vo = w == 0 && lane < 24 ? (bi * 24 + lane) * 128 : 0x80000000u;
    asm volatile("buffer_load_dword %0, %1, %2, 0 offen" : "=v"(cpf) : "v"(vo), "s"(mkrs((const void*)pc, CODE_BYTES)) : "memory");
  }
#pragma unroll
  for (int k = 0; k < 5; k++) issueA(1, k);
  issueA(2, 0);
  asm volatile("s_waitcnt vmcnt(12)" : "+v"(xrw), "+v"(Ev) :: "memory");
  const unsigned E = __builtin_amdgcn_readfirstlane(Ev);
  {
    float s1 = 0.f, s2 = 0.f;
#pragma unroll
    for (int i = 0; i < 4; i++) { s1 = dot2(xrw[i], 0x3f803f80u, s1); s2 = dot2(xrw[i], xrw[i], s2); }
    s1 = wavesum63(s1); s2 = wavesum63(s2);
    const float mean = s1 * (1.f / 448.f), var = __builtin_fmaxf(s2 * (1.f / 448.f) - mean * mean, 0.f);
    u32x2 gr = {pk2(-mean, __builtin_amdgcn_sqrtf(rbf(rbf(var) + 1e-5f))), E + 1};
    __builtin_amdgcn_raw_buffer_store_b64(gr, rsWS, lane == 63 ? 0u : 0x80000000u, WS_GR + srow * 8, 16);
  }

  auto prog = [&](auto at_, auto gg_) AI {
  constexpr bool AT = decltype(at_)::value; constexpr int GG = decltype(gg_)::value;
  constexpr unsigned lcstep = AT ? 224 : 128;
  auto issueL = [&](int hc) AI {
    const int st = hc >> 1; const unsigned ch = cidx(st);
    if (hc & 1) dma(lw1, lc1_base(st) + 8192, vU, rsU, sG0 + ch * 64 * wstr, lcstep);
    else dma(lw1, lc1_base(st), vG, rsG, sG0 + ch * 64 * wstr, lcstep);
  };
  auto issueG = [&](int bar, int k) AI {
    if (bar == 0) issueA(2 + k / 5, k % 5);
    else if (bar <= 3) issueA(bar + 3, k);
    else if (k < 4) issueL(4 * (bar - 4) + k);
    else if (bar == 4) dma(LB + (w & 1) * 1024, L_WPK, lane * 16, mkrs(pphi, 16 * 1568), head * 1568 + (w & 1) * 1024, 0);
    else issueL(12 + (bar - 5));
  };

  const bool hh = lane >= 32;
  const unsigned hu = lane >> 5, blk = (lane >> 4) & 1, q4 = (lane >> 2) & 3, p4 = lane & 3, ta = (q4 >> 1) & 1, l31 = lane & 31;
  const unsigned perm = wr ^ (2 * GG);
  const unsigned rowoff = (8 * hu + q4) * 128 + (perm << 11);
  const unsigned pc = (4 * blk + p4) < 6 ? (4 * blk + p4) : 6;
  auto tileaddr = [&](unsigned t) AI -> unsigned {
    if constexpr (AT) { unsigned o = 56 * t + 8 * pc; return (o >> 7) * 8192 + ((((o >> 4) & 7) ^ (4 * ta)) << 4) + (o & 8) + rowoff; }
    else return blk * 8192 + (((2 * t + (p4 >> 1)) ^ (4 * blk + 2 * ta)) << 4) + 8 * (p4 & 1) + rowoff;
  };
  unsigned aA[3], aB[2];
  {
    unsigned o = (head & 1) * 8 + 8 * pc;
    unsigned ak = L_KV + (((o >> 4) ^ (4 * ta)) << 4) + (o & 8) + rowoff;
    aA[0] = LB + (GG ? L_GU + tileaddr(1) : ak);
    aA[1] = LB + (GG ? L_GU + tileaddr(2) : (ak ^ 64u));
    aA[2] = LB + L_GU + tileaddr(GG ? 3 : 0);
    aB[0] = LB + tileaddr(2 * GG); aB[1] = LB + tileaddr(2 * GG + 1);
  }
  const unsigned xrow = 32 * wr + l31;
  const unsigned aX = LB + xrow * 128 + (((2 * perm + hu) ^ ((xrow >> 1) & 7)) << 4);
  const unsigned selv = (((lane >> 4) ^ (lane >> 2)) & 1) ? 0u : 0x3f803f80u;
  const u32x4 sel4 = {selv, selv, selv, selv};
  const bf16x8 sel = __builtin_bit_cast(bf16x8, sel4);

  bf16x8 xf[28], wa[2][3], wb[3][2];
  f32x16 accA[3], accB[2];
  f32x4 csA[3], csB[2];
#pragma unroll
  for (int j = 0; j < 3; j++) { for (int i = 0; i < 16; i++) accA[j][i] = 0.f; for (int i = 0; i < 4; i++) csA[j][i] = 0.f; }
#pragma unroll
  for (int j = 0; j < 2; j++) { for (int i = 0; i < 16; i++) accB[j][i] = 0.f; for (int i = 0; i < 4; i++) csB[j][i] = 0.f; }
  u32x2 grn;

  const rsrc_t rsWD = mkrs(pwd, INSZ(1792 * XROW)), rsWO = mkrs(pwo, INSZ(448 * XROW));
  const unsigned vW2 = r8 * XROW + (lc << 4) + ci * 128;
  constexpr int NS = GG ? 16 : 20;
#define RHO ((rbl + cp) & 3)
  const unsigned rho = RHO;
  auto t_of = [](int i) constexpr { return (0x76543021u >> (4 * (i >> 1))) & 7; };
  auto ksb = [&](int i) AI -> unsigned {
    if (GG) return 16 * wr + ((t_of(i) & 3) ^ rho);
    if (i < 12) { const unsigned cc = i % 6 + rho + (rho >> 1); return wr + 8 * (cc >= 6 ? cc - 6 : cc); }
    return 8 * wr + 2 * (((i - 12) >> 1) ^ rho);
  };
  auto ksi = [&](int i) AI -> unsigned { return GG ? 8 * (i & 1) + (t_of(i) & 4) : i < 12 ? 64 + 4 * (i / 6) : 112 + (i & 1); };
  auto issueW2 = [&](int i, int hf) AI {
    if (GG == 0 && i >= 12) dma(lw2, ring_area(i % RING) + hf * 1024, vW2, rsWO, ksb(i) * 14 * XROW, (16 * (i & 1) + 8 * hf) * XROW);
    else dma(lw2, ring_area(i % RING) + hf * 1024, vW2, rsWD, ksb(i) * 16 * XROW, (ksi(i) * 16 + 8 * hf) * XROW);
  };

  const unsigned aCSw = LB + L_CS + l31 * 16 + wr * 4 + GG * 2048, aCSr = LB + L_CS + l31 * 16 + GG * 2048;
  auto readX = [&](int n) AI { xf[n] = ldsr<bf16x8>(aX ^ (unsigned)((n & 3) << 5), ((n >> 2) & 3) * 16384); };
  auto readA = [&](int n, int j) AI {
    const unsigned o = ((n >> 2) & 3) * ((GG == 0 && j < 2) ? 8192 : 16384);
    wa[n & 1][j] = tr2(aA[j] ^ (unsigned)((n & 3) << 11), o, o + 512);
  };
  auto readB = [&](int n, int j) AI {
    const unsigned base = lc1_base(n >> 2);
    if ((n & 3) == 0) asm volatile("" : "+v"(aB[j]));
    wb[n % 3][j] = tr2(aB[j] ^ (((n & 3) << 11) | (base & 0x30000u)), base & 0xffffu, (base & 0xffffu) + 512);
  };
  WAITV(8);
  BAR(); MEMFENCE();
  STAMP(1);
  readX(0); readA(0, 0); readA(0, 1); readA(0, 2);
  SFOR(n, 28)
    SB();
    if constexpr (n == 27) {
#pragma unroll
      for (int j = 0; j < 3; j++) ldsw<unsigned>(aCSw, j * 512, hilo(csA[j][0]));
    }
    if constexpr ((n & 3) == 3) {
      if constexpr (n == 3) asm volatile("s_waitcnt vmcnt(11)" : "+v"(cpf) :: "memory");
      else if constexpr (n == 27) asm volatile("s_waitcnt vmcnt(5)" : "+v"(grn) :: "memory");
      else if constexpr (n == 23) WAITV(11); else WAITV(10);
      { WAITL0(); BAR(); MEMFENCE(); }
      if constexpr (n == 27) STAMP(2);
    }
    constexpr int bar = (n + 1) >> 2, ph = (n + 1) & 3;
#pragma unroll
    for (int j = 0; j < 3; j++) {
      if constexpr (n < 27) { if (j == 0) readX(n + 1); readA(n + 1, j); }
      else if (j < 2) { readB(j, 0); readB(j, 1); }
      SB();
      accA[j] = MFMA(wa[n & 1][j], xf[n], accA[j]);
      const int slot = 3 * ph + j;
      if (bar == 0) issueG(0, slot - 2);
      else if (bar <= 6) { if (slot == 0) issueG(bar, 0); if (slot == 2) issueG(bar, 1); if (slot == 5) issueG(bar, 2); if (slot == 7) issueG(bar, 3); if (slot == 10) issueG(bar, 4); }
      else if (j < 2) {
        if constexpr (AT) dma(lw2, L_FREE + j * 1024, lane * 16, mkrs(pphi, 16 * 1568), half * 8 * 1568 + w * 2048, j * 1024);
        else issueW2(0, j);
      }
      if ((n & 3) == 0) csA[j] = MFMA16(sel, wa[n & 1][j], csA[j]);
      if (n == 22 && j == 2) asm volatile("buffer_load_dwordx2 %0, %1, %2, %3 offen sc1" : "=v"(grn) : "v"(xrow * 8), "s"(rsWS), "s"(WS_GR + rb * 1024) : "memory");
      SB();
    }
  SEND

  RowC rc; bf16x8 xe; unsigned denb;
  auto rowconst = [&]() AI {
#pragma nounroll
    for (int it = 0; it < (1 << 16) && !ALL(grn[1] == E + 1); ++it) {
      __builtin_amdgcn_s_sleep(SLEEPN);
      asm volatile("buffer_load_dwordx2 %0, %1, %2, %3 offen sc1\n\ts_waitcnt vmcnt(0)" : "=v"(grn) : "v"(xrow * 8), "s"(rsWS), "s"(WS_GR + rb * 1024) : "memory");
    }
    const float den = U2F(grn[0] & 0xffff0000u);
    const unsigned nm = hh ? 0u : __builtin_amdgcn_perm(grn[0], grn[0], 0x01000100u);
    u32x4 t = {nm, nm, nm, nm}; xe = __builtin_bit_cast(bf16x8, t);
    denb = grn[0] >> 16;
    rc.r = __builtin_amdgcn_rcpf(den);
    const float k1 = -2.f * 1.4426950408889634f * 0.7978845608028654f;
    rc.K1 = k1 * rc.r; rc.K2 = (k1 * 0.044715f) * rc.r * (rc.r * rc.r); rc.Ri = den * den; rc.Lr = 1.4426950408889634f * rc.r;
  };
  auto fixmean = [&](f32x16& acc, int slot) AI { acc = MFMA(ldsr<bf16x8>(aCSr, slot * 512), xe, acc); };
  const unsigned rt = 4 * rb + wr;
  const unsigned sA2 = WS_A2 + rt * 144 * 1024 + (AT ? half * 16384 : c * 8192), vL16 = lane * 16;
  const unsigned sFown = WS_FH + (rt * 32 + (AT ? 28 + 2 * half : 2 * c) + GG) * FS;
  const unsigned aWP = LB + (4 * hu + q4) * 56 + 8 * pc;
  const unsigned T12 = (E + 1) & 0xfffu, T16 = (E + 1) & 0xffffu;
  const unsigned rt2 = 2 * (8 * grp + 4 * half + rbl);
  const unsigned sF = WS_FH + rt2 * 32 * FS;
  const unsigned fc = l31 >> 1;
  const bool nd1 = GG ? (fc >> 1) == wr : (fc >= 8 && fc < 14) || fc == 14 + (wr >> 1);
  const u32x4 sent = {~0u, ~0u, ~0u, ~0u};
  auto clr = [&](int ks) AI { st16(sent, rsWS, vL16, sA2, ks * 1024); };

  const unsigned aIW = LB + L_PHI + xrow * 64 + ((hu ^ ((xrow >> 1) & 7)) << 3);
  SmS sm;
  auto kstep = [&](auto s_) AI {
    constexpr int s = decltype(s_)::value;
    if constexpr (s == 0) sm.p = featproj(accA[0], aWP + L_WPK, 0, hh);
    if constexpr (s == 1 || s == 2) {
#pragma unroll
      for (int k = 2 * s - 2; k < 2 * s; k++) {
        u32x2 vv = {pk2(accA[1][4 * k], accA[1][4 * k + 1]), pk2(accA[1][4 * k + 2], accA[1][4 * k + 3])};
        if (k == 3) vv[0] = hh ? denb : vv[0];
        ldsw<u32x2>(aIW ^ (unsigned)(k << 4), L_V - L_PHI, vv);
      }
    }
    if constexpr (s == 3) sm.bx = -max16(sm.p) * rc.Lr;
    if constexpr (s == 4) sm.bn = min16(sm.p) * rc.Lr;
    if constexpr (s >= 5 && s < 13) {
      constexpr int i = 2 * (s - 5);
      sm.e[i] = __builtin_amdgcn_exp2f(__builtin_fmaf(sm.p[i], rc.Lr, sm.bx)); sm.e[i + 1] = __builtin_amdgcn_exp2f(__builtin_fmaf(sm.p[i + 1], rc.Lr, sm.bx));
      sm.en[i] = __builtin_amdgcn_exp2f(__builtin_fmaf(sm.p[i], -rc.Lr, sm.bn)); sm.en[i + 1] = __builtin_amdgcn_exp2f(__builtin_fmaf(sm.p[i + 1], -rc.Lr, sm.bn));
      if constexpr (i == 0) { sm.sp = sm.e[0] + sm.e[1]; sm.sn = sm.en[0] + sm.en[1]; }
      else if constexpr (i < 12) { sm.sp = (sm.sp + sm.e[i]) + sm.e[i + 1]; sm.sn = (sm.sn + sm.en[i]) + sm.en[i + 1]; }
      else if constexpr (i == 12) { sm.tp = sm.e[12] + sm.e[13]; sm.tn = sm.en[12] + sm.en[13]; }
      else { sm.tp = (sm.tp + sm.e[14]) + sm.e[15]; sm.tn = (sm.tn + sm.en[14]) + sm.en[15]; }
    }
    if constexpr (s == 13) {
      sm.sp = __builtin_amdgcn_rcpf(xsum(sm.sp + (hh ? 0.f : sm.tp))) * rc.r; sm.sn = __builtin_amdgcn_rcpf(xsum(sm.sn + (hh ? 0.f : sm.tn))) * rc.r;
    }
    if constexpr (s >= 14 && s < 18) {
      constexpr int k = s - 14;
      u32x2 t0 = {pk2s(sm.e[4 * k] * sm.sp, sm.e[4 * k + 1] * sm.sp), pk2s(sm.e[4 * k + 2] * sm.sp, sm.e[4 * k + 3] * sm.sp)};
      u32x2 t1 = {pk2s(sm.en[4 * k] * sm.sn, sm.en[4 * k + 1] * sm.sn), pk2s(sm.en[4 * k + 2] * sm.sn, sm.en[4 * k + 3] * sm.sn)};
      ldsw<u32x2>(aIW ^ (unsigned)(k << 4), 0, t0); ldsw<u32x2>(aIW ^ (unsigned)(k << 4), 8192, t1);
    }
  };
  auto qstep = [&](auto s_, f32x16& a, unsigned lh) AI {
    constexpr int s = decltype(s_)::value;
    if constexpr (s == 0) sm.p = featproj(a, aWP + L_FREE, lh * 1568, hh);
    if constexpr (s == 3) sm.bx = -max16(sm.p) * rc.Lr;
    if constexpr (s == 4) sm.bn = min16(sm.p) * rc.Lr;
    if constexpr (s >= 5 && s < 21) {
      constexpr int i = (s - 5) >> 1;
      float t0, t1;
      if constexpr ((s - 5) & 1) { t0 = __builtin_fmaf(sm.p[2 * i], -rc.Lr, sm.bn); t1 = __builtin_fmaf(sm.p[2 * i + 1], -rc.Lr, sm.bn); }
      else { t0 = __builtin_fmaf(sm.p[2 * i], rc.Lr, sm.bx); t1 = __builtin_fmaf(sm.p[2 * i + 1], rc.Lr, sm.bx); }
      asm volatile("" : "+v"(t0));
      a[8 * ((s - 5) & 1) + i] = U2F(pk2(__builtin_amdgcn_exp2f(t0), __builtin_amdgcn_exp2f(t1)));
      if constexpr (s == 20) asm volatile("" : "+v"(a));
    }
  };
  auto kvpart = [&]() AI {
    if (wr < 2) {
      f32x16 kv;
#pragma unroll
      for (int i = 0; i < 16; i++) kv[i] = 0.f;
      const unsigned r0 = 8 * hu + q4, u0 = 4 * blk + p4;
      const unsigned b0 = LB + L_PHI + r0 * 64 + ((u0 ^ (r0 >> 1)) << 3), b1 = LB + L_PHI + (r0 + 4) * 64 + ((u0 ^ ((r0 + 4) >> 1)) << 3);
#pragma unroll
      for (int h2 = 0; h2 < 2; h2++) {
        bf16x8 fa[4], fb[4];
#pragma unroll
        for (int s = 0; s < 4; s++) {
          const unsigned o = (4 * h2 + s) * 1024;
          fa[s] = __builtin_shufflevector(trd(b0 + wr * 8192 + o), trd(b1 + wr * 8192 + o), 0, 1, 2, 3, 4, 5, 6, 7);
          fb[s] = __builtin_shufflevector(trd(b0 + (L_V - L_PHI) + o), trd(b1 + (L_V - L_PHI) + o), 0, 1, 2, 3, 4, 5, 6, 7);
        }
#pragma unroll
        for (int s = 0; s < 4; s++) kv = MFMA(fa[s], fb[s], kv);
      }
#pragma unroll
      for (int k = 0; k < 4; k++) {
        u32x4 t;
#pragma unroll
        for (int i = 0; i < 4; i++) t[i] = (F2U(kv[4 * k + i]) & ~0xfffu) | T12;
        st16(t, rsWS, vL16, ((head * 16 + rb) * 2 + wr) * 4096, WS_KVP + k * 1024);
      }
    }
  };
  auto qexp2 = [&](f32x16& a0, unsigned l0, f32x16& a1, unsigned l1) AI {
    const f32x16 p0 = featproj(a0, aWP + L_FREE, l0 * 1568, hh), p1 = featproj(a1, aWP + L_FREE, l1 * 1568, hh);
    a0 = dualexpP(p0, rc.Lr); a1 = dualexpP(p1, rc.Lr); asm volatile("" : "+v"(a0), "+v"(a1));
  };

  unsigned fl1 = 0;
  EpiS es;
  u32x4 pv[8];
  const unsigned vKP = l31 * 16 + hu * 8192, sKP = (rb * 32 + half) * 4096 + wr * 1024 + GG * 512;
  if constexpr (GG == 0) __builtin_amdgcn_s_setprio(1);
  SFOR(n, 28)
    if constexpr (n == 27) { ldsw<unsigned>(aCSw, 3 * 512, hilo(csB[0][0])); ldsw<unsigned>(aCSw, 0, hilo(csB[1][0])); }
    if constexpr (n == 11) waitv<AT ? 0 : 4>();
    if constexpr (AT && n == 7) waitv<GG ? 10 : 6>();
    if constexpr ((n & 3) == 3 && n != 15) {
      if constexpr (n == 27 || n == 11) WAITL0(); else asm volatile("s_waitcnt lgkmcnt(4)" ::: "memory");
      BAR(); MEMFENCE();
    }
    if constexpr (n == 11) stflag(E + 1, rsWS, sFown);
    if constexpr (n == 11) { STAMP(3); if constexpr (GG == 0) { kvpart(); __builtin_amdgcn_s_setprio(0); } else __builtin_amdgcn_s_setprio(1); STAMP(4); }
    SFOR(j, 2)
      constexpr int q = 2 * n + j;
      if constexpr (n + 2 < 28) readB(n + 2, j);
      SB();
      accB[j] = MFMA(wb[n % 3][j], xf[n], accB[j]);
      if constexpr ((n & 3) == 0) csB[j] = MFMA16(sel, wb[n % 3][j], csB[j]);
      if constexpr (q == 0) rowconst();
      if constexpr (q == 1) { fixmean(accA[0], 0); fixmean(accA[1], 1); }
      if constexpr (q == 2) fixmean(accA[2], 2);
      if constexpr (GG == 0 && q >= 4 && q < 22) kstep(IntC<q - 4>{});
      if constexpr (AT) {
        if constexpr (GG == 0 && q < 6) clr(q < 2 ? 112 + q : 118 + q);
        if constexpr (GG == 1 && q < 10) clr(q < 6 ? 114 + q : 118 + q);
        if constexpr (GG == 0 && q >= 33 && q < 54) qstep(IntC<q - 33>{}, accA[2], 0);
        if constexpr (GG == 1 && q >= 14 && q < 35) qstep(IntC<q - 14>{}, accA[0], 1);
        if constexpr (GG == 1 && q >= 35) qstep(IntC<q - 35>{}, accA[1], 2);
      } else {
        if constexpr (GG == 0 && q < 3) clr(q == 0 ? 0 : 3 + q);
        if constexpr (GG == 1 && q < 5) clr(q < 3 ? 1 + q : 3 + q);
        if constexpr (GG == 0 && q >= 36 && q < 52) { epiSlice<((q - 36) >> 2) & 3, q & 3>(accA[2], rc, es); if constexpr (q == 51) st16(es.r, rsWS, vL16, sA2, 0); }
        if constexpr (GG == 1) {
          SFOR(u, cumG1(q + 1) - cumG1(q))
            constexpr int e = cumG1(q) + u;
            epiSlice<(e >> 2) & 3, e & 3>(accA[e >> 4], rc, es);
            if constexpr ((e & 15) == 15) st16(es.r, rsWS, vL16, sA2, (1 + (e >> 4)) * 1024);
          SEND
        }
        if constexpr (j == 0 && (n == 3 || n == 5)) issueW2(1, (n - 3) >> 1);
        if constexpr (j == 0 && (n == 8 || n == 10)) issueW2(2, (n - 8) >> 1);
        if constexpr (j == 0 && (n == 12 || n == 14)) issueW2(3, (n - 12) >> 1);
        if constexpr (q >= 38 && q < 44) issueW2(4 + ((q - 38) >> 1), q & 1);
        if constexpr (j == 0 && (n == 23 || n == 24)) issueW2(7, n - 23);
        if constexpr (q == 37) asm volatile("buffer_load_dword %0, %1, %2, %3 offen sc1" : "=v"(fl1) : "v"(nd1 ? lane * FS : 0x80000000u), "s"(rsWS), "s"(sF) : "memory");
        if constexpr (n == 27) issueW2(8, j);
      }
      SB();
    SEND
  SEND
  __builtin_amdgcn_s_setprio(0);
  fixmean(accB[0], 3); fixmean(accB[1], 0);

  if constexpr (AT) {
#pragma unroll
    for (int i = 0; i < 8; i++) pv[i] = ld16(rsWS, vKP, sKP, WS_KVP + i * 16384);
    if constexpr (GG) { accA[2] = dualexpP(featproj(accA[2], aWP + L_FREE, 3 * 1568, hh), rc.Lr); asm volatile("" : "+v"(accA[2])); }
    STAMP(5);
    waittag<16384>(pv, rsWS, vKP, sKP + WS_KVP, T12, 0xfffu);
    STAMP(6);
    {
      u32x4 o;
#pragma unroll
      for (int k = 0; k < 4; k++) {
        float sum = 0.f;
#pragma unroll
        for (int i = 0; i < 8; i++) sum += U2F(pv[i][k] ^ T12);
        sum = xsum(sum);
        sum = l31 == 29 ? 1.f : sum;
        if (GG == 1) sum = wr == 3 ? 0.f : sum;
        o[k] = (pk2(sum, sum) & 0xffff0000u) | T16;
      }
      st16(o, rsWS, hh ? 0x80000000u : l31 * 16, (rb * 2 + half) * 4096 + wr * 1024 + GG * 512, WS_KVR);
    }
    qexp2(accB[0], 4 + 2 * GG, accB[1], 5 + 2 * GG);
    const unsigned sKR = (8 * half + w) * 8192;
#pragma unroll
    for (int i = 0; i < 8; i++) pv[i] = ld16(rsWS, vL16, sKR, WS_KVR + i * 1024);
    STAMP(7);
    waittag<1024>(pv, rsWS, vL16, sKR + WS_KVR, T16, 0xffffu);
    STAMP(8);
#pragma unroll
    for (int i = 0; i < 4; i++) {
      u32x4 f = {__builtin_amdgcn_perm(pv[2 * i][1], pv[2 * i][0], 0x07060302u), __builtin_amdgcn_perm(pv[2 * i][3], pv[2 * i][2], 0x07060302u),
                 __builtin_amdgcn_perm(pv[2 * i + 1][1], pv[2 * i + 1][0], 0x07060302u), __builtin_amdgcn_perm(pv[2 * i + 1][3], pv[2 * i + 1][2], 0x07060302u)};
      ldsw<u32x4>(LB + w * 4096 + vL16, i * 1024, f);
    }
    WAITL0(); BAR(); MEMFENCE();
    auto ohead = [&](const f32x16& phi, unsigned lh) AI {
      f32x16 op, on;
#pragma unroll
      for (int i = 0; i < 16; i++) { op[i] = 0.f; on[i] = 0.f; }
#pragma unroll
      for (int i = 0; i < 2; i++) {
        op = MFMA(ldsr<bf16x8>(LB + vL16, lh * 4096 + i * 1024), sub4(phi, i), op);
        on = MFMA(ldsr<bf16x8>(LB + vL16, lh * 4096 + (2 + i) * 1024), sub4(phi, 2 + i), on);
      }
      const float ia = __builtin_amdgcn_rcpf(op[13]), ib = __builtin_amdgcn_rcpf(on[13]);
      const float id = __builtin_amdgcn_rcpf(__builtin_fmaf(op[12], ia, __builtin_fmaf(on[12], ib, 1e-6f)));
      const float ca = xhi(ia * id), cb = xhi(ib * id);
      const f32x16 o = op * ca + on * cb;
      u32x4 f0, f1;
#pragma unroll
      for (int i = 0; i < 4; i++) { f0[i] = pk2(o[2 * i], o[2 * i + 1]); f1[i] = pk2(o[8 + 2 * i], o[9 + 2 * i]); }
      f1[2] = hh ? 0u : f1[2]; f1[3] = hh ? 0u : f1[3];
      st16(f0, rsWS, vL16, sA2, (112 + 2 * lh) * 1024); st16(f1, rsWS, vL16, sA2, (113 + 2 * lh) * 1024);
    };
    ohead(accA[2], GG ? 3 : 0); ohead(accB[0], 4 + 2 * GG); ohead(accB[1], 5 + 2 * GG);
    if constexpr (GG) { ohead(accA[0], 1); ohead(accA[1], 2); }
    STAMP(9);
  } else {
    STAMP(5);
    const unsigned sA = WS_A2 + rt2 * 144 * 1024;
    unsigned ln = vL16; asm volatile("" : "+v"(ln));
    const unsigned aW = lw2 + (ln & 0x200) + ((ln & 0xc0) << 1) + ((((ln >> 3) & 0x20) | ((ln >> 1) & 0x10)) ^ ((ln >> 1) & 0x40)) + ((ln >> 1) & 8);
    f32x16 acc[2][2];
#pragma unroll
    for (int i = 0; i < 4; i++)
#pragma unroll
      for (int k = 0; k < 16; k++) acc[i >> 1][i & 1][k] = 0.f;
    u32x4 af[NS][2];
    u32x2 xr[2];
    auto loadA = [&](int i) AI { af[i][0] = ld16(rsWS, vL16, sA + ksb(i) * 1024, ksi(i) * 1024); af[i][1] = ld16(rsWS, vL16, sA + ksb(i) * 1024, (144 + ksi(i)) * 1024); };
    const unsigned nt = wr >> 1, mt = wr & 1;
    const unsigned vO = (32 * mt + ((ln >> 4) & 31)) * XROW + ((ln >> 6) & 8) + 32 * GG, sO = rt2 * 32 * XROW + ci * 128 + nt * 64;
    st16(epiP(accB[0], rc), rsWS, vL16, sA2, (4 + 2 * GG) * 1024);
    st16(epiP(accB[1], rc), rsWS, vL16, sA2, (5 + 2 * GG) * 1024);
    STAMP(6);
    asm volatile("s_waitcnt vmcnt(%c1)" : "+v"(fl1) : "i"(GG ? 14 : 13) : "memory");
    if (!ALL(!nd1 || fl1 == E + 1u)) poll(rsWS, lane * FS, sF, E, nd1 ? 1u : 0u);
#pragma unroll
    for (int j = 0; j < PD; j++) loadA(j);
#pragma unroll
    for (int k = 0; k < 2; k++) asm volatile("buffer_load_dwordx2 %0, %1, %2, %3 offen offset:%c4" : "=v"(xr[k]) : "v"(vO), "s"(rsX), "s"(sO), "i"(16 * k) : "memory");
    STAMP(7);
    SFOR(i, NS)
      asm volatile("s_waitcnt vmcnt(%c2)" : "+v"(af[i][0]), "+v"(af[i][1]) : "i"(later_ops(i, NS, 0, NS) + (i < PD ? 2 : 0)) : "memory");
      constexpr unsigned ra = ring_area(i % RING);
      const unsigned la = aW + (ra & 0x30000u);
      bf16x8 w0 = tr2(la, ra & 0xffffu, (ra & 0xffffu) + 1024), w1 = tr2(la ^ 64u, ra & 0xffffu, (ra & 0xffffu) + 1024);
      auto stale = [&]() AI {
        unsigned m = UMAX(UMAX(UMAX(af[i][0][0], af[i][0][1]), UMAX(af[i][0][2], af[i][0][3])), UMAX(UMAX(af[i][1][0], af[i][1][1]), UMAX(af[i][1][2], af[i][1][3])));
        return ANY(m == ~0u);
      };
      if (__builtin_expect(stale(), 0)) {
        constexpr int i1 = i + 1 < NS ? i + 1 : i, i2 = i + 2 < NS ? i + 2 : i1, i3 = i + 3 < NS ? i + 3 : i2;
        const unsigned o0 = sA + (ksb(i) + ksi(i)) * 1024;
        int it = 0;
#pragma nounroll
        do { __builtin_amdgcn_s_sleep(SLEEPR); reld<1>(af[i][0], af[i][1], af[i][0], af[i][1], af[i][0], af[i][1], af[i][0], af[i][1], rsWS, vL16, o0, o0, o0, o0); } while (++it < (1 << 14) && stale());
        if constexpr (i + 1 < NS) {
          __builtin_amdgcn_s_sleep(0);
          reld<(NS - 1 - i < 3 ? NS - 1 - i : 3)>(af[i1][0], af[i1][1], af[i2][0], af[i2][1], af[i3][0], af[i3][1], af[i3][0], af[i3][1], rsWS, vL16,
                sA + (ksb(i1) + ksi(i1)) * 1024, sA + (ksb(i2) + ksi(i2)) * 1024, sA + (ksb(i3) + ksi(i3)) * 1024, o0);
        }
      }
      if constexpr (i == 6) STAMP(8);
      if constexpr (i == 12) STAMP(9);
      SB();
      if constexpr (i + PD < NS) loadA(i + PD);
#pragma unroll
      for (int mm = 0; mm < 2; mm++) acc[0][mm] = MFMA(w0, __builtin_bit_cast(bf16x8, af[i][mm]), acc[0][mm]);
      if constexpr (i + RING < NS) { WAITL0(); SB(); issueW2(i + RING, 0); issueW2(i + RING, 1); }
#pragma unroll
      for (int mm = 0; mm < 2; mm++) acc[1][mm] = MFMA(w1, __builtin_bit_cast(bf16x8, af[i][mm]), acc[1][mm]);
      SB();
    SEND
    STAMP(10);
#pragma unroll
    for (int qd = 0; qd < 4; qd++)
#pragma unroll
      for (int k = 0; k < 4; k++) {
        const unsigned ra = ring_area(2 * qd + (k >> 1)) + (k & 1) * 1024;
        ldsw<bf16x8>(lw2 + vL16 + (ra & 0x30000u), ra & 0xffffu, sub4(acc[qd >> 1][qd & 1], k));
      }
    WAITL0(); BAR(); MEMFENCE();
    STAMP(12);
    const unsigned j2 = 2 * wr + GG;
    const unsigned ra = ((0x56140732u >> (4 * j2)) & 7) * 16384;
    asm volatile("" : "+v"(xr[0]), "+v"(xr[1]));
    const rsrc_t rsO = mkrs(pout, 2048 * XROW);
#pragma unroll
    for (int k = 0; k < 2; k++) {
      f32x4 sum = {U2F(xr[k][0] << 16), U2F(xr[k][0] & 0xffff0000u), U2F(xr[k][1] << 16), U2F(xr[k][1] & 0xffff0000u)};
#pragma unroll
      for (int v = 0; v < 8; v++) sum += ldsr<f32x4>(LB + ra + vL16, v * 2048 + k * 1024);
      u32x2 o = {pk2(sum[0], sum[1]), pk2(sum[2], sum[3])};
      __builtin_amdgcn_raw_buffer_store_b64(o, rsO, vO + 16 * k, sO, 0);
    }
    if (b == 32 && w == 0) stflag(E + 4, rsWS, WS_EPOCH);
  }
  STAMPS_OUT();
  };
  if (!attn && g) prog(BoolC<false>{}, IntC<1>{});
  else if (!attn) prog(BoolC<false>{}, IntC<0>{});
  else if (g) prog(BoolC<true>{}, IntC<1>{});
  else prog(BoolC<true>{}, IntC<0>{});
}

__global__ __launch_bounds__(256) void warmk(const char* p0, const char* p1, const char* p2, const char* p3, const char* p4, const char* p5, char* ws, char* out) {
  const unsigned b = blockIdx.x, t = threadIdx.x;
  unsigned acc = 0;
  const char* ps[6] = {p0, p1, p2, p3, p4, p5};
  const unsigned sz[6] = {2048 * 896, 448 * 2688, 448 * 3584, 448 * 3584, 1792 * 896, 448 * 896};
  if (acc == 0x12345679u) *(unsigned*)(ws + (200u << 20)) = acc;
}
extern "C" void kernel_launch(void* const* d_in, const int* in_sizes, int n_in, void* d_out, int out_size, void* d_ws, size_t ws_size,
                              hipStream_t stream) {
  warmk<<<256, 256, 0, stream>>>((const char*)d_in[0], (const char*)d_in[1], (const char*)d_in[5], (const char*)d_in[6], (const char*)d_in[7], (const char*)d_in[3], (char*)d_ws, (char*)d_out);
  fused_kernel<<<256, 512, 0, stream>>>((const char*)d_in[0], (const char*)d_in[1], (const char*)d_in[5], (const char*)d_in[6], (char*)d_ws,
                                        (const char*)d_in[2], (const char*)d_in[7], (const char*)d_in[3], (char*)d_out);
}
```

```cpp
#define FIXT 2200
#include <hip/hip_runtime.h>
#include <hip/hip_bf16.h>
#include <utility>

#define DI __device__ __forceinline__
#define LAS __attribute__((address_space(3)))
typedef __attribute__((ext_vector_type(8))) short bf16x8;
typedef __attribute__((ext_vector_type(4))) short s16x4;
typedef __attribute__((ext_vector_type(2))) __bf16 bf2;
typedef __attribute__((ext_vector_type(2))) float f32x2;
typedef __attribute__((ext_vector_type(4))) float f32x4;
typedef __attribute__((ext_vector_type(16))) float f32x16;
typedef __attribute__((ext_vector_type(2))) unsigned u32x2;
typedef __attribute__((ext_vector_type(4))) unsigned u32x4;
typedef __amdgpu_buffer_rsrc_t rsrc_t;
#define F2U(x) __builtin_bit_cast(unsigned, (float)(x))
#define U2F(x) __builtin_bit_cast(float, (unsigned)(x))
#define ALL(x) (__builtin_amdgcn_ballot_w64(!(x)) == 0ull)
#define ANY(x) (__builtin_amdgcn_ballot_w64(x) != 0ull)
#define UMAX(a, b) __builtin_elementwise_max((unsigned)(a), (unsigned)(b))

constexpr unsigned XROW = 896, QROW = 2688, FROW = 3584;
constexpr unsigned FS = 128;
constexpr unsigned WS_EPOCH = 0;
constexpr unsigned WS_GR = 4096;
constexpr unsigned WS_KVP = 131072;
constexpr unsigned WS_A2 = WS_KVP + 16 * 16 * 8192;
constexpr unsigned WS_ST = WS_A2 + 64 * 144 * 1024;
constexpr unsigned WS_FH = WS_ST + 262144;
constexpr unsigned WS_KVR = WS_FH + 2048 * FS;
constexpr unsigned WS_END = WS_KVR + 16 * 8192;
constexpr unsigned L_GU = 65536, L_KV = 131072;
constexpr unsigned L_TAIL = L_KV + 3 * 8192;
constexpr unsigned L_WPK = L_TAIL;
constexpr unsigned L_ST = L_TAIL + 2048;
constexpr unsigned L_CS = L_TAIL + 4096;
constexpr unsigned L_PHI = L_GU + 32768;
constexpr unsigned L_V = L_KV + 16384;
constexpr unsigned L_FREE = 32768;

#define MFMA(a, b, c) __builtin_amdgcn_mfma_f32_32x32x16_bf16((a), (b), (c), 0, 0, 0)
#define MFMA16(a, b, c) __builtin_amdgcn_mfma_f32_16x16x32_bf16((a), (b), (c), 0, 0, 0)
#define AI __attribute__((always_inline))
#define BAR() __builtin_amdgcn_s_barrier()
#define WAITV(n) asm volatile("s_waitcnt vmcnt(" #n ")" ::: "memory")
#define WAITL0() asm volatile("s_waitcnt lgkmcnt(0)" ::: "memory")
#define SB() __builtin_amdgcn_sched_barrier(0)
#define MEMFENCE() asm volatile("" ::: "memory")

template <int N> DI void waitv() { asm volatile("s_waitcnt vmcnt(%c0)" :: "i"(N) : "memory"); }

#define INSZ(x) (x)
DI rsrc_t mkrs(const void* p, unsigned bytes) { return __builtin_amdgcn_make_buffer_rsrc((void*)p, (short)0, (int)bytes, 0x00020000); }
DI void dma(unsigned lds, unsigned limm, unsigned voff, rsrc_t rs, unsigned soff, unsigned simm) {
  unsigned t;
  asm volatile("s_add_u32 m0, %1, %c5\n\ts_add_u32 %0, %4, %c6\n\tbuffer_load_dwordx4 %2, %3, %0 offen lds"
               : "=&s"(t) : "s"(lds), "v"(voff), "s"(rs), "s"(soff), "i"(limm), "i"(simm) : "memory", "scc");
}
DI s16x4 trd(unsigned a) { return __builtin_amdgcn_ds_read_tr16_b64_v4i16((LAS s16x4*)(size_t)a); }
DI s16x4 trd2(unsigned a, unsigned off) { return __builtin_amdgcn_ds_read_tr16_b64_v4i16((LAS s16x4*)((LAS char*)(size_t)a + off)); }
DI bf16x8 tr2(unsigned a, unsigned off0, unsigned off1) {
  LAS char* p = (LAS char*)(size_t)a;
  s16x4 lo = __builtin_amdgcn_ds_read_tr16_b64_v4i16((LAS s16x4*)(p + off0));
  s16x4 hi = __builtin_amdgcn_ds_read_tr16_b64_v4i16((LAS s16x4*)(p + off1));
  return __builtin_shufflevector(lo, hi, 0, 1, 2, 3, 4, 5, 6, 7);
}
template <class T> DI T ldsr(unsigned a, unsigned off) { return *(LAS T*)((LAS char*)(size_t)a + off); }
template <class T> DI void ldsw(unsigned a, unsigned off, T v) { *(LAS T*)((LAS char*)(size_t)a + off) = v; }
DI unsigned pk2(float a, float b) { f32x2 f = {a, b}; return __builtin_bit_cast(unsigned, __builtin_convertvector(f, bf2)); }
DI float rbf(float a) { return U2F(pk2(a, a) << 16); }
DI float dot2(unsigned a, unsigned b, float c) { return __builtin_amdgcn_fdot2_f32_bf16(__builtin_bit_cast(bf2, a), __builtin_bit_cast(bf2, b), c, false); }
DI float xsum(float v) { auto r = __builtin_amdgcn_permlane32_swap(F2U(v), F2U(v), false, false); return U2F(r[0]) + U2F(r[1]); }
DI float xmax(float v) { auto r = __builtin_amdgcn_permlane32_swap(F2U(v), F2U(v), false, false); return __builtin_fmaxf(U2F(r[0]), U2F(r[1])); }
DI float xmin(float v) { auto r = __builtin_amdgcn_permlane32_swap(F2U(v), F2U(v), false, false); return __builtin_fminf(U2F(r[0]), U2F(r[1])); }
DI float xhi(float v) { auto r = __builtin_amdgcn_permlane32_swap(F2U(v), F2U(v), false, false); return U2F(r[1]); }
DI void st16(u32x4 v, rsrc_t rs, unsigned voff, unsigned soff, unsigned simm) {
  unsigned t;
  asm volatile("s_add_u32 %0, %4, %c5\n\tbuffer_store_dwordx4 %1, %2, %3, %0 offen sc1\n\ts_nop 1" : "=&s"(t) : "v"(v), "v"(voff), "s"(rs), "s"(soff), "i"(simm) : "memory", "scc");
}
DI void stflag(unsigned v, rsrc_t rs, unsigned soff) {
  unsigned voff = (__builtin_amdgcn_mbcnt_hi(~0u, __builtin_amdgcn_mbcnt_lo(~0u, 0u)) == 0u) ? 0u : 0x80000000u;
  __builtin_amdgcn_raw_buffer_store_b32(v, rs, voff, soff, 16);
}
#define SLEEPR 4
#define SLEEPN 2
DI u32x4 ld16(rsrc_t rs, unsigned voff, unsigned soff, unsigned simm) {
  u32x4 r; unsigned t;
  asm volatile("s_add_u32 %1, %4, %c5\n\tbuffer_load_dwordx4 %0, %2, %3, %1 offen sc1" : "=v"(r), "=&s"(t) : "v"(voff), "s"(rs), "s"(soff), "i"(simm) : "memory", "scc");
  return r;
}
template <unsigned STEP> DI void ld8w(u32x4 (&p)[8], rsrc_t rs, unsigned voff, unsigned soff) {
  unsigned t;
#define L1(k) "buffer_load_dwordx4 %" #k ", %9, %10, %8 offen sc1\n\ts_add_u32 %8, %8, %c12\n\t"
  asm volatile("s_mov_b32 %8, %11\n\t" L1(0) L1(1) L1(2) L1(3) L1(4) L1(5) L1(6) L1(7) "s_waitcnt vmcnt(0)"
               : "=&v"(p[0]), "=&v"(p[1]), "=&v"(p[2]), "=&v"(p[3]), "=&v"(p[4]), "=&v"(p[5]), "=&v"(p[6]), "=&v"(p[7]), "=&s"(t)
               : "v"(voff), "s"(rs), "s"(soff), "i"(STEP) : "memory", "scc");
#undef L1
}
DI bool untag(u32x4 (&p)[8], unsigned tag, unsigned mask) {
  unsigned chk = 0;
#pragma unroll
  for (int i = 0; i < 8; i++)
#pragma unroll
    for (int k = 0; k < 4; k++) { p[i][k] ^= tag; chk |= p[i][k]; }
  return ALL((chk & mask) == 0u);
}
template <unsigned STEP> DI void waittag(u32x4 (&p)[8], rsrc_t rs, unsigned voff, unsigned soff, unsigned tag, unsigned mask) {
  asm volatile("s_waitcnt vmcnt(0)" : "+v"(p[0]), "+v"(p[1]), "+v"(p[2]), "+v"(p[3]), "+v"(p[4]), "+v"(p[5]), "+v"(p[6]), "+v"(p[7]) :: "memory");
#pragma nounroll
  for (int it = 0; !untag(p, tag, mask) && it < (1 << 16); ++it) { __builtin_amdgcn_s_sleep(SLEEPN); ld8w<STEP>(p, rs, voff, soff); }
}
#define RL(a, b, so) "buffer_load_dwordx4 %" #a ", %[v], %[r], %" #so " offen sc1\n\ts_add_u32 %[t], %" #so ", 147456\n\tbuffer_load_dwordx4 %" #b ", %[v], %[r], %[t] offen sc1\n\t"
template <int N> DI void reld(u32x4& a0, u32x4& b0, u32x4& a1, u32x4& b1, u32x4& a2, u32x4& b2, u32x4& a3, u32x4& b3, rsrc_t rs, unsigned voff,
                              unsigned s0, unsigned s1, unsigned s2, unsigned s3) {
  unsigned t;
  if constexpr (N == 4) asm volatile(RL(0, 1, 9) RL(2, 3, 10) RL(4, 5, 11) RL(6, 7, 12) "s_waitcnt vmcnt(0)"
      : "+v"(a0), "+v"(b0), "+v"(a1), "+v"(b1), "+v"(a2), "+v"(b2), "+v"(a3), "+v"(b3), [t] "=&s"(t) : "s"(s0), "s"(s1), "s"(s2), "s"(s3), [v] "v"(voff), [r] "s"(rs) : "memory", "scc");
  if constexpr (N == 3) asm volatile(RL(0, 1, 7) RL(2, 3, 8) RL(4, 5, 9) "s_waitcnt vmcnt(0)"
      : "+v"(a0), "+v"(b0), "+v"(a1), "+v"(b1), "+v"(a2), "+v"(b2), [t] "=&s"(t) : "s"(s0), "s"(s1), "s"(s2), [v] "v"(voff), [r] "s"(rs) : "memory", "scc");
  if constexpr (N == 2) asm volatile(RL(0, 1, 5) RL(2, 3, 6) "s_waitcnt vmcnt(0)"
      : "+v"(a0), "+v"(b0), "+v"(a1), "+v"(b1), [t] "=&s"(t) : "s"(s0), "s"(s1), [v] "v"(voff), [r] "s"(rs) : "memory", "scc");
  if constexpr (N == 1) asm volatile(RL(0, 1, 3) "s_waitcnt vmcnt(0)"
      : "+v"(a0), "+v"(b0), [t] "=&s"(t) : "s"(s0), [v] "v"(voff), [r] "s"(rs) : "memory", "scc");
}
DI void poll(rsrc_t rs, unsigned voff, unsigned soff, unsigned E, unsigned need) {
  voff = need ? voff : 0x80000000u;
#pragma nounroll
  for (int it = 0; it < (1 << 16); ++it) {
    unsigned v;
    asm volatile("buffer_load_dword %0, %1, %2, %3 offen sc1\n\ts_waitcnt vmcnt(0)" : "=v"(v) : "v"(voff), "s"(rs), "s"(soff) : "memory");
    bool ok = (need == 0u) || ((v - E - need) <= (3u - need));
    if (ALL(ok)) break;
    __builtin_amdgcn_s_sleep(SLEEPN);
  }
}

template <bool B> struct BoolC { static constexpr bool value = B; };
template <int I> struct IntC { static constexpr int value = I; };
template <int... I, class F> DI void sfor_impl(std::integer_sequence<int, I...>, F&& f) { (f(IntC<I>{}), ...); }
template <int N, class F> DI void sfor(F&& f) { sfor_impl(std::make_integer_sequence<int, N>{}, f); }
#define SFOR(n, N) sfor<N>([&](auto n##_) AI { constexpr int n = decltype(n##_)::value;
#define SEND });
struct RowC { float r, K1, K2, Ri, Lr; };

DI unsigned hilo(float v) { float hi = rbf(v); return pk2(hi, v - hi); }
DI float max3(float a, float b, float c) { return __builtin_fmaxf(__builtin_fmaxf(a, b), c); }
DI float min3(float a, float b, float c) { return __builtin_fminf(__builtin_fminf(a, b), c); }
DI unsigned pk2s(float a, float b) { unsigned r; asm("v_cvt_pk_bf16_f32 %0, %1, %2" : "=v"(r) : "v"(a), "v"(b)); return r; }
DI f32x2 sub2(const f32x16& v, int k) { f32x2 t = {v[2 * k], v[2 * k + 1]}; return t; }
DI f32x2 exp2v(f32x2 v) { f32x2 r = {__builtin_amdgcn_exp2f(v[0]), __builtin_amdgcn_exp2f(v[1])}; return r; }
DI f32x2 fma2(f32x2 a, float b, float c) { f32x2 bb = {b, b}, cc = {c, c}; return __builtin_elementwise_fma(a, bb, cc); }
DI u32x4 epiP(const f32x16& a, const RowC& c) {
  u32x4 r;
#pragma unroll
  for (int i = 0; i < 4; i++) {
    const f32x2 g = sub2(a, i), d = fma2(exp2v(g * fma2(g * g, c.K2, c.K1)), c.Ri, c.Ri);
    const f32x2 s = {__builtin_amdgcn_rcpf(d[0]), __builtin_amdgcn_rcpf(d[1])}, h = (g * sub2(a, 4 + i)) * s;
    r[i] = pk2(h[0], h[1]);
  }
  return r;
}
struct EpiS { float x0, x1, m0, m1; u32x4 r; };
template <int P, int PH> DI void epiSlice(const f32x16& a, const RowC& c, EpiS& s) {
  const float g0 = a[2 * P], g1 = a[2 * P + 1];
  if constexpr (PH == 0) { s.x0 = g0 * __builtin_fmaf(g0 * g0, c.K2, c.K1); s.x1 = g1 * __builtin_fmaf(g1 * g1, c.K2, c.K1); }
  if constexpr (PH == 1) { s.x0 = __builtin_amdgcn_exp2f(s.x0); s.x1 = __builtin_amdgcn_exp2f(s.x1); s.m0 = g0 * a[8 + 2 * P]; s.m1 = g1 * a[9 + 2 * P]; }
  if constexpr (PH == 2) { s.x0 = __builtin_amdgcn_rcpf(__builtin_fmaf(s.x0, c.Ri, c.Ri)); s.x1 = __builtin_amdgcn_rcpf(__builtin_fmaf(s.x1, c.Ri, c.Ri)); }
  if constexpr (PH == 3) s.r[P] = pk2s(s.m0 * s.x0, s.m1 * s.x1);
}
DI f32x16 featproj2(const f32x16& a, bf16x8 w0, bf16x8 w1, bool hh) {
  u32x4 q0, q1;
#pragma unroll
  for (int i = 0; i < 4; i++) { q0[i] = pk2(a[2 * i], a[2 * i + 1]); q1[i] = pk2(a[8 + 2 * i], a[9 + 2 * i]); }
  q1[2] = hh ? 0u : q1[2]; q1[3] = hh ? 0u : q1[3];
  f32x16 z;
#pragma unroll
  for (int i = 0; i < 16; i++) z[i] = 0.f;
  z = MFMA(w0, __builtin_bit_cast(bf16x8, q0), z);
  z = MFMA(w1, __builtin_bit_cast(bf16x8, q1), z);
  return z;
}
DI f32x16 featproj(const f32x16& a, unsigned wpaddr, unsigned off, bool hh) {
  return featproj2(a, tr2(wpaddr, off, off + 8 * 56), tr2(wpaddr, off + 16 * 56, off + 24 * 56), hh);
}
DI float max16(const f32x16& p) { float m = max3(p[0], p[1], p[2]); _Pragma("unroll") for (int i = 3; i < 15; i += 2) m = max3(m, p[i], p[i + 1]); return xmax(__builtin_fmaxf(m, p[15])); }
DI float min16(const f32x16& p) { float m = min3(p[0], p[1], p[2]); _Pragma("unroll") for (int i = 3; i < 15; i += 2) m = min3(m, p[i], p[i + 1]); return xmin(__builtin_fminf(m, p[15])); }
DI f32x16 dualexpP(const f32x16& p, float Lr) {
  const float bx = -max16(p) * Lr, bn = min16(p) * Lr;
  f32x16 o;
#pragma unroll
  for (int i = 0; i < 8; i++) {
    const f32x2 ep = exp2v(fma2(sub2(p, i), Lr, bx)), en = exp2v(fma2(sub2(p, i), -Lr, bn));
    o[i] = U2F(pk2(ep[0], ep[1])); o[8 + i] = U2F(pk2(en[0], en[1]));
  }
  return o;
}
struct SmS { f32x16 p, e; float en[16], bx, bn, sp, sn, tp, tn; bf16x8 w0, w1; };
DI bf16x8 sub4(const f32x16& v, int k) { f32x4 t = {v[4 * k], v[4 * k + 1], v[4 * k + 2], v[4 * k + 3]}; return __builtin_bit_cast(bf16x8, t); }

DI constexpr unsigned lc1_base(int ch) { return ch == 6 ? L_KV : ((ch & 1) ? L_GU : 0u) + (ch < 2 ? 49152u : ch < 4 ? 0u : 16384u); }
template <int CTRL, int RM> DI float dppadd(float v) { return v + U2F(__builtin_amdgcn_update_dpp(0u, F2U(v), CTRL, RM, 0xf, false)); }
DI float wavesum63(float v) {
  v = dppadd<0x111, 0xf>(v); v = dppadd<0x112, 0xf>(v); v = dppadd<0x114, 0xf>(v); v = dppadd<0x118, 0xf>(v);
  v = dppadd<0x142, 0xa>(v); return dppadd<0x143, 0xc>(v);
}

#define STAMP(i)
#define STAMPS_OUT()
#define CSTAMP(i)

constexpr int PD = 4, RING = 9;
constexpr int later_ops(int m, int n, int n0, int NS) {
  int cnt = 0; bool seen = false;
  for (int j = 0; j < PD && j < n; j++) { if (seen) cnt += 2; if (j == m) seen = true; }
  for (int st = 0; st < m; st++) {
    if (st + PD < n) { if (seen) cnt += 2; if (st + PD == m) seen = true; }
    if (n0 + st + RING < NS && seen) cnt += 2;
  }
  return cnt;
}
DI constexpr unsigned ring_area(int j) { return j == 0 ? L_FREE : j == 1 ? L_PHI : lc1_base(j - 2); }
constexpr int cumG1(int s) { return s < 4 ? 0 : s < 22 ? (s - 4) / 2 : (9 + (s - 22) * 5 / 4 < 48 ? 9 + (s - 22) * 5 / 4 : 48); }

#define CODE_BYTES 65536
__global__ __launch_bounds__(512, 2) void fused_kernel(const char* px_, const char* pqkv_, const char* pwg_, const char* pwu_, char* ws_, const char* pphi_,
                                                       const char* pwd_, const char* pwo_, char* pout_) {
  typedef __attribute__((ext_vector_type(8))) unsigned long long u64x8;
  u64x8 ka; unsigned long long kb;
  asm volatile("s_load_dwordx16 %0, %2, 0\n\ts_load_dwordx2 %1, %2, 64" : "=&s"(ka), "=&s"(kb) : "s"(__builtin_amdgcn_kernarg_segment_ptr()));
  __shared__ __attribute__((aligned(16))) char smem[163840];
  const unsigned LB = (unsigned)(size_t)smem;
  const unsigned tid = __builtin_amdgcn_workitem_id_x(), lane = tid & 63;
  const unsigned w = __builtin_amdgcn_readfirstlane(tid >> 6), g = w >> 2, wr = w & 3;
  const unsigned b = __builtin_amdgcn_workgroup_id_x(), xcd = b & 7, bi = b >> 3, grp = xcd >> 1, half = xcd & 1, rbl = bi & 3, cp = bi >> 2, ci = cp - 1;
  const unsigned rb = 4 * grp + rbl, c = 7 * half + ci;
  const unsigned head = 8 * half + (((half ? 0x76425310u : 0x75316420u) >> (4 * cp)) & 7);
  const bool attn = cp == 0;
  const unsigned long long tst = __builtin_amdgcn_s_memrealtime();
  const unsigned wstr = attn ? QROW : FROW;
  const unsigned sG0 = half * (attn ? 448 : 1792) + (attn ? 0 : ci * 256), sX0 = rb * 128 * XROW;
  unsigned vx0, vx1, vG, vU, vKV;
  auto voffs = [&](unsigned ln) AI {
    const unsigned r8 = ln >> 3, c8 = ln & 7, sa = (r8 >> 1) & 1, lc = c8 ^ (4 * sa);
    vx0 = (16 * w + r8) * XROW + ((c8 ^ (r8 >> 1)) << 4); vx1 = (vx0 ^ 64u) + 8 * XROW;
    const unsigned cg = attn ? lc : (c8 ^ (2 * sa)), cu = attn ? (lc >= 6 ? lc - 2 : lc) : (cg ^ 4);
    vG = (8 * w + r8) * wstr + (cg << 4); vU = (8 * w + r8) * wstr + (cu << 4);
    vKV = (8 * w + r8) * QROW + 896 * (1 + (lc >> 2)) + ((lc & 3) << 4) + ((56 * head) & ~15u);
  };
  voffs(lane);
  const unsigned lw1 = LB + w * 1024, lw2 = LB + w * 2048;
#define ROT (((0x24924u >> (2 * (cp + rbl))) & 3) * 2)
  const unsigned rot = ROT;
  const unsigned srow = 128 * rb + 64 * half + 8 * cp + w;
  unsigned vrow = lane < 56 ? lane * 16 : 0x80000000u;
  asm volatile("s_waitcnt lgkmcnt(0)" : "+s"(ka), "+s"(kb), "+v"(vx0), "+v"(vx1), "+v"(vG), "+v"(vU), "+v"(vKV), "+v"(vrow));
  const char *px = (const char*)ka[0], *pqkv = (const char*)ka[1], *pwg = (const char*)ka[2], *pwu = (const char*)ka[3], *pphi = (const char*)ka[5],
             *pwd = (const char*)ka[6], *pwo = (const char*)ka[7];
  char *ws = (char*)ka[4], *pout = (char*)kb;
  const rsrc_t rsX = mkrs(px, INSZ(2048 * XROW)), rsQ = mkrs(pqkv, INSZ(448 * QROW));
  const rsrc_t rsG = mkrs(attn ? pqkv : pwg, INSZ(448 * wstr)), rsU = mkrs(attn ? pqkv + 128 : pwu, INSZ(448 * wstr - (attn ? 128 : 0)));
  auto cidx = [&](int st) AI -> unsigned { unsigned t = rot + st; return t >= 7 ? t - 7 : t; };
  auto issueA = [&](int st, int k) AI {
    const unsigned sl = st & 3, ch = cidx(st);
    if (k == 0) dma(lw2, sl * 16384, vx0, rsX, sX0 + ch * 128, 0);
    if (k == 1) dma(lw2, sl * 16384 + 1024, vx1, rsX, sX0 + ch * 128, 0);
    if (k == 2) dma(lw1, L_GU + sl * 16384, vG, rsG, sG0 + ch * 64 * wstr, 0);
    if (k == 3) dma(lw1, L_GU + sl * 16384 + 8192, vU, rsU, sG0 + ch * 64 * wstr, 0);
    if (k == 4) dma(lw1, L_KV + sl * 8192, vKV, rsQ, ch * 64 * QROW, 0);
  };
  const rsrc_t rsWS = mkrs(ws, WS_END);
  unsigned Ev, cpf; u32x4 xrw;
  asm volatile("buffer_load_dwordx4 %0, %1, %2, %3 offen" : "=v"(xrw) : "v"(vrow), "s"(rsX), "s"(srow * XROW) : "memory");
  asm volatile("buffer_load_dword %0, off, %1, 0 sc1" : "=v"(Ev) : "s"(rsWS) : "memory");
  {
    unsigned long long pc; asm volatile("s_getpc_b64 %0" : "=s"(pc));
    const unsigned vo = w == 0 && lane < 24 ? (bi * 24 + lane) * 128 : 0x80000000u;
    asm volatile("buffer_load_dword %0, %1, %2, 0 offen" : "=v"(cpf) : "v"(vo), "s"(mkrs((const void*)pc, CODE_BYTES)) : "memory");
  }
#pragma unroll
  for (int k = 0; k < 5; k++) issueA(0, k);
#pragma unroll
  for (int k = 0; k < 5; k++) issueA(1, k);
  issueA(2, 0);
  asm volatile("s_waitcnt vmcnt(11)" : "+v"(xrw), "+v"(Ev), "+v"(cpf) :: "memory");
  const unsigned E = __builtin_amdgcn_readfirstlane(Ev);
  {
    float s1 = 0.f, s2 = 0.f;
#pragma unroll
    for (int i = 0; i < 4; i++) { s1 = dot2(xrw[i], 0x3f803f80u, s1); s2 = dot2(xrw[i], xrw[i], s2); }
    s1 = wavesum63(s1); s2 = wavesum63(s2);
    const float mean = s1 * (1.f / 448.f), var = __builtin_fmaxf(s2 * (1.f / 448.f) - mean * mean, 0.f);
    u32x2 gr = {pk2(-mean, __builtin_amdgcn_sqrtf(rbf(rbf(var) + 1e-5f))), E + 1};
    __builtin_amdgcn_raw_buffer_store_b64(gr, rsWS, lane == 63 ? 0u : 0x80000000u, WS_GR + srow * 8, 16);
  }

  auto prog = [&](auto at_, auto gg_) AI {
  constexpr bool AT = decltype(at_)::value; constexpr int GG = decltype(gg_)::value;
  constexpr unsigned lcstep = AT ? 224 : 128;
  unsigned lane;
  asm volatile("v_mbcnt_lo_u32_b32 %0, -1, 0\n\tv_mbcnt_hi_u32_b32 %0, -1, %0" : "=v"(lane));
  __builtin_assume(lane < 64);
  voffs(lane);
  auto issueL = [&](int hc) AI {
    const int st = hc >> 1; const unsigned ch = cidx(st);
    if (hc & 1) dma(lw1, lc1_base(st) + 8192, vU, rsU, sG0 + ch * 64 * wstr, lcstep);
    else dma(lw1, lc1_base(st), vG, rsG, sG0 + ch * 64 * wstr, lcstep);
  };
  auto issueG = [&](int bar, int k) AI {
    if (bar == 0) issueA(2 + k / 5, k % 5);
    else if (bar <= 3) issueA(bar + 3, k);
    else if (k < 4) issueL(4 * (bar - 4) + k);
    else if (bar == 4) dma(LB + (w & 1) * 1024, L_WPK, lane * 16, mkrs(pphi, 16 * 1568), head * 1568 + (w & 1) * 1024, 0);
    else issueL(12 + (bar - 5));
  };

  const unsigned rt = 4 * rb + wr;
  const unsigned sA2 = WS_A2 + rt * 144 * 1024 + (AT ? half * 16384 : c * 8192), vL16 = lane * 16;
  const u32x4 sent = {~0u, ~0u, ~0u, ~0u};
  auto clr = [&](int ks) AI { st16(sent, rsWS, vL16, sA2, ks * 1024); };

  constexpr int NCLR = AT ? (GG ? 10 : 6) : (GG ? 5 : 3);
  auto clri = [&](int q) AI { clr(AT ? (GG ? (q < 6 ? 114 + q : 118 + q) : (q < 2 ? 112 + q : 118 + q)) : (GG ? (q < 3 ? 1 + q : 3 + q) : (q == 0 ? 0 : 3 + q))); };
#pragma unroll
  for (int q = 0; q < NCLR; q++) clri(q);

  const bool hh = lane >= 32;
  const unsigned hu = lane >> 5, blk = (lane >> 4) & 1, q4 = (lane >> 2) & 3, p4 = lane & 3, ta = (q4 >> 1) & 1, l31 = lane & 31;
  const unsigned perm = wr ^ (2 * GG);
  const unsigned rowoff = (8 * hu + q4) * 128 + (perm << 11);
  const unsigned pc = (4 * blk + p4) < 6 ? (4 * blk + p4) : 6;
  auto tileaddr = [&](unsigned t) AI -> unsigned {
    if constexpr (AT) { unsigned o = 56 * t + 8 * pc; return (o >> 7) * 8192 + ((((o >> 4) & 7) ^ (4 * ta)) << 4) + (o & 8) + rowoff; }
    else return blk * 8192 + (((2 * t + (p4 >> 1)) ^ (4 * blk + 2 * ta)) << 4) + 8 * (p4 & 1) + rowoff;
  };
  unsigned aA[3], aB[2];
  {
    unsigned o = (head & 1) * 8 + 8 * pc;
    unsigned ak = L_KV + (((o >> 4) ^ (4 * ta)) << 4) + (o & 8) + rowoff;
    aA[0] = LB + (GG ? L_GU + tileaddr(1) : ak);
    aA[1] = LB + (GG ? L_GU + tileaddr(2) : (ak ^ 64u));
    aA[2] = LB + L_GU + tileaddr(GG ? 3 : 0);
    aB[0] = LB + tileaddr(2 * GG); aB[1] = LB + tileaddr(2 * GG + 1);
  }
  const unsigned xrow = 32 * wr + l31;
  const unsigned aX = LB + xrow * 128 + (((2 * perm + hu) ^ ((xrow >> 1) & 7)) << 4);
  const unsigned selv = (((lane >> 4) ^ (lane >> 2)) & 1) ? 0u : 0x3f803f80u;
  const u32x4 sel4 = {selv, selv, selv, selv};
  const bf16x8 sel = __builtin_bit_cast(bf16x8, sel4);

  bf16x8 xf[28], wa[2][3], wb[3][2];
  f32x16 accA[3], accB[2];
  f32x4 csA[3], csB[2];
#pragma unroll
  for (int j = 0; j < 3; j++) { for (int i = 0; i < 16; i++) accA[j][i] = 0.f; for (int i = 0; i < 4; i++) csA[j][i] = 0.f; }
#pragma unroll
  for (int j = 0; j < 2; j++) { for (int i = 0; i < 16; i++) accB[j][i] = 0.f; for (int i = 0; i < 4; i++) csB[j][i] = 0.f; }
  u32x2 grn;

  const rsrc_t rsWD = mkrs(pwd, INSZ(1792 * XROW)), rsWO = mkrs(pwo, INSZ(448 * XROW));
  const unsigned vW2 = (lane >> 3) * XROW + (((lane & 7) ^ ((lane >> 2) & 4)) << 4) + ci * 128;
  constexpr int NS = GG ? 16 : 20;
#define RHO ((rbl + cp) & 3)
  const unsigned rho = RHO;
  auto t_of = [](int i) constexpr { return (0x54763210u >> (4 * (i >> 1))) & 7; };
  auto ksb = [&](int i) AI -> unsigned {
    if (GG) return 16 * wr + ((t_of(i) & 3) ^ rho);
    if (i < 12) { const unsigned cc = i % 6 + rho + (rho >> 1); return wr + 8 * (cc >= 6 ? cc - 6 : cc); }
    return 8 * wr + 2 * (((i - 12) >> 1) ^ rho);
  };
  auto ksi = [&](int i) AI -> unsigned { return GG ? 8 * (i & 1) + (t_of(i) & 4) : i < 12 ? 64 + 4 * (i / 6) : 112 + (i & 1); };
  auto issueW2 = [&](int i, int hf) AI {
    if (GG == 0 && i >= 12) dma(lw2, ring_area(i % RING) + hf * 1024, vW2, rsWO, ksb(i) * 14 * XROW, (16 * (i & 1) + 8 * hf) * XROW);
    else dma(lw2, ring_area(i % RING) + hf * 1024, vW2, rsWD, ksb(i) * 16 * XROW, (ksi(i) * 16 + 8 * hf) * XROW);
  };

  const unsigned aCSw = LB + L_CS + l31 * 16 + wr * 4 + GG * 2048, aCSr = LB + L_CS + l31 * 16 + GG * 2048;
  auto readX = [&](int n) AI { xf[n] = ldsr<bf16x8>(aX ^ (unsigned)((n & 3) << 5), ((n >> 2) & 3) * 16384); };
  auto readA = [&](int n, int j) AI {
    const unsigned o = ((n >> 2) & 3) * ((GG == 0 && j < 2) ? 8192 : 16384);
    wa[n & 1][j] = tr2(aA[j] ^ (unsigned)((n & 3) << 11), o, o + 512);
  };
  auto readB = [&](int n, int j) AI {
    const unsigned base = lc1_base(n >> 2);
    if ((n & 3) == 0) asm volatile("" : "+v"(aB[j]));
    wb[n % 3][j] = tr2(aB[j] ^ (((n & 3) << 11) | (base & 0x30000u)), base & 0xffffu, (base & 0xffffu) + 512);
  };
  RowC rc; bf16x8 xe; unsigned denb;
  auto rowconst = [&]() AI {
#pragma nounroll
    for (int it = 0; it < (1 << 16) && !ALL(grn[1] == E + 1); ++it) {
      __builtin_amdgcn_s_sleep(SLEEPN);
      asm volatile("buffer_load_dwordx2 %0, %1, %2, %3 offen sc1\n\ts_waitcnt vmcnt(0)" : "=v"(grn) : "v"(xrow * 8), "s"(rsWS), "s"(WS_GR + rb * 1024) : "memory");
    }
    const float den = U2F(grn[0] & 0xffff0000u);
    const unsigned nm = hh ? 0u : __builtin_amdgcn_perm(grn[0], grn[0], 0x01000100u);
    u32x4 t = {nm, nm, nm, nm}; xe = __builtin_bit_cast(bf16x8, t);
    denb = grn[0] >> 16;
    rc.r = __builtin_amdgcn_rcpf(den);
    const float k1 = -2.f * 1.4426950408889634f * 0.7978845608028654f;
    rc.K1 = k1 * rc.r; rc.K2 = (k1 * 0.044715f) * rc.r * (rc.r * rc.r); rc.Ri = den * den; rc.Lr = 1.4426950408889634f * rc.r;
  };
  waitv<7 + NCLR>();
  BAR(); MEMFENCE();
  STAMP(1);
  readX(0); readA(0, 0); readA(0, 1); readA(0, 2);
  SFOR(n, 28)
    SB();
    if constexpr (n == 27) {
#pragma unroll
      for (int j = 0; j < 3; j++) ldsw<unsigned>(aCSw, j * 512, hilo(csA[j][0]));
    }
    if constexpr ((n & 3) == 3) {
      if constexpr (n == 27) WAITV(5);
      else if constexpr (n == 23) asm volatile("s_waitcnt vmcnt(10)" : "+v"(grn) :: "memory");
      else if constexpr (n == 3) waitv<11 + NCLR>(); else if constexpr (n == 15 || n == 19) WAITV(11); else WAITV(10);
      { WAITL0(); BAR(); MEMFENCE(); }
      if constexpr (n == 27) STAMP(2);
    }
    constexpr int bar = (n + 1) >> 2, ph = (n + 1) & 3;
#pragma unroll
    for (int j = 0; j < 3; j++) {
      if constexpr (n < 27) { if (j == 0) readX(n + 1); readA(n + 1, j); }
      else { wa[0][j] = ldsr<bf16x8>(aCSr, j * 512); if (GG == 1 && j < 2) { readB(j, 0); readB(j, 1); } }
      SB();
      accA[j] = MFMA(wa[n & 1][j], xf[n], accA[j]);
      const int slot = 3 * ph + j;
      if (bar == 0) issueG(0, slot - 2);
      else if (bar <= 6) { if (slot == 0) issueG(bar, 0); if (slot == 2) issueG(bar, 1); if (slot == 5) issueG(bar, 2); if (slot == 7) issueG(bar, 3); if (slot == 10) issueG(bar, 4); }
      else if (j < 2) {
        if constexpr (AT) dma(lw2, L_FREE + j * 1024, lane * 16, mkrs(pphi, 16 * 1568), half * 8 * 1568 + w * 2048, j * 1024);
        else issueW2(0, j);
      }
      if ((n & 3) == 0) csA[j] = MFMA16(sel, wa[n & 1][j], csA[j]);
      if (n == 25 && j == 0) rowconst();
      if (n == 14 && j == 2) asm volatile("buffer_load_dwordx2 %0, %1, %2, %3 offen sc1" : "=v"(grn) : "v"(xrow * 8), "s"(rsWS), "s"(WS_GR + rb * 1024) : "memory");
      SB();
    }
  SEND
#pragma unroll
  for (int j = 0; j < 3; j++) accA[j] = MFMA(wa[0][j], xe, accA[j]);
  asm volatile("" : "+v"(accA[0]), "+v"(accA[1]), "+v"(accA[2]));
  SB();

  const unsigned sFown = WS_FH + (rt * 32 + (AT ? 28 + 2 * half : 2 * c) + GG) * FS;
  const unsigned aWP = LB + (4 * hu + q4) * 56 + 8 * pc;
  const unsigned T12 = (E + 1) & 0xfffu, T16 = (E + 1) & 0xffffu;
  const unsigned rt2 = 2 * (8 * grp + 4 * half + rbl);
  const unsigned sF = WS_FH + rt2 * 32 * FS;
  const unsigned fc = l31 >> 1;
  const bool nd1 = GG ? (fc >> 1) == wr : (fc >= 8 && fc < 14) || fc == 14 + (wr >> 1);
  const unsigned aIW = LB + L_PHI + xrow * 64 + ((hu ^ ((xrow >> 1) & 7)) << 3);
  SmS sm;
  unsigned aWK = aWP + L_WPK, aWQ = aWP + L_FREE;
  asm volatile("" : "+v"(aWK)); if constexpr (AT) asm volatile("" : "+v"(aWQ));
  auto kstep = [&](auto s_) AI {
    constexpr int s = decltype(s_)::value;
    if constexpr (s == 0) { sm.w0 = tr2(aWK, 0, 8 * 56); sm.w1 = tr2(aWK, 16 * 56, 24 * 56); }
    if constexpr (s == 1) sm.p = featproj2(accA[0], sm.w0, sm.w1, hh);
    if constexpr (s == 2 || s == 3) {
#pragma unroll
      for (int k = 2 * s - 4; k < 2 * s - 2; k++) {
        u32x2 vv = {pk2(accA[1][4 * k], accA[1][4 * k + 1]), pk2(accA[1][4 * k + 2], accA[1][4 * k + 3])};
        if (k == 3) vv[0] = hh ? denb : vv[0];
        ldsw<u32x2>(aIW ^ (unsigned)(k << 4), L_V - L_PHI, vv);
      }
    }
    if constexpr (s == 4) sm.bx = -max16(sm.p) * rc.Lr;
    if constexpr (s == 5) sm.bn = min16(sm.p) * rc.Lr;
    if constexpr (s >= 6 && s < 22) {
      constexpr int i = s - 6;
      sm.e[i] = __builtin_amdgcn_exp2f(__builtin_fmaf(sm.p[i], rc.Lr, sm.bx)); sm.en[i] = __builtin_amdgcn_exp2f(__builtin_fmaf(sm.p[i], -rc.Lr, sm.bn));
      if constexpr (i == 0) { sm.sp = sm.e[0]; sm.sn = sm.en[0]; } else if constexpr (i < 12) { sm.sp += sm.e[i]; sm.sn += sm.en[i]; }
      else if constexpr (i == 12) { sm.tp = sm.e[12]; sm.tn = sm.en[12]; } else { sm.tp += sm.e[i]; sm.tn += sm.en[i]; }
    }
    if constexpr (s == 22) { sm.sp = __builtin_amdgcn_rcpf(xsum(sm.sp + (hh ? 0.f : sm.tp))) * rc.r; sm.sn = __builtin_amdgcn_rcpf(xsum(sm.sn + (hh ? 0.f : sm.tn))) * rc.r; }
    if constexpr (s >= 23) {
#pragma unroll
      for (int u = (s == 29 ? 6 : s - 23); u <= (s == 29 ? 7 : s - 23); u++) {
        const int k = u >> 1;
        if (u & 1) { u32x2 t = {pk2s(sm.en[4 * k] * sm.sn, sm.en[4 * k + 1] * sm.sn), pk2s(sm.en[4 * k + 2] * sm.sn, sm.en[4 * k + 3] * sm.sn)}; ldsw<u32x2>(aIW ^ (unsigned)(k << 4), 8192, t); }
        else { u32x2 t = {pk2s(sm.e[4 * k] * sm.sp, sm.e[4 * k + 1] * sm.sp), pk2s(sm.e[4 * k + 2] * sm.sp, sm.e[4 * k + 3] * sm.sp)}; ldsw<u32x2>(aIW ^ (unsigned)(k << 4), 0, t); }
      }
    }
  };
  auto qstep = [&](auto s_, f32x16& a, unsigned lh) AI {
    constexpr int s = decltype(s_)::value;
    if constexpr (s == 0) { sm.w0 = tr2(aWQ, lh * 1568, lh * 1568 + 8 * 56); sm.w1 = tr2(aWQ, lh * 1568 + 16 * 56, lh * 1568 + 24 * 56); }
    if constexpr (s == 1) sm.p = featproj2(a, sm.w0, sm.w1, hh);
    if constexpr (s == 3) sm.bx = -max16(sm.p) * rc.Lr;
    if constexpr (s == 4) sm.bn = min16(sm.p) * rc.Lr;
    if constexpr (s >= 5 && s < 21) {
      constexpr int i = (s - 5) >> 1;
      float t0, t1;
      if constexpr ((s - 5) & 1) { t0 = __builtin_fmaf(sm.p[2 * i], -rc.Lr, sm.bn); t1 = __builtin_fmaf(sm.p[2 * i + 1], -rc.Lr, sm.bn); }
      else { t0 = __builtin_fmaf(sm.p[2 * i], rc.Lr, sm.bx); t1 = __builtin_fmaf(sm.p[2 * i + 1], rc.Lr, sm.bx); }
      a[8 * ((s - 5) & 1) + i] = U2F(pk2(__builtin_amdgcn_exp2f(t0), __builtin_amdgcn_exp2f(t1)));
      if constexpr (s == 20) asm volatile("" : "+v"(a));
    }
  };
  bf16x8 ka_[4], kb_[2][4]; f32x4 kd[2];
  auto kvstep = [&](auto s_) AI {
    constexpr int s = decltype(s_)::value;
    if constexpr (s == 0) {
      const unsigned g4 = lane >> 4, r0 = 8 * g4 + q4, sw = 4 * blk + (q4 >> 1);
      unsigned b0 = LB + L_PHI + r0 * 64 + ((p4 ^ sw) << 3);
      unsigned a0 = (b0 ^ ((wr >> 1) << 5)) + (wr & 1) * 8192;
#pragma unroll
      for (int k = 0; k < 4; k++) {
        ka_[k] = __builtin_shufflevector(trd2(a0, k * 2048), trd2(a0 ^ 16u, k * 2048 + 256), 0, 1, 2, 3, 4, 5, 6, 7);
        kb_[0][k] = __builtin_shufflevector(trd2(b0, L_V - L_PHI + k * 2048), trd2(b0 ^ 16u, L_V - L_PHI + k * 2048 + 256), 0, 1, 2, 3, 4, 5, 6, 7);
        kb_[1][k] = __builtin_shufflevector(trd2(b0 ^ 32u, L_V - L_PHI + k * 2048), trd2(b0 ^ 48u, L_V - L_PHI + k * 2048 + 256), 0, 1, 2, 3, 4, 5, 6, 7);
      }
    }
    if constexpr (s == 1) {
      f32x4 z = {0.f, 0.f, 0.f, 0.f}; kd[0] = z; kd[1] = z;
#pragma unroll
      for (int k = 0; k < 4; k++) { kd[0] = MFMA16(ka_[k], kb_[0][k], kd[0]); kd[1] = MFMA16(ka_[k], kb_[1][k], kd[1]); }
    }
    if constexpr (s == 3) {
#pragma unroll
      for (int d = 0; d < 2; d++) {
        u32x4 t;
#pragma unroll
        for (int i = 0; i < 4; i++) t[i] = (F2U(kd[d][i]) & ~0xfffu) | T12;
        st16(t, rsWS, (lane >> 4) * 512 + (lane & 15) * 16, ((head * 16 + rb) * 2 + (wr & 1)) * 4096 + (wr >> 1) * 2048, WS_KVP + d * 256);
      }
    }
  };
  auto qexp2 = [&](f32x16& a0, unsigned l0, f32x16& a1, unsigned l1) AI {
    const f32x16 p0 = featproj(a0, aWQ, l0 * 1568, hh), p1 = featproj(a1, aWQ, l1 * 1568, hh);
    a0 = dualexpP(p0, rc.Lr); a1 = dualexpP(p1, rc.Lr); asm volatile("" : "+v"(a0), "+v"(a1));
  };

  unsigned fl1 = 0;
  EpiS es;
  u32x4 pv[8];
  const unsigned vKP = l31 * 16 + hu * 8192, sKP = (rb * 32 + half) * 4096 + wr * 1024 + GG * 512;
  bf16x8 cb[2];
  auto ldflag = [&]() AI { asm volatile("buffer_load_dword %0, %1, %2, %3 offen sc1" : "=v"(fl1) : "v"(nd1 ? lane * FS : 0x80000000u), "s"(rsWS), "s"(sF) : "memory"); };
  auto touch = [&]() AI {
    const unsigned rw = (rbl * 8 + w) * 44 + lane;
    const bool dn = rw < 896;
    const unsigned vo = lane < 44 ? (dn ? ((rw >> 6) * 128 + 64 + (rw & 63)) : rw - 896) * XROW + ci * 128 : 0x80000000u;
    asm volatile("s_add_u32 m0, %0, %c5\n\ts_nop 0\n\tbuffer_load_dword %1, %3, 0 offen lds\n\tbuffer_load_dword %2, %4, 0 offen lds"
                 :: "s"(LB + w * 256), "v"(dn ? vo : 0x80000000u), "v"(dn ? 0x80000000u : vo), "s"(rsWD), "s"(rsWO), "i"(L_ST) : "memory", "scc");
  };
  auto stream = [&]() AI {
    __builtin_amdgcn_s_setprio(1);
    SFOR(n, 28)
      if constexpr (n == 27) { ldsw<unsigned>(aCSw, 3 * 512, hilo(csB[0][0])); ldsw<unsigned>(aCSw, 0, hilo(csB[1][0])); WAITL0(); BAR(); MEMFENCE(); }
      else if constexpr (GG == 1) {
        if constexpr (n == 13) { waitv<AT ? 0 : 3>(); BAR(); MEMFENCE(); }
        if constexpr (n == 18) { BAR(); MEMFENCE(); }
      } else if constexpr (!AT && (n & 3) == 3) { asm volatile("s_waitcnt lgkmcnt(4)" ::: "memory"); BAR(); MEMFENCE(); }
      SFOR(j, 2)
        if constexpr (n + 2 < 28) readB(n + 2, j);
        if constexpr (n == 27) cb[j] = ldsr<bf16x8>(aCSr, j ? 0 : 3 * 512);
        SB();
        accB[j] = MFMA(wb[n % 3][j], xf[n], accB[j]);
        if constexpr ((n & 3) == 0) csB[j] = MFMA16(sel, wb[n % 3][j], csB[j]);
        if constexpr (GG == 1 && n == 1 && j == 0) stflag(E + 1, rsWS, sFown);
        if constexpr (!AT) {
          if constexpr (GG == 1 && n == 20 && j == 0) touch();
          if constexpr (GG == 0 && j == 0 && n < 2) issueW2(1, n);
          if constexpr (GG == 0 && j == 0 && n >= 3 && n < 27 && (n & 1)) issueW2(1 + ((n + 1) >> 2), ((n + 1) >> 1) & 1);
          if constexpr (GG == 0 && n == 22 && j == 0) ldflag();
          if constexpr (GG == 0 && n == 27) issueW2(8, j);
        } else if constexpr (GG == 0 && n >= 24) pv[2 * (n - 24) + j] = ld16(rsWS, vKP, sKP, WS_KVP + (2 * (n - 24) + j) * 16384);
        SB();
      SEND
    SEND
    accB[0] = MFMA(cb[0], xe, accB[0]); accB[1] = MFMA(cb[1], xe, accB[1]);
    asm volatile("" : "+v"(accB[0]), "+v"(accB[1]));
    __builtin_amdgcn_s_setprio(0);
    SB();
  };
  auto pairF = [&](auto p_, const f32x16& a) AI { constexpr int p = decltype(p_)::value; epiSlice<p, 0>(a, rc, es); epiSlice<p, 1>(a, rc, es); epiSlice<p, 2>(a, rc, es); epiSlice<p, 3>(a, rc, es); };
  if constexpr (GG == 0) {
    stflag(E + 1, rsWS, sFown);
    SFOR(s, 23) kstep(s_); SEND
    asm volatile("" : "+v"(sm.sp), "+v"(sm.sn));
    SB(); waitv<AT ? 0 : 3>(); BAR(); SB();
    SFOR(s, 7) kstep(IntC<23 + s>{}); SEND
    WAITL0(); SB(); BAR(); SB();
    STAMP(3);
    SFOR(s, 4) kvstep(s_); SEND
    STAMP(4);
    if constexpr (AT) { SFOR(s, 21) qstep(s_, accA[2], 0); SEND }
    else { touch(); SFOR(p, 4) pairF(p_, accA[2]); SEND st16(es.r, rsWS, vL16, sA2, 0); }
    readB(0, 0); readB(0, 1); readB(1, 0); readB(1, 1);
    SB(); BAR(); SB();
    stream();
  } else {
    stream();
    STAMP(3);
    if constexpr (AT) {
      SFOR(s, 21) qstep(s_, accA[0], 1); SEND
      SFOR(s, 21) qstep(s_, accA[1], 2); SEND
#pragma unroll
      for (int i = 0; i < 8; i++) pv[i] = ld16(rsWS, vKP, sKP, WS_KVP + i * 16384);
      SB(); BAR(); SB();
    } else {
      issueW2(1, 0); issueW2(1, 1);
      SFOR(e, 21)
        if constexpr (e < 20) {
          if constexpr (e < 12) pairF(IntC<e & 3>{}, accA[e >> 2]); else pairF(IntC<e & 3>{}, accB[(e >> 2) - 3]);
          if constexpr ((e & 3) == 3) st16(es.r, rsWS, vL16, sA2, (e < 12 ? 1 + (e >> 2) : 3 + (e >> 2)) * 1024);
        }
        if constexpr (e == 16) ldflag();
        if constexpr (e % 3 == 2) { SB(); BAR(); SB(); issueW2(2 + e / 3, 0); issueW2(2 + e / 3, 1); }
      SEND
    }
  }

  if constexpr (AT) {
    STAMP(5);
    waittag<16384>(pv, rsWS, vKP, sKP + WS_KVP, T12, 0xfffu);
    STAMP(6);
    {
      u32x4 o;
      const f32x4 s4 = ((__builtin_bit_cast(f32x4, pv[0]) + __builtin_bit_cast(f32x4, pv[1])) + (__builtin_bit_cast(f32x4, pv[2]) + __builtin_bit_cast(f32x4, pv[3]))) +
                       ((__builtin_bit_cast(f32x4, pv[4]) + __builtin_bit_cast(f32x4, pv[5])) + (__builtin_bit_cast(f32x4, pv[6]) + __builtin_bit_cast(f32x4, pv[7])));
#pragma unroll
      for (int k = 0; k < 4; k++) {
        float sum = xsum(s4[k]);
        sum = l31 == 29 ? 1.f : sum;
        if (GG == 1) sum = wr == 3 ? 0.f : sum;
        o[k] = (pk2(sum, sum) & 0xffff0000u) | T16;
      }
      st16(o, rsWS, hh ? 0x80000000u : l31 * 16, (rb * 2 + half) * 4096 + wr * 1024 + GG * 512, WS_KVR);
    }
    qexp2(accB[0], 4 + 2 * GG, accB[1], 5 + 2 * GG);
    if constexpr (GG) { accA[2] = dualexpP(featproj(accA[2], aWQ, 3 * 1568, hh), rc.Lr); asm volatile("" : "+v"(accA[2])); }
    const unsigned sKR = (8 * half + w) * 8192;
#pragma unroll
    for (int i = 0; i < 8; i++) pv[i] = ld16(rsWS, vL16, sKR, WS_KVR + i * 1024);
    STAMP(7);
    waittag<1024>(pv, rsWS, vL16, sKR + WS_KVR, T16, 0xffffu);
    STAMP(8);
#pragma unroll
    for (int i = 0; i < 4; i++) {
      u32x4 f = {__builtin_amdgcn_perm(pv[2 * i][1], pv[2 * i][0], 0x07060302u), __builtin_amdgcn_perm(pv[2 * i][3], pv[2 * i][2], 0x07060302u),
                 __builtin_amdgcn_perm(pv[2 * i + 1][1], pv[2 * i + 1][0], 0x07060302u), __builtin_amdgcn_perm(pv[2 * i + 1][3], pv[2 * i + 1][2], 0x07060302u)};
      ldsw<u32x4>(LB + w * 4096 + vL16, i * 1024, f);
    }
    WAITL0(); BAR(); MEMFENCE();
    f32x16 op[2], on[2];
    auto omm = [&](const f32x16& phi, unsigned lh, int z) AI {
#pragma unroll
      for (int i = 0; i < 16; i++) { op[z][i] = 0.f; on[z][i] = 0.f; }
      bf16x8 kf[4];
#pragma unroll
      for (int i = 0; i < 4; i++) kf[i] = ldsr<bf16x8>(LB + vL16, lh * 4096 + i * 1024);
#pragma unroll
      for (int i = 0; i < 2; i++) { op[z] = MFMA(kf[i], sub4(phi, i), op[z]); on[z] = MFMA(kf[2 + i], sub4(phi, 2 + i), on[z]); }
      SB();
    };
    auto ofin = [&](unsigned lh, int z) AI {
      const float ia = __builtin_amdgcn_rcpf(op[z][13]), ib = __builtin_amdgcn_rcpf(on[z][13]);
      const float id = __builtin_amdgcn_rcpf(__builtin_fmaf(op[z][12], ia, __builtin_fmaf(on[z][12], ib, 1e-6f)));
      const float ca = xhi(ia * id), cb2 = xhi(ib * id);
      const f32x16 o = op[z] * ca + on[z] * cb2;
      u32x4 f0, f1;
#pragma unroll
      for (int i = 0; i < 4; i++) { f0[i] = pk2(o[2 * i], o[2 * i + 1]); f1[i] = pk2(o[8 + 2 * i], o[9 + 2 * i]); }
      f1[2] = hh ? 0u : f1[2]; f1[3] = hh ? 0u : f1[3];
      st16(f0, rsWS, vL16, sA2, (112 + 2 * lh) * 1024); st16(f1, rsWS, vL16, sA2, (113 + 2 * lh) * 1024);
      SB();
    };
    if constexpr (GG) __builtin_amdgcn_s_setprio(1);
    omm(accA[2], GG ? 3 : 0, 0); omm(accB[0], 4 + 2 * GG, 1); ofin(GG ? 3 : 0, 0); omm(accB[1], 5 + 2 * GG, 0); ofin(4 + 2 * GG, 1);
    if constexpr (GG) { omm(accA[0], 1, 1); ofin(7, 0); omm(accA[1], 2, 0); ofin(1, 1); ofin(2, 0); } else ofin(5, 0);
    STAMP(9);
  } else {
    STAMP(5);
    const unsigned sA = WS_A2 + rt2 * 144 * 1024;
    unsigned ln = vL16; asm volatile("" : "+v"(ln));
    const unsigned aW = lw2 + (ln & 0x200) + ((ln & 0xc0) << 1) + ((((ln >> 3) & 0x20) | ((ln >> 1) & 0x10)) ^ ((ln >> 1) & 0x40)) + ((ln >> 1) & 8);
    f32x16 acc[2][2];
#pragma unroll
    for (int i = 0; i < 4; i++)
#pragma unroll
      for (int k = 0; k < 16; k++) acc[i >> 1][i & 1][k] = 0.f;
    u32x4 af[NS][2];
    u32x2 xr[2];
    auto loadA = [&](int i) AI { af[i][0] = ld16(rsWS, vL16, sA + ksb(i) * 1024, ksi(i) * 1024); af[i][1] = ld16(rsWS, vL16, sA + ksb(i) * 1024, (144 + ksi(i)) * 1024); };
    const unsigned nt = wr >> 1, mt = wr & 1;
    const unsigned vO = (32 * mt + ((ln >> 4) & 31)) * XROW + ((ln >> 6) & 8) + 32 * GG, sO = rt2 * 32 * XROW + ci * 128 + nt * 64;
    if constexpr (GG == 0) { st16(epiP(accB[0], rc), rsWS, vL16, sA2, 4 * 1024); st16(epiP(accB[1], rc), rsWS, vL16, sA2, 5 * 1024); }
    STAMP(6);
    asm volatile("s_waitcnt vmcnt(%c1)" : "+v"(fl1) : "i"(GG ? 5 : 6) : "memory");
    if (!ALL(!nd1 || fl1 == E + 1u)) poll(rsWS, lane * FS, sF, E, nd1 ? 1u : 0u);
#pragma unroll
    for (int j = 0; j < PD; j++) loadA(j);
#pragma unroll
    for (int k = 0; k < 2; k++) asm volatile("buffer_load_dwordx2 %0, %1, %2, %3 offen offset:%c4" : "=v"(xr[k]) : "v"(vO), "s"(rsX), "s"(sO), "i"(16 * k) : "memory");
    STAMP(7);
    SFOR(i, NS)
      asm volatile("s_waitcnt vmcnt(%c2)" : "+v"(af[i][0]), "+v"(af[i][1]) : "i"(later_ops(i, NS, 0, NS) + (i < PD ? 2 : 0)) : "memory");
      constexpr unsigned ra = ring_area(i % RING);
      const unsigned la = aW + (ra & 0x30000u);
      bf16x8 w0 = tr2(la, ra & 0xffffu, (ra & 0xffffu) + 1024), w1 = tr2(la ^ 64u, ra & 0xffffu, (ra & 0xffffu) + 1024);
      auto stale = [&]() AI {
        unsigned m = UMAX(UMAX(UMAX(af[i][0][0], af[i][0][1]), UMAX(af[i][0][2], af[i][0][3])), UMAX(UMAX(af[i][1][0], af[i][1][1]), UMAX(af[i][1][2], af[i][1][3])));
        return ANY(m == ~0u);
      };
      if (__builtin_expect(stale(), 0)) {
        constexpr int i1 = i + 1 < NS ? i + 1 : i, i2 = i + 2 < NS ? i + 2 : i1, i3 = i + 3 < NS ? i + 3 : i2;
        const unsigned o0 = sA + (ksb(i) + ksi(i)) * 1024;
        int it = 0;
#pragma nounroll
        do { __builtin_amdgcn_s_sleep(SLEEPR); reld<1>(af[i][0], af[i][1], af[i][0], af[i][1], af[i][0], af[i][1], af[i][0], af[i][1], rsWS, vL16, o0, o0, o0, o0); } while (++it < (1 << 14) && stale());
        if constexpr (i + 1 < NS) {
          __builtin_amdgcn_s_sleep(0);
          reld<(NS - 1 - i < 3 ? NS - 1 - i : 3)>(af[i1][0], af[i1][1], af[i2][0], af[i2][1], af[i3][0], af[i3][1], af[i3][0], af[i3][1], rsWS, vL16,
                sA + (ksb(i1) + ksi(i1)) * 1024, sA + (ksb(i2) + ksi(i2)) * 1024, sA + (ksb(i3) + ksi(i3)) * 1024, o0);
        }
      }
      if constexpr (i == 6) STAMP(8);
      if constexpr (i == 12) STAMP(9);
      SB();
      if constexpr (i + PD < NS) loadA(i + PD);
#pragma unroll
      for (int mm = 0; mm < 2; mm++) acc[0][mm] = MFMA(w0, __builtin_bit_cast(bf16x8, af[i][mm]), acc[0][mm]);
      if constexpr (i + RING < NS) { WAITL0(); SB(); issueW2(i + RING, 0); issueW2(i + RING, 1); }
#pragma unroll
      for (int mm = 0; mm < 2; mm++) acc[1][mm] = MFMA(w1, __builtin_bit_cast(bf16x8, af[i][mm]), acc[1][mm]);
      SB();
    SEND
    STAMP(10);
#pragma unroll
    for (int qd = 0; qd < 4; qd++)
#pragma unroll
      for (int k = 0; k < 4; k++) {
        const unsigned ra = ring_area(2 * qd + (k >> 1)) + (k & 1) * 1024;
        ldsw<bf16x8>(lw2 + vL16 + (ra & 0x30000u), ra & 0xffffu, sub4(acc[qd >> 1][qd & 1], k));
      }
    WAITL0(); BAR(); MEMFENCE();
    STAMP(12);
    const unsigned j2 = 2 * wr + GG;
    const unsigned ra = ((0x51407362u >> (4 * j2)) & 7) * 16384;
    asm volatile("" : "+v"(xr[0]), "+v"(xr[1]));
    const rsrc_t rsO = mkrs(pout, 2048 * XROW);
#pragma unroll
    for (int k = 0; k < 2; k++) {
      f32x4 sum = {U2F(xr[k][0] << 16), U2F(xr[k][0] & 0xffff0000u), U2F(xr[k][1] << 16), U2F(xr[k][1] & 0xffff0000u)};
#pragma unroll
      for (int v = 0; v < 8; v++) sum += ldsr<f32x4>(LB + ra + vL16, v * 2048 + k * 1024);
      u32x2 o = {pk2(sum[0], sum[1]), pk2(sum[2], sum[3])};
      __builtin_amdgcn_raw_buffer_store_b64(o, rsO, vO + 16 * k, sO, 0);
    }
    if (b == 32 && w == 0) stflag(E + 4, rsWS, WS_EPOCH);
  }
  while (__builtin_amdgcn_s_memrealtime() - tst < FIXT) __builtin_amdgcn_s_sleep(1);
  };
  if (!attn && g) prog(BoolC<false>{}, IntC<1>{});
  else if (!attn) prog(BoolC<false>{}, IntC<0>{});
  else if (g) prog(BoolC<true>{}, IntC<1>{});
  else prog(BoolC<true>{}, IntC<0>{});
}

extern "C" void kernel_launch(void* const* d_in, const int* in_sizes, int n_in, void* d_out, int out_size, void* d_ws, size_t ws_size,
                              hipStream_t stream) {
  fused_kernel<<<256, 512, 0, stream>>>((const char*)d_in[0], (const char*)d_in[1], (const char*)d_in[5], (const char*)d_in[6], (char*)d_ws,
                                        (const char*)d_in[2], (const char*)d_in[7], (const char*)d_in[3], (char*)d_out);
}
```

```cpp
#define PT 256
#define PW 1
#define PL 0
#define PV 0
#define PG 256
#include <hip/hip_runtime.h>
#include <hip/hip_bf16.h>
#include <utility>

#define DI __device__ __forceinline__
#define LAS __attribute__((address_space(3)))
typedef __attribute__((ext_vector_type(8))) short bf16x8;
typedef __attribute__((ext_vector_type(4))) short s16x4;
typedef __attribute__((ext_vector_type(2))) __bf16 bf2;
typedef __attribute__((ext_vector_type(2))) float f32x2;
typedef __attribute__((ext_vector_type(4))) float f32x4;
typedef __attribute__((ext_vector_type(16))) float f32x16;
typedef __attribute__((ext_vector_type(2))) unsigned u32x2;
typedef __attribute__((ext_vector_type(4))) unsigned u32x4;
typedef __amdgpu_buffer_rsrc_t rsrc_t;
#define F2U(x) __builtin_bit_cast(unsigned, (float)(x))
#define U2F(x) __builtin_bit_cast(float, (unsigned)(x))
#define ALL(x) (__builtin_amdgcn_ballot_w64(!(x)) == 0ull)
#define ANY(x) (__builtin_amdgcn_ballot_w64(x) != 0ull)
#define UMAX(a, b) __builtin_elementwise_max((unsigned)(a), (unsigned)(b))

constexpr unsigned XROW = 896, QROW = 2688, FROW = 3584;
constexpr unsigned FS = 128;
constexpr unsigned WS_EPOCH = 0;
constexpr unsigned WS_GR = 4096;
constexpr unsigned WS_KVP = 131072;
constexpr unsigned WS_A2 = WS_KVP + 16 * 16 * 8192;
constexpr unsigned WS_ST = WS_A2 + 64 * 144 * 1024;
constexpr unsigned WS_FH = WS_ST + 262144;
constexpr unsigned WS_KVR = WS_FH + 2048 * FS;
constexpr unsigned WS_END = WS_KVR + 16 * 8192;
constexpr unsigned L_GU = 65536, L_KV = 131072;
constexpr unsigned L_TAIL = L_KV + 3 * 8192;
constexpr unsigned L_WPK = L_TAIL;
constexpr unsigned L_ST = L_TAIL + 2048;
constexpr unsigned L_CS = L_TAIL + 4096;
constexpr unsigned L_PHI = L_GU + 32768;
constexpr unsigned L_V = L_KV + 16384;
constexpr unsigned L_FREE = 32768;

#define MFMA(a, b, c) __builtin_amdgcn_mfma_f32_32x32x16_bf16((a), (b), (c), 0, 0, 0)
#define MFMA16(a, b, c) __builtin_amdgcn_mfma_f32_16x16x32_bf16((a), (b), (c), 0, 0, 0)
#define AI __attribute__((always_inline))
#define BAR() __builtin_amdgcn_s_barrier()
#define WAITV(n) asm volatile("s_waitcnt vmcnt(" #n ")" ::: "memory")
#define WAITL0() asm volatile("s_waitcnt lgkmcnt(0)" ::: "memory")
#define SB() __builtin_amdgcn_sched_barrier(0)
#define MEMFENCE() asm volatile("" ::: "memory")

template <int N> DI void waitv() { asm volatile("s_waitcnt vmcnt(%c0)" :: "i"(N) : "memory"); }

#define INSZ(x) (x)
DI rsrc_t mkrs(const void* p, unsigned bytes) { return __builtin_amdgcn_make_buffer_rsrc((void*)p, (short)0, (int)bytes, 0x00020000); }
DI void dma(unsigned lds, unsigned limm, unsigned voff, rsrc_t rs, unsigned soff, unsigned simm) {
  unsigned t;
  asm volatile("s_add_u32 m0, %1, %c5\n\ts_add_u32 %0, %4, %c6\n\tbuffer_load_dwordx4 %2, %3, %0 offen lds"
               : "=&s"(t) : "s"(lds), "v"(voff), "s"(rs), "s"(soff), "i"(limm), "i"(simm) : "memory", "scc");
}
DI s16x4 trd(unsigned a) { return __builtin_amdgcn_ds_read_tr16_b64_v4i16((LAS s16x4*)(size_t)a); }
DI s16x4 trd2(unsigned a, unsigned off) { return __builtin_amdgcn_ds_read_tr16_b64_v4i16((LAS s16x4*)((LAS char*)(size_t)a + off)); }
DI bf16x8 tr2(unsigned a, unsigned off0, unsigned off1) {
  LAS char* p = (LAS char*)(size_t)a;
  s16x4 lo = __builtin_amdgcn_ds_read_tr16_b64_v4i16((LAS s16x4*)(p + off0));
  s16x4 hi = __builtin_amdgcn_ds_read_tr16_b64_v4i16((LAS s16x4*)(p + off1));
  return __builtin_shufflevector(lo, hi, 0, 1, 2, 3, 4, 5, 6, 7);
}
template <class T> DI T ldsr(unsigned a, unsigned off) { return *(LAS T*)((LAS char*)(size_t)a + off); }
template <class T> DI void ldsw(unsigned a, unsigned off, T v) { *(LAS T*)((LAS char*)(size_t)a + off) = v; }
DI unsigned pk2(float a, float b) { f32x2 f = {a, b}; return __builtin_bit_cast(unsigned, __builtin_convertvector(f, bf2)); }
DI float rbf(float a) { return U2F(pk2(a, a) << 16); }
DI float dot2(unsigned a, unsigned b, float c) { return __builtin_amdgcn_fdot2_f32_bf16(__builtin_bit_cast(bf2, a), __builtin_bit_cast(bf2, b), c, false); }
DI float xsum(float v) { auto r = __builtin_amdgcn_permlane32_swap(F2U(v), F2U(v), false, false); return U2F(r[0]) + U2F(r[1]); }
DI float xmax(float v) { auto r = __builtin_amdgcn_permlane32_swap(F2U(v), F2U(v), false, false); return __builtin_fmaxf(U2F(r[0]), U2F(r[1])); }
DI float xmin(float v) { auto r = __builtin_amdgcn_permlane32_swap(F2U(v), F2U(v), false, false); return __builtin_fminf(U2F(r[0]), U2F(r[1])); }
DI float xhi(float v) { auto r = __builtin_amdgcn_permlane32_swap(F2U(v), F2U(v), false, false); return U2F(r[1]); }
DI void st16(u32x4 v, rsrc_t rs, unsigned voff, unsigned soff, unsigned simm) {
  unsigned t;
  asm volatile("s_add_u32 %0, %4, %c5\n\tbuffer_store_dwordx4 %1, %2, %3, %0 offen sc1\n\ts_nop 1" : "=&s"(t) : "v"(v), "v"(voff), "s"(rs), "s"(soff), "i"(simm) : "memory", "scc");
}
DI void stflag(unsigned v, rsrc_t rs, unsigned soff) {
  unsigned voff = (__builtin_amdgcn_mbcnt_hi(~0u, __builtin_amdgcn_mbcnt_lo(~0u, 0u)) == 0u) ? 0u : 0x80000000u;
  __builtin_amdgcn_raw_buffer_store_b32(v, rs, voff, soff, 16);
}
#define SLEEPR 4
#define SLEEPN 2
DI u32x4 ld16(rsrc_t rs, unsigned voff, unsigned soff, unsigned simm) {
  u32x4 r; unsigned t;
  asm volatile("s_add_u32 %1, %4, %c5\n\tbuffer_load_dwordx4 %0, %2, %3, %1 offen sc1" : "=v"(r), "=&s"(t) : "v"(voff), "s"(rs), "s"(soff), "i"(simm) : "memory", "scc");
  return r;
}
template <unsigned STEP> DI void ld8w(u32x4 (&p)[8], rsrc_t rs, unsigned voff, unsigned soff) {
  unsigned t;
#define L1(k) "buffer_load_dwordx4 %" #k ", %9, %10, %8 offen sc1\n\ts_add_u32 %8, %8, %c12\n\t"
  asm volatile("s_mov_b32 %8, %11\n\t" L1(0) L1(1) L1(2) L1(3) L1(4) L1(5) L1(6) L1(7) "s_waitcnt vmcnt(0)"
               : "=&v"(p[0]), "=&v"(p[1]), "=&v"(p[2]), "=&v"(p[3]), "=&v"(p[4]), "=&v"(p[5]), "=&v"(p[6]), "=&v"(p[7]), "=&s"(t)
               : "v"(voff), "s"(rs), "s"(soff), "i"(STEP) : "memory", "scc");
#undef L1
}
DI bool untag(u32x4 (&p)[8], unsigned tag, unsigned mask) {
  unsigned chk = 0;
#pragma unroll
  for (int i = 0; i < 8; i++)
#pragma unroll
    for (int k = 0; k < 4; k++) { p[i][k] ^= tag; chk |= p[i][k]; }
  return ALL((chk & mask) == 0u);
}
template <unsigned STEP> DI void waittag(u32x4 (&p)[8], rsrc_t rs, unsigned voff, unsigned soff, unsigned tag, unsigned mask) {
  asm volatile("s_waitcnt vmcnt(0)" : "+v"(p[0]), "+v"(p[1]), "+v"(p[2]), "+v"(p[3]), "+v"(p[4]), "+v"(p[5]), "+v"(p[6]), "+v"(p[7]) :: "memory");
#pragma nounroll
  for (int it = 0; !untag(p, tag, mask) && it < (1 << 16); ++it) { __builtin_amdgcn_s_sleep(SLEEPN); ld8w<STEP>(p, rs, voff, soff); }
}
#define RL(a, b, so) "buffer_load_dwordx4 %" #a ", %[v], %[r], %" #so " offen sc1\n\ts_add_u32 %[t], %" #so ", 147456\n\tbuffer_load_dwordx4 %" #b ", %[v], %[r], %[t] offen sc1\n\t"
template <int N> DI void reld(u32x4& a0, u32x4& b0, u32x4& a1, u32x4& b1, u32x4& a2, u32x4& b2, u32x4& a3, u32x4& b3, rsrc_t rs, unsigned voff,
                              unsigned s0, unsigned s1, unsigned s2, unsigned s3) {
  unsigned t;
  if constexpr (N == 4) asm volatile(RL(0, 1, 9) RL(2, 3, 10) RL(4, 5, 11) RL(6, 7, 12) "s_waitcnt vmcnt(0)"
      : "+v"(a0), "+v"(b0), "+v"(a1), "+v"(b1), "+v"(a2), "+v"(b2), "+v"(a3), "+v"(b3), [t] "=&s"(t) : "s"(s0), "s"(s1), "s"(s2), "s"(s3), [v] "v"(voff), [r] "s"(rs) : "memory", "scc");
  if constexpr (N == 3) asm volatile(RL(0, 1, 7) RL(2, 3, 8) RL(4, 5, 9) "s_waitcnt vmcnt(0)"
      : "+v"(a0), "+v"(b0), "+v"(a1), "+v"(b1), "+v"(a2), "+v"(b2), [t] "=&s"(t) : "s"(s0), "s"(s1), "s"(s2), [v] "v"(voff), [r] "s"(rs) : "memory", "scc");
  if constexpr (N == 2) asm volatile(RL(0, 1, 5) RL(2, 3, 6) "s_waitcnt vmcnt(0)"
      : "+v"(a0), "+v"(b0), "+v"(a1), "+v"(b1), [t] "=&s"(t) : "s"(s0), "s"(s1), [v] "v"(voff), [r] "s"(rs) : "memory", "scc");
  if constexpr (N == 1) asm volatile(RL(0, 1, 3) "s_waitcnt vmcnt(0)"
      : "+v"(a0), "+v"(b0), [t] "=&s"(t) : "s"(s0), [v] "v"(voff), [r] "s"(rs) : "memory", "scc");
}
DI void poll(rsrc_t rs, unsigned voff, unsigned soff, unsigned E, unsigned need) {
  voff = need ? voff : 0x80000000u;
#pragma nounroll
  for (int it = 0; it < (1 << 16); ++it) {
    unsigned v;
    asm volatile("buffer_load_dword %0, %1, %2, %3 offen sc1\n\ts_waitcnt vmcnt(0)" : "=v"(v) : "v"(voff), "s"(rs), "s"(soff) : "memory");
    bool ok = (need == 0u) || ((v - E - need) <= (3u - need));
    if (ALL(ok)) break;
    __builtin_amdgcn_s_sleep(SLEEPN);
  }
}

template <bool B> struct BoolC { static constexpr bool value = B; };
template <int I> struct IntC { static constexpr int value = I; };
template <int... I, class F> DI void sfor_impl(std::integer_sequence<int, I...>, F&& f) { (f(IntC<I>{}), ...); }
template <int N, class F> DI void sfor(F&& f) { sfor_impl(std::make_integer_sequence<int, N>{}, f); }
#define SFOR(n, N) sfor<N>([&](auto n##_) AI { constexpr int n = decltype(n##_)::value;
#define SEND });
struct RowC { float r, K1, K2, Ri, Lr; };

DI unsigned hilo(float v) { float hi = rbf(v); return pk2(hi, v - hi); }
DI float max3(float a, float b, float c) { return __builtin_fmaxf(__builtin_fmaxf(a, b), c); }
DI float min3(float a, float b, float c) { return __builtin_fminf(__builtin_fminf(a, b), c); }
DI unsigned pk2s(float a, float b) { unsigned r; asm("v_cvt_pk_bf16_f32 %0, %1, %2" : "=v"(r) : "v"(a), "v"(b)); return r; }
DI f32x2 sub2(const f32x16& v, int k) { f32x2 t = {v[2 * k], v[2 * k + 1]}; return t; }
DI f32x2 exp2v(f32x2 v) { f32x2 r = {__builtin_amdgcn_exp2f(v[0]), __builtin_amdgcn_exp2f(v[1])}; return r; }
DI f32x2 fma2(f32x2 a, float b, float c) { f32x2 bb = {b, b}, cc = {c, c}; return __builtin_elementwise_fma(a, bb, cc); }
DI u32x4 epiP(const f32x16& a, const RowC& c) {
  u32x4 r;
#pragma unroll
  for (int i = 0; i < 4; i++) {
    const f32x2 g = sub2(a, i), d = fma2(exp2v(g * fma2(g * g, c.K2, c.K1)), c.Ri, c.Ri);
    const f32x2 s = {__builtin_amdgcn_rcpf(d[0]), __builtin_amdgcn_rcpf(d[1])}, h = (g * sub2(a, 4 + i)) * s;
    r[i] = pk2(h[0], h[1]);
  }
  return r;
}
struct EpiS { float x0, x1, m0, m1; u32x4 r; };
template <int P, int PH> DI void epiSlice(const f32x16& a, const RowC& c, EpiS& s) {
  const float g0 = a[2 * P], g1 = a[2 * P + 1];
  if constexpr (PH == 0) { s.x0 = g0 * __builtin_fmaf(g0 * g0, c.K2, c.K1); s.x1 = g1 * __builtin_fmaf(g1 * g1, c.K2, c.K1); }
  if constexpr (PH == 1) { s.x0 = __builtin_amdgcn_exp2f(s.x0); s.x1 = __builtin_amdgcn_exp2f(s.x1); s.m0 = g0 * a[8 + 2 * P]; s.m1 = g1 * a[9 + 2 * P]; }
  if constexpr (PH == 2) { s.x0 = __builtin_amdgcn_rcpf(__builtin_fmaf(s.x0, c.Ri, c.Ri)); s.x1 = __builtin_amdgcn_rcpf(__builtin_fmaf(s.x1, c.Ri, c.Ri)); }
  if constexpr (PH == 3) s.r[P] = pk2s(s.m0 * s.x0, s.m1 * s.x1);
}
DI f32x16 featproj2(const f32x16& a, bf16x8 w0, bf16x8 w1, bool hh) {
  u32x4 q0, q1;
#pragma unroll
  for (int i = 0; i < 4; i++) { q0[i] = pk2(a[2 * i], a[2 * i + 1]); q1[i] = pk2(a[8 + 2 * i], a[9 + 2 * i]); }
  q1[2] = hh ? 0u : q1[2]; q1[3] = hh ? 0u : q1[3];
  f32x16 z;
#pragma unroll
  for (int i = 0; i < 16; i++) z[i] = 0.f;
  z = MFMA(w0, __builtin_bit_cast(bf16x8, q0), z);
  z = MFMA(w1, __builtin_bit_cast(bf16x8, q1), z);
  return z;
}
DI f32x16 featproj(const f32x16& a, unsigned wpaddr, unsigned off, bool hh) {
  return featproj2(a, tr2(wpaddr, off, off + 8 * 56), tr2(wpaddr, off + 16 * 56, off + 24 * 56), hh);
}
DI float max16(const f32x16& p) { float m = max3(p[0], p[1], p[2]); _Pragma("unroll") for (int i = 3; i < 15; i += 2) m = max3(m, p[i], p[i + 1]); return xmax(__builtin_fmaxf(m, p[15])); }
DI float min16(const f32x16& p) { float m = min3(p[0], p[1], p[2]); _Pragma("unroll") for (int i = 3; i < 15; i += 2) m = min3(m, p[i], p[i + 1]); return xmin(__builtin_fminf(m, p[15])); }
DI f32x16 dualexpP(const f32x16& p, float Lr) {
  const float bx = -max16(p) * Lr, bn = min16(p) * Lr;
  f32x16 o;
#pragma unroll
  for (int i = 0; i < 8; i++) {
    const f32x2 ep = exp2v(fma2(sub2(p, i), Lr, bx)), en = exp2v(fma2(sub2(p, i), -Lr, bn));
    o[i] = U2F(pk2(ep[0], ep[1])); o[8 + i] = U2F(pk2(en[0], en[1]));
  }
  return o;
}
struct SmS { f32x16 p, e; float en[16], bx, bn, sp, sn, tp, tn; bf16x8 w0, w1; };
DI bf16x8 sub4(const f32x16& v, int k) { f32x4 t = {v[4 * k], v[4 * k + 1], v[4 * k + 2], v[4 * k + 3]}; return __builtin_bit_cast(bf16x8, t); }

DI constexpr unsigned lc1_base(int ch) { return ch == 6 ? L_KV : ((ch & 1) ? L_GU : 0u) + (ch < 2 ? 49152u : ch < 4 ? 0u : 16384u); }
template <int CTRL, int RM> DI float dppadd(float v) { return v + U2F(__builtin_amdgcn_update_dpp(0u, F2U(v), CTRL, RM, 0xf, false)); }
DI float wavesum63(float v) {
  v = dppadd<0x111, 0xf>(v); v = dppadd<0x112, 0xf>(v); v = dppadd<0x114, 0xf>(v); v = dppadd<0x118, 0xf>(v);
  v = dppadd<0x142, 0xa>(v); return dppadd<0x143, 0xc>(v);
}

#define STAMP(i)
#define STAMPS_OUT()
#define CSTAMP(i)

constexpr int PD = 4, RING = 9;
constexpr int later_ops(int m, int n, int n0, int NS) {
  int cnt = 0; bool seen = false;
  for (int j = 0; j < PD && j < n; j++) { if (seen) cnt += 2; if (j == m) seen = true; }
  for (int st = 0; st < m; st++) {
    if (st + PD < n) { if (seen) cnt += 2; if (st + PD == m) seen = true; }
    if (n0 + st + RING < NS && seen) cnt += 2;
  }
  return cnt;
}
DI constexpr unsigned ring_area(int j) { return j == 0 ? L_FREE : j == 1 ? L_PHI : lc1_base(j - 2); }
constexpr int cumG1(int s) { return s < 4 ? 0 : s < 22 ? (s - 4) / 2 : (9 + (s - 22) * 5 / 4 < 48 ? 9 + (s - 22) * 5 / 4 : 48); }

#define CODE_BYTES 65536
__global__ __launch_bounds__(512, 2) void fused_kernel(const char* px_, const char* pqkv_, const char* pwg_, const char* pwu_, char* ws_, const char* pphi_,
                                                       const char* pwd_, const char* pwo_, char* pout_) {
  typedef __attribute__((ext_vector_type(8))) unsigned long long u64x8;
  u64x8 ka; unsigned long long kb;
  asm volatile("s_load_dwordx16 %0, %2, 0\n\ts_load_dwordx2 %1, %2, 64" : "=&s"(ka), "=&s"(kb) : "s"(__builtin_amdgcn_kernarg_segment_ptr()));
  __shared__ __attribute__((aligned(16))) char smem[163840];
  const unsigned LB = (unsigned)(size_t)smem;
  const unsigned tid = __builtin_amdgcn_workitem_id_x(), lane = tid & 63;
  const unsigned w = __builtin_amdgcn_readfirstlane(tid >> 6), g = w >> 2, wr = w & 3;
  const unsigned b = __builtin_amdgcn_workgroup_id_x(), xcd = b & 7, bi = b >> 3, grp = xcd >> 1, half = xcd & 1, rbl = bi & 3, cp = bi >> 2, ci = cp - 1;
  const unsigned rb = 4 * grp + rbl, c = 7 * half + ci;
  const unsigned head = 8 * half + (((half ? 0x76425310u : 0x75316420u) >> (4 * cp)) & 7);
  const bool attn = cp == 0;
  STAMP(0); CSTAMP(14);
  const unsigned wstr = attn ? QROW : FROW;
  const unsigned sG0 = half * (attn ? 448 : 1792) + (attn ? 0 : ci * 256), sX0 = rb * 128 * XROW;
  unsigned vx0, vx1, vG, vU, vKV;
  auto voffs = [&](unsigned ln) AI {
    const unsigned r8 = ln >> 3, c8 = ln & 7, sa = (r8 >> 1) & 1, lc = c8 ^ (4 * sa);
    vx0 = (16 * w + r8) * XROW + ((c8 ^ (r8 >> 1)) << 4); vx1 = (vx0 ^ 64u) + 8 * XROW;
    const unsigned cg = attn ? lc : (c8 ^ (2 * sa)), cu = attn ? (lc >= 6 ? lc - 2 : lc) : (cg ^ 4);
    vG = (8 * w + r8) * wstr + (cg << 4); vU = (8 * w + r8) * wstr + (cu << 4);
    vKV = (8 * w + r8) * QROW + 896 * (1 + (lc >> 2)) + ((lc & 3) << 4) + ((56 * head) & ~15u);
  };
  voffs(lane);
  const unsigned lw1 = LB + w * 1024, lw2 = LB + w * 2048;
#define ROT (((0x24924u >> (2 * (cp + rbl))) & 3) * 2)
  const unsigned rot = ROT;
  const unsigned srow = 128 * rb + 64 * half + 8 * cp + w;
  unsigned vrow = lane < 56 ? lane * 16 : 0x80000000u;
  asm volatile("s_waitcnt lgkmcnt(0)" : "+s"(ka), "+s"(kb), "+v"(vx0), "+v"(vx1), "+v"(vG), "+v"(vU), "+v"(vKV), "+v"(vrow));
  const char *px = (const char*)ka[0], *pqkv = (const char*)ka[1], *pwg = (const char*)ka[2], *pwu = (const char*)ka[3], *pphi = (const char*)ka[5],
             *pwd = (const char*)ka[6], *pwo = (const char*)ka[7];
  char *ws = (char*)ka[4], *pout = (char*)kb;
  const rsrc_t rsX = mkrs(px, INSZ(2048 * XROW)), rsQ = mkrs(pqkv, INSZ(448 * QROW));
  const rsrc_t rsG = mkrs(attn ? pqkv : pwg, INSZ(448 * wstr)), rsU = mkrs(attn ? pqkv + 128 : pwu, INSZ(448 * wstr - (attn ? 128 : 0)));
  auto cidx = [&](int st) AI -> unsigned { unsigned t = rot + st; return t >= 7 ? t - 7 : t; };
  auto issueA = [&](int st, int k) AI {
    const unsigned sl = st & 3, ch = cidx(st);
    if (k == 0) dma(lw2, sl * 16384, vx0, rsX, sX0 + ch * 128, 0);
    if (k == 1) dma(lw2, sl * 16384 + 1024, vx1, rsX, sX0 + ch * 128, 0);
    if (k == 2) dma(lw1, L_GU + sl * 16384, vG, rsG, sG0 + ch * 64 * wstr, 0);
    if (k == 3) dma(lw1, L_GU + sl * 16384 + 8192, vU, rsU, sG0 + ch * 64 * wstr, 0);
    if (k == 4) dma(lw1, L_KV + sl * 8192, vKV, rsQ, ch * 64 * QROW, 0);
  };
  const rsrc_t rsWS = mkrs(ws, WS_END);
  unsigned Ev, cpf; u32x4 xrw;
  asm volatile("buffer_load_dwordx4 %0, %1, %2, %3 offen" : "=v"(xrw) : "v"(vrow), "s"(rsX), "s"(srow * XROW) : "memory");
  asm volatile("buffer_load_dword %0, off, %1, 0 sc1" : "=v"(Ev) : "s"(rsWS) : "memory");
  {
    unsigned long long pc; asm volatile("s_getpc_b64 %0" : "=s"(pc));
    const unsigned vo = w == 0 && lane < 24 ? (bi * 24 + lane) * 128 : 0x80000000u;
    asm volatile("buffer_load_dword %0, %1, %2, 0 offen" : "=v"(cpf) : "v"(vo), "s"(mkrs((const void*)pc, CODE_BYTES)) : "memory");
  }
#pragma unroll
  for (int k = 0; k < 5; k++) issueA(0, k);
#pragma unroll
  for (int k = 0; k < 5; k++) issueA(1, k);
  issueA(2, 0);
  asm volatile("s_waitcnt vmcnt(11)" : "+v"(xrw), "+v"(Ev), "+v"(cpf) :: "memory");
  const unsigned E = __builtin_amdgcn_readfirstlane(Ev);
  {
    float s1 = 0.f, s2 = 0.f;
#pragma unroll
    for (int i = 0; i < 4; i++) { s1 = dot2(xrw[i], 0x3f803f80u, s1); s2 = dot2(xrw[i], xrw[i], s2); }
    s1 = wavesum63(s1); s2 = wavesum63(s2);
    const float mean = s1 * (1.f / 448.f), var = __builtin_fmaxf(s2 * (1.f / 448.f) - mean * mean, 0.f);
    u32x2 gr = {pk2(-mean, __builtin_amdgcn_sqrtf(rbf(rbf(var) + 1e-5f))), E + 1};
    __builtin_amdgcn_raw_buffer_store_b64(gr, rsWS, lane == 63 ? 0u : 0x80000000u, WS_GR + srow * 8, 16);
  }

  auto prog = [&](auto at_, auto gg_) AI {
  constexpr bool AT = decltype(at_)::value; constexpr int GG = decltype(gg_)::value;
  constexpr unsigned lcstep = AT ? 224 : 128;
  unsigned lane;
  asm volatile("v_mbcnt_lo_u32_b32 %0, -1, 0\n\tv_mbcnt_hi_u32_b32 %0, -1, %0" : "=v"(lane));
  __builtin_assume(lane < 64);
  voffs(lane);
  auto issueL = [&](int hc) AI {
    const int st = hc >> 1; const unsigned ch = cidx(st);
    if (hc & 1) dma(lw1, lc1_base(st) + 8192, vU, rsU, sG0 + ch * 64 * wstr, lcstep);
    else dma(lw1, lc1_base(st), vG, rsG, sG0 + ch * 64 * wstr, lcstep);
  };
  auto issueG = [&](int bar, int k) AI {
    if (bar == 0) issueA(2 + k / 5, k % 5);
    else if (bar <= 3) issueA(bar + 3, k);
    else if (k < 4) issueL(4 * (bar - 4) + k);
    else if (bar == 4) dma(LB + (w & 1) * 1024, L_WPK, lane * 16, mkrs(pphi, 16 * 1568), head * 1568 + (w & 1) * 1024, 0);
    else issueL(12 + (bar - 5));
  };

  const unsigned rt = 4 * rb + wr;
  const unsigned sA2 = WS_A2 + rt * 144 * 1024 + (AT ? half * 16384 : c * 8192), vL16 = lane * 16;
  const u32x4 sent = {~0u, ~0u, ~0u, ~0u};
  auto clr = [&](int ks) AI { st16(sent, rsWS, vL16, sA2, ks * 1024); };

  constexpr int NCLR = AT ? (GG ? 10 : 6) : (GG ? 5 : 3);
  auto clri = [&](int q) AI { clr(AT ? (GG ? (q < 6 ? 114 + q : 118 + q) : (q < 2 ? 112 + q : 118 + q)) : (GG ? (q < 3 ? 1 + q : 3 + q) : (q == 0 ? 0 : 3 + q))); };
#pragma unroll
  for (int q = 0; q < NCLR; q++) clri(q);

  const bool hh = lane >= 32;
  const unsigned hu = lane >> 5, blk = (lane >> 4) & 1, q4 = (lane >> 2) & 3, p4 = lane & 3, ta = (q4 >> 1) & 1, l31 = lane & 31;
  const unsigned perm = wr ^ (2 * GG);
  const unsigned rowoff = (8 * hu + q4) * 128 + (perm << 11);
  const unsigned pc = (4 * blk + p4) < 6 ? (4 * blk + p4) : 6;
  auto tileaddr = [&](unsigned t) AI -> unsigned {
    if constexpr (AT) { unsigned o = 56 * t + 8 * pc; return (o >> 7) * 8192 + ((((o >> 4) & 7) ^ (4 * ta)) << 4) + (o & 8) + rowoff; }
    else return blk * 8192 + (((2 * t + (p4 >> 1)) ^ (4 * blk + 2 * ta)) << 4) + 8 * (p4 & 1) + rowoff;
  };
  unsigned aA[3], aB[2];
  {
    unsigned o = (head & 1) * 8 + 8 * pc;
    unsigned ak = L_KV + (((o >> 4) ^ (4 * ta)) << 4) + (o & 8) + rowoff;
    aA[0] = LB + (GG ? L_GU + tileaddr(1) : ak);
    aA[1] = LB + (GG ? L_GU + tileaddr(2) : (ak ^ 64u));
    aA[2] = LB + L_GU + tileaddr(GG ? 3 : 0);
    aB[0] = LB + tileaddr(2 * GG); aB[1] = LB + tileaddr(2 * GG + 1);
  }
  const unsigned xrow = 32 * wr + l31;
  const unsigned aX = LB + xrow * 128 + (((2 * perm + hu) ^ ((xrow >> 1) & 7)) << 4);
  const unsigned selv = (((lane >> 4) ^ (lane >> 2)) & 1) ? 0u : 0x3f803f80u;
  const u32x4 sel4 = {selv, selv, selv, selv};
  const bf16x8 sel = __builtin_bit_cast(bf16x8, sel4);

  bf16x8 xf[28], wa[2][3], wb[3][2];
  f32x16 accA[3], accB[2];
  f32x4 csA[3], csB[2];
#pragma unroll
  for (int j = 0; j < 3; j++) { for (int i = 0; i < 16; i++) accA[j][i] = 0.f; for (int i = 0; i < 4; i++) csA[j][i] = 0.f; }
#pragma unroll
  for (int j = 0; j < 2; j++) { for (int i = 0; i < 16; i++) accB[j][i] = 0.f; for (int i = 0; i < 4; i++) csB[j][i] = 0.f; }
  u32x2 grn;

  const rsrc_t rsWD = mkrs(pwd, INSZ(1792 * XROW)), rsWO = mkrs(pwo, INSZ(448 * XROW));
  const unsigned vW2 = (lane >> 3) * XROW + (((lane & 7) ^ ((lane >> 2) & 4)) << 4) + ci * 128;
  constexpr int NS = GG ? 16 : 20;
#define RHO ((rbl + cp) & 3)
  const unsigned rho = RHO;
  auto t_of = [](int i) constexpr { return (0x54763210u >> (4 * (i >> 1))) & 7; };
  auto ksb = [&](int i) AI -> unsigned {
    if (GG) return 16 * wr + ((t_of(i) & 3) ^ rho);
    if (i < 12) { const unsigned cc = i % 6 + rho + (rho >> 1); return wr + 8 * (cc >= 6 ? cc - 6 : cc); }
    return 8 * wr + 2 * (((i - 12) >> 1) ^ rho);
  };
  auto ksi = [&](int i) AI -> unsigned { return GG ? 8 * (i & 1) + (t_of(i) & 4) : i < 12 ? 64 + 4 * (i / 6) : 112 + (i & 1); };
  auto issueW2 = [&](int i, int hf) AI {
    if (GG == 0 && i >= 12) dma(lw2, ring_area(i % RING) + hf * 1024, vW2, rsWO, ksb(i) * 14 * XROW, (16 * (i & 1) + 8 * hf) * XROW);
    else dma(lw2, ring_area(i % RING) + hf * 1024, vW2, rsWD, ksb(i) * 16 * XROW, (ksi(i) * 16 + 8 * hf) * XROW);
  };

  const unsigned aCSw = LB + L_CS + l31 * 16 + wr * 4 + GG * 2048, aCSr = LB + L_CS + l31 * 16 + GG * 2048;
  auto readX = [&](int n) AI { xf[n] = ldsr<bf16x8>(aX ^ (unsigned)((n & 3) << 5), ((n >> 2) & 3) * 16384); };
  auto readA = [&](int n, int j) AI {
    const unsigned o = ((n >> 2) & 3) * ((GG == 0 && j < 2) ? 8192 : 16384);
    wa[n & 1][j] = tr2(aA[j] ^ (unsigned)((n & 3) << 11), o, o + 512);
  };
  auto readB = [&](int n, int j) AI {
    const unsigned base = lc1_base(n >> 2);
    if ((n & 3) == 0) asm volatile("" : "+v"(aB[j]));
    wb[n % 3][j] = tr2(aB[j] ^ (((n & 3) << 11) | (base & 0x30000u)), base & 0xffffu, (base & 0xffffu) + 512);
  };
  RowC rc; bf16x8 xe; unsigned denb;
  auto rowconst = [&]() AI {
#pragma nounroll
    for (int it = 0; it < (1 << 16) && !ALL(grn[1] == E + 1); ++it) {
      __builtin_amdgcn_s_sleep(SLEEPN);
      asm volatile("buffer_load_dwordx2 %0, %1, %2, %3 offen sc1\n\ts_waitcnt vmcnt(0)" : "=v"(grn) : "v"(xrow * 8), "s"(rsWS), "s"(WS_GR + rb * 1024) : "memory");
    }
    const float den = U2F(grn[0] & 0xffff0000u);
    const unsigned nm = hh ? 0u : __builtin_amdgcn_perm(grn[0], grn[0], 0x01000100u);
    u32x4 t = {nm, nm, nm, nm}; xe = __builtin_bit_cast(bf16x8, t);
    denb = grn[0] >> 16;
    rc.r = __builtin_amdgcn_rcpf(den);
    const float k1 = -2.f * 1.4426950408889634f * 0.7978845608028654f;
    rc.K1 = k1 * rc.r; rc.K2 = (k1 * 0.044715f) * rc.r * (rc.r * rc.r); rc.Ri = den * den; rc.Lr = 1.4426950408889634f * rc.r;
  };
  waitv<7 + NCLR>();
  BAR(); MEMFENCE();
  STAMP(1);
  readX(0); readA(0, 0); readA(0, 1); readA(0, 2);
  SFOR(n, 28)
    SB();
    if constexpr (n == 27) {
#pragma unroll
      for (int j = 0; j < 3; j++) ldsw<unsigned>(aCSw, j * 512, hilo(csA[j][0]));
    }
    if constexpr ((n & 3) == 3) {
      if constexpr (n == 27) WAITV(5);
      else if constexpr (n == 23) asm volatile("s_waitcnt vmcnt(10)" : "+v"(grn) :: "memory");
      else if constexpr (n == 3) waitv<11 + NCLR>(); else if constexpr (n == 15 || n == 19) WAITV(11); else WAITV(10);
      { WAITL0(); BAR(); MEMFENCE(); }
      if constexpr (n == 27) STAMP(2);
    }
    constexpr int bar = (n + 1) >> 2, ph = (n + 1) & 3;
#pragma unroll
    for (int j = 0; j < 3; j++) {
      if constexpr (n < 27) { if (j == 0) readX(n + 1); readA(n + 1, j); }
      else { wa[0][j] = ldsr<bf16x8>(aCSr, j * 512); if (GG == 1 && j < 2) { readB(j, 0); readB(j, 1); } }
      SB();
      accA[j] = MFMA(wa[n & 1][j], xf[n], accA[j]);
      const int slot = 3 * ph + j;
      if (bar == 0) issueG(0, slot - 2);
      else if (bar <= 6) { if (slot == 0) issueG(bar, 0); if (slot == 2) issueG(bar, 1); if (slot == 5) issueG(bar, 2); if (slot == 7) issueG(bar, 3); if (slot == 10) issueG(bar, 4); }
      else if (j < 2) {
        if constexpr (AT) dma(lw2, L_FREE + j * 1024, lane * 16, mkrs(pphi, 16 * 1568), half * 8 * 1568 + w * 2048, j * 1024);
        else issueW2(0, j);
      }
      if ((n & 3) == 0) csA[j] = MFMA16(sel, wa[n & 1][j], csA[j]);
      if (n == 25 && j == 0) rowconst();
      if (n == 14 && j == 2) asm volatile("buffer_load_dwordx2 %0, %1, %2, %3 offen sc1" : "=v"(grn) : "v"(xrow * 8), "s"(rsWS), "s"(WS_GR + rb * 1024) : "memory");
      SB();
    }
  SEND
#pragma unroll
  for (int j = 0; j < 3; j++) accA[j] = MFMA(wa[0][j], xe, accA[j]);
  asm volatile("" : "+v"(accA[0]), "+v"(accA[1]), "+v"(accA[2]));
  SB();

  const unsigned sFown = WS_FH + (rt * 32 + (AT ? 28 + 2 * half : 2 * c) + GG) * FS;
  const unsigned aWP = LB + (4 * hu + q4) * 56 + 8 * pc;
  const unsigned T12 = (E + 1) & 0xfffu, T16 = (E + 1) & 0xffffu;
  const unsigned rt2 = 2 * (8 * grp + 4 * half + rbl);
  const unsigned sF = WS_FH + rt2 * 32 * FS;
  const unsigned fc = l31 >> 1;
  const bool nd1 = GG ? (fc >> 1) == wr : (fc >= 8 && fc < 14) || fc == 14 + (wr >> 1);
  const unsigned aIW = LB + L_PHI + xrow * 64 + ((hu ^ ((xrow >> 1) & 7)) << 3);
  SmS sm;
  unsigned aWK = aWP + L_WPK, aWQ = aWP + L_FREE;
  asm volatile("" : "+v"(aWK)); if constexpr (AT) asm volatile("" : "+v"(aWQ));
  auto kstep = [&](auto s_) AI {
    constexpr int s = decltype(s_)::value;
    if constexpr (s == 0) { sm.w0 = tr2(aWK, 0, 8 * 56); sm.w1 = tr2(aWK, 16 * 56, 24 * 56); }
    if constexpr (s == 1) sm.p = featproj2(accA[0], sm.w0, sm.w1, hh);
    if constexpr (s == 2 || s == 3) {
#pragma unroll
      for (int k = 2 * s - 4; k < 2 * s - 2; k++) {
        u32x2 vv = {pk2(accA[1][4 * k], accA[1][4 * k + 1]), pk2(accA[1][4 * k + 2], accA[1][4 * k + 3])};
        if (k == 3) vv[0] = hh ? denb : vv[0];
        ldsw<u32x2>(aIW ^ (unsigned)(k << 4), L_V - L_PHI, vv);
      }
    }
    if constexpr (s == 4) sm.bx = -max16(sm.p) * rc.Lr;
    if constexpr (s == 5) sm.bn = min16(sm.p) * rc.Lr;
    if constexpr (s >= 6 && s < 22) {
      constexpr int i = s - 6;
      sm.e[i] = __builtin_amdgcn_exp2f(__builtin_fmaf(sm.p[i], rc.Lr, sm.bx)); sm.en[i] = __builtin_amdgcn_exp2f(__builtin_fmaf(sm.p[i], -rc.Lr, sm.bn));
      if constexpr (i == 0) { sm.sp = sm.e[0]; sm.sn = sm.en[0]; } else if constexpr (i < 12) { sm.sp += sm.e[i]; sm.sn += sm.en[i]; }
      else if constexpr (i == 12) { sm.tp = sm.e[12]; sm.tn = sm.en[12]; } else { sm.tp += sm.e[i]; sm.tn += sm.en[i]; }
    }
    if constexpr (s == 22) { sm.sp = __builtin_amdgcn_rcpf(xsum(sm.sp + (hh ? 0.f : sm.tp))) * rc.r; sm.sn = __builtin_amdgcn_rcpf(xsum(sm.sn + (hh ? 0.f : sm.tn))) * rc.r; }
    if constexpr (s >= 23) {
#pragma unroll
      for (int u = (s == 29 ? 6 : s - 23); u <= (s == 29 ? 7 : s - 23); u++) {
        const int k = u >> 1;
        if (u & 1) { u32x2 t = {pk2s(sm.en[4 * k] * sm.sn, sm.en[4 * k + 1] * sm.sn), pk2s(sm.en[4 * k + 2] * sm.sn, sm.en[4 * k + 3] * sm.sn)}; ldsw<u32x2>(aIW ^ (unsigned)(k << 4), 8192, t); }
        else { u32x2 t = {pk2s(sm.e[4 * k] * sm.sp, sm.e[4 * k + 1] * sm.sp), pk2s(sm.e[4 * k + 2] * sm.sp, sm.e[4 * k + 3] * sm.sp)}; ldsw<u32x2>(aIW ^ (unsigned)(k << 4), 0, t); }
      }
    }
  };
  auto qstep = [&](auto s_, f32x16& a, unsigned lh) AI {
    constexpr int s = decltype(s_)::value;
    if constexpr (s == 0) { sm.w0 = tr2(aWQ, lh * 1568, lh * 1568 + 8 * 56); sm.w1 = tr2(aWQ, lh * 1568 + 16 * 56, lh * 1568 + 24 * 56); }
    if constexpr (s == 1) sm.p = featproj2(a, sm.w0, sm.w1, hh);
    if constexpr (s == 3) sm.bx = -max16(sm.p) * rc.Lr;
    if constexpr (s == 4) sm.bn = min16(sm.p) * rc.Lr;
    if constexpr (s >= 5 && s < 21) {
      constexpr int i = (s - 5) >> 1;
      float t0, t1;
      if constexpr ((s - 5) & 1) { t0 = __builtin_fmaf(sm.p[2 * i], -rc.Lr, sm.bn); t1 = __builtin_fmaf(sm.p[2 * i + 1], -rc.Lr, sm.bn); }
      else { t0 = __builtin_fmaf(sm.p[2 * i], rc.Lr, sm.bx); t1 = __builtin_fmaf(sm.p[2 * i + 1], rc.Lr, sm.bx); }
      a[8 * ((s - 5) & 1) + i] = U2F(pk2(__builtin_amdgcn_exp2f(t0), __builtin_amdgcn_exp2f(t1)));
      if constexpr (s == 20) asm volatile("" : "+v"(a));
    }
  };
  bf16x8 ka_[4], kb_[2][4]; f32x4 kd[2];
  auto kvstep = [&](auto s_) AI {
    constexpr int s = decltype(s_)::value;
    if constexpr (s == 0) {
      const unsigned g4 = lane >> 4, r0 = 8 * g4 + q4, sw = 4 * blk + (q4 >> 1);
      unsigned b0 = LB + L_PHI + r0 * 64 + ((p4 ^ sw) << 3);
      unsigned a0 = (b0 ^ ((wr >> 1) << 5)) + (wr & 1) * 8192;
#pragma unroll
      for (int k = 0; k < 4; k++) {
        ka_[k] = __builtin_shufflevector(trd2(a0, k * 2048), trd2(a0 ^ 16u, k * 2048 + 256), 0, 1, 2, 3, 4, 5, 6, 7);
        kb_[0][k] = __builtin_shufflevector(trd2(b0, L_V - L_PHI + k * 2048), trd2(b0 ^ 16u, L_V - L_PHI + k * 2048 + 256), 0, 1, 2, 3, 4, 5, 6, 7);
        kb_[1][k] = __builtin_shufflevector(trd2(b0 ^ 32u, L_V - L_PHI + k * 2048), trd2(b0 ^ 48u, L_V - L_PHI + k * 2048 + 256), 0, 1, 2, 3, 4, 5, 6, 7);
      }
    }
    if constexpr (s == 1) {
      f32x4 z = {0.f, 0.f, 0.f, 0.f}; kd[0] = z; kd[1] = z;
#pragma unroll
      for (int k = 0; k < 4; k++) { kd[0] = MFMA16(ka_[k], kb_[0][k], kd[0]); kd[1] = MFMA16(ka_[k], kb_[1][k], kd[1]); }
    }
    if constexpr (s == 3) {
#pragma unroll
      for (int d = 0; d < 2; d++) {
        u32x4 t;
#pragma unroll
        for (int i = 0; i < 4; i++) t[i] = (F2U(kd[d][i]) & ~0xfffu) | T12;
        st16(t, rsWS, (lane >> 4) * 512 + (lane & 15) * 16, ((head * 16 + rb) * 2 + (wr & 1)) * 4096 + (wr >> 1) * 2048, WS_KVP + d * 256);
      }
    }
  };
  auto qexp2 = [&](f32x16& a0, unsigned l0, f32x16& a1, unsigned l1) AI {
    const f32x16 p0 = featproj(a0, aWQ, l0 * 1568, hh), p1 = featproj(a1, aWQ, l1 * 1568, hh);
    a0 = dualexpP(p0, rc.Lr); a1 = dualexpP(p1, rc.Lr); asm volatile("" : "+v"(a0), "+v"(a1));
  };

  unsigned fl1 = 0;
  EpiS es;
  u32x4 pv[8];
  const unsigned vKP = l31 * 16 + hu * 8192, sKP = (rb * 32 + half) * 4096 + wr * 1024 + GG * 512;
  bf16x8 cb[2];
  auto ldflag = [&]() AI { asm volatile("buffer_load_dword %0, %1, %2, %3 offen sc1" : "=v"(fl1) : "v"(nd1 ? lane * FS : 0x80000000u), "s"(rsWS), "s"(sF) : "memory"); };
  auto touch = [&]() AI {
    const unsigned rw = (rbl * 8 + w) * 44 + lane;
    const bool dn = rw < 896;
    const unsigned vo = lane < 44 ? (dn ? ((rw >> 6) * 128 + 64 + (rw & 63)) : rw - 896) * XROW + ci * 128 : 0x80000000u;
    asm volatile("s_add_u32 m0, %0, %c5\n\ts_nop 0\n\tbuffer_load_dword %1, %3, 0 offen lds\n\tbuffer_load_dword %2, %4, 0 offen lds"
                 :: "s"(LB + w * 256), "v"(dn ? vo : 0x80000000u), "v"(dn ? 0x80000000u : vo), "s"(rsWD), "s"(rsWO), "i"(L_ST) : "memory", "scc");
  };
  auto stream = [&]() AI {
    __builtin_amdgcn_s_setprio(1);
    SFOR(n, 28)
      if constexpr (n == 27) { ldsw<unsigned>(aCSw, 3 * 512, hilo(csB[0][0])); ldsw<unsigned>(aCSw, 0, hilo(csB[1][0])); WAITL0(); BAR(); MEMFENCE(); }
      else if constexpr (GG == 1) {
        if constexpr (n == 13) { waitv<AT ? 0 : 3>(); BAR(); MEMFENCE(); }
        if constexpr (n == 18) { BAR(); MEMFENCE(); }
      } else if constexpr (!AT && (n & 3) == 3) { asm volatile("s_waitcnt lgkmcnt(4)" ::: "memory"); BAR(); MEMFENCE(); }
      SFOR(j, 2)
        if constexpr (n + 2 < 28) readB(n + 2, j);
        if constexpr (n == 27) cb[j] = ldsr<bf16x8>(aCSr, j ? 0 : 3 * 512);
        SB();
        accB[j] = MFMA(wb[n % 3][j], xf[n], accB[j]);
        if constexpr ((n & 3) == 0) csB[j] = MFMA16(sel, wb[n % 3][j], csB[j]);
        if constexpr (GG == 1 && n == 1 && j == 0) stflag(E + 1, rsWS, sFown);
        if constexpr (!AT) {
          if constexpr (GG == 1 && n == 20 && j == 0) touch();
          if constexpr (GG == 0 && j == 0 && n < 2) issueW2(1, n);
          if constexpr (GG == 0 && j == 0 && n >= 3 && n < 27 && (n & 1)) issueW2(1 + ((n + 1) >> 2), ((n + 1) >> 1) & 1);
          if constexpr (GG == 0 && n == 22 && j == 0) ldflag();
          if constexpr (GG == 0 && n == 27) issueW2(8, j);
        } else if constexpr (GG == 0 && n >= 24) pv[2 * (n - 24) + j] = ld16(rsWS, vKP, sKP, WS_KVP + (2 * (n - 24) + j) * 16384);
        SB();
      SEND
    SEND
    accB[0] = MFMA(cb[0], xe, accB[0]); accB[1] = MFMA(cb[1], xe, accB[1]);
    asm volatile("" : "+v"(accB[0]), "+v"(accB[1]));
    __builtin_amdgcn_s_setprio(0);
    SB();
  };
  auto pairF = [&](auto p_, const f32x16& a) AI { constexpr int p = decltype(p_)::value; epiSlice<p, 0>(a, rc, es); epiSlice<p, 1>(a, rc, es); epiSlice<p, 2>(a, rc, es); epiSlice<p, 3>(a, rc, es); };
  if constexpr (GG == 0) {
    stflag(E + 1, rsWS, sFown);
    SFOR(s, 23) kstep(s_); SEND
    asm volatile("" : "+v"(sm.sp), "+v"(sm.sn));
    SB(); waitv<AT ? 0 : 3>(); BAR(); SB();
    SFOR(s, 7) kstep(IntC<23 + s>{}); SEND
    WAITL0(); SB(); BAR(); SB();
    STAMP(3);
    SFOR(s, 4) kvstep(s_); SEND
    STAMP(4);
    if constexpr (AT) { SFOR(s, 21) qstep(s_, accA[2], 0); SEND }
    else { touch(); SFOR(p, 4) pairF(p_, accA[2]); SEND st16(es.r, rsWS, vL16, sA2, 0); }
    readB(0, 0); readB(0, 1); readB(1, 0); readB(1, 1);
    SB(); BAR(); SB();
    stream();
  } else {
    stream();
    STAMP(3);
    if constexpr (AT) {
      SFOR(s, 21) qstep(s_, accA[0], 1); SEND
      SFOR(s, 21) qstep(s_, accA[1], 2); SEND
#pragma unroll
      for (int i = 0; i < 8; i++) pv[i] = ld16(rsWS, vKP, sKP, WS_KVP + i * 16384);
      SB(); BAR(); SB();
    } else {
      issueW2(1, 0); issueW2(1, 1);
      SFOR(e, 21)
        if constexpr (e < 20) {
          if constexpr (e < 12) pairF(IntC<e & 3>{}, accA[e >> 2]); else pairF(IntC<e & 3>{}, accB[(e >> 2) - 3]);
          if constexpr ((e & 3) == 3) st16(es.r, rsWS, vL16, sA2, (e < 12 ? 1 + (e >> 2) : 3 + (e >> 2)) * 1024);
        }
        if constexpr (e == 16) ldflag();
        if constexpr (e % 3 == 2) { SB(); BAR(); SB(); issueW2(2 + e / 3, 0); issueW2(2 + e / 3, 1); }
      SEND
    }
  }

  if constexpr (AT) {
    STAMP(5);
    waittag<16384>(pv, rsWS, vKP, sKP + WS_KVP, T12, 0xfffu);
    STAMP(6);
    {
      u32x4 o;
      const f32x4 s4 = ((__builtin_bit_cast(f32x4, pv[0]) + __builtin_bit_cast(f32x4, pv[1])) + (__builtin_bit_cast(f32x4, pv[2]) + __builtin_bit_cast(f32x4, pv[3]))) +
                       ((__builtin_bit_cast(f32x4, pv[4]) + __builtin_bit_cast(f32x4, pv[5])) + (__builtin_bit_cast(f32x4, pv[6]) + __builtin_bit_cast(f32x4, pv[7])));
#pragma unroll
      for (int k = 0; k < 4; k++) {
        float sum = xsum(s4[k]);
        sum = l31 == 29 ? 1.f : sum;
        if (GG == 1) sum = wr == 3 ? 0.f : sum;
        o[k] = (pk2(sum, sum) & 0xffff0000u) | T16;
      }
      st16(o, rsWS, hh ? 0x80000000u : l31 * 16, (rb * 2 + half) * 4096 + wr * 1024 + GG * 512, WS_KVR);
    }
    qexp2(accB[0], 4 + 2 * GG, accB[1], 5 + 2 * GG);
    if constexpr (GG) { accA[2] = dualexpP(featproj(accA[2], aWQ, 3 * 1568, hh), rc.Lr); asm volatile("" : "+v"(accA[2])); }
    const unsigned sKR = (8 * half + w) * 8192;
#pragma unroll
    for (int i = 0; i < 8; i++) pv[i] = ld16(rsWS, vL16, sKR, WS_KVR + i * 1024);
    STAMP(7);
    waittag<1024>(pv, rsWS, vL16, sKR + WS_KVR, T16, 0xffffu);
    STAMP(8);
#pragma unroll
    for (int i = 0; i < 4; i++) {
      u32x4 f = {__builtin_amdgcn_perm(pv[2 * i][1], pv[2 * i][0], 0x07060302u), __builtin_amdgcn_perm(pv[2 * i][3], pv[2 * i][2], 0x07060302u),
                 __builtin_amdgcn_perm(pv[2 * i + 1][1], pv[2 * i + 1][0], 0x07060302u), __builtin_amdgcn_perm(pv[2 * i + 1][3], pv[2 * i + 1][2], 0x07060302u)};
      ldsw<u32x4>(LB + w * 4096 + vL16, i * 1024, f);
    }
    WAITL0(); BAR(); MEMFENCE();
    f32x16 op[2], on[2];
    auto omm = [&](const f32x16& phi, unsigned lh, int z) AI {
#pragma unroll
      for (int i = 0; i < 16; i++) { op[z][i] = 0.f; on[z][i] = 0.f; }
      bf16x8 kf[4];
#pragma unroll
      for (int i = 0; i < 4; i++) kf[i] = ldsr<bf16x8>(LB + vL16, lh * 4096 + i * 1024);
#pragma unroll
      for (int i = 0; i < 2; i++) { op[z] = MFMA(kf[i], sub4(phi, i), op[z]); on[z] = MFMA(kf[2 + i], sub4(phi, 2 + i), on[z]); }
      SB();
    };
    auto ofin = [&](unsigned lh, int z) AI {
      const float ia = __builtin_amdgcn_rcpf(op[z][13]), ib = __builtin_amdgcn_rcpf(on[z][13]);
      const float id = __builtin_amdgcn_rcpf(__builtin_fmaf(op[z][12], ia, __builtin_fmaf(on[z][12], ib, 1e-6f)));
      const float ca = xhi(ia * id), cb2 = xhi(ib * id);
      const f32x16 o = op[z] * ca + on[z] * cb2;
      u32x4 f0, f1;
#pragma unroll
      for (int i = 0; i < 4; i++) { f0[i] = pk2(o[2 * i], o[2 * i + 1]); f1[i] = pk2(o[8 + 2 * i], o[9 + 2 * i]); }
      f1[2] = hh ? 0u : f1[2]; f1[3] = hh ? 0u : f1[3];
      st16(f0, rsWS, vL16, sA2, (112 + 2 * lh) * 1024); st16(f1, rsWS, vL16, sA2, (113 + 2 * lh) * 1024);
      SB();
    };
    if constexpr (GG) __builtin_amdgcn_s_setprio(1);
    omm(accA[2], GG ? 3 : 0, 0); omm(accB[0], 4 + 2 * GG, 1); ofin(GG ? 3 : 0, 0); omm(accB[1], 5 + 2 * GG, 0); ofin(4 + 2 * GG, 1);
    if constexpr (GG) { omm(accA[0], 1, 1); ofin(7, 0); omm(accA[1], 2, 0); ofin(1, 1); ofin(2, 0); } else ofin(5, 0);
    STAMP(9);
  } else {
    STAMP(5);
    const unsigned sA = WS_A2 + rt2 * 144 * 1024;
    unsigned ln = vL16; asm volatile("" : "+v"(ln));
    const unsigned aW = lw2 + (ln & 0x200) + ((ln & 0xc0) << 1) + ((((ln >> 3) & 0x20) | ((ln >> 1) & 0x10)) ^ ((ln >> 1) & 0x40)) + ((ln >> 1) & 8);
    f32x16 acc[2][2];
#pragma unroll
    for (int i = 0; i < 4; i++)
#pragma unroll
      for (int k = 0; k < 16; k++) acc[i >> 1][i & 1][k] = 0.f;
    u32x4 af[NS][2];
    u32x2 xr[2];
    auto loadA = [&](int i) AI { af[i][0] = ld16(rsWS, vL16, sA + ksb(i) * 1024, ksi(i) * 1024); af[i][1] = ld16(rsWS, vL16, sA + ksb(i) * 1024, (144 + ksi(i)) * 1024); };
    const unsigned nt = wr >> 1, mt = wr & 1;
    const unsigned vO = (32 * mt + ((ln >> 4) & 31)) * XROW + ((ln >> 6) & 8) + 32 * GG, sO = rt2 * 32 * XROW + ci * 128 + nt * 64;
    if constexpr (GG == 0) { st16(epiP(accB[0], rc), rsWS, vL16, sA2, 4 * 1024); st16(epiP(accB[1], rc), rsWS, vL16, sA2, 5 * 1024); }
    STAMP(6);
    asm volatile("s_waitcnt vmcnt(%c1)" : "+v"(fl1) : "i"(GG ? 5 : 6) : "memory");
    if (!ALL(!nd1 || fl1 == E + 1u)) poll(rsWS, lane * FS, sF, E, nd1 ? 1u : 0u);
#pragma unroll
    for (int j = 0; j < PD; j++) loadA(j);
#pragma unroll
    for (int k = 0; k < 2; k++) asm volatile("buffer_load_dwordx2 %0, %1, %2, %3 offen offset:%c4" : "=v"(xr[k]) : "v"(vO), "s"(rsX), "s"(sO), "i"(16 * k) : "memory");
    STAMP(7);
    SFOR(i, NS)
      asm volatile("s_waitcnt vmcnt(%c2)" : "+v"(af[i][0]), "+v"(af[i][1]) : "i"(later_ops(i, NS, 0, NS) + (i < PD ? 2 : 0)) : "memory");
      constexpr unsigned ra = ring_area(i % RING);
      const unsigned la = aW + (ra & 0x30000u);
      bf16x8 w0 = tr2(la, ra & 0xffffu, (ra & 0xffffu) + 1024), w1 = tr2(la ^ 64u, ra & 0xffffu, (ra & 0xffffu) + 1024);
      auto stale = [&]() AI {
        unsigned m = UMAX(UMAX(UMAX(af[i][0][0], af[i][0][1]), UMAX(af[i][0][2], af[i][0][3])), UMAX(UMAX(af[i][1][0], af[i][1][1]), UMAX(af[i][1][2], af[i][1][3])));
        return ANY(m == ~0u);
      };
      if (__builtin_expect(stale(), 0)) {
        constexpr int i1 = i + 1 < NS ? i + 1 : i, i2 = i + 2 < NS ? i + 2 : i1, i3 = i + 3 < NS ? i + 3 : i2;
        const unsigned o0 = sA + (ksb(i) + ksi(i)) * 1024;
        int it = 0;
#pragma nounroll
        do { __builtin_amdgcn_s_sleep(SLEEPR); reld<1>(af[i][0], af[i][1], af[i][0], af[i][1], af[i][0], af[i][1], af[i][0], af[i][1], rsWS, vL16, o0, o0, o0, o0); } while (++it < (1 << 14) && stale());
        if constexpr (i + 1 < NS) {
          __builtin_amdgcn_s_sleep(0);
          reld<(NS - 1 - i < 3 ? NS - 1 - i : 3)>(af[i1][0], af[i1][1], af[i2][0], af[i2][1], af[i3][0], af[i3][1], af[i3][0], af[i3][1], rsWS, vL16,
                sA + (ksb(i1) + ksi(i1)) * 1024, sA + (ksb(i2) + ksi(i2)) * 1024, sA + (ksb(i3) + ksi(i3)) * 1024, o0);
        }
      }
      if constexpr (i == 6) STAMP(8);
      if constexpr (i == 12) STAMP(9);
      SB();
      if constexpr (i + PD < NS) loadA(i + PD);
#pragma unroll
      for (int mm = 0; mm < 2; mm++) acc[0][mm] = MFMA(w0, __builtin_bit_cast(bf16x8, af[i][mm]), acc[0][mm]);
      if constexpr (i + RING < NS) { WAITL0(); SB(); issueW2(i + RING, 0); issueW2(i + RING, 1); }
#pragma unroll
      for (int mm = 0; mm < 2; mm++) acc[1][mm] = MFMA(w1, __builtin_bit_cast(bf16x8, af[i][mm]), acc[1][mm]);
      SB();
    SEND
    STAMP(10);
#pragma unroll
    for (int qd = 0; qd < 4; qd++)
#pragma unroll
      for (int k = 0; k < 4; k++) {
        const unsigned ra = ring_area(2 * qd + (k >> 1)) + (k & 1) * 1024;
        ldsw<bf16x8>(lw2 + vL16 + (ra & 0x30000u), ra & 0xffffu, sub4(acc[qd >> 1][qd & 1], k));
      }
    WAITL0(); BAR(); MEMFENCE();
    STAMP(12);
    const unsigned j2 = 2 * wr + GG;
    const unsigned ra = ((0x51407362u >> (4 * j2)) & 7) * 16384;
    asm volatile("" : "+v"(xr[0]), "+v"(xr[1]));
    const rsrc_t rsO = mkrs(pout, 2048 * XROW);
#pragma unroll
    for (int k = 0; k < 2; k++) {
      f32x4 sum = {U2F(xr[k][0] << 16), U2F(xr[k][0] & 0xffff0000u), U2F(xr[k][1] << 16), U2F(xr[k][1] & 0xffff0000u)};
#pragma unroll
      for (int v = 0; v < 8; v++) sum += ldsr<f32x4>(LB + ra + vL16, v * 2048 + k * 1024);
      u32x2 o = {pk2(sum[0], sum[1]), pk2(sum[2], sum[3])};
      __builtin_amdgcn_raw_buffer_store_b64(o, rsO, vO + 16 * k, sO, 0);
    }
    if (b == 32 && w == 0) stflag(E + 4, rsWS, WS_EPOCH);
  }
  STAMPS_OUT();
  };
  if (!attn && g) prog(BoolC<false>{}, IntC<1>{});
  else if (!attn) prog(BoolC<false>{}, IntC<0>{});
  else if (g) prog(BoolC<true>{}, IntC<1>{});
  else prog(BoolC<true>{}, IntC<0>{});
}

__global__ __launch_bounds__(PT, PW) void probe(char* ws) {
  const unsigned long long t0 = __builtin_amdgcn_s_memrealtime();
  while (__builtin_amdgcn_s_memrealtime() - t0 < 1000) __builtin_amdgcn_s_sleep(1);
}
extern "C" void kernel_launch(void* const* d_in, const int* in_sizes, int n_in, void* d_out, int out_size, void* d_ws, size_t ws_size,
                              hipStream_t stream) {
  probe<<<PG, PT, 0, stream>>>((char*)d_ws);
  fused_kernel<<<256, 512, 0, stream>>>((const char*)d_in[0], (const char*)d_in[1], (const char*)d_in[5], (const char*)d_in[6], (char*)d_ws,
                                        (const char*)d_in[2], (const char*)d_in[7], (const char*)d_in[3], (char*)d_out);
}
```

```cpp
#include <hip/hip_runtime.h>
#include <hip/hip_bf16.h>
#include <utility>

#define DI __device__ __forceinline__
#define LAS __attribute__((address_space(3)))
typedef __attribute__((ext_vector_type(8))) short bf16x8;
typedef __attribute__((ext_vector_type(4))) short s16x4;
typedef __attribute__((ext_vector_type(2))) __bf16 bf2;
typedef __attribute__((ext_vector_type(2))) float f32x2;
typedef __attribute__((ext_vector_type(4))) float f32x4;
typedef __attribute__((ext_vector_type(16))) float f32x16;
typedef __attribute__((ext_vector_type(2))) unsigned u32x2;
typedef __attribute__((ext_vector_type(4))) unsigned u32x4;
typedef __amdgpu_buffer_rsrc_t rsrc_t;
#define F2U(x) __builtin_bit_cast(unsigned, (float)(x))
#define U2F(x) __builtin_bit_cast(float, (unsigned)(x))
#define ALL(x) (__builtin_amdgcn_ballot_w64(!(x)) == 0ull)
#define ANY(x) (__builtin_amdgcn_ballot_w64(x) != 0ull)
#define UMAX(a, b) __builtin_elementwise_max((unsigned)(a), (unsigned)(b))

constexpr unsigned XROW = 896, QROW = 2688, FROW = 3584;
constexpr unsigned FS = 128;
constexpr unsigned WS_EPOCH = 0;
constexpr unsigned WS_GR = 4096;
constexpr unsigned WS_KVP = 131072;
constexpr unsigned WS_A2 = WS_KVP + 16 * 16 * 8192;
constexpr unsigned WS_ST = WS_A2 + 64 * 144 * 1024;
constexpr unsigned WS_FH = WS_ST + 262144;
constexpr unsigned WS_KVR = WS_FH + 2048 * FS;
constexpr unsigned WS_END = WS_KVR + 16 * 8192;
constexpr unsigned L_GU = 65536, L_KV = 131072;
constexpr unsigned L_TAIL = L_KV + 3 * 8192;
constexpr unsigned L_WPK = L_TAIL;
constexpr unsigned L_ST = L_TAIL + 2048;
constexpr unsigned L_CS = L_TAIL + 4096;
constexpr unsigned L_PHI = L_GU + 32768;
constexpr unsigned L_V = L_KV + 16384;
constexpr unsigned L_FREE = 32768;

#define MFMA(a, b, c) __builtin_amdgcn_mfma_f32_32x32x16_bf16((a), (b), (c), 0, 0, 0)
#define MFMA16(a, b, c) __builtin_amdgcn_mfma_f32_16x16x32_bf16((a), (b), (c), 0, 0, 0)
#define AI __attribute__((always_inline))
#define BAR() __builtin_amdgcn_s_barrier()
#define WAITV(n) asm volatile("s_waitcnt vmcnt(" #n ")" ::: "memory")
#define WAITL0() asm volatile("s_waitcnt lgkmcnt(0)" ::: "memory")
#define SB() __builtin_amdgcn_sched_barrier(0)
#define MEMFENCE() asm volatile("" ::: "memory")

template <int N> DI void waitv() { asm volatile("s_waitcnt vmcnt(%c0)" :: "i"(N) : "memory"); }

#define INSZ(x) (x)
DI rsrc_t mkrs(const void* p, unsigned bytes) { return __builtin_amdgcn_make_buffer_rsrc((void*)p, (short)0, (int)bytes, 0x00020000); }
DI void dma(unsigned lds, unsigned limm, unsigned voff, rsrc_t rs, unsigned soff, unsigned simm) {
  unsigned t;
  asm volatile("s_add_u32 m0, %1, %c5\n\ts_add_u32 %0, %4, %c6\n\tbuffer_load_dwordx4 %2, %3, %0 offen lds"
               : "=&s"(t) : "s"(lds), "v"(voff), "s"(rs), "s"(soff), "i"(limm), "i"(simm) : "memory", "scc");
}
DI s16x4 trd(unsigned a) { return __builtin_amdgcn_ds_read_tr16_b64_v4i16((LAS s16x4*)(size_t)a); }
DI s16x4 trd2(unsigned a, unsigned off) { return __builtin_amdgcn_ds_read_tr16_b64_v4i16((LAS s16x4*)((LAS char*)(size_t)a + off)); }
DI bf16x8 tr2(unsigned a, unsigned off0, unsigned off1) {
  LAS char* p = (LAS char*)(size_t)a;
  s16x4 lo = __builtin_amdgcn_ds_read_tr16_b64_v4i16((LAS s16x4*)(p + off0));
  s16x4 hi = __builtin_amdgcn_ds_read_tr16_b64_v4i16((LAS s16x4*)(p + off1));
  return __builtin_shufflevector(lo, hi, 0, 1, 2, 3, 4, 5, 6, 7);
}
template <class T> DI T ldsr(unsigned a, unsigned off) { return *(LAS T*)((LAS char*)(size_t)a + off); }
template <class T> DI void ldsw(unsigned a, unsigned off, T v) { *(LAS T*)((LAS char*)(size_t)a + off) = v; }
DI unsigned pk2(float a, float b) { f32x2 f = {a, b}; return __builtin_bit_cast(unsigned, __builtin_convertvector(f, bf2)); }
DI float rbf(float a) { return U2F(pk2(a, a) << 16); }
DI float dot2(unsigned a, unsigned b, float c) { return __builtin_amdgcn_fdot2_f32_bf16(__builtin_bit_cast(bf2, a), __builtin_bit_cast(bf2, b), c, false); }
DI float xsum(float v) { auto r = __builtin_amdgcn_permlane32_swap(F2U(v), F2U(v), false, false); float a = U2F(r[0]); asm("" : "+v"(a)); return a + U2F(r[1]); }
DI float xmax(float v) { auto r = __builtin_amdgcn_permlane32_swap(F2U(v), F2U(v), false, false); return __builtin_fmaxf(U2F(r[0]), U2F(r[1])); }
DI float xmin(float v) { auto r = __builtin_amdgcn_permlane32_swap(F2U(v), F2U(v), false, false); return __builtin_fminf(U2F(r[0]), U2F(r[1])); }
DI float xhi(float v) { auto r = __builtin_amdgcn_permlane32_swap(F2U(v), F2U(v), false, false); return U2F(r[1]); }
DI void st16(u32x4 v, rsrc_t rs, unsigned voff, unsigned soff, unsigned simm) {
  unsigned t;
  asm volatile("s_add_u32 %0, %4, %c5\n\tbuffer_store_dwordx4 %1, %2, %3, %0 offen sc1\n\ts_nop 1" : "=&s"(t) : "v"(v), "v"(voff), "s"(rs), "s"(soff), "i"(simm) : "memory", "scc");
}
DI void stflag(unsigned v, rsrc_t rs, unsigned soff) {
  unsigned voff = (__builtin_amdgcn_mbcnt_hi(~0u, __builtin_amdgcn_mbcnt_lo(~0u, 0u)) == 0u) ? 0u : 0x80000000u;
  __builtin_amdgcn_raw_buffer_store_b32(v, rs, voff, soff, 16);
}
#define XORCOND (n == 24)
#define PS 0
#define PV 0
#define KEEP2 0
#define S2N 14
#define B1N 23
#define DIST 2
#define P2G1 1
#define SLEEPR 4
#define SLEEPN 2
DI u32x4 ld16(rsrc_t rs, unsigned voff, unsigned soff, unsigned simm) {
  u32x4 r; unsigned t;
  asm volatile("s_add_u32 %1, %4, %c5\n\tbuffer_load_dwordx4 %0, %2, %3, %1 offen sc1" : "=v"(r), "=&s"(t) : "v"(voff), "s"(rs), "s"(soff), "i"(simm) : "memory", "scc");
  return r;
}
template <unsigned STEP> DI void ld8w(u32x4 (&p)[8], rsrc_t rs, unsigned voff, unsigned soff) {
  unsigned t;
#define L1(k) "buffer_load_dwordx4 %" #k ", %9, %10, %8 offen sc1\n\ts_add_u32 %8, %8, %c12\n\t"
  asm volatile("s_mov_b32 %8, %11\n\t" L1(0) L1(1) L1(2) L1(3) L1(4) L1(5) L1(6) L1(7) "s_waitcnt vmcnt(0)"
               : "=&v"(p[0]), "=&v"(p[1]), "=&v"(p[2]), "=&v"(p[3]), "=&v"(p[4]), "=&v"(p[5]), "=&v"(p[6]), "=&v"(p[7]), "=&s"(t)
               : "v"(voff), "s"(rs), "s"(soff), "i"(STEP) : "memory", "scc");
#undef L1
}
DI bool untag(u32x4 (&p)[8], unsigned tag, unsigned mask) {
  unsigned chk = 0;
#pragma unroll
  for (int i = 0; i < 8; i++)
#pragma unroll
    for (int k = 0; k < 4; k++) { p[i][k] ^= tag; chk |= p[i][k]; }
  return ALL((chk & mask) == 0u);
}
template <unsigned STEP> DI void waittag(u32x4 (&p)[8], rsrc_t rs, unsigned voff, unsigned soff, unsigned tag, unsigned mask) {
  asm volatile("s_waitcnt vmcnt(0)" : "+v"(p[0]), "+v"(p[1]), "+v"(p[2]), "+v"(p[3]), "+v"(p[4]), "+v"(p[5]), "+v"(p[6]), "+v"(p[7]) :: "memory");
#pragma nounroll
  for (int it = 0; !untag(p, tag, mask) && it < (1 << 16); ++it) { __builtin_amdgcn_s_sleep(SLEEPN); ld8w<STEP>(p, rs, voff, soff); }
}
template <bool WAIT> DI void rel1(u32x4& a, u32x4& b, rsrc_t rs, unsigned voff, unsigned so) {
  unsigned t;
  if constexpr (WAIT) asm volatile("buffer_load_dwordx4 %0, %3, %4, %5 offen sc1\n\ts_add_u32 %2, %5, 147456\n\tbuffer_load_dwordx4 %1, %3, %4, %2 offen sc1\n\ts_waitcnt vmcnt(0)"
                                   : "+v"(a), "+v"(b), "=&s"(t) : "v"(voff), "s"(rs), "s"(so) : "memory", "scc");
  else asm volatile("buffer_load_dwordx4 %0, %3, %4, %5 offen sc1\n\ts_add_u32 %2, %5, 147456\n\tbuffer_load_dwordx4 %1, %3, %4, %2 offen sc1"
                    : "+v"(a), "+v"(b), "=&s"(t) : "v"(voff), "s"(rs), "s"(so) : "memory", "scc");
}
DI void poll(rsrc_t rs, unsigned voff, unsigned soff, unsigned E, unsigned need) {
  voff = need ? voff : 0x80000000u;
#pragma nounroll
  for (int it = 0; it < (1 << 16); ++it) {
    unsigned v;
    asm volatile("buffer_load_dword %0, %1, %2, %3 offen sc1\n\ts_waitcnt vmcnt(0)" : "=v"(v) : "v"(voff), "s"(rs), "s"(soff) : "memory");
    bool ok = (need == 0u) || ((v - E - need) <= (3u - need));
    if (ALL(ok)) break;
    __builtin_amdgcn_s_sleep(SLEEPN);
  }
}

template <bool B> struct BoolC { static constexpr bool value = B; };
template <int I> struct IntC { static constexpr int value = I; };
template <int... I, class F> DI void sfor_impl(std::integer_sequence<int, I...>, F&& f) { (f(IntC<I>{}), ...); }
template <int N, class F> DI void sfor(F&& f) { sfor_impl(std::make_integer_sequence<int, N>{}, f); }
#define SFOR(n, N) sfor<N>([&](auto n##_) AI { constexpr int n = decltype(n##_)::value;
#define SEND });
struct RowC { float r, K1, K2, Ri, Lr; };

DI unsigned hilo(float v) { float hi = rbf(v); return pk2(hi, v - hi); }
DI float max3(float a, float b, float c) { return __builtin_fmaxf(__builtin_fmaxf(a, b), c); }
DI float min3(float a, float b, float c) { return __builtin_fminf(__builtin_fminf(a, b), c); }
DI unsigned pk2s(float a, float b) { unsigned r; asm("v_cvt_pk_bf16_f32 %0, %1, %2" : "=v"(r) : "v"(a), "v"(b)); return r; }
DI f32x2 sub2(const f32x16& v, int k) { f32x2 t = {v[2 * k], v[2 * k + 1]}; return t; }
DI f32x2 exp2v(f32x2 v) { f32x2 r = {__builtin_amdgcn_exp2f(v[0]), __builtin_amdgcn_exp2f(v[1])}; return r; }
DI f32x2 dup2(float a) { f32x2 r = {a, a}; asm("" : "+v"(r)); return r; }
DI f32x2 fma2(f32x2 a, f32x2 b, f32x2 c) { return __builtin_elementwise_fma(a, b, c); }
DI u32x4 epiP(const f32x16& a, const RowC& c) {
  u32x4 r;
  const f32x2 K1 = dup2(c.K1), K2 = dup2(c.K2), Ri = dup2(c.Ri);
#pragma unroll
  for (int i = 0; i < 4; i++) {
    const f32x2 g = sub2(a, i), d = fma2(exp2v(g * fma2(g * g, K2, K1)), Ri, Ri);
    const f32x2 s = {__builtin_amdgcn_rcpf(d[0]), __builtin_amdgcn_rcpf(d[1])}, h = (g * sub2(a, 4 + i)) * s;
    r[i] = pk2(h[0], h[1]);
  }
  return r;
}
struct EpiS { float x0, x1, m0, m1; u32x4 r; };
template <int P, int PH> DI void epiSlice(const f32x16& a, const RowC& c, EpiS& s) {
  const float g0 = a[2 * P], g1 = a[2 * P + 1];
  if constexpr (PH == 0) { s.x0 = g0 * __builtin_fmaf(g0 * g0, c.K2, c.K1); s.x1 = g1 * __builtin_fmaf(g1 * g1, c.K2, c.K1); }
  if constexpr (PH == 1) { s.x0 = __builtin_amdgcn_exp2f(s.x0); s.x1 = __builtin_amdgcn_exp2f(s.x1); s.m0 = g0 * a[8 + 2 * P]; s.m1 = g1 * a[9 + 2 * P]; }
  if constexpr (PH == 2) { s.x0 = __builtin_amdgcn_rcpf(__builtin_fmaf(s.x0, c.Ri, c.Ri)); s.x1 = __builtin_amdgcn_rcpf(__builtin_fmaf(s.x1, c.Ri, c.Ri)); }
  if constexpr (PH == 3) s.r[P] = pk2s(s.m0 * s.x0, s.m1 * s.x1);
}
DI f32x16 featproj2(const f32x16& a, bf16x8 w0, bf16x8 w1, bool hh) {
  u32x4 q0, q1;
#pragma unroll
  for (int i = 0; i < 4; i++) { q0[i] = pk2(a[2 * i], a[2 * i + 1]); q1[i] = pk2(a[8 + 2 * i], a[9 + 2 * i]); }
  q1[2] = hh ? 0u : q1[2]; q1[3] = hh ? 0u : q1[3];
  f32x16 z;
#pragma unroll
  for (int i = 0; i < 16; i++) z[i] = 0.f;
  z = MFMA(w0, __builtin_bit_cast(bf16x8, q0), z);
  z = MFMA(w1, __builtin_bit_cast(bf16x8, q1), z);
  return z;
}
DI f32x16 featproj(const f32x16& a, unsigned wpaddr, unsigned off, bool hh) {
  return featproj2(a, tr2(wpaddr, off, off + 8 * 56), tr2(wpaddr, off + 16 * 56, off + 24 * 56), hh);
}
DI float max16(const f32x16& p) { float m = max3(p[0], p[1], p[2]); _Pragma("unroll") for (int i = 3; i < 15; i += 2) m = max3(m, p[i], p[i + 1]); return xmax(__builtin_fmaxf(m, p[15])); }
DI float min16(const f32x16& p) { float m = min3(p[0], p[1], p[2]); _Pragma("unroll") for (int i = 3; i < 15; i += 2) m = min3(m, p[i], p[i + 1]); return xmin(__builtin_fminf(m, p[15])); }
DI f32x16 dualexpP(const f32x16& p, float Lr) {
  const f32x2 bx = dup2(-max16(p) * Lr), bn = dup2(min16(p) * Lr), L2 = dup2(Lr);
  f32x16 o;
#pragma unroll
  for (int i = 0; i < 8; i++) {
    const f32x2 ep = exp2v(fma2(sub2(p, i), L2, bx)), en = exp2v(fma2(sub2(p, i), -L2, bn));
    o[i] = U2F(pk2(ep[0], ep[1])); o[8 + i] = U2F(pk2(en[0], en[1]));
  }
  return o;
}
struct SmS { f32x16 p, e; float en[16], bx, bn, sp, sn, tp, tn; bf16x8 w0, w1; };
DI bf16x8 sub4(const f32x16& v, int k) { f32x4 t = {v[4 * k], v[4 * k + 1], v[4 * k + 2], v[4 * k + 3]}; return __builtin_bit_cast(bf16x8, t); }

DI constexpr unsigned lc1_base(int ch) { return ch == 6 ? L_KV : ((ch & 1) ? L_GU : 0u) + (ch < 2 ? 49152u : ch < 4 ? 0u : 16384u); }
template <int CTRL, int RM> DI float dppadd(float v) { return v + U2F(__builtin_amdgcn_update_dpp(0u, F2U(v), CTRL, RM, 0xf, false)); }
DI float wavesum63(float v) {
  v = dppadd<0x111, 0xf>(v); v = dppadd<0x112, 0xf>(v); v = dppadd<0x114, 0xf>(v); v = dppadd<0x118, 0xf>(v);
  v = dppadd<0x142, 0xa>(v); return dppadd<0x143, 0xc>(v);
}

#define STAMP(i)
#define STAMPS_OUT()
#define CSTAMP(i)

#define PDV 5
constexpr int PD = PDV, RING = 9;
constexpr int later2(int i, int NS, int GG) {
  const int younger = 2 * ((i + PD <= NS ? i + PD : NS) - 1 - i);
  int extra = i < PD ? 2 : 0;
  if (GG == 0 && (i == 10 || i == 11)) extra += 2 * (i - 9);
  return younger + extra;
}
DI constexpr unsigned ring_area(int j) { return j == 0 ? L_FREE : j == 1 ? L_PHI : lc1_base(j - 2); }
constexpr int cumG1(int s) { return s < 4 ? 0 : s < 22 ? (s - 4) / 2 : (9 + (s - 22) * 5 / 4 < 48 ? 9 + (s - 22) * 5 / 4 : 48); }

#define CODE_BYTES 65536
__global__ __launch_bounds__(512, 2) void fused_kernel(const char* px_, const char* pqkv_, const char* pwg_, const char* pwu_, char* ws_, const char* pphi_,
                                                       const char* pwd_, const char* pwo_, char* pout_) {
  typedef __attribute__((ext_vector_type(8))) unsigned long long u64x8;
  u64x8 ka; unsigned long long kb;
  asm volatile("s_load_dwordx16 %0, %2, 0\n\ts_load_dwordx2 %1, %2, 64" : "=&s"(ka), "=&s"(kb) : "s"(__builtin_amdgcn_kernarg_segment_ptr()));
  __shared__ __attribute__((aligned(16))) char smem[163840];
  const unsigned LB = (unsigned)(size_t)smem;
  const unsigned tid = __builtin_amdgcn_workitem_id_x(), lane = tid & 63;
  const unsigned w = __builtin_amdgcn_readfirstlane(tid >> 6), g = w >> 2, wr = w & 3;
  const unsigned b = __builtin_amdgcn_workgroup_id_x(), xcd = b & 7, bi = b >> 3, grp = xcd >> 1, half = xcd & 1, rbl = bi & 3, cp = bi >> 2, ci = cp - 1;
  const unsigned rb = 4 * grp + rbl, c = 7 * half + ci;
  const unsigned head = 8 * half + (((half ? 0x76425310u : 0x75316420u) >> (4 * cp)) & 7);
  const bool attn = cp == 0;
  STAMP(0); CSTAMP(14);
  const unsigned wstr = attn ? QROW : FROW;
  const unsigned sG0 = half * (attn ? 448 : 1792) + (attn ? 0 : ci * 256), sX0 = rb * 128 * XROW;
  unsigned vx0, vx1, vG, vU, vKV;
  auto voffs = [&](unsigned ln) AI {
    const unsigned r8 = ln >> 3, c8 = ln & 7, sa = (r8 >> 1) & 1, lc = c8 ^ (4 * sa);
    vx0 = (16 * w + r8) * XROW + ((c8 ^ (r8 >> 1)) << 4); vx1 = (vx0 ^ 64u) + 8 * XROW;
    const unsigned cg = attn ? lc : (c8 ^ (2 * sa)), cu = attn ? (lc >= 6 ? lc - 2 : lc) : (cg ^ 4);
    vG = (8 * w + r8) * wstr + (cg << 4); vU = (8 * w + r8) * wstr + (cu << 4);
    vKV = (8 * w + r8) * QROW + 896 * (1 + (lc >> 2)) + ((lc & 3) << 4) + ((56 * head) & ~15u);
  };
  voffs(lane);
  const unsigned lw1 = LB + w * 1024, lw2 = LB + w * 2048;
#define ROT (((0x24924u >> (2 * (cp + rbl))) & 3) * 2)
  const unsigned rot = ROT;
  const unsigned srow = 128 * rb + 64 * half + 8 * cp + w;
  unsigned vrow = lane < 56 ? lane * 16 : 0x80000000u;
  asm volatile("s_waitcnt lgkmcnt(0)" : "+s"(ka), "+s"(kb), "+v"(vx0), "+v"(vx1), "+v"(vG), "+v"(vU), "+v"(vKV), "+v"(vrow));
  const char *px = (const char*)ka[0], *pqkv = (const char*)ka[1], *pwg = (const char*)ka[2], *pwu = (const char*)ka[3], *pphi = (const char*)ka[5],
             *pwd = (const char*)ka[6], *pwo = (const char*)ka[7];
  char *ws = (char*)ka[4], *pout = (char*)kb;
  const rsrc_t rsX = mkrs(px, INSZ(2048 * XROW)), rsQ = mkrs(pqkv, INSZ(448 * QROW));
  const rsrc_t rsG = mkrs(attn ? pqkv : pwg, INSZ(448 * wstr)), rsU = mkrs(attn ? pqkv + 128 : pwu, INSZ(448 * wstr - (attn ? 128 : 0)));
  auto cidx = [&](int st) AI -> unsigned { unsigned t = rot + st; return t >= 7 ? t - 7 : t; };
  auto issueA = [&](int st, int k) AI {
    const unsigned sl = st & 3, ch = cidx(st);
    if (k == 0) dma(lw2, sl * 16384, vx0, rsX, sX0 + ch * 128, 0);
    if (k == 1) dma(lw2, sl * 16384 + 1024, vx1, rsX, sX0 + ch * 128, 0);
    if (k == 2) dma(lw1, L_GU + sl * 16384, vG, rsG, sG0 + ch * 64 * wstr, 0);
    if (k == 3) dma(lw1, L_GU + sl * 16384 + 8192, vU, rsU, sG0 + ch * 64 * wstr, 0);
    if (k == 4) dma(lw1, L_KV + sl * 8192, vKV, rsQ, ch * 64 * QROW, 0);
  };
  const rsrc_t rsWS = mkrs(ws, WS_END);
  unsigned Ev, cpf; u32x4 xrw;
  asm volatile("buffer_load_dwordx4 %0, %1, %2, %3 offen" : "=v"(xrw) : "v"(vrow), "s"(rsX), "s"(srow * XROW) : "memory");
  asm volatile("buffer_load_dword %0, off, %1, 0 sc1" : "=v"(Ev) : "s"(rsWS) : "memory");
  {
    unsigned long long pc; asm volatile("s_getpc_b64 %0" : "=s"(pc));
    const unsigned vo = w == 0 && lane < 24 ? (bi * 24 + lane) * 128 : 0x80000000u;
    asm volatile("buffer_load_dword %0, %1, %2, 0 offen" : "=v"(cpf) : "v"(vo), "s"(mkrs((const void*)pc, CODE_BYTES)) : "memory");
  }
#pragma unroll
  for (int k = 0; k < 5; k++) issueA(0, k);
  asm volatile("s_waitcnt vmcnt(5)" : "+v"(xrw), "+v"(Ev), "+v"(cpf) :: "memory");
#pragma unroll
  for (int k = 0; k < 5; k++) issueA(1, k);
  issueA(2, 0);
  const unsigned E = __builtin_amdgcn_readfirstlane(Ev);
  {
    float s1 = 0.f, s2 = 0.f;
#pragma unroll
    for (int i = 0; i < 4; i++) { s1 = dot2(xrw[i], 0x3f803f80u, s1); s2 = dot2(xrw[i], xrw[i], s2); }
    s1 = wavesum63(s1); s2 = wavesum63(s2);
    const float mean = s1 * (1.f / 448.f), var = __builtin_fmaxf(s2 * (1.f / 448.f) - mean * mean, 0.f);
    u32x2 gr = {pk2(-mean, __builtin_amdgcn_sqrtf(rbf(rbf(var) + 1e-5f))), E + 1};
    __builtin_amdgcn_raw_buffer_store_b64(gr, rsWS, lane == 63 ? 0u : 0x80000000u, WS_GR + srow * 8, 16);
  }

  auto prog = [&](auto at_, auto gg_) AI {
  constexpr bool AT = decltype(at_)::value; constexpr int GG = decltype(gg_)::value;
  constexpr unsigned lcstep = AT ? 224 : 128;
  unsigned lane;
  asm volatile("v_mbcnt_lo_u32_b32 %0, -1, 0\n\tv_mbcnt_hi_u32_b32 %0, -1, %0" : "=v"(lane));
  __builtin_assume(lane < 64);
  voffs(lane);
  auto issueL = [&](int hc) AI {
    const int st = hc >> 1; const unsigned ch = cidx(st);
    if (hc & 1) dma(lw1, lc1_base(st) + 8192, vU, rsU, sG0 + ch * 64 * wstr, lcstep);
    else dma(lw1, lc1_base(st), vG, rsG, sG0 + ch * 64 * wstr, lcstep);
  };
  auto issueG = [&](int bar, int k) AI {
    if (bar == 0) issueA(2 + k / 5, k % 5);
    else if (bar <= 3) issueA(bar + 3, k);
    else if (k < 4) issueL(4 * (bar - 4) + k);
    else if (bar == 4) dma(LB + (w & 1) * 1024, L_WPK, lane * 16, mkrs(pphi, 16 * 1568), head * 1568 + (w & 1) * 1024, 0);
    else issueL(12 + (bar - 5));
  };

  const unsigned rt = 4 * rb + wr;
  const unsigned sA2 = WS_A2 + rt * 144 * 1024 + (AT ? half * 16384 : c * 8192), vL16 = lane * 16;
  const u32x4 sent = {~0u, ~0u, ~0u, ~0u};
  auto clr = [&](int ks) AI { st16(sent, rsWS, vL16, sA2, ks * 1024); };

  auto clri = [&](int q) AI { clr(GG ? (q < 6 ? 114 + q : 118 + q) : (q < 2 ? 112 + q : 118 + q)); };

  const bool hh = lane >= 32;
  const unsigned hu = lane >> 5, blk = (lane >> 4) & 1, q4 = (lane >> 2) & 3, p4 = lane & 3, ta = (q4 >> 1) & 1, l31 = lane & 31;
  const unsigned perm = wr ^ (2 * GG);
  const unsigned rowoff = (8 * hu + q4) * 128 + (perm << 11);
  const unsigned pc = (4 * blk + p4) < 6 ? (4 * blk + p4) : 6;
  auto tileaddr = [&](unsigned t) AI -> unsigned {
    if constexpr (AT) { unsigned o = 56 * t + 8 * pc; return (o >> 7) * 8192 + ((((o >> 4) & 7) ^ (4 * ta)) << 4) + (o & 8) + rowoff; }
    else return blk * 8192 + (((2 * t + (p4 >> 1)) ^ (4 * blk + 2 * ta)) << 4) + 8 * (p4 & 1) + rowoff;
  };
  unsigned aA[3], aB[2];
  {
    unsigned o = (head & 1) * 8 + 8 * pc;
    unsigned ak = L_KV + (((o >> 4) ^ (4 * ta)) << 4) + (o & 8) + rowoff;
    aA[0] = LB + (GG ? L_GU + tileaddr(1) : ak);
    aA[1] = LB + (GG ? L_GU + tileaddr(2) : (ak ^ 64u));
    aA[2] = LB + L_GU + tileaddr(GG ? 3 : 0);
    aB[0] = LB + tileaddr(2 * GG); aB[1] = LB + tileaddr(2 * GG + 1);
  }
  const unsigned xrow = 32 * wr + l31;
  const unsigned aX = LB + xrow * 128 + (((2 * perm + hu) ^ ((xrow >> 1) & 7)) << 4);
  const unsigned selv = (((lane >> 4) ^ (lane >> 2)) & 1) ? 0u : 0x3f803f80u;
  const u32x4 sel4 = {selv, selv, selv, selv};
  const bf16x8 sel = __builtin_bit_cast(bf16x8, sel4);

  bf16x8 xf[28], wa[2][3], wb[8][2];
  f32x16 accA[3], accB[2];
  f32x4 csA[3], csB[2];
#pragma unroll
  for (int j = 0; j < 3; j++) { for (int i = 0; i < 16; i++) accA[j][i] = 0.f; for (int i = 0; i < 4; i++) csA[j][i] = 0.f; }
#pragma unroll
  for (int j = 0; j < 2; j++) { for (int i = 0; i < 16; i++) accB[j][i] = 0.f; for (int i = 0; i < 4; i++) csB[j][i] = 0.f; }
  u32x2 grn;

  const rsrc_t rsWD = mkrs(pwd, INSZ(1792 * XROW)), rsWO = mkrs(pwo, INSZ(448 * XROW));
  const unsigned vW2 = (lane >> 3) * XROW + (((lane & 7) ^ ((lane >> 2) & 4)) << 4) + ci * 128;
  constexpr int NS = GG ? 16 : 20;
#define RHO ((rbl + cp) & 3)
  const unsigned rho = RHO;
  auto t_of = [](int i) constexpr { return (0x54763210u >> (4 * (i >> 1))) & 7; };
  auto ksb = [&](int i) AI -> unsigned {
    if (GG) return 16 * wr + ((t_of(i) & 3) ^ rho);
    if (i < 12) { const unsigned cc = i % 6 + rho + (rho >> 1); return wr + 8 * (cc >= 6 ? cc - 6 : cc); }
    return 8 * wr + 2 * (((i - 12) >> 1) ^ rho);
  };
  auto ksi = [&](int i) AI -> unsigned { return GG ? 8 * (i & 1) + (t_of(i) & 4) : i < 12 ? 64 + 4 * (i / 6) : 112 + (i & 1); };
  auto issueW2 = [&](int i, int hf) AI {
    if (GG == 0 && i >= 12) dma(lw2, ring_area(i % RING) + hf * 1024, vW2, rsWO, ksb(i) * 14 * XROW, (16 * (i & 1) + 8 * hf) * XROW);
    else dma(lw2, ring_area(i % RING) + hf * 1024, vW2, rsWD, ksb(i) * 16 * XROW, (ksi(i) * 16 + 8 * hf) * XROW);
  };

  const unsigned aCSw = LB + L_CS + l31 * 16 + wr * 4 + GG * 2048, aCSr = LB + L_CS + l31 * 16 + GG * 2048;
  auto readX = [&](int n) AI { xf[n] = ldsr<bf16x8>(aX ^ (unsigned)((n & 3) << 5), ((n >> 2) & 3) * 16384); };
  auto readA = [&](int n, int j) AI {
    const unsigned o = ((n >> 2) & 3) * ((GG == 0 && j < 2) ? 8192 : 16384);
    wa[n & 1][j] = tr2(aA[j] ^ (unsigned)((n & 3) << 11), o, o + 512);
  };
  auto readB = [&](int n, int j) AI {
    const unsigned base = lc1_base(n >> 2);
    if (XORCOND) asm volatile("" : "+v"(aB[j]));
    wb[n & 7][j] = tr2(aB[j] ^ (((n & 3) << 11) | (base & 0x30000u)), base & 0xffffu, (base & 0xffffu) + 512);
  };
  RowC rc; bf16x8 xe; unsigned denb;
  auto rowconst = [&]() AI {
#pragma nounroll
    for (int it = 0; it < (1 << 16) && !ALL(grn[1] == E + 1); ++it) {
      __builtin_amdgcn_s_sleep(SLEEPN);
      asm volatile("buffer_load_dwordx2 %0, %1, %2, %3 offen sc1\n\ts_waitcnt vmcnt(0)" : "=v"(grn) : "v"(xrow * 8), "s"(rsWS), "s"(WS_GR + rb * 1024) : "memory");
    }
    const float den = U2F(grn[0] & 0xffff0000u);
    const unsigned nm = hh ? 0u : __builtin_amdgcn_perm(grn[0], grn[0], 0x01000100u);
    u32x4 t = {nm, nm, nm, nm}; xe = __builtin_bit_cast(bf16x8, t);
    denb = grn[0] >> 16;
    rc.r = __builtin_amdgcn_rcpf(den);
    const float k1 = -2.f * 1.4426950408889634f * 0.7978845608028654f;
    rc.K1 = k1 * rc.r; rc.K2 = (k1 * 0.044715f) * rc.r * (rc.r * rc.r); rc.Ri = den * den; rc.Lr = 1.4426950408889634f * rc.r;
  };
  WAITV(7);
  BAR(); MEMFENCE();
  STAMP(1);
  readX(0); readA(0, 0); readA(0, 1); readA(0, 2);
  SFOR(n, 28)
    SB();
    if constexpr (n == 27) {
#pragma unroll
      for (int j = 0; j < 3; j++) ldsw<unsigned>(aCSw, j * 512, hilo(csA[j][0]));
    }
    if constexpr ((n & 3) == 3) {
      if constexpr (n == 27) WAITV(0);
      else if constexpr (n == 23) asm volatile("s_waitcnt vmcnt(10)" : "+v"(grn) :: "memory");
      else if constexpr (n == 3 || n == 15 || n == 19) WAITV(11); else WAITV(10);
      { WAITL0(); BAR(); MEMFENCE(); }
      if constexpr (n == 27) STAMP(2);
    }
    constexpr int bar = (n + 1) >> 2, ph = (n + 1) & 3;
#pragma unroll
    for (int j = 0; j < 3; j++) {
      if constexpr (n < 27) { if (j == 0) readX(n + 1); readA(n + 1, j); }
      else { wa[0][j] = ldsr<bf16x8>(aCSr, j * 512); if (GG == 1 && j < 2) { readB(j, 0); readB(j, 1); } }
      SB();
      accA[j] = MFMA(wa[n & 1][j], xf[n], accA[j]);
      const int slot = 3 * ph + j;
      if (bar == 0) issueG(0, slot - 2);
      else if (bar == 6) { if (slot < 5) issueG(bar, slot); }
      else if (bar <= 6) { if (slot == 0) issueG(bar, 0); if (slot == 2) issueG(bar, 1); if (slot == 5) issueG(bar, 2); if (slot == 7) issueG(bar, 3); if (slot == 10) issueG(bar, 4); }
      else if (j < 2) {
        if constexpr (AT) dma(lw2, L_FREE + j * 1024, lane * 16, mkrs(pphi, 16 * 1568), half * 8 * 1568 + w * 2048, j * 1024);
        else issueW2(0, j);
      }
      if ((n & 3) == 0) csA[j] = MFMA16(sel, wa[n & 1][j], csA[j]);
      if (n == 25 && j == 0) rowconst();
      if (n == 14 && j == 2) asm volatile("buffer_load_dwordx2 %0, %1, %2, %3 offen sc1" : "=v"(grn) : "v"(xrow * 8), "s"(rsWS), "s"(WS_GR + rb * 1024) : "memory");
      SB();
    }
  SEND
#pragma unroll
  for (int j = 0; j < 3; j++) accA[j] = MFMA(wa[0][j], xe, accA[j]);
  asm volatile("" : "+v"(accA[0]), "+v"(accA[1]), "+v"(accA[2]));
  SB();

  const unsigned sFown = WS_FH + (rt * 32 + (AT ? 28 + 2 * half : 2 * c) + GG) * FS;
  const unsigned aWP = LB + (4 * hu + q4) * 56 + 8 * pc;
  const unsigned T12 = (E + 1) & 0xfffu, T16 = (E + 1) & 0xffffu;
  const unsigned rt2 = 2 * (8 * grp + 4 * half + rbl);
  const unsigned sF = WS_FH + rt2 * 32 * FS;
  const unsigned fc = l31 >> 1;
  const bool nd1 = GG ? (fc >> 1) == wr : (fc >= 8 && fc < 14) || fc == 14 + (wr >> 1);
  const unsigned aIW = LB + L_PHI + xrow * 64 + ((hu ^ ((xrow >> 1) & 7)) << 3);
  SmS sm;
  unsigned aWK = aWP + L_WPK, aWQ = aWP + L_FREE;
  asm volatile("" : "+v"(aWK)); if constexpr (AT) asm volatile("" : "+v"(aWQ));
  auto kstep = [&](auto s_) AI {
    constexpr int s = decltype(s_)::value;
    if constexpr (s == 0) { sm.w0 = tr2(aWK, 0, 8 * 56); sm.w1 = tr2(aWK, 16 * 56, 24 * 56); }
    if constexpr (s == 1) sm.p = featproj2(accA[0], sm.w0, sm.w1, hh);
    if constexpr (s == 2 || s == 3) {
#pragma unroll
      for (int k = 2 * s - 4; k < 2 * s - 2; k++) {
        u32x2 vv = {pk2(accA[1][4 * k], accA[1][4 * k + 1]), pk2(accA[1][4 * k + 2], accA[1][4 * k + 3])};
        if (k == 3) vv[0] = hh ? denb : vv[0];
        ldsw<u32x2>(aIW ^ (unsigned)(k << 4), L_V - L_PHI, vv);
      }
    }
    if constexpr (s == 4) sm.bx = -max16(sm.p) * rc.Lr;
    if constexpr (s == 5) sm.bn = min16(sm.p) * rc.Lr;
    if constexpr (s >= 6 && s < 22) {
      constexpr int i = s - 6;
      sm.e[i] = __builtin_amdgcn_exp2f(__builtin_fmaf(sm.p[i], rc.Lr, sm.bx)); sm.en[i] = __builtin_amdgcn_exp2f(__builtin_fmaf(sm.p[i], -rc.Lr, sm.bn));
      if constexpr (i == 0) { sm.sp = sm.e[0]; sm.sn = sm.en[0]; } else if constexpr (i < 12) { sm.sp += sm.e[i]; sm.sn += sm.en[i]; }
      else if constexpr (i == 12) { sm.tp = sm.e[12]; sm.tn = sm.en[12]; } else { sm.tp += sm.e[i]; sm.tn += sm.en[i]; }
    }
    if constexpr (s == 22) { sm.sp = __builtin_amdgcn_rcpf(xsum(sm.sp + (hh ? 0.f : sm.tp))) * rc.r; sm.sn = __builtin_amdgcn_rcpf(xsum(sm.sn + (hh ? 0.f : sm.tn))) * rc.r; }
    if constexpr (s >= 23) {
#pragma unroll
      for (int u = (s == 29 ? 6 : s - 23); u <= (s == 29 ? 7 : s - 23); u++) {
        const int k = u >> 1;
        if (u & 1) { u32x2 t = {pk2s(sm.en[4 * k] * sm.sn, sm.en[4 * k + 1] * sm.sn), pk2s(sm.en[4 * k + 2] * sm.sn, sm.en[4 * k + 3] * sm.sn)}; ldsw<u32x2>(aIW ^ (unsigned)(k << 4), 8192, t); }
        else { u32x2 t = {pk2s(sm.e[4 * k] * sm.sp, sm.e[4 * k + 1] * sm.sp), pk2s(sm.e[4 * k + 2] * sm.sp, sm.e[4 * k + 3] * sm.sp)}; ldsw<u32x2>(aIW ^ (unsigned)(k << 4), 0, t); }
      }
    }
  };
  auto qstep = [&](auto s_, f32x16& a, unsigned lh) AI {
    constexpr int s = decltype(s_)::value;
    if constexpr (s == 0) { sm.w0 = tr2(aWQ, lh * 1568, lh * 1568 + 8 * 56); sm.w1 = tr2(aWQ, lh * 1568 + 16 * 56, lh * 1568 + 24 * 56); }
    if constexpr (s == 1) sm.p = featproj2(a, sm.w0, sm.w1, hh);
    if constexpr (s == 3) sm.bx = -max16(sm.p) * rc.Lr;
    if constexpr (s == 4) sm.bn = min16(sm.p) * rc.Lr;
    if constexpr (s >= 5 && s < 21) {
      constexpr int i = (s - 5) >> 1;
      float t0, t1;
      if constexpr ((s - 5) & 1) { t0 = __builtin_fmaf(sm.p[2 * i], -rc.Lr, sm.bn); t1 = __builtin_fmaf(sm.p[2 * i + 1], -rc.Lr, sm.bn); }
      else { t0 = __builtin_fmaf(sm.p[2 * i], rc.Lr, sm.bx); t1 = __builtin_fmaf(sm.p[2 * i + 1], rc.Lr, sm.bx); }
      a[8 * ((s - 5) & 1) + i] = U2F(pk2(__builtin_amdgcn_exp2f(t0), __builtin_amdgcn_exp2f(t1)));
      if constexpr (s == 20) asm volatile("" : "+v"(a));
    }
  };
  bf16x8 ka_[4], kb_[2][4]; f32x4 kd[2];
  auto kvstep = [&](auto s_) AI {
    constexpr int s = decltype(s_)::value;
    if constexpr (s == 0) {
      const unsigned g4 = lane >> 4, r0 = 8 * g4 + q4, sw = 4 * blk + (q4 >> 1);
      unsigned b0 = LB + L_PHI + r0 * 64 + ((p4 ^ sw) << 3);
      unsigned a0 = (b0 ^ ((wr >> 1) << 5)) + (wr & 1) * 8192;
#pragma unroll
      for (int k = 0; k < 4; k++) {
        ka_[k] = __builtin_shufflevector(trd2(a0, k * 2048), trd2(a0 ^ 16u, k * 2048 + 256), 0, 1, 2, 3, 4, 5, 6, 7);
        kb_[0][k] = __builtin_shufflevector(trd2(b0, L_V - L_PHI + k * 2048), trd2(b0 ^ 16u, L_V - L_PHI + k * 2048 + 256), 0, 1, 2, 3, 4, 5, 6, 7);
        kb_[1][k] = __builtin_shufflevector(trd2(b0 ^ 32u, L_V - L_PHI + k * 2048), trd2(b0 ^ 48u, L_V - L_PHI + k * 2048 + 256), 0, 1, 2, 3, 4, 5, 6, 7);
      }
    }
    if constexpr (s == 1) {
      f32x4 z = {0.f, 0.f, 0.f, 0.f}; kd[0] = z; kd[1] = z;
#pragma unroll
      for (int k = 0; k < 4; k++) { kd[0] = MFMA16(ka_[k], kb_[0][k], kd[0]); kd[1] = MFMA16(ka_[k], kb_[1][k], kd[1]); }
    }
    if constexpr (s == 3) {
#pragma unroll
      for (int d = 0; d < 2; d++) {
        u32x4 t;
#pragma unroll
        for (int i = 0; i < 4; i++) t[i] = (F2U(kd[d][i]) & ~0xfffu) | T12;
        st16(t, rsWS, (lane >> 4) * 512 + (lane & 15) * 16, ((head * 16 + rb) * 2 + (wr & 1)) * 4096 + (wr >> 1) * 2048, WS_KVP + d * 256);
      }
    }
  };
  auto qexp2 = [&](f32x16& a0, unsigned l0, f32x16& a1, unsigned l1) AI {
    const f32x16 p0 = featproj(a0, aWQ, l0 * 1568, hh), p1 = featproj(a1, aWQ, l1 * 1568, hh);
    a0 = dualexpP(p0, rc.Lr); a1 = dualexpP(p1, rc.Lr); asm volatile("" : "+v"(a0), "+v"(a1));
  };

  unsigned fl1 = 0;
  EpiS es;
  u32x4 pv[8];
  const unsigned vKP = l31 * 16 + hu * 8192, sKP = (rb * 32 + half) * 4096 + wr * 1024 + GG * 512;
  bf16x8 cb[2];
  auto ldflag = [&]() AI { asm volatile("buffer_load_dword %0, %1, %2, %3 offen sc1" : "=v"(fl1) : "v"(nd1 ? lane * FS : 0x80000000u), "s"(rsWS), "s"(sF) : "memory"); };
  auto touch = [&](unsigned wo4) AI {
    const unsigned rw = (rbl * 8 + w + wo4) * 44 + lane;
    const bool dn = rw < 896;
    const unsigned vo = lane < 44 ? (dn ? ((rw >> 6) * 128 + 64 + (rw & 63)) : rw - 896) * XROW + ci * 128 : 0x80000000u;
    asm volatile("s_add_u32 m0, %0, %c5\n\ts_nop 0\n\tbuffer_load_dword %1, %3, 0 offen lds\n\tbuffer_load_dword %2, %4, 0 offen lds"
                 :: "s"(LB + w * 256), "v"(dn ? vo : 0x80000000u), "v"(dn ? 0x80000000u : vo), "s"(rsWD), "s"(rsWO), "i"(L_ST) : "memory", "scc");
  };
  auto stream = [&]() AI {
    __builtin_amdgcn_s_setprio(PS);
    SFOR(n, 28)
      if constexpr (n == 27) { ldsw<unsigned>(aCSw, 3 * 512, hilo(csB[0][0])); ldsw<unsigned>(aCSw, 0, hilo(csB[1][0])); WAITL0(); BAR(); MEMFENCE(); }
      else if constexpr (GG == 1) { if constexpr (n == S2N || (!AT && n == B1N)) { if constexpr (AT) WAITV(10); BAR(); MEMFENCE(); } }
      else if constexpr (AT ? n == 7 : (n & 3) == 3) { asm volatile("s_waitcnt lgkmcnt(%c0)" :: "i"(4 * DIST - 4) : "memory"); BAR(); MEMFENCE(); }
      SFOR(j, 2)
        constexpr int q = 2 * n + j;
        if constexpr (DIST == 3 && n == 0) readB(2, j);
        if constexpr (n + DIST < 28) readB(n + DIST, j);
        if constexpr (n == 27) cb[j] = ldsr<bf16x8>(aCSr, j ? 0 : 3 * 512);
        SB();
        accB[j] = MFMA(wb[n & 7][j], xf[n], accB[j]);
        if constexpr ((n & 3) == 0) csB[j] = MFMA16(sel, wb[n & 7][j], csB[j]);
        if constexpr (!AT) {

          if constexpr (GG == 1 && j == 0 && n >= B1N && n < B1N + 4) issueW2(1 + ((n - B1N) >> 1), (n - B1N) & 1);

          if constexpr (GG == 0) {
            if constexpr (j == 1 && n < 16) { epiSlice<(n >> 2) & 3, n & 3>(accA[2], rc, es); if constexpr (n == 15) st16(es.r, rsWS, vL16, sA2, 0); }
            if constexpr (j == 0 && n >= 3 && n < 7) issueW2(1 + ((n - 3) >> 1), (n - 3) & 1);
            if constexpr (j == 0 && n >= 7 && n < 27 && (n & 1)) issueW2(1 + ((n + 1) >> 2), ((n + 1) >> 1) & 1);
            if constexpr (n == 27) issueW2(8, j);
          }
        } else {
          if constexpr (GG == 1 && j == 0 && n < 10) clri(n);
          if constexpr (GG == 0 && j == 1 && n < 21) qstep(n_, accA[2], 0);
          if constexpr (GG == 0 && n >= 24) pv[q - 48] = ld16(rsWS, vKP, sKP, WS_KVP + (q - 48) * 16384);
        }
        SB();
      SEND
    SEND
    accB[0] = MFMA(cb[0], xe, accB[0]); accB[1] = MFMA(cb[1], xe, accB[1]);
    asm volatile("" : "+v"(accB[0]), "+v"(accB[1]));
    __builtin_amdgcn_s_setprio(GG ? PV : 0);
    SB();
  };
  auto pairF = [&](auto p_, const f32x16& a) AI { constexpr int p = decltype(p_)::value; epiSlice<p, 0>(a, rc, es); epiSlice<p, 1>(a, rc, es); epiSlice<p, 2>(a, rc, es); epiSlice<p, 3>(a, rc, es); };
  if constexpr (GG == 0) {
    __builtin_amdgcn_s_setprio(PV);
    if constexpr (AT) { SFOR(q, 6) clri(q); SEND }
    SFOR(s, 30)
      kstep(s_);
    SEND
    if constexpr (!AT) { touch(0); touch(4); }
    if constexpr (AT) WAITV(6);
    WAITL0(); SB(); BAR(); SB();
    STAMP(3);
    kvstep(IntC<0>{});
    readB(0, 0); readB(0, 1); readB(1, 0); readB(1, 1);
    kvstep(IntC<1>{}); kvstep(IntC<3>{});
    if constexpr (AT) { WAITV(2); stflag(E + 1, rsWS, sFown); }
    STAMP(4);
    SB();
    stream();
  } else {
    stream();
    STAMP(3);
    if constexpr (AT) {
      WAITV(0); stflag(E + 1, rsWS, sFown);
      SFOR(s, 21) qstep(s_, accA[0], 1); SEND
      SFOR(s, 21) qstep(s_, accA[1], 2); SEND
#pragma unroll
      for (int i = 0; i < 8; i++) pv[i] = ld16(rsWS, vKP, sKP, WS_KVP + i * 16384);
      SFOR(s, 21) qstep(s_, accA[2], 3); SEND
      SB(); BAR(); SB();
    } else {
      SFOR(e, 20)
        if constexpr (e < 12) pairF(IntC<e & 3>{}, accA[e >> 2]); else pairF(IntC<e & 3>{}, accB[(e >> 2) - 3]);
        if constexpr ((e & 3) == 3) st16(es.r, rsWS, vL16, sA2, (e < 12 ? 1 + (e >> 2) : 3 + (e >> 2)) * 1024);
        if constexpr (e == 0) { issueW2(3, 0); issueW2(3, 1); }
        if constexpr ((e & 3) == 2) { SB(); BAR(); SB(); issueW2(4 + (e >> 2), 0); issueW2(4 + (e >> 2), 1); }
      SEND
    }
  }

  if constexpr (AT) {
    __builtin_amdgcn_s_setprio(0);
    STAMP(5);
    waittag<16384>(pv, rsWS, vKP, sKP + WS_KVP, T12, 0xfffu);
    STAMP(6);
    {
      u32x4 o;
      f32x4 s4 = __builtin_bit_cast(f32x4, pv[0]);
#pragma unroll
      for (int i = 1; i < 8; i++) s4 += __builtin_bit_cast(f32x4, pv[i]);
      asm("" : "+v"(s4));
#pragma unroll
      for (int k = 0; k < 4; k++) {
        float sum = xsum(s4[k]);
        sum = l31 == 29 ? 1.f : sum;
        if (GG == 1) sum = wr == 3 ? 0.f : sum;
        o[k] = (pk2(sum, sum) & 0xffff0000u) | T16;
      }
      st16(o, rsWS, hh ? 0x80000000u : l31 * 16, (rb * 2 + half) * 4096 + wr * 1024 + GG * 512, WS_KVR);
    }
    const unsigned sKR = (8 * half + w) * 8192;
    {
      const f32x16 p0 = featproj(accB[0], aWQ, (4 + 2 * GG) * 1568, hh), p1 = featproj(accB[1], aWQ, (5 + 2 * GG) * 1568, hh);
      accB[0] = dualexpP(p0, rc.Lr); asm volatile("" : "+v"(accB[0]));
#pragma unroll
      for (int i = 0; i < 8; i++) pv[i] = ld16(rsWS, vL16, sKR, WS_KVR + i * 1024);
      accB[1] = dualexpP(p1, rc.Lr); asm volatile("" : "+v"(accB[1]));
    }
    STAMP(7);
    waittag<1024>(pv, rsWS, vL16, sKR + WS_KVR, T16, 0xffffu);
    STAMP(8);
#pragma unroll
    for (int i = 0; i < 4; i++) {
      u32x4 f = {__builtin_amdgcn_perm(pv[2 * i][1], pv[2 * i][0], 0x07060302u), __builtin_amdgcn_perm(pv[2 * i][3], pv[2 * i][2], 0x07060302u),
                 __builtin_amdgcn_perm(pv[2 * i + 1][1], pv[2 * i + 1][0], 0x07060302u), __builtin_amdgcn_perm(pv[2 * i + 1][3], pv[2 * i + 1][2], 0x07060302u)};
      ldsw<u32x4>(LB + w * 4096 + vL16, i * 1024, f);
    }
    WAITL0(); BAR(); MEMFENCE();
    f32x16 op[2], on[2];
    auto omm = [&](const f32x16& phi, unsigned lh, int z) AI {
#pragma unroll
      for (int i = 0; i < 16; i++) { op[z][i] = 0.f; on[z][i] = 0.f; }
      bf16x8 kf[4];
#pragma unroll
      for (int i = 0; i < 4; i++) kf[i] = ldsr<bf16x8>(LB + vL16, lh * 4096 + i * 1024);
#pragma unroll
      for (int i = 0; i < 2; i++) { op[z] = MFMA(kf[i], sub4(phi, i), op[z]); on[z] = MFMA(kf[2 + i], sub4(phi, 2 + i), on[z]); }
      SB();
    };
    auto ofin = [&](unsigned lh, int z) AI {
      const float ia = __builtin_amdgcn_rcpf(op[z][13]), ib = __builtin_amdgcn_rcpf(on[z][13]);
      const float id = __builtin_amdgcn_rcpf(__builtin_fmaf(op[z][12], ia, __builtin_fmaf(on[z][12], ib, 1e-6f)));
      const float ca = xhi(ia * id), cb2 = xhi(ib * id);
      u32x4 f0, f1;
#pragma unroll
      for (int i = 0; i < 4; i++) {
        f0[i] = pk2s(__builtin_fmaf(op[z][2 * i], ca, on[z][2 * i] * cb2), __builtin_fmaf(op[z][2 * i + 1], ca, on[z][2 * i + 1] * cb2));
        f1[i] = pk2s(__builtin_fmaf(op[z][8 + 2 * i], ca, on[z][8 + 2 * i] * cb2), __builtin_fmaf(op[z][9 + 2 * i], ca, on[z][9 + 2 * i] * cb2));
      }
      f1[2] = hh ? 0u : f1[2]; f1[3] = hh ? 0u : f1[3];
      st16(f0, rsWS, vL16, sA2, (112 + 2 * lh) * 1024); st16(f1, rsWS, vL16, sA2, (113 + 2 * lh) * 1024);
      SB();
    };
    if constexpr (GG) __builtin_amdgcn_s_setprio(1);
    omm(accA[2], GG ? 3 : 0, 0); omm(accB[0], 4 + 2 * GG, 1); ofin(GG ? 3 : 0, 0); omm(accB[1], 5 + 2 * GG, 0); ofin(4 + 2 * GG, 1);
    if constexpr (GG) { omm(accA[0], 1, 1); ofin(7, 0); omm(accA[1], 2, 0); ofin(1, 1); ofin(2, 0); } else ofin(5, 0);
    STAMP(9);
  } else {
    __builtin_amdgcn_s_setprio(0);
    STAMP(5);
    const unsigned sA = WS_A2 + rt2 * 144 * 1024;
    unsigned ln = vL16; asm volatile("" : "+v"(ln));
    const unsigned aW = lw2 + (ln & 0x200) + ((ln & 0xc0) << 1) + ((((ln >> 3) & 0x20) | ((ln >> 1) & 0x10)) ^ ((ln >> 1) & 0x40)) + ((ln >> 1) & 8);
    f32x16 acc[2][2];
#pragma unroll
    for (int i = 0; i < 4; i++)
#pragma unroll
      for (int k = 0; k < 16; k++) acc[i >> 1][i & 1][k] = 0.f;
    u32x4 af[NS][2];
    u32x2 xr[2];
    auto loadA = [&](int i) AI { af[i][0] = ld16(rsWS, vL16, sA + ksb(i) * 1024, ksi(i) * 1024); af[i][1] = ld16(rsWS, vL16, sA + ksb(i) * 1024, (144 + ksi(i)) * 1024); };
    const unsigned nt = wr >> 1, mt = wr & 1;
    const unsigned vO = (32 * mt + ((ln >> 4) & 31)) * XROW + ((ln >> 6) & 8) + 32 * GG, sO = rt2 * 32 * XROW + ci * 128 + nt * 64;
    if constexpr (GG == 0) { st16(epiP(accB[0], rc), rsWS, vL16, sA2, 4 * 1024); st16(epiP(accB[1], rc), rsWS, vL16, sA2, 5 * 1024); }
    STAMP(6);
    constexpr int MV = GG ? 7 : 9;
    bf16x8 wf[NS][2];
    auto readW = [&](int i) AI {
      const unsigned ra = ring_area(i % RING), la = aW + (ra & 0x30000u);
      wf[i][0] = tr2(la, ra & 0xffffu, (ra & 0xffffu) + 1024); wf[i][1] = tr2(la ^ 64u, ra & 0xffffu, (ra & 0xffffu) + 1024);
    };
    waitv<GG ? 7 : 4>();
    SFOR(i, GG ? 7 : 8) readW(i); SEND
    WAITV(0); stflag(E + 1, rsWS, sFown);
    if constexpr (!GG) readW(8);
    WAITL0(); SB();
    BAR();
    SB(); STAMP(11);
    if constexpr (GG) __builtin_amdgcn_s_setprio(P2G1);
    SFOR(i, MV)
      if constexpr (i == MV - 2) ldflag();
      issueW2(9 + i, 0); issueW2(9 + i, 1);
    SEND
    asm volatile("s_waitcnt vmcnt(4)" : "+v"(fl1) :: "memory");
    if (!ALL(!nd1 || fl1 == E + 1u)) poll(rsWS, lane * FS, sF, E, nd1 ? 1u : 0u);
    STAMP(7);
#pragma unroll
    for (int j = 0; j < PD; j++) loadA(j);
#pragma unroll
    for (int k = 0; k < 2; k++) asm volatile("buffer_load_dwordx2 %0, %1, %2, %3 offen offset:%c4" : "=v"(xr[k]) : "v"(vO), "s"(rsX), "s"(sO), "i"(16 * k) : "memory");
    SFOR(i, NS)
      asm volatile("s_waitcnt vmcnt(%c2)" : "+v"(af[i][0]), "+v"(af[i][1]) : "i"(later2(i, NS, GG)) : "memory");
      if constexpr (i + 1 >= MV && i + 1 < NS) readW(i + 1);
      if constexpr (GG == 0 && i >= 12) {
        auto stale = [&]() AI {
          unsigned m = UMAX(UMAX(UMAX(af[i][0][0], af[i][0][1]), UMAX(af[i][0][2], af[i][0][3])), UMAX(UMAX(af[i][1][0], af[i][1][1]), UMAX(af[i][1][2], af[i][1][3])));
          return ANY(m == ~0u);
        };
        if (__builtin_expect(stale(), 0)) {
          int it = 0;
#pragma nounroll
          do { __builtin_amdgcn_s_sleep(SLEEPR); rel1<true>(af[i][0], af[i][1], rsWS, vL16, sA + (ksb(i) + ksi(i)) * 1024); } while (++it < (1 << 14) && stale());
          SFOR(u, PD - 1) if constexpr (i + 1 + u < NS) rel1<false>(af[i + 1 + u][0], af[i + 1 + u][1], rsWS, vL16, sA + (ksb(i + 1 + u) + ksi(i + 1 + u)) * 1024); SEND
        }
      }
      if constexpr (i == 6) STAMP(8);
      if constexpr (i == 12) STAMP(9);
      SB();
      if constexpr (i + PD < NS) loadA(i + PD);
#pragma unroll
      for (int mm = 0; mm < 2; mm++) acc[0][mm] = MFMA(wf[i][0], __builtin_bit_cast(bf16x8, af[i][mm]), acc[0][mm]);
#pragma unroll
      for (int mm = 0; mm < 2; mm++) acc[1][mm] = MFMA(wf[i][1], __builtin_bit_cast(bf16x8, af[i][mm]), acc[1][mm]);
      if constexpr (GG == 0 && (i == 9 || i == 10)) { SB(); issueW2(i + 9, 0); issueW2(i + 9, 1); }
      SB();
    SEND
    STAMP(10);
    __builtin_amdgcn_s_setprio(0);
#pragma unroll
    for (int qd = 0; qd < 4; qd++)
#pragma unroll
      for (int k = 0; k < 4; k++) {
        const unsigned ra = ring_area(2 * qd + (k >> 1)) + (k & 1) * 1024;
        ldsw<bf16x8>(lw2 + vL16 + (ra & 0x30000u), ra & 0xffffu, sub4(acc[qd >> 1][qd & 1], k));
      }
    WAITL0(); BAR(); MEMFENCE();
    STAMP(12);
    const unsigned j2 = 2 * wr + GG;
    const unsigned ra = ((0x51407362u >> (4 * j2)) & 7) * 16384;
    asm volatile("" : "+v"(xr[0]), "+v"(xr[1]));
    const rsrc_t rsO = mkrs(pout, 2048 * XROW);
#pragma unroll
    for (int k = 0; k < 2; k++) {
      f32x4 sum = {U2F(xr[k][0] << 16), U2F(xr[k][0] & 0xffff0000u), U2F(xr[k][1] << 16), U2F(xr[k][1] & 0xffff0000u)};
#pragma unroll
      for (int v = 0; v < 8; v++) sum += ldsr<f32x4>(LB + ra + vL16, v * 2048 + k * 1024);
      u32x2 o = {pk2(sum[0], sum[1]), pk2(sum[2], sum[3])};
      __builtin_amdgcn_raw_buffer_store_b64(o, rsO, vO + 16 * k, sO, 0);
    }
    if (b == 32 && w == 0) stflag(E + 4, rsWS, WS_EPOCH);
  }
  STAMPS_OUT();
  };
  if (!attn && g) prog(BoolC<false>{}, IntC<1>{});
  else if (!attn) prog(BoolC<false>{}, IntC<0>{});
  else if (g) prog(BoolC<true>{}, IntC<1>{});
  else prog(BoolC<true>{}, IntC<0>{});
}

extern "C" void kernel_launch(void* const* d_in, const int* in_sizes, int n_in, void* d_out, int out_size, void* d_ws, size_t ws_size,
                              hipStream_t stream) {
  fused_kernel<<<256, 512, 0, stream>>>((const char*)d_in[0], (const char*)d_in[1], (const char*)d_in[5], (const char*)d_in[6], (char*)d_ws,
                                        (const char*)d_in[2], (const char*)d_in[7], (const char*)d_in[3], (char*)d_out);
}
```

```cpp
#define ENC 1
#define ENCAT 0
#include <hip/hip_runtime.h>
#include <hip/hip_bf16.h>
#include <utility>

#define DI __device__ __forceinline__
#define LAS __attribute__((address_space(3)))
typedef __attribute__((ext_vector_type(8))) short bf16x8;
typedef __attribute__((ext_vector_type(4))) short s16x4;
typedef __attribute__((ext_vector_type(2))) __bf16 bf2;
typedef __attribute__((ext_vector_type(2))) float f32x2;
typedef __attribute__((ext_vector_type(4))) float f32x4;
typedef __attribute__((ext_vector_type(16))) float f32x16;
typedef __attribute__((ext_vector_type(2))) unsigned u32x2;
typedef __attribute__((ext_vector_type(4))) unsigned u32x4;
typedef __amdgpu_buffer_rsrc_t rsrc_t;
#define F2U(x) __builtin_bit_cast(unsigned, (float)(x))
#define U2F(x) __builtin_bit_cast(float, (unsigned)(x))
#define ALL(x) (__builtin_amdgcn_ballot_w64(!(x)) == 0ull)
#define ANY(x) (__builtin_amdgcn_ballot_w64(x) != 0ull)
#define UMAX(a, b) __builtin_elementwise_max((unsigned)(a), (unsigned)(b))

constexpr unsigned XROW = 896, QROW = 2688, FROW = 3584;
constexpr unsigned FS = 128;
constexpr unsigned WS_EPOCH = 0;
constexpr unsigned WS_GR = 4096;
constexpr unsigned WS_KVP = 131072;
constexpr unsigned WS_A2 = WS_KVP + 16 * 16 * 8192;
constexpr unsigned WS_ST = WS_A2 + 64 * 144 * 1024;
constexpr unsigned WS_FH = WS_ST + 262144;
constexpr unsigned WS_KVR = WS_FH + 2048 * FS;
constexpr unsigned WS_END = WS_KVR + 16 * 8192;
constexpr unsigned L_GU = 65536, L_KV = 131072;
constexpr unsigned L_TAIL = L_KV + 3 * 8192;
constexpr unsigned L_WPK = L_TAIL;
constexpr unsigned L_ST = L_TAIL + 2048;
constexpr unsigned L_CS = L_TAIL + 4096;
constexpr unsigned L_PHI = L_GU + 32768;
constexpr unsigned L_V = L_KV + 16384;
constexpr unsigned L_FREE = 32768;

#define MFMA(a, b, c) __builtin_amdgcn_mfma_f32_32x32x16_bf16((a), (b), (c), 0, 0, 0)
#define MFMA16(a, b, c) __builtin_amdgcn_mfma_f32_16x16x32_bf16((a), (b), (c), 0, 0, 0)
#define AI __attribute__((always_inline))
#define BAR() __builtin_amdgcn_s_barrier()
#define WAITV(n) asm volatile("s_waitcnt vmcnt(" #n ")" ::: "memory")
#define WAITL0() asm volatile("s_waitcnt lgkmcnt(0)" ::: "memory")
#define SB() __builtin_amdgcn_sched_barrier(0)
#define MEMFENCE() asm volatile("" ::: "memory")

template <int N> DI void waitv() { asm volatile("s_waitcnt vmcnt(%c0)" :: "i"(N) : "memory"); }

#define INSZ(x) (x)
DI rsrc_t mkrs(const void* p, unsigned bytes) { return __builtin_amdgcn_make_buffer_rsrc((void*)p, (short)0, (int)bytes, 0x00020000); }
DI void dma(unsigned lds, unsigned limm, unsigned voff, rsrc_t rs, unsigned soff, unsigned simm) {
  unsigned t;
  asm volatile("s_add_u32 m0, %1, %c5\n\ts_add_u32 %0, %4, %c6\n\tbuffer_load_dwordx4 %2, %3, %0 offen lds"
               : "=&s"(t) : "s"(lds), "v"(voff), "s"(rs), "s"(soff), "i"(limm), "i"(simm) : "memory", "scc");
}
DI s16x4 trd(unsigned a) { return __builtin_amdgcn_ds_read_tr16_b64_v4i16((LAS s16x4*)(size_t)a); }
DI s16x4 trd2(unsigned a, unsigned off) { return __builtin_amdgcn_ds_read_tr16_b64_v4i16((LAS s16x4*)((LAS char*)(size_t)a + off)); }
DI bf16x8 tr2(unsigned a, unsigned off0, unsigned off1) {
  LAS char* p = (LAS char*)(size_t)a;
  s16x4 lo = __builtin_amdgcn_ds_read_tr16_b64_v4i16((LAS s16x4*)(p + off0));
  s16x4 hi = __builtin_amdgcn_ds_read_tr16_b64_v4i16((LAS s16x4*)(p + off1));
  return __builtin_shufflevector(lo, hi, 0, 1, 2, 3, 4, 5, 6, 7);
}
template <class T> DI T ldsr(unsigned a, unsigned off) { return *(LAS T*)((LAS char*)(size_t)a + off); }
template <class T> DI void ldsw(unsigned a, unsigned off, T v) { *(LAS T*)((LAS char*)(size_t)a + off) = v; }
DI unsigned pk2(float a, float b) { f32x2 f = {a, b}; return __builtin_bit_cast(unsigned, __builtin_convertvector(f, bf2)); }
DI float rbf(float a) { return U2F(pk2(a, a) << 16); }
DI float dot2(unsigned a, unsigned b, float c) { return __builtin_amdgcn_fdot2_f32_bf16(__builtin_bit_cast(bf2, a), __builtin_bit_cast(bf2, b), c, false); }
DI float xsum(float v) { auto r = __builtin_amdgcn_permlane32_swap(F2U(v), F2U(v), false, false); float a = U2F(r[0]); asm("" : "+v"(a)); return a + U2F(r[1]); }
DI float xmax(float v) { auto r = __builtin_amdgcn_permlane32_swap(F2U(v), F2U(v), false, false); return __builtin_fmaxf(U2F(r[0]), U2F(r[1])); }
DI float xmin(float v) { auto r = __builtin_amdgcn_permlane32_swap(F2U(v), F2U(v), false, false); return __builtin_fminf(U2F(r[0]), U2F(r[1])); }
DI float xhi(float v) { auto r = __builtin_amdgcn_permlane32_swap(F2U(v), F2U(v), false, false); return U2F(r[1]); }
DI void st16(u32x4 v, rsrc_t rs, unsigned voff, unsigned soff, unsigned simm) {
  unsigned t;
  asm volatile("s_add_u32 %0, %4, %c5\n\tbuffer_store_dwordx4 %1, %2, %3, %0 offen sc1\n\ts_nop 1" : "=&s"(t) : "v"(v), "v"(voff), "s"(rs), "s"(soff), "i"(simm) : "memory", "scc");
}
DI void stflag(unsigned v, rsrc_t rs, unsigned soff) {
  unsigned voff = (__builtin_amdgcn_mbcnt_hi(~0u, __builtin_amdgcn_mbcnt_lo(~0u, 0u)) == 0u) ? 0u : 0x80000000u;
  __builtin_amdgcn_raw_buffer_store_b32(v, rs, voff, soff, 16);
}
#define XORCOND (n == 24)
#define PS 0
#define PV 0
#define KEEP2 0
#define S2N 14
#define B1N 23
#define DIST 2
#define P2G1 1
#define SLEEPR 4
#define SLEEPN 2
DI u32x4 ld16(rsrc_t rs, unsigned voff, unsigned soff, unsigned simm) {
  u32x4 r; unsigned t;
  asm volatile("s_add_u32 %1, %4, %c5\n\tbuffer_load_dwordx4 %0, %2, %3, %1 offen sc1" : "=v"(r), "=&s"(t) : "v"(voff), "s"(rs), "s"(soff), "i"(simm) : "memory", "scc");
  return r;
}
template <unsigned STEP> DI void ld8w(u32x4 (&p)[8], rsrc_t rs, unsigned voff, unsigned soff) {
  unsigned t;
#define L1(k) "buffer_load_dwordx4 %" #k ", %9, %10, %8 offen sc1\n\ts_add_u32 %8, %8, %c12\n\t"
  asm volatile("s_mov_b32 %8, %11\n\t" L1(0) L1(1) L1(2) L1(3) L1(4) L1(5) L1(6) L1(7) "s_waitcnt vmcnt(0)"
               : "=&v"(p[0]), "=&v"(p[1]), "=&v"(p[2]), "=&v"(p[3]), "=&v"(p[4]), "=&v"(p[5]), "=&v"(p[6]), "=&v"(p[7]), "=&s"(t)
               : "v"(voff), "s"(rs), "s"(soff), "i"(STEP) : "memory", "scc");
#undef L1
}
DI bool untag(u32x4 (&p)[8], unsigned tag, unsigned mask) {
  unsigned chk = 0;
#pragma unroll
  for (int i = 0; i < 8; i++)
#pragma unroll
    for (int k = 0; k < 4; k++) { p[i][k] ^= tag; chk |= p[i][k]; }
  return ALL((chk & mask) == 0u);
}
template <unsigned STEP> DI void waittag(u32x4 (&p)[8], rsrc_t rs, unsigned voff, unsigned soff, unsigned tag, unsigned mask) {
  asm volatile("s_waitcnt vmcnt(0)" : "+v"(p[0]), "+v"(p[1]), "+v"(p[2]), "+v"(p[3]), "+v"(p[4]), "+v"(p[5]), "+v"(p[6]), "+v"(p[7]) :: "memory");
#pragma nounroll
  for (int it = 0; !untag(p, tag, mask) && it < (1 << 16); ++it) { __builtin_amdgcn_s_sleep(SLEEPN); ld8w<STEP>(p, rs, voff, soff); }
}
template <bool WAIT> DI void rel1(u32x4& a, u32x4& b, rsrc_t rs, unsigned voff, unsigned so) {
  unsigned t;
  if constexpr (WAIT) asm volatile("buffer_load_dwordx4 %0, %3, %4, %5 offen sc1\n\ts_add_u32 %2, %5, 147456\n\tbuffer_load_dwordx4 %1, %3, %4, %2 offen sc1\n\ts_waitcnt vmcnt(0)"
                                   : "+v"(a), "+v"(b), "=&s"(t) : "v"(voff), "s"(rs), "s"(so) : "memory", "scc");
  else asm volatile("buffer_load_dwordx4 %0, %3, %4, %5 offen sc1\n\ts_add_u32 %2, %5, 147456\n\tbuffer_load_dwordx4 %1, %3, %4, %2 offen sc1"
                    : "+v"(a), "+v"(b), "=&s"(t) : "v"(voff), "s"(rs), "s"(so) : "memory", "scc");
}
DI void poll(rsrc_t rs, unsigned voff, unsigned soff, unsigned E, unsigned need) {
  voff = need ? voff : 0x80000000u;
#pragma nounroll
  for (int it = 0; it < (1 << 16); ++it) {
    unsigned v;
    asm volatile("buffer_load_dword %0, %1, %2, %3 offen sc1\n\ts_waitcnt vmcnt(0)" : "=v"(v) : "v"(voff), "s"(rs), "s"(soff) : "memory");
    bool ok = (need == 0u) || ((v - E - need) <= (3u - need));
    if (ALL(ok)) break;
    __builtin_amdgcn_s_sleep(SLEEPN);
  }
}

template <bool B> struct BoolC { static constexpr bool value = B; };
template <int I> struct IntC { static constexpr int value = I; };
template <int... I, class F> DI void sfor_impl(std::integer_sequence<int, I...>, F&& f) { (f(IntC<I>{}), ...); }
template <int N, class F> DI void sfor(F&& f) { sfor_impl(std::make_integer_sequence<int, N>{}, f); }
#define SFOR(n, N) sfor<N>([&](auto n##_) AI { constexpr int n = decltype(n##_)::value;
#define SEND });
struct RowC { float r, K1, K2, Ri, Lr; };

DI unsigned hilo(float v) { float hi = rbf(v); return pk2(hi, v - hi); }
DI float max3(float a, float b, float c) { return __builtin_fmaxf(__builtin_fmaxf(a, b), c); }
DI float min3(float a, float b, float c) { return __builtin_fminf(__builtin_fminf(a, b), c); }
DI unsigned pk2s(float a, float b) { unsigned r; asm("v_cvt_pk_bf16_f32 %0, %1, %2" : "=v"(r) : "v"(a), "v"(b)); return r; }
DI f32x2 sub2(const f32x16& v, int k) { f32x2 t = {v[2 * k], v[2 * k + 1]}; return t; }
DI f32x2 exp2v(f32x2 v) { f32x2 r = {__builtin_amdgcn_exp2f(v[0]), __builtin_amdgcn_exp2f(v[1])}; return r; }
DI f32x2 dup2(float a) { f32x2 r = {a, a}; asm("" : "+v"(r)); return r; }
DI f32x2 fma2(f32x2 a, f32x2 b, f32x2 c) { return __builtin_elementwise_fma(a, b, c); }
DI u32x4 epiP(const f32x16& a, const RowC& c) {
  u32x4 r;
  const f32x2 K1 = dup2(c.K1), K2 = dup2(c.K2), Ri = dup2(c.Ri);
#pragma unroll
  for (int i = 0; i < 4; i++) {
    const f32x2 g = sub2(a, i), d = fma2(exp2v(g * fma2(g * g, K2, K1)), Ri, Ri);
    const f32x2 s = {__builtin_amdgcn_rcpf(d[0]), __builtin_amdgcn_rcpf(d[1])}, h = (g * sub2(a, 4 + i)) * s;
    r[i] = pk2(h[0], h[1]);
  }
  return r;
}
struct EpiS { float x0, x1, m0, m1; u32x4 r; };
template <int P, int PH> DI void epiSlice(const f32x16& a, const RowC& c, EpiS& s) {
  const float g0 = a[2 * P], g1 = a[2 * P + 1];
  if constexpr (PH == 0) { s.x0 = g0 * __builtin_fmaf(g0 * g0, c.K2, c.K1); s.x1 = g1 * __builtin_fmaf(g1 * g1, c.K2, c.K1); }
  if constexpr (PH == 1) { s.x0 = __builtin_amdgcn_exp2f(s.x0); s.x1 = __builtin_amdgcn_exp2f(s.x1); s.m0 = g0 * a[8 + 2 * P]; s.m1 = g1 * a[9 + 2 * P]; }
  if constexpr (PH == 2) { s.x0 = __builtin_amdgcn_rcpf(__builtin_fmaf(s.x0, c.Ri, c.Ri)); s.x1 = __builtin_amdgcn_rcpf(__builtin_fmaf(s.x1, c.Ri, c.Ri)); }
  if constexpr (PH == 3) s.r[P] = pk2s(s.m0 * s.x0, s.m1 * s.x1);
}
DI f32x16 featproj2(const f32x16& a, bf16x8 w0, bf16x8 w1, bool hh) {
  u32x4 q0, q1;
#pragma unroll
  for (int i = 0; i < 4; i++) { q0[i] = pk2(a[2 * i], a[2 * i + 1]); q1[i] = pk2(a[8 + 2 * i], a[9 + 2 * i]); }
  q1[2] = hh ? 0u : q1[2]; q1[3] = hh ? 0u : q1[3];
  f32x16 z;
#pragma unroll
  for (int i = 0; i < 16; i++) z[i] = 0.f;
  z = MFMA(w0, __builtin_bit_cast(bf16x8, q0), z);
  z = MFMA(w1, __builtin_bit_cast(bf16x8, q1), z);
  return z;
}
DI f32x16 featproj(const f32x16& a, unsigned wpaddr, unsigned off, bool hh) {
  return featproj2(a, tr2(wpaddr, off, off + 8 * 56), tr2(wpaddr, off + 16 * 56, off + 24 * 56), hh);
}
DI float max16(const f32x16& p) { float m = max3(p[0], p[1], p[2]); _Pragma("unroll") for (int i = 3; i < 15; i += 2) m = max3(m, p[i], p[i + 1]); return xmax(__builtin_fmaxf(m, p[15])); }
DI float min16(const f32x16& p) { float m = min3(p[0], p[1], p[2]); _Pragma("unroll") for (int i = 3; i < 15; i += 2) m = min3(m, p[i], p[i + 1]); return xmin(__builtin_fminf(m, p[15])); }
DI f32x16 dualexpP(const f32x16& p, float Lr) {
  const f32x2 bx = dup2(-max16(p) * Lr), bn = dup2(min16(p) * Lr), L2 = dup2(Lr);
  f32x16 o;
#pragma unroll
  for (int i = 0; i < 8; i++) {
    const f32x2 ep = exp2v(fma2(sub2(p, i), L2, bx)), en = exp2v(fma2(sub2(p, i), -L2, bn));
    o[i] = U2F(pk2(ep[0], ep[1])); o[8 + i] = U2F(pk2(en[0], en[1]));
  }
  return o;
}
struct SmS { f32x16 p, e; float en[16], bx, bn, sp, sn, tp, tn; bf16x8 w0, w1; };
DI bf16x8 sub4(const f32x16& v, int k) { f32x4 t = {v[4 * k], v[4 * k + 1], v[4 * k + 2], v[4 * k + 3]}; return __builtin_bit_cast(bf16x8, t); }

DI constexpr unsigned lc1_base(int ch) { return ch == 6 ? L_KV : ((ch & 1) ? L_GU : 0u) + (ch < 2 ? 49152u : ch < 4 ? 0u : 16384u); }
template <int CTRL, int RM> DI float dppadd(float v) { return v + U2F(__builtin_amdgcn_update_dpp(0u, F2U(v), CTRL, RM, 0xf, false)); }
DI float wavesum63(float v) {
  v = dppadd<0x111, 0xf>(v); v = dppadd<0x112, 0xf>(v); v = dppadd<0x114, 0xf>(v); v = dppadd<0x118, 0xf>(v);
  v = dppadd<0x142, 0xa>(v); return dppadd<0x143, 0xc>(v);
}

#define STAMP(i) do { if ((i) == 0 || (i) == ENC) { unsigned long long t_; asm volatile("s_memrealtime %0\n\ts_waitcnt lgkmcnt(0)" : "=s"(t_) :: "memory"); stv[(i) != 0] = (unsigned)t_; } } while (0)
#define CSTAMP(i)
#define STAMPS_OUT() do { STAMP(13); const unsigned lim_ = 3000u + ((stv[1] && (ENCAT ? AT : !AT)) ? stv[1] - stv[0] : 0u); while ((unsigned)__builtin_amdgcn_s_memrealtime() - stv[0] < lim_) __builtin_amdgcn_s_sleep(1); } while (0)

#define PDV 5
constexpr int PD = PDV, RING = 9;
constexpr int later2(int i, int NS, int GG) {
  const int younger = 2 * ((i + PD <= NS ? i + PD : NS) - 1 - i);
  int extra = i < PD ? 2 : 0;
  if (GG == 0 && (i == 10 || i == 11)) extra += 2 * (i - 9);
  return younger + extra;
}
DI constexpr unsigned ring_area(int j) { return j == 0 ? L_FREE : j == 1 ? L_PHI : lc1_base(j - 2); }
constexpr int cumG1(int s) { return s < 4 ? 0 : s < 22 ? (s - 4) / 2 : (9 + (s - 22) * 5 / 4 < 48 ? 9 + (s - 22) * 5 / 4 : 48); }

#define CODE_BYTES 65536
__global__ __launch_bounds__(512, 2) void fused_kernel(const char* px_, const char* pqkv_, const char* pwg_, const char* pwu_, char* ws_, const char* pphi_,
                                                       const char* pwd_, const char* pwo_, char* pout_) {
  typedef __attribute__((ext_vector_type(8))) unsigned long long u64x8;
  u64x8 ka; unsigned long long kb;
  asm volatile("s_load_dwordx16 %0, %2, 0\n\ts_load_dwordx2 %1, %2, 64" : "=&s"(ka), "=&s"(kb) : "s"(__builtin_amdgcn_kernarg_segment_ptr()));
  __shared__ __attribute__((aligned(16))) char smem[163840];
  const unsigned LB = (unsigned)(size_t)smem;
  const unsigned tid = __builtin_amdgcn_workitem_id_x(), lane = tid & 63;
  const unsigned w = __builtin_amdgcn_readfirstlane(tid >> 6), g = w >> 2, wr = w & 3;
  const unsigned b = __builtin_amdgcn_workgroup_id_x(), xcd = b & 7, bi = b >> 3, grp = xcd >> 1, half = xcd & 1, rbl = bi & 3, cp = bi >> 2, ci = cp - 1;
  const unsigned rb = 4 * grp + rbl, c = 7 * half + ci;
  const unsigned head = 8 * half + (((half ? 0x76425310u : 0x75316420u) >> (4 * cp)) & 7);
  const bool attn = cp == 0;
  unsigned stv[2] = {0, 0};
  STAMP(0); CSTAMP(14);
  const unsigned wstr = attn ? QROW : FROW;
  const unsigned sG0 = half * (attn ? 448 : 1792) + (attn ? 0 : ci * 256), sX0 = rb * 128 * XROW;
  unsigned vx0, vx1, vG, vU, vKV;
  auto voffs = [&](unsigned ln) AI {
    const unsigned r8 = ln >> 3, c8 = ln & 7, sa = (r8 >> 1) & 1, lc = c8 ^ (4 * sa);
    vx0 = (16 * w + r8) * XROW + ((c8 ^ (r8 >> 1)) << 4); vx1 = (vx0 ^ 64u) + 8 * XROW;
    const unsigned cg = attn ? lc : (c8 ^ (2 * sa)), cu = attn ? (lc >= 6 ? lc - 2 : lc) : (cg ^ 4);
    vG = (8 * w + r8) * wstr + (cg << 4); vU = (8 * w + r8) * wstr + (cu << 4);
    vKV = (8 * w + r8) * QROW + 896 * (1 + (lc >> 2)) + ((lc & 3) << 4) + ((56 * head) & ~15u);
  };
  voffs(lane);
  const unsigned lw1 = LB + w * 1024, lw2 = LB + w * 2048;
#define ROT (((0x24924u >> (2 * (cp + rbl))) & 3) * 2)
  const unsigned rot = ROT;
  const unsigned srow = 128 * rb + 64 * half + 8 * cp + w;
  unsigned vrow = lane < 56 ? lane * 16 : 0x80000000u;
  asm volatile("s_waitcnt lgkmcnt(0)" : "+s"(ka), "+s"(kb), "+v"(vx0), "+v"(vx1), "+v"(vG), "+v"(vU), "+v"(vKV), "+v"(vrow));
  const char *px = (const char*)ka[0], *pqkv = (const char*)ka[1], *pwg = (const char*)ka[2], *pwu = (const char*)ka[3], *pphi = (const char*)ka[5],
             *pwd = (const char*)ka[6], *pwo = (const char*)ka[7];
  char *ws = (char*)ka[4], *pout = (char*)kb;
  const rsrc_t rsX = mkrs(px, INSZ(2048 * XROW)), rsQ = mkrs(pqkv, INSZ(448 * QROW));
  const rsrc_t rsG = mkrs(attn ? pqkv : pwg, INSZ(448 * wstr)), rsU = mkrs(attn ? pqkv + 128 : pwu, INSZ(448 * wstr - (attn ? 128 : 0)));
  auto cidx = [&](int st) AI -> unsigned { unsigned t = rot + st; return t >= 7 ? t - 7 : t; };
  auto issueA = [&](int st, int k) AI {
    const unsigned sl = st & 3, ch = cidx(st);
    if (k == 0) dma(lw2, sl * 16384, vx0, rsX, sX0 + ch * 128, 0);
    if (k == 1) dma(lw2, sl * 16384 + 1024, vx1, rsX, sX0 + ch * 128, 0);
    if (k == 2) dma(lw1, L_GU + sl * 16384, vG, rsG, sG0 + ch * 64 * wstr, 0);
    if (k == 3) dma(lw1, L_GU + sl * 16384 + 8192, vU, rsU, sG0 + ch * 64 * wstr, 0);
    if (k == 4) dma(lw1, L_KV + sl * 8192, vKV, rsQ, ch * 64 * QROW, 0);
  };
  const rsrc_t rsWS = mkrs(ws, WS_END);
  unsigned Ev, cpf; u32x4 xrw;
  asm volatile("buffer_load_dwordx4 %0, %1, %2, %3 offen" : "=v"(xrw) : "v"(vrow), "s"(rsX), "s"(srow * XROW) : "memory");
  asm volatile("buffer_load_dword %0, off, %1, 0 sc1" : "=v"(Ev) : "s"(rsWS) : "memory");
  {
    unsigned long long pc; asm volatile("s_getpc_b64 %0" : "=s"(pc));
    const unsigned vo = w == 0 && lane < 24 ? (bi * 24 + lane) * 128 : 0x80000000u;
    asm volatile("buffer_load_dword %0, %1, %2, 0 offen" : "=v"(cpf) : "v"(vo), "s"(mkrs((const void*)pc, CODE_BYTES)) : "memory");
  }
#pragma unroll
  for (int k = 0; k < 5; k++) issueA(0, k);
  asm volatile("s_waitcnt vmcnt(5)" : "+v"(xrw), "+v"(Ev), "+v"(cpf) :: "memory");
#pragma unroll
  for (int k = 0; k < 5; k++) issueA(1, k);
  issueA(2, 0);
  const unsigned E = __builtin_amdgcn_readfirstlane(Ev);
  {
    float s1 = 0.f, s2 = 0.f;
#pragma unroll
    for (int i = 0; i < 4; i++) { s1 = dot2(xrw[i], 0x3f803f80u, s1); s2 = dot2(xrw[i], xrw[i], s2); }
    s1 = wavesum63(s1); s2 = wavesum63(s2);
    const float mean = s1 * (1.f / 448.f), var = __builtin_fmaxf(s2 * (1.f / 448.f) - mean * mean, 0.f);
    u32x2 gr = {pk2(-mean, __builtin_amdgcn_sqrtf(rbf(rbf(var) + 1e-5f))), E + 1};
    __builtin_amdgcn_raw_buffer_store_b64(gr, rsWS, lane == 63 ? 0u : 0x80000000u, WS_GR + srow * 8, 16);
  }

  auto prog = [&](auto at_, auto gg_) AI {
  constexpr bool AT = decltype(at_)::value; constexpr int GG = decltype(gg_)::value;
  constexpr unsigned lcstep = AT ? 224 : 128;
  unsigned lane;
  asm volatile("v_mbcnt_lo_u32_b32 %0, -1, 0\n\tv_mbcnt_hi_u32_b32 %0, -1, %0" : "=v"(lane));
  __builtin_assume(lane < 64);
  voffs(lane);
  auto issueL = [&](int hc) AI {
    const int st = hc >> 1; const unsigned ch = cidx(st);
    if (hc & 1) dma(lw1, lc1_base(st) + 8192, vU, rsU, sG0 + ch * 64 * wstr, lcstep);
    else dma(lw1, lc1_base(st), vG, rsG, sG0 + ch * 64 * wstr, lcstep);
  };
  auto issueG = [&](int bar, int k) AI {
    if (bar == 0) issueA(2 + k / 5, k % 5);
    else if (bar <= 3) issueA(bar + 3, k);
    else if (k < 4) issueL(4 * (bar - 4) + k);
    else if (bar == 4) dma(LB + (w & 1) * 1024, L_WPK, lane * 16, mkrs(pphi, 16 * 1568), head * 1568 + (w & 1) * 1024, 0);
    else issueL(12 + (bar - 5));
  };

  const unsigned rt = 4 * rb + wr;
  const unsigned sA2 = WS_A2 + rt * 144 * 1024 + (AT ? half * 16384 : c * 8192), vL16 = lane * 16;
  const u32x4 sent = {~0u, ~0u, ~0u, ~0u};
  auto clr = [&](int ks) AI { st16(sent, rsWS, vL16, sA2, ks * 1024); };

  auto clri = [&](int q) AI { clr(GG ? (q < 6 ? 114 + q : 118 + q) : (q < 2 ? 112 + q : 118 + q)); };

  const bool hh = lane >= 32;
  const unsigned hu = lane >> 5, blk = (lane >> 4) & 1, q4 = (lane >> 2) & 3, p4 = lane & 3, ta = (q4 >> 1) & 1, l31 = lane & 31;
  const unsigned perm = wr ^ (2 * GG);
  const unsigned rowoff = (8 * hu + q4) * 128 + (perm << 11);
  const unsigned pc = (4 * blk + p4) < 6 ? (4 * blk + p4) : 6;
  auto tileaddr = [&](unsigned t) AI -> unsigned {
    if constexpr (AT) { unsigned o = 56 * t + 8 * pc; return (o >> 7) * 8192 + ((((o >> 4) & 7) ^ (4 * ta)) << 4) + (o & 8) + rowoff; }
    else return blk * 8192 + (((2 * t + (p4 >> 1)) ^ (4 * blk + 2 * ta)) << 4) + 8 * (p4 & 1) + rowoff;
  };
  unsigned aA[3], aB[2];
  {
    unsigned o = (head & 1) * 8 + 8 * pc;
    unsigned ak = L_KV + (((o >> 4) ^ (4 * ta)) << 4) + (o & 8) + rowoff;
    aA[0] = LB + (GG ? L_GU + tileaddr(1) : ak);
    aA[1] = LB + (GG ? L_GU + tileaddr(2) : (ak ^ 64u));
    aA[2] = LB + L_GU + tileaddr(GG ? 3 : 0);
    aB[0] = LB + tileaddr(2 * GG); aB[1] = LB + tileaddr(2 * GG + 1);
  }
  const unsigned xrow = 32 * wr + l31;
  const unsigned aX = LB + xrow * 128 + (((2 * perm + hu) ^ ((xrow >> 1) & 7)) << 4);
  const unsigned selv = (((lane >> 4) ^ (lane >> 2)) & 1) ? 0u : 0x3f803f80u;
  const u32x4 sel4 = {selv, selv, selv, selv};
  const bf16x8 sel = __builtin_bit_cast(bf16x8, sel4);

  bf16x8 xf[28], wa[2][3], wb[8][2];
  f32x16 accA[3], accB[2];
  f32x4 csA[3], csB[2];
#pragma unroll
  for (int j = 0; j < 3; j++) { for (int i = 0; i < 16; i++) accA[j][i] = 0.f; for (int i = 0; i < 4; i++) csA[j][i] = 0.f; }
#pragma unroll
  for (int j = 0; j < 2; j++) { for (int i = 0; i < 16; i++) accB[j][i] = 0.f; for (int i = 0; i < 4; i++) csB[j][i] = 0.f; }
  u32x2 grn;

  const rsrc_t rsWD = mkrs(pwd, INSZ(1792 * XROW)), rsWO = mkrs(pwo, INSZ(448 * XROW));
  const unsigned vW2 = (lane >> 3) * XROW + (((lane & 7) ^ ((lane >> 2) & 4)) << 4) + ci * 128;
  constexpr int NS = GG ? 16 : 20;
#define RHO ((rbl + cp) & 3)
  const unsigned rho = RHO;
  auto t_of = [](int i) constexpr { return (0x54763210u >> (4 * (i >> 1))) & 7; };
  auto ksb = [&](int i) AI -> unsigned {
    if (GG) return 16 * wr + ((t_of(i) & 3) ^ rho);
    if (i < 12) { const unsigned cc = i % 6 + rho + (rho >> 1); return wr + 8 * (cc >= 6 ? cc - 6 : cc); }
    return 8 * wr + 2 * (((i - 12) >> 1) ^ rho);
  };
  auto ksi = [&](int i) AI -> unsigned { return GG ? 8 * (i & 1) + (t_of(i) & 4) : i < 12 ? 64 + 4 * (i / 6) : 112 + (i & 1); };
  auto issueW2 = [&](int i, int hf) AI {
    if (GG == 0 && i >= 12) dma(lw2, ring_area(i % RING) + hf * 1024, vW2, rsWO, ksb(i) * 14 * XROW, (16 * (i & 1) + 8 * hf) * XROW);
    else dma(lw2, ring_area(i % RING) + hf * 1024, vW2, rsWD, ksb(i) * 16 * XROW, (ksi(i) * 16 + 8 * hf) * XROW);
  };

  const unsigned aCSw = LB + L_CS + l31 * 16 + wr * 4 + GG * 2048, aCSr = LB + L_CS + l31 * 16 + GG * 2048;
  auto readX = [&](int n) AI { xf[n] = ldsr<bf16x8>(aX ^ (unsigned)((n & 3) << 5), ((n >> 2) & 3) * 16384); };
  auto readA = [&](int n, int j) AI {
    const unsigned o = ((n >> 2) & 3) * ((GG == 0 && j < 2) ? 8192 : 16384);
    wa[n & 1][j] = tr2(aA[j] ^ (unsigned)((n & 3) << 11), o, o + 512);
  };
  auto readB = [&](int n, int j) AI {
    const unsigned base = lc1_base(n >> 2);
    if (XORCOND) asm volatile("" : "+v"(aB[j]));
    wb[n & 7][j] = tr2(aB[j] ^ (((n & 3) << 11) | (base & 0x30000u)), base & 0xffffu, (base & 0xffffu) + 512);
  };
  RowC rc; bf16x8 xe; unsigned denb;
  auto rowconst = [&]() AI {
#pragma nounroll
    for (int it = 0; it < (1 << 16) && !ALL(grn[1] == E + 1); ++it) {
      __builtin_amdgcn_s_sleep(SLEEPN);
      asm volatile("buffer_load_dwordx2 %0, %1, %2, %3 offen sc1\n\ts_waitcnt vmcnt(0)" : "=v"(grn) : "v"(xrow * 8), "s"(rsWS), "s"(WS_GR + rb * 1024) : "memory");
    }
    const float den = U2F(grn[0] & 0xffff0000u);
    const unsigned nm = hh ? 0u : __builtin_amdgcn_perm(grn[0], grn[0], 0x01000100u);
    u32x4 t = {nm, nm, nm, nm}; xe = __builtin_bit_cast(bf16x8, t);
    denb = grn[0] >> 16;
    rc.r = __builtin_amdgcn_rcpf(den);
    const float k1 = -2.f * 1.4426950408889634f * 0.7978845608028654f;
    rc.K1 = k1 * rc.r; rc.K2 = (k1 * 0.044715f) * rc.r * (rc.r * rc.r); rc.Ri = den * den; rc.Lr = 1.4426950408889634f * rc.r;
  };
  WAITV(7);
  BAR(); MEMFENCE();
  STAMP(1);
  readX(0); readA(0, 0); readA(0, 1); readA(0, 2);
  SFOR(n, 28)
    SB();
    if constexpr (n == 27) {
#pragma unroll
      for (int j = 0; j < 3; j++) ldsw<unsigned>(aCSw, j * 512, hilo(csA[j][0]));
    }
    if constexpr ((n & 3) == 3) {
      if constexpr (n == 27) WAITV(0);
      else if constexpr (n == 23) asm volatile("s_waitcnt vmcnt(10)" : "+v"(grn) :: "memory");
      else if constexpr (n == 3 || n == 15 || n == 19) WAITV(11); else WAITV(10);
      { WAITL0(); BAR(); MEMFENCE(); }
      if constexpr (n == 27) STAMP(2);
    }
    constexpr int bar = (n + 1) >> 2, ph = (n + 1) & 3;
#pragma unroll
    for (int j = 0; j < 3; j++) {
      if constexpr (n < 27) { if (j == 0) readX(n + 1); readA(n + 1, j); }
      else { wa[0][j] = ldsr<bf16x8>(aCSr, j * 512); if (GG == 1 && j < 2) { readB(j, 0); readB(j, 1); } }
      SB();
      accA[j] = MFMA(wa[n & 1][j], xf[n], accA[j]);
      const int slot = 3 * ph + j;
      if (bar == 0) issueG(0, slot - 2);
      else if (bar == 6) { if (slot < 5) issueG(bar, slot); }
      else if (bar <= 6) { if (slot == 0) issueG(bar, 0); if (slot == 2) issueG(bar, 1); if (slot == 5) issueG(bar, 2); if (slot == 7) issueG(bar, 3); if (slot == 10) issueG(bar, 4); }
      else if (j < 2) {
        if constexpr (AT) dma(lw2, L_FREE + j * 1024, lane * 16, mkrs(pphi, 16 * 1568), half * 8 * 1568 + w * 2048, j * 1024);
        else issueW2(0, j);
      }
      if ((n & 3) == 0) csA[j] = MFMA16(sel, wa[n & 1][j], csA[j]);
      if (n == 25 && j == 0) rowconst();
      if (n == 14 && j == 2) asm volatile("buffer_load_dwordx2 %0, %1, %2, %3 offen sc1" : "=v"(grn) : "v"(xrow * 8), "s"(rsWS), "s"(WS_GR + rb * 1024) : "memory");
      SB();
    }
  SEND
#pragma unroll
  for (int j = 0; j < 3; j++) accA[j] = MFMA(wa[0][j], xe, accA[j]);
  asm volatile("" : "+v"(accA[0]), "+v"(accA[1]), "+v"(accA[2]));
  SB();

  const unsigned sFown = WS_FH + (rt * 32 + (AT ? 28 + 2 * half : 2 * c) + GG) * FS;
  const unsigned aWP = LB + (4 * hu + q4) * 56 + 8 * pc;
  const unsigned T12 = (E + 1) & 0xfffu, T16 = (E + 1) & 0xffffu;
  const unsigned rt2 = 2 * (8 * grp + 4 * half + rbl);
  const unsigned sF = WS_FH + rt2 * 32 * FS;
  const unsigned fc = l31 >> 1;
  const bool nd1 = GG ? (fc >> 1) == wr : (fc >= 8 && fc < 14) || fc == 14 + (wr >> 1);
  const unsigned aIW = LB + L_PHI + xrow * 64 + ((hu ^ ((xrow >> 1) & 7)) << 3);
  SmS sm;
  unsigned aWK = aWP + L_WPK, aWQ = aWP + L_FREE;
  asm volatile("" : "+v"(aWK)); if constexpr (AT) asm volatile("" : "+v"(aWQ));
  auto kstep = [&](auto s_) AI {
    constexpr int s = decltype(s_)::value;
    if constexpr (s == 0) { sm.w0 = tr2(aWK, 0, 8 * 56); sm.w1 = tr2(aWK, 16 * 56, 24 * 56); }
    if constexpr (s == 1) sm.p = featproj2(accA[0], sm.w0, sm.w1, hh);
    if constexpr (s == 2 || s == 3) {
#pragma unroll
      for (int k = 2 * s - 4; k < 2 * s - 2; k++) {
        u32x2 vv = {pk2(accA[1][4 * k], accA[1][4 * k + 1]), pk2(accA[1][4 * k + 2], accA[1][4 * k + 3])};
        if (k == 3) vv[0] = hh ? denb : vv[0];
        ldsw<u32x2>(aIW ^ (unsigned)(k << 4), L_V - L_PHI, vv);
      }
    }
    if constexpr (s == 4) sm.bx = -max16(sm.p) * rc.Lr;
    if constexpr (s == 5) sm.bn = min16(sm.p) * rc.Lr;
    if constexpr (s >= 6 && s < 22) {
      constexpr int i = s - 6;
      sm.e[i] = __builtin_amdgcn_exp2f(__builtin_fmaf(sm.p[i], rc.Lr, sm.bx)); sm.en[i] = __builtin_amdgcn_exp2f(__builtin_fmaf(sm.p[i], -rc.Lr, sm.bn));
      if constexpr (i == 0) { sm.sp = sm.e[0]; sm.sn = sm.en[0]; } else if constexpr (i < 12) { sm.sp += sm.e[i]; sm.sn += sm.en[i]; }
      else if constexpr (i == 12) { sm.tp = sm.e[12]; sm.tn = sm.en[12]; } else { sm.tp += sm.e[i]; sm.tn += sm.en[i]; }
    }
    if constexpr (s == 22) { sm.sp = __builtin_amdgcn_rcpf(xsum(sm.sp + (hh ? 0.f : sm.tp))) * rc.r; sm.sn = __builtin_amdgcn_rcpf(xsum(sm.sn + (hh ? 0.f : sm.tn))) * rc.r; }
    if constexpr (s >= 23) {
#pragma unroll
      for (int u = (s == 29 ? 6 : s - 23); u <= (s == 29 ? 7 : s - 23); u++) {
        const int k = u >> 1;
        if (u & 1) { u32x2 t = {pk2s(sm.en[4 * k] * sm.sn, sm.en[4 * k + 1] * sm.sn), pk2s(sm.en[4 * k + 2] * sm.sn, sm.en[4 * k + 3] * sm.sn)}; ldsw<u32x2>(aIW ^ (unsigned)(k << 4), 8192, t); }
        else { u32x2 t = {pk2s(sm.e[4 * k] * sm.sp, sm.e[4 * k + 1] * sm.sp), pk2s(sm.e[4 * k + 2] * sm.sp, sm.e[4 * k + 3] * sm.sp)}; ldsw<u32x2>(aIW ^ (unsigned)(k << 4), 0, t); }
      }
    }
  };
  auto qstep = [&](auto s_, f32x16& a, unsigned lh) AI {
    constexpr int s = decltype(s_)::value;
    if constexpr (s == 0) { sm.w0 = tr2(aWQ, lh * 1568, lh * 1568 + 8 * 56); sm.w1 = tr2(aWQ, lh * 1568 + 16 * 56, lh * 1568 + 24 * 56); }
    if constexpr (s == 1) sm.p = featproj2(a, sm.w0, sm.w1, hh);
    if constexpr (s == 3) sm.bx = -max16(sm.p) * rc.Lr;
    if constexpr (s == 4) sm.bn = min16(sm.p) * rc.Lr;
    if constexpr (s >= 5 && s < 21) {
      constexpr int i = (s - 5) >> 1;
      float t0, t1;
      if constexpr ((s - 5) & 1) { t0 = __builtin_fmaf(sm.p[2 * i], -rc.Lr, sm.bn); t1 = __builtin_fmaf(sm.p[2 * i + 1], -rc.Lr, sm.bn); }
      else { t0 = __builtin_fmaf(sm.p[2 * i], rc.Lr, sm.bx); t1 = __builtin_fmaf(sm.p[2 * i + 1], rc.Lr, sm.bx); }
      a[8 * ((s - 5) & 1) + i] = U2F(pk2(__builtin_amdgcn_exp2f(t0), __builtin_amdgcn_exp2f(t1)));
      if constexpr (s == 20) asm volatile("" : "+v"(a));
    }
  };
  bf16x8 ka_[4], kb_[2][4]; f32x4 kd[2];
  auto kvstep = [&](auto s_) AI {
    constexpr int s = decltype(s_)::value;
    if constexpr (s == 0) {
      const unsigned g4 = lane >> 4, r0 = 8 * g4 + q4, sw = 4 * blk + (q4 >> 1);
      unsigned b0 = LB + L_PHI + r0 * 64 + ((p4 ^ sw) << 3);
      unsigned a0 = (b0 ^ ((wr >> 1) << 5)) + (wr & 1) * 8192;
#pragma unroll
      for (int k = 0; k < 4; k++) {
        ka_[k] = __builtin_shufflevector(trd2(a0, k * 2048), trd2(a0 ^ 16u, k * 2048 + 256), 0, 1, 2, 3, 4, 5, 6, 7);
        kb_[0][k] = __builtin_shufflevector(trd2(b0, L_V - L_PHI + k * 2048), trd2(b0 ^ 16u, L_V - L_PHI + k * 2048 + 256), 0, 1, 2, 3, 4, 5, 6, 7);
        kb_[1][k] = __builtin_shufflevector(trd2(b0 ^ 32u, L_V - L_PHI + k * 2048), trd2(b0 ^ 48u, L_V - L_PHI + k * 2048 + 256), 0, 1, 2, 3, 4, 5, 6, 7);
      }
    }
    if constexpr (s == 1) {
      f32x4 z = {0.f, 0.f, 0.f, 0.f}; kd[0] = z; kd[1] = z;
#pragma unroll
      for (int k = 0; k < 4; k++) { kd[0] = MFMA16(ka_[k], kb_[0][k], kd[0]); kd[1] = MFMA16(ka_[k], kb_[1][k], kd[1]); }
    }
    if constexpr (s == 3) {
#pragma unroll
      for (int d = 0; d < 2; d++) {
        u32x4 t;
#pragma unroll
        for (int i = 0; i < 4; i++) t[i] = (F2U(kd[d][i]) & ~0xfffu) | T12;
        st16(t, rsWS, (lane >> 4) * 512 + (lane & 15) * 16, ((head * 16 + rb) * 2 + (wr & 1)) * 4096 + (wr >> 1) * 2048, WS_KVP + d * 256);
      }
    }
  };
  auto qexp2 = [&](f32x16& a0, unsigned l0, f32x16& a1, unsigned l1) AI {
    const f32x16 p0 = featproj(a0, aWQ, l0 * 1568, hh), p1 = featproj(a1, aWQ, l1 * 1568, hh);
    a0 = dualexpP(p0, rc.Lr); a1 = dualexpP(p1, rc.Lr); asm volatile("" : "+v"(a0), "+v"(a1));
  };

  unsigned fl1 = 0;
  EpiS es;
  u32x4 pv[8];
  const unsigned vKP = l31 * 16 + hu * 8192, sKP = (rb * 32 + half) * 4096 + wr * 1024 + GG * 512;
  bf16x8 cb[2];
  auto ldflag = [&]() AI { asm volatile("buffer_load_dword %0, %1, %2, %3 offen sc1" : "=v"(fl1) : "v"(nd1 ? lane * FS : 0x80000000u), "s"(rsWS), "s"(sF) : "memory"); };
  auto touch = [&](unsigned wo4) AI {
    const unsigned rw = (rbl * 8 + w + wo4) * 44 + lane;
    const bool dn = rw < 896;
    const unsigned vo = lane < 44 ? (dn ? ((rw >> 6) * 128 + 64 + (rw & 63)) : rw - 896) * XROW + ci * 128 : 0x80000000u;
    asm volatile("s_add_u32 m0, %0, %c5\n\ts_nop 0\n\tbuffer_load_dword %1, %3, 0 offen lds\n\tbuffer_load_dword %2, %4, 0 offen lds"
                 :: "s"(LB + w * 256), "v"(dn ? vo : 0x80000000u), "v"(dn ? 0x80000000u : vo), "s"(rsWD), "s"(rsWO), "i"(L_ST) : "memory", "scc");
  };
  auto stream = [&]() AI {
    __builtin_amdgcn_s_setprio(PS);
    SFOR(n, 28)
      if constexpr (n == 27) { ldsw<unsigned>(aCSw, 3 * 512, hilo(csB[0][0])); ldsw<unsigned>(aCSw, 0, hilo(csB[1][0])); WAITL0(); BAR(); MEMFENCE(); }
      else if constexpr (GG == 1) { if constexpr (n == S2N || (!AT && n == B1N)) { if constexpr (AT) WAITV(10); BAR(); MEMFENCE(); } }
      else if constexpr (AT ? n == 7 : (n & 3) == 3) { asm volatile("s_waitcnt lgkmcnt(%c0)" :: "i"(4 * DIST - 4) : "memory"); BAR(); MEMFENCE(); }
      SFOR(j, 2)
        constexpr int q = 2 * n + j;
        if constexpr (DIST == 3 && n == 0) readB(2, j);
        if constexpr (n + DIST < 28) readB(n + DIST, j);
        if constexpr (n == 27) cb[j] = ldsr<bf16x8>(aCSr, j ? 0 : 3 * 512);
        SB();
        accB[j] = MFMA(wb[n & 7][j], xf[n], accB[j]);
        if constexpr ((n & 3) == 0) csB[j] = MFMA16(sel, wb[n & 7][j], csB[j]);
        if constexpr (!AT) {

          if constexpr (GG == 1 && j == 0 && n >= B1N && n < B1N + 4) issueW2(1 + ((n - B1N) >> 1), (n - B1N) & 1);

          if constexpr (GG == 0) {
            if constexpr (j == 1 && n < 16) { epiSlice<(n >> 2) & 3, n & 3>(accA[2], rc, es); if constexpr (n == 15) st16(es.r, rsWS, vL16, sA2, 0); }
            if constexpr (j == 0 && n >= 3 && n < 7) issueW2(1 + ((n - 3) >> 1), (n - 3) & 1);
            if constexpr (j == 0 && n >= 7 && n < 27 && (n & 1)) issueW2(1 + ((n + 1) >> 2), ((n + 1) >> 1) & 1);
            if constexpr (n == 27) issueW2(8, j);
          }
        } else {
          if constexpr (GG == 1 && j == 0 && n < 10) clri(n);
          if constexpr (GG == 0 && j == 1 && n < 21) qstep(n_, accA[2], 0);
          if constexpr (GG == 0 && n >= 24) pv[q - 48] = ld16(rsWS, vKP, sKP, WS_KVP + (q - 48) * 16384);
        }
        SB();
      SEND
    SEND
    accB[0] = MFMA(cb[0], xe, accB[0]); accB[1] = MFMA(cb[1], xe, accB[1]);
    asm volatile("" : "+v"(accB[0]), "+v"(accB[1]));
    __builtin_amdgcn_s_setprio(GG ? PV : 0);
    SB();
  };
  auto pairF = [&](auto p_, const f32x16& a) AI { constexpr int p = decltype(p_)::value; epiSlice<p, 0>(a, rc, es); epiSlice<p, 1>(a, rc, es); epiSlice<p, 2>(a, rc, es); epiSlice<p, 3>(a, rc, es); };
  if constexpr (GG == 0) {
    __builtin_amdgcn_s_setprio(PV);
    if constexpr (AT) { SFOR(q, 6) clri(q); SEND }
    SFOR(s, 30)
      kstep(s_);
    SEND
    if constexpr (!AT) { touch(0); touch(4); }
    if constexpr (AT) WAITV(6);
    WAITL0(); SB(); BAR(); SB();
    STAMP(3);
    kvstep(IntC<0>{});
    readB(0, 0); readB(0, 1); readB(1, 0); readB(1, 1);
    kvstep(IntC<1>{}); kvstep(IntC<3>{});
    if constexpr (AT) { WAITV(2); stflag(E + 1, rsWS, sFown); }
    STAMP(4);
    SB();
    stream();
  } else {
    stream();
    STAMP(3);
    if constexpr (AT) {
      WAITV(0); stflag(E + 1, rsWS, sFown);
      SFOR(s, 21) qstep(s_, accA[0], 1); SEND
      SFOR(s, 21) qstep(s_, accA[1], 2); SEND
#pragma unroll
      for (int i = 0; i < 8; i++) pv[i] = ld16(rsWS, vKP, sKP, WS_KVP + i * 16384);
      SFOR(s, 21) qstep(s_, accA[2], 3); SEND
      SB(); BAR(); SB();
    } else {
      SFOR(e, 20)
        if constexpr (e < 12) pairF(IntC<e & 3>{}, accA[e >> 2]); else pairF(IntC<e & 3>{}, accB[(e >> 2) - 3]);
        if constexpr ((e & 3) == 3) st16(es.r, rsWS, vL16, sA2, (e < 12 ? 1 + (e >> 2) : 3 + (e >> 2)) * 1024);
        if constexpr (e == 0) { issueW2(3, 0); issueW2(3, 1); }
        if constexpr ((e & 3) == 2) { SB(); BAR(); SB(); issueW2(4 + (e >> 2), 0); issueW2(4 + (e >> 2), 1); }
      SEND
    }
  }

  if constexpr (AT) {
    __builtin_amdgcn_s_setprio(0);
    STAMP(5);
    waittag<16384>(pv, rsWS, vKP, sKP + WS_KVP, T12, 0xfffu);
    STAMP(6);
    {
      u32x4 o;
      f32x4 s4 = __builtin_bit_cast(f32x4, pv[0]);
#pragma unroll
      for (int i = 1; i < 8; i++) s4 += __builtin_bit_cast(f32x4, pv[i]);
      asm("" : "+v"(s4));
#pragma unroll
      for (int k = 0; k < 4; k++) {
        float sum = xsum(s4[k]);
        sum = l31 == 29 ? 1.f : sum;
        if (GG == 1) sum = wr == 3 ? 0.f : sum;
        o[k] = (pk2(sum, sum) & 0xffff0000u) | T16;
      }
      st16(o, rsWS, hh ? 0x80000000u : l31 * 16, (rb * 2 + half) * 4096 + wr * 1024 + GG * 512, WS_KVR);
    }
    const unsigned sKR = (8 * half + w) * 8192;
    {
      const f32x16 p0 = featproj(accB[0], aWQ, (4 + 2 * GG) * 1568, hh), p1 = featproj(accB[1], aWQ, (5 + 2 * GG) * 1568, hh);
      accB[0] = dualexpP(p0, rc.Lr); asm volatile("" : "+v"(accB[0]));
#pragma unroll
      for (int i = 0; i < 8; i++) pv[i] = ld16(rsWS, vL16, sKR, WS_KVR + i * 1024);
      accB[1] = dualexpP(p1, rc.Lr); asm volatile("" : "+v"(accB[1]));
    }
    STAMP(7);
    waittag<1024>(pv, rsWS, vL16, sKR + WS_KVR, T16, 0xffffu);
    STAMP(8);
#pragma unroll
    for (int i = 0; i < 4; i++) {
      u32x4 f = {__builtin_amdgcn_perm(pv[2 * i][1], pv[2 * i][0], 0x07060302u), __builtin_amdgcn_perm(pv[2 * i][3], pv[2 * i][2], 0x07060302u),
                 __builtin_amdgcn_perm(pv[2 * i + 1][1], pv[2 * i + 1][0], 0x07060302u), __builtin_amdgcn_perm(pv[2 * i + 1][3], pv[2 * i + 1][2], 0x07060302u)};
      ldsw<u32x4>(LB + w * 4096 + vL16, i * 1024, f);
    }
    WAITL0(); BAR(); MEMFENCE();
    f32x16 op[2], on[2];
    auto omm = [&](const f32x16& phi, unsigned lh, int z) AI {
#pragma unroll
      for (int i = 0; i < 16; i++) { op[z][i] = 0.f; on[z][i] = 0.f; }
      bf16x8 kf[4];
#pragma unroll
      for (int i = 0; i < 4; i++) kf[i] = ldsr<bf16x8>(LB + vL16, lh * 4096 + i * 1024);
#pragma unroll
      for (int i = 0; i < 2; i++) { op[z] = MFMA(kf[i], sub4(phi, i), op[z]); on[z] = MFMA(kf[2 + i], sub4(phi, 2 + i), on[z]); }
      SB();
    };
    auto ofin = [&](unsigned lh, int z) AI {
      const float ia = __builtin_amdgcn_rcpf(op[z][13]), ib = __builtin_amdgcn_rcpf(on[z][13]);
      const float id = __builtin_amdgcn_rcpf(__builtin_fmaf(op[z][12], ia, __builtin_fmaf(on[z][12], ib, 1e-6f)));
      const float ca = xhi(ia * id), cb2 = xhi(ib * id);
      u32x4 f0, f1;
#pragma unroll
      for (int i = 0; i < 4; i++) {
        f0[i] = pk2s(__builtin_fmaf(op[z][2 * i], ca, on[z][2 * i] * cb2), __builtin_fmaf(op[z][2 * i + 1], ca, on[z][2 * i + 1] * cb2));
        f1[i] = pk2s(__builtin_fmaf(op[z][8 + 2 * i], ca, on[z][8 + 2 * i] * cb2), __builtin_fmaf(op[z][9 + 2 * i], ca, on[z][9 + 2 * i] * cb2));
      }
      f1[2] = hh ? 0u : f1[2]; f1[3] = hh ? 0u : f1[3];
      st16(f0, rsWS, vL16, sA2, (112 + 2 * lh) * 1024); st16(f1, rsWS, vL16, sA2, (113 + 2 * lh) * 1024);
      SB();
    };
    if constexpr (GG) __builtin_amdgcn_s_setprio(1);
    omm(accA[2], GG ? 3 : 0, 0); omm(accB[0], 4 + 2 * GG, 1); ofin(GG ? 3 : 0, 0); omm(accB[1], 5 + 2 * GG, 0); ofin(4 + 2 * GG, 1);
    if constexpr (GG) { omm(accA[0], 1, 1); ofin(7, 0); omm(accA[1], 2, 0); ofin(1, 1); ofin(2, 0); } else ofin(5, 0);
    STAMP(9);
  } else {
    __builtin_amdgcn_s_setprio(0);
    STAMP(5);
    const unsigned sA = WS_A2 + rt2 * 144 * 1024;
    unsigned ln = vL16; asm volatile("" : "+v"(ln));
    const unsigned aW = lw2 + (ln & 0x200) + ((ln & 0xc0) << 1) + ((((ln >> 3) & 0x20) | ((ln >> 1) & 0x10)) ^ ((ln >> 1) & 0x40)) + ((ln >> 1) & 8);
    f32x16 acc[2][2];
#pragma unroll
    for (int i = 0; i < 4; i++)
#pragma unroll
      for (int k = 0; k < 16; k++) acc[i >> 1][i & 1][k] = 0.f;
    u32x4 af[NS][2];
    u32x2 xr[2];
    auto loadA = [&](int i) AI { af[i][0] = ld16(rsWS, vL16, sA + ksb(i) * 1024, ksi(i) * 1024); af[i][1] = ld16(rsWS, vL16, sA + ksb(i) * 1024, (144 + ksi(i)) * 1024); };
    const unsigned nt = wr >> 1, mt = wr & 1;
    const unsigned vO = (32 * mt + ((ln >> 4) & 31)) * XROW + ((ln >> 6) & 8) + 32 * GG, sO = rt2 * 32 * XROW + ci * 128 + nt * 64;
    if constexpr (GG == 0) { st16(epiP(accB[0], rc), rsWS, vL16, sA2, 4 * 1024); st16(epiP(accB[1], rc), rsWS, vL16, sA2, 5 * 1024); }
    STAMP(6);
    constexpr int MV = GG ? 7 : 9;
    bf16x8 wf[NS][2];
    auto readW = [&](int i) AI {
      const unsigned ra = ring_area(i % RING), la = aW + (ra & 0x30000u);
      wf[i][0] = tr2(la, ra & 0xffffu, (ra & 0xffffu) + 1024); wf[i][1] = tr2(la ^ 64u, ra & 0xffffu, (ra & 0xffffu) + 1024);
    };
    waitv<GG ? 7 : 4>();
    SFOR(i, GG ? 7 : 8) readW(i); SEND
    WAITV(0); stflag(E + 1, rsWS, sFown);
    if constexpr (!GG) readW(8);
    WAITL0(); SB();
    BAR();
    SB(); STAMP(11);
    if constexpr (GG) __builtin_amdgcn_s_setprio(P2G1);
    SFOR(i, MV)
      if constexpr (i == MV - 2) ldflag();
      issueW2(9 + i, 0); issueW2(9 + i, 1);
    SEND
    asm volatile("s_waitcnt vmcnt(4)" : "+v"(fl1) :: "memory");
    if (!ALL(!nd1 || fl1 == E + 1u)) poll(rsWS, lane * FS, sF, E, nd1 ? 1u : 0u);
    STAMP(7);
#pragma unroll
    for (int j = 0; j < PD; j++) loadA(j);
#pragma unroll
    for (int k = 0; k < 2; k++) asm volatile("buffer_load_dwordx2 %0, %1, %2, %3 offen offset:%c4" : "=v"(xr[k]) : "v"(vO), "s"(rsX), "s"(sO), "i"(16 * k) : "memory");
    SFOR(i, NS)
      asm volatile("s_waitcnt vmcnt(%c2)" : "+v"(af[i][0]), "+v"(af[i][1]) : "i"(later2(i, NS, GG)) : "memory");
      if constexpr (i + 1 >= MV && i + 1 < NS) readW(i + 1);
      if constexpr (GG == 0 && i >= 12) {
        auto stale = [&]() AI {
          unsigned m = UMAX(UMAX(UMAX(af[i][0][0], af[i][0][1]), UMAX(af[i][0][2], af[i][0][3])), UMAX(UMAX(af[i][1][0], af[i][1][1]), UMAX(af[i][1][2], af[i][1][3])));
          return ANY(m == ~0u);
        };
        if (__builtin_expect(stale(), 0)) {
          int it = 0;
#pragma nounroll
          do { __builtin_amdgcn_s_sleep(SLEEPR); rel1<true>(af[i][0], af[i][1], rsWS, vL16, sA + (ksb(i) + ksi(i)) * 1024); } while (++it < (1 << 14) && stale());
          SFOR(u, PD - 1) if constexpr (i + 1 + u < NS) rel1<false>(af[i + 1 + u][0], af[i + 1 + u][1], rsWS, vL16, sA + (ksb(i + 1 + u) + ksi(i + 1 + u)) * 1024); SEND
        }
      }
      if constexpr (i == 6) STAMP(8);
      if constexpr (i == 12) STAMP(9);
      SB();
      if constexpr (i + PD < NS) loadA(i + PD);
#pragma unroll
      for (int mm = 0; mm < 2; mm++) acc[0][mm] = MFMA(wf[i][0], __builtin_bit_cast(bf16x8, af[i][mm]), acc[0][mm]);
#pragma unroll
      for (int mm = 0; mm < 2; mm++) acc[1][mm] = MFMA(wf[i][1], __builtin_bit_cast(bf16x8, af[i][mm]), acc[1][mm]);
      if constexpr (GG == 0 && (i == 9 || i == 10)) { SB(); issueW2(i + 9, 0); issueW2(i + 9, 1); }
      SB();
    SEND
    STAMP(10);
    __builtin_amdgcn_s_setprio(0);
#pragma unroll
    for (int qd = 0; qd < 4; qd++)
#pragma unroll
      for (int k = 0; k < 4; k++) {
        const unsigned ra = ring_area(2 * qd + (k >> 1)) + (k & 1) * 1024;
        ldsw<bf16x8>(lw2 + vL16 + (ra & 0x30000u), ra & 0xffffu, sub4(acc[qd >> 1][qd & 1], k));
      }
    WAITL0(); BAR(); MEMFENCE();
    STAMP(12);
    const unsigned j2 = 2 * wr + GG;
    const unsigned ra = ((0x51407362u >> (4 * j2)) & 7) * 16384;
    asm volatile("" : "+v"(xr[0]), "+v"(xr[1]));
    const rsrc_t rsO = mkrs(pout, 2048 * XROW);
#pragma unroll
    for (int k = 0; k < 2; k++) {
      f32x4 sum = {U2F(xr[k][0] << 16), U2F(xr[k][0] & 0xffff0000u), U2F(xr[k][1] << 16), U2F(xr[k][1] & 0xffff0000u)};
#pragma unroll
      for (int v = 0; v < 8; v++) sum += ldsr<f32x4>(LB + ra + vL16, v * 2048 + k * 1024);
      u32x2 o = {pk2(sum[0], sum[1]), pk2(sum[2], sum[3])};
      __builtin_amdgcn_raw_buffer_store_b64(o, rsO, vO + 16 * k, sO, 0);
    }
    if (b == 32 && w == 0) stflag(E + 4, rsWS, WS_EPOCH);
  }
  STAMPS_OUT();
  };
  if (!attn && g) prog(BoolC<false>{}, IntC<1>{});
  else if (!attn) prog(BoolC<false>{}, IntC<0>{});
  else if (g) prog(BoolC<true>{}, IntC<1>{});
  else prog(BoolC<true>{}, IntC<0>{});
}

extern "C" void kernel_launch(void* const* d_in, const int* in_sizes, int n_in, void* d_out, int out_size, void* d_ws, size_t ws_size,
                              hipStream_t stream) {
  fused_kernel<<<256, 512, 0, stream>>>((const char*)d_in[0], (const char*)d_in[1], (const char*)d_in[5], (const char*)d_in[6], (char*)d_ws,
                                        (const char*)d_in[2], (const char*)d_in[7], (const char*)d_in[3], (char*)d_out);
}
```

```cpp
#define OAUX 17
#include <hip/hip_runtime.h>
#include <hip/hip_bf16.h>
#include <utility>

#define DI __device__ __forceinline__
#define LAS __attribute__((address_space(3)))
typedef __attribute__((ext_vector_type(8))) short bf16x8;
typedef __attribute__((ext_vector_type(4))) short s16x4;
typedef __attribute__((ext_vector_type(2))) __bf16 bf2;
typedef __attribute__((ext_vector_type(2))) float f32x2;
typedef __attribute__((ext_vector_type(4))) float f32x4;
typedef __attribute__((ext_vector_type(16))) float f32x16;
typedef __attribute__((ext_vector_type(2))) unsigned u32x2;
typedef __attribute__((ext_vector_type(4))) unsigned u32x4;
typedef __amdgpu_buffer_rsrc_t rsrc_t;
#define F2U(x) __builtin_bit_cast(unsigned, (float)(x))
#define U2F(x) __builtin_bit_cast(float, (unsigned)(x))
#define ALL(x) (__builtin_amdgcn_ballot_w64(!(x)) == 0ull)
#define ANY(x) (__builtin_amdgcn_ballot_w64(x) != 0ull)
#define UMAX(a, b) __builtin_elementwise_max((unsigned)(a), (unsigned)(b))

constexpr unsigned XROW = 896, QROW = 2688, FROW = 3584;
constexpr unsigned FS = 128;
constexpr unsigned WS_EPOCH = 0;
constexpr unsigned WS_GR = 4096;
constexpr unsigned WS_KVP = 131072;
constexpr unsigned WS_A2 = WS_KVP + 16 * 16 * 8192;
constexpr unsigned WS_ST = WS_A2 + 64 * 144 * 1024;
constexpr unsigned WS_FH = WS_ST + 262144;
constexpr unsigned WS_KVR = WS_FH + 2048 * FS;
constexpr unsigned WS_END = WS_KVR + 16 * 8192;
constexpr unsigned L_GU = 65536, L_KV = 131072;
constexpr unsigned L_TAIL = L_KV + 3 * 8192;
constexpr unsigned L_WPK = L_TAIL;
constexpr unsigned L_ST = L_TAIL + 2048;
constexpr unsigned L_CS = L_TAIL + 4096;
constexpr unsigned L_PHI = L_GU + 32768;
constexpr unsigned L_V = L_KV + 16384;
constexpr unsigned L_FREE = 32768;

#define MFMA(a, b, c) __builtin_amdgcn_mfma_f32_32x32x16_bf16((a), (b), (c), 0, 0, 0)
#define MFMA16(a, b, c) __builtin_amdgcn_mfma_f32_16x16x32_bf16((a), (b), (c), 0, 0, 0)
#define AI __attribute__((always_inline))
#define BAR() __builtin_amdgcn_s_barrier()
#define WAITV(n) asm volatile("s_waitcnt vmcnt(" #n ")" ::: "memory")
#define WAITL0() asm volatile("s_waitcnt lgkmcnt(0)" ::: "memory")
#define SB() __builtin_amdgcn_sched_barrier(0)
#define MEMFENCE() asm volatile("" ::: "memory")

template <int N> DI void waitv() { asm volatile("s_waitcnt vmcnt(%c0)" :: "i"(N) : "memory"); }

#define INSZ(x) (x)
DI rsrc_t mkrs(const void* p, unsigned bytes) { return __builtin_amdgcn_make_buffer_rsrc((void*)p, (short)0, (int)bytes, 0x00020000); }
DI void dma(unsigned lds, unsigned limm, unsigned voff, rsrc_t rs, unsigned soff, unsigned simm) {
  unsigned t;
  asm volatile("s_add_u32 m0, %1, %c5\n\ts_add_u32 %0, %4, %c6\n\tbuffer_load_dwordx4 %2, %3, %0 offen lds"
               : "=&s"(t) : "s"(lds), "v"(voff), "s"(rs), "s"(soff), "i"(limm), "i"(simm) : "memory", "scc");
}
DI s16x4 trd(unsigned a) { return __builtin_amdgcn_ds_read_tr16_b64_v4i16((LAS s16x4*)(size_t)a); }
DI s16x4 trd2(unsigned a, unsigned off) { return __builtin_amdgcn_ds_read_tr16_b64_v4i16((LAS s16x4*)((LAS char*)(size_t)a + off)); }
DI bf16x8 tr2(unsigned a, unsigned off0, unsigned off1) {
  LAS char* p = (LAS char*)(size_t)a;
  s16x4 lo = __builtin_amdgcn_ds_read_tr16_b64_v4i16((LAS s16x4*)(p + off0));
  s16x4 hi = __builtin_amdgcn_ds_read_tr16_b64_v4i16((LAS s16x4*)(p + off1));
  return __builtin_shufflevector(lo, hi, 0, 1, 2, 3, 4, 5, 6, 7);
}
template <class T> DI T ldsr(unsigned a, unsigned off) { return *(LAS T*)((LAS char*)(size_t)a + off); }
template <class T> DI void ldsw(unsigned a, unsigned off, T v) { *(LAS T*)((LAS char*)(size_t)a + off) = v; }
DI unsigned pk2(float a, float b) { f32x2 f = {a, b}; return __builtin_bit_cast(unsigned, __builtin_convertvector(f, bf2)); }
DI float rbf(float a) { return U2F(pk2(a, a) << 16); }
DI float dot2(unsigned a, unsigned b, float c) { return __builtin_amdgcn_fdot2_f32_bf16(__builtin_bit_cast(bf2, a), __builtin_bit_cast(bf2, b), c, false); }
DI float xsum(float v) { auto r = __builtin_amdgcn_permlane32_swap(F2U(v), F2U(v), false, false); float a = U2F(r[0]); asm("" : "+v"(a)); return a + U2F(r[1]); }
DI float xmax(float v) { auto r = __builtin_amdgcn_permlane32_swap(F2U(v), F2U(v), false, false); return __builtin_fmaxf(U2F(r[0]), U2F(r[1])); }
DI float xmin(float v) { auto r = __builtin_amdgcn_permlane32_swap(F2U(v), F2U(v), false, false); return __builtin_fminf(U2F(r[0]), U2F(r[1])); }
DI float xhi(float v) { auto r = __builtin_amdgcn_permlane32_swap(F2U(v), F2U(v), false, false); return U2F(r[1]); }
DI void st16(u32x4 v, rsrc_t rs, unsigned voff, unsigned soff, unsigned simm) {
  unsigned t;
  asm volatile("s_add_u32 %0, %4, %c5\n\tbuffer_store_dwordx4 %1, %2, %3, %0 offen sc1\n\ts_nop 1" : "=&s"(t) : "v"(v), "v"(voff), "s"(rs), "s"(soff), "i"(simm) : "memory", "scc");
}
DI void stflag(unsigned v, rsrc_t rs, unsigned soff) {
  unsigned voff = (__builtin_amdgcn_mbcnt_hi(~0u, __builtin_amdgcn_mbcnt_lo(~0u, 0u)) == 0u) ? 0u : 0x80000000u;
  __builtin_amdgcn_raw_buffer_store_b32(v, rs, voff, soff, 16);
}
#define XORCOND (n == 24)
#define PS 0
#define PV 0
#define KEEP2 0
#define S2N 14
#define B1N 23
#define DIST 2
#define P2G1 1
#define SLEEPR 4
#define SLEEPN 2
DI u32x4 ld16(rsrc_t rs, unsigned voff, unsigned soff, unsigned simm) {
  u32x4 r; unsigned t;
  asm volatile("s_add_u32 %1, %4, %c5\n\tbuffer_load_dwordx4 %0, %2, %3, %1 offen sc1" : "=v"(r), "=&s"(t) : "v"(voff), "s"(rs), "s"(soff), "i"(simm) : "memory", "scc");
  return r;
}
template <unsigned STEP> DI void ld8w(u32x4 (&p)[8], rsrc_t rs, unsigned voff, unsigned soff) {
  unsigned t;
#define L1(k) "buffer_load_dwordx4 %" #k ", %9, %10, %8 offen sc1\n\ts_add_u32 %8, %8, %c12\n\t"
  asm volatile("s_mov_b32 %8, %11\n\t" L1(0) L1(1) L1(2) L1(3) L1(4) L1(5) L1(6) L1(7) "s_waitcnt vmcnt(0)"
               : "=&v"(p[0]), "=&v"(p[1]), "=&v"(p[2]), "=&v"(p[3]), "=&v"(p[4]), "=&v"(p[5]), "=&v"(p[6]), "=&v"(p[7]), "=&s"(t)
               : "v"(voff), "s"(rs), "s"(soff), "i"(STEP) : "memory", "scc");
#undef L1
}
DI bool untag(u32x4 (&p)[8], unsigned tag, unsigned mask) {
  unsigned chk = 0;
#pragma unroll
  for (int i = 0; i < 8; i++)
#pragma unroll
    for (int k = 0; k < 4; k++) { p[i][k] ^= tag; chk |= p[i][k]; }
  return ALL((chk & mask) == 0u);
}
template <unsigned STEP> DI void waittag(u32x4 (&p)[8], rsrc_t rs, unsigned voff, unsigned soff, unsigned tag, unsigned mask) {
  asm volatile("s_waitcnt vmcnt(0)" : "+v"(p[0]), "+v"(p[1]), "+v"(p[2]), "+v"(p[3]), "+v"(p[4]), "+v"(p[5]), "+v"(p[6]), "+v"(p[7]) :: "memory");
#pragma nounroll
  for (int it = 0; !untag(p, tag, mask) && it < (1 << 16); ++it) { __builtin_amdgcn_s_sleep(SLEEPN); ld8w<STEP>(p, rs, voff, soff); }
}
template <bool WAIT> DI void rel1(u32x4& a, u32x4& b, rsrc_t rs, unsigned voff, unsigned so) {
  unsigned t;
  if constexpr (WAIT) asm volatile("buffer_load_dwordx4 %0, %3, %4, %5 offen sc1\n\ts_add_u32 %2, %5, 147456\n\tbuffer_load_dwordx4 %1, %3, %4, %2 offen sc1\n\ts_waitcnt vmcnt(0)"
                                   : "+v"(a), "+v"(b), "=&s"(t) : "v"(voff), "s"(rs), "s"(so) : "memory", "scc");
  else asm volatile("buffer_load_dwordx4 %0, %3, %4, %5 offen sc1\n\ts_add_u32 %2, %5, 147456\n\tbuffer_load_dwordx4 %1, %3, %4, %2 offen sc1"
                    : "+v"(a), "+v"(b), "=&s"(t) : "v"(voff), "s"(rs), "s"(so) : "memory", "scc");
}
DI void poll(rsrc_t rs, unsigned voff, unsigned soff, unsigned E, unsigned need) {
  voff = need ? voff : 0x80000000u;
#pragma nounroll
  for (int it = 0; it < (1 << 16); ++it) {
    unsigned v;
    asm volatile("buffer_load_dword %0, %1, %2, %3 offen sc1\n\ts_waitcnt vmcnt(0)" : "=v"(v) : "v"(voff), "s"(rs), "s"(soff) : "memory");
    bool ok = (need == 0u) || ((v - E - need) <= (3u - need));
    if (ALL(ok)) break;
    __builtin_amdgcn_s_sleep(SLEEPN);
  }
}

template <bool B> struct BoolC { static constexpr bool value = B; };
template <int I> struct IntC { static constexpr int value = I; };
template <int... I, class F> DI void sfor_impl(std::integer_sequence<int, I...>, F&& f) { (f(IntC<I>{}), ...); }
template <int N, class F> DI void sfor(F&& f) { sfor_impl(std::make_integer_sequence<int, N>{}, f); }
#define SFOR(n, N) sfor<N>([&](auto n##_) AI { constexpr int n = decltype(n##_)::value;
#define SEND });
struct RowC { float r, K1, K2, Ri, Lr; };

DI unsigned hilo(float v) { float hi = rbf(v); return pk2(hi, v - hi); }
DI float max3(float a, float b, float c) { return __builtin_fmaxf(__builtin_fmaxf(a, b), c); }
DI float min3(float a, float b, float c) { return __builtin_fminf(__builtin_fminf(a, b), c); }
DI unsigned pk2s(float a, float b) { unsigned r; asm("v_cvt_pk_bf16_f32 %0, %1, %2" : "=v"(r) : "v"(a), "v"(b)); return r; }
DI f32x2 sub2(const f32x16& v, int k) { f32x2 t = {v[2 * k], v[2 * k + 1]}; return t; }
DI f32x2 exp2v(f32x2 v) { f32x2 r = {__builtin_amdgcn_exp2f(v[0]), __builtin_amdgcn_exp2f(v[1])}; return r; }
DI f32x2 dup2(float a) { f32x2 r = {a, a}; asm("" : "+v"(r)); return r; }
DI f32x2 fma2(f32x2 a, f32x2 b, f32x2 c) { return __builtin_elementwise_fma(a, b, c); }
DI u32x4 epiP(const f32x16& a, const RowC& c) {
  u32x4 r;
  const f32x2 K1 = dup2(c.K1), K2 = dup2(c.K2), Ri = dup2(c.Ri);
#pragma unroll
  for (int i = 0; i < 4; i++) {
    const f32x2 g = sub2(a, i), d = fma2(exp2v(g * fma2(g * g, K2, K1)), Ri, Ri);
    const f32x2 s = {__builtin_amdgcn_rcpf(d[0]), __builtin_amdgcn_rcpf(d[1])}, h = (g * sub2(a, 4 + i)) * s;
    r[i] = pk2(h[0], h[1]);
  }
  return r;
}
struct EpiS { float x0, x1, m0, m1; u32x4 r; };
template <int P, int PH> DI void epiSlice(const f32x16& a, const RowC& c, EpiS& s) {
  const float g0 = a[2 * P], g1 = a[2 * P + 1];
  if constexpr (PH == 0) { s.x0 = g0 * __builtin_fmaf(g0 * g0, c.K2, c.K1); s.x1 = g1 * __builtin_fmaf(g1 * g1, c.K2, c.K1); }
  if constexpr (PH == 1) { s.x0 = __builtin_amdgcn_exp2f(s.x0); s.x1 = __builtin_amdgcn_exp2f(s.x1); s.m0 = g0 * a[8 + 2 * P]; s.m1 = g1 * a[9 + 2 * P]; }
  if constexpr (PH == 2) { s.x0 = __builtin_amdgcn_rcpf(__builtin_fmaf(s.x0, c.Ri, c.Ri)); s.x1 = __builtin_amdgcn_rcpf(__builtin_fmaf(s.x1, c.Ri, c.Ri)); }
  if constexpr (PH == 3) s.r[P] = pk2s(s.m0 * s.x0, s.m1 * s.x1);
}
DI f32x16 featproj2(const f32x16& a, bf16x8 w0, bf16x8 w1, bool hh) {
  u32x4 q0, q1;
#pragma unroll
  for (int i = 0; i < 4; i++) { q0[i] = pk2(a[2 * i], a[2 * i + 1]); q1[i] = pk2(a[8 + 2 * i], a[9 + 2 * i]); }
  q1[2] = hh ? 0u : q1[2]; q1[3] = hh ? 0u : q1[3];
  f32x16 z;
#pragma unroll
  for (int i = 0; i < 16; i++) z[i] = 0.f;
  z = MFMA(w0, __builtin_bit_cast(bf16x8, q0), z);
  z = MFMA(w1, __builtin_bit_cast(bf16x8, q1), z);
  return z;
}
DI f32x16 featproj(const f32x16& a, unsigned wpaddr, unsigned off, bool hh) {
  return featproj2(a, tr2(wpaddr, off, off + 8 * 56), tr2(wpaddr, off + 16 * 56, off + 24 * 56), hh);
}
DI float max16(const f32x16& p) { float m = max3(p[0], p[1], p[2]); _Pragma("unroll") for (int i = 3; i < 15; i += 2) m = max3(m, p[i], p[i + 1]); return xmax(__builtin_fmaxf(m, p[15])); }
DI float min16(const f32x16& p) { float m = min3(p[0], p[1], p[2]); _Pragma("unroll") for (int i = 3; i < 15; i += 2) m = min3(m, p[i], p[i + 1]); return xmin(__builtin_fminf(m, p[15])); }
DI f32x16 dualexpP(const f32x16& p, float Lr) {
  const f32x2 bx = dup2(-max16(p) * Lr), bn = dup2(min16(p) * Lr), L2 = dup2(Lr);
  f32x16 o;
#pragma unroll
  for (int i = 0; i < 8; i++) {
    const f32x2 ep = exp2v(fma2(sub2(p, i), L2, bx)), en = exp2v(fma2(sub2(p, i), -L2, bn));
    o[i] = U2F(pk2(ep[0], ep[1])); o[8 + i] = U2F(pk2(en[0], en[1]));
  }
  return o;
}
struct SmS { f32x16 p, e; float en[16], bx, bn, sp, sn, tp, tn; bf16x8 w0, w1; };
DI bf16x8 sub4(const f32x16& v, int k) { f32x4 t = {v[4 * k], v[4 * k + 1], v[4 * k + 2], v[4 * k + 3]}; return __builtin_bit_cast(bf16x8, t); }

DI constexpr unsigned lc1_base(int ch) { return ch == 6 ? L_KV : ((ch & 1) ? L_GU : 0u) + (ch < 2 ? 49152u : ch < 4 ? 0u : 16384u); }
template <int CTRL, int RM> DI float dppadd(float v) { return v + U2F(__builtin_amdgcn_update_dpp(0u, F2U(v), CTRL, RM, 0xf, false)); }
DI float wavesum63(float v) {
  v = dppadd<0x111, 0xf>(v); v = dppadd<0x112, 0xf>(v); v = dppadd<0x114, 0xf>(v); v = dppadd<0x118, 0xf>(v);
  v = dppadd<0x142, 0xa>(v); return dppadd<0x143, 0xc>(v);
}

#define STAMP(i)
#define STAMPS_OUT()
#define CSTAMP(i)

#define PDV 5
constexpr int PD = PDV, RING = 9;
constexpr int later2(int i, int NS, int GG) {
  const int younger = 2 * ((i + PD <= NS ? i + PD : NS) - 1 - i);
  int extra = i < PD ? 1 : 0;
  if (GG == 0 && (i == 10 || i == 11)) extra += 2 * (i - 9);
  return younger + extra;
}
DI constexpr unsigned ring_area(int j) { return j == 0 ? L_FREE : j == 1 ? L_PHI : lc1_base(j - 2); }
constexpr int cumG1(int s) { return s < 4 ? 0 : s < 22 ? (s - 4) / 2 : (9 + (s - 22) * 5 / 4 < 48 ? 9 + (s - 22) * 5 / 4 : 48); }

#define CODE_BYTES 65536
__global__ __launch_bounds__(512, 2) void fused_kernel(const char* px_, const char* pqkv_, const char* pwg_, const char* pwu_, char* ws_, const char* pphi_,
                                                       const char* pwd_, const char* pwo_, char* pout_) {
  typedef __attribute__((ext_vector_type(8))) unsigned long long u64x8;
  u64x8 ka; unsigned long long kb;
  asm volatile("s_load_dwordx16 %0, %2, 0\n\ts_load_dwordx2 %1, %2, 64" : "=&s"(ka), "=&s"(kb) : "s"(__builtin_amdgcn_kernarg_segment_ptr()));
  __shared__ __attribute__((aligned(16))) char smem[163840];
  const unsigned LB = (unsigned)(size_t)smem;
  const unsigned tid = __builtin_amdgcn_workitem_id_x(), lane = tid & 63;
  const unsigned w = __builtin_amdgcn_readfirstlane(tid >> 6), g = w >> 2, wr = w & 3;
  const unsigned b = __builtin_amdgcn_workgroup_id_x(), xcd = b & 7, bi = b >> 3, grp = xcd >> 1, half = xcd & 1, rbl = bi & 3, cp = bi >> 2, ci = cp - 1;
  const unsigned rb = 4 * grp + rbl, c = 7 * half + ci;
  const unsigned head = 8 * half + (((half ? 0x76425310u : 0x75316420u) >> (4 * cp)) & 7);
  const bool attn = cp == 0;
  STAMP(0); CSTAMP(14);
  const unsigned wstr = attn ? QROW : FROW;
  const unsigned sG0 = half * (attn ? 448 : 1792) + (attn ? 0 : ci * 256), sX0 = rb * 128 * XROW;
  unsigned vx0, vx1, vG, vU, vKV;
  auto voffs = [&](unsigned ln) AI {
    const unsigned r8 = ln >> 3, c8 = ln & 7, sa = (r8 >> 1) & 1, lc = c8 ^ (4 * sa);
    vx0 = (16 * w + r8) * XROW + ((c8 ^ (r8 >> 1)) << 4); vx1 = (vx0 ^ 64u) + 8 * XROW;
    const unsigned cg = attn ? lc : (c8 ^ (2 * sa)), cu = attn ? (lc >= 6 ? lc - 2 : lc) : (cg ^ 4);
    vG = (8 * w + r8) * wstr + (cg << 4); vU = (8 * w + r8) * wstr + (cu << 4);
    vKV = (8 * w + r8) * QROW + 896 * (1 + (lc >> 2)) + ((lc & 3) << 4) + ((56 * head) & ~15u);
  };
  voffs(lane);
  const unsigned lw1 = LB + w * 1024, lw2 = LB + w * 2048;
#define ROT (((0x24924u >> (2 * (cp + rbl))) & 3) * 2)
  const unsigned rot = ROT;
  const unsigned srow = 128 * rb + 64 * half + 8 * cp + w;
  unsigned vrow = lane < 56 ? lane * 16 : 0x80000000u;
  asm volatile("s_waitcnt lgkmcnt(0)" : "+s"(ka), "+s"(kb), "+v"(vx0), "+v"(vx1), "+v"(vG), "+v"(vU), "+v"(vKV), "+v"(vrow));
  const char *px = (const char*)ka[0], *pqkv = (const char*)ka[1], *pwg = (const char*)ka[2], *pwu = (const char*)ka[3], *pphi = (const char*)ka[5],
             *pwd = (const char*)ka[6], *pwo = (const char*)ka[7];
  char *ws = (char*)ka[4], *pout = (char*)kb;
  const rsrc_t rsX = mkrs(px, INSZ(2048 * XROW)), rsQ = mkrs(pqkv, INSZ(448 * QROW));
  const rsrc_t rsG = mkrs(attn ? pqkv : pwg, INSZ(448 * wstr)), rsU = mkrs(attn ? pqkv + 128 : pwu, INSZ(448 * wstr - (attn ? 128 : 0)));
  auto cidx = [&](int st) AI -> unsigned { unsigned t = rot + st; return t >= 7 ? t - 7 : t; };
  auto issueA = [&](int st, int k) AI {
    const unsigned sl = st & 3, ch = cidx(st);
    if (k == 0) dma(lw2, sl * 16384, vx0, rsX, sX0 + ch * 128, 0);
    if (k == 1) dma(lw2, sl * 16384 + 1024, vx1, rsX, sX0 + ch * 128, 0);
    if (k == 2) dma(lw1, L_GU + sl * 16384, vG, rsG, sG0 + ch * 64 * wstr, 0);
    if (k == 3) dma(lw1, L_GU + sl * 16384 + 8192, vU, rsU, sG0 + ch * 64 * wstr, 0);
    if (k == 4) dma(lw1, L_KV + sl * 8192, vKV, rsQ, ch * 64 * QROW, 0);
  };
  const rsrc_t rsWS = mkrs(ws, WS_END);
  unsigned Ev, cpf; u32x4 xrw;
  asm volatile("buffer_load_dwordx4 %0, %1, %2, %3 offen" : "=v"(xrw) : "v"(vrow), "s"(rsX), "s"(srow * XROW) : "memory");
  asm volatile("buffer_load_dword %0, off, %1, 0 sc1" : "=v"(Ev) : "s"(rsWS) : "memory");
  {
    unsigned long long pc; asm volatile("s_getpc_b64 %0" : "=s"(pc));
    const unsigned vo = w == 0 && lane < 24 ? (bi * 24 + lane) * 128 : 0x80000000u;
    asm volatile("buffer_load_dword %0, %1, %2, 0 offen" : "=v"(cpf) : "v"(vo), "s"(mkrs((const void*)pc, CODE_BYTES)) : "memory");
  }
#pragma unroll
  for (int k = 0; k < 5; k++) issueA(0, k);
  asm volatile("s_waitcnt vmcnt(5)" : "+v"(xrw), "+v"(Ev), "+v"(cpf) :: "memory");
#pragma unroll
  for (int k = 0; k < 5; k++) issueA(1, k);
  issueA(2, 0);
  const unsigned E = __builtin_amdgcn_readfirstlane(Ev);
  {
    float s1 = 0.f, s2 = 0.f;
#pragma unroll
    for (int i = 0; i < 4; i++) { s1 = dot2(xrw[i], 0x3f803f80u, s1); s2 = dot2(xrw[i], xrw[i], s2); }
    s1 = wavesum63(s1); s2 = wavesum63(s2);
    const float mean = s1 * (1.f / 448.f), var = __builtin_fmaxf(s2 * (1.f / 448.f) - mean * mean, 0.f);
    u32x2 gr = {pk2(-mean, __builtin_amdgcn_sqrtf(rbf(rbf(var) + 1e-5f))), E + 1};
    __builtin_amdgcn_raw_buffer_store_b64(gr, rsWS, lane == 63 ? 0u : 0x80000000u, WS_GR + srow * 8, 16);
  }

  auto prog = [&](auto at_, auto gg_) AI {
  constexpr bool AT = decltype(at_)::value; constexpr int GG = decltype(gg_)::value;
  constexpr unsigned lcstep = AT ? 224 : 128;
  unsigned lane;
  asm volatile("v_mbcnt_lo_u32_b32 %0, -1, 0\n\tv_mbcnt_hi_u32_b32 %0, -1, %0" : "=v"(lane));
  __builtin_assume(lane < 64);
  voffs(lane);
  auto issueL = [&](int hc) AI {
    const int st = hc >> 1; const unsigned ch = cidx(st);
    if (hc & 1) dma(lw1, lc1_base(st) + 8192, vU, rsU, sG0 + ch * 64 * wstr, lcstep);
    else dma(lw1, lc1_base(st), vG, rsG, sG0 + ch * 64 * wstr, lcstep);
  };
  auto issueG = [&](int bar, int k) AI {
    if (bar == 0) issueA(2 + k / 5, k % 5);
    else if (bar <= 3) issueA(bar + 3, k);
    else if (k < 4) issueL(4 * (bar - 4) + k);
    else if (bar == 4) dma(LB + (w & 1) * 1024, L_WPK, lane * 16, mkrs(pphi, 16 * 1568), head * 1568 + (w & 1) * 1024, 0);
    else issueL(12 + (bar - 5));
  };

  const unsigned rt = 4 * rb + wr;
  const unsigned sA2 = WS_A2 + rt * 144 * 1024 + (AT ? half * 16384 : c * 8192), vL16 = lane * 16;
  const u32x4 sent = {~0u, ~0u, ~0u, ~0u};
  auto clr = [&](int ks) AI { st16(sent, rsWS, vL16, sA2, ks * 1024); };

  auto clri = [&](int q) AI { clr(GG ? (q < 6 ? 114 + q : 118 + q) : (q < 2 ? 112 + q : 118 + q)); };

  const bool hh = lane >= 32;
  const unsigned hu = lane >> 5, blk = (lane >> 4) & 1, q4 = (lane >> 2) & 3, p4 = lane & 3, ta = (q4 >> 1) & 1, l31 = lane & 31;
  const unsigned perm = wr ^ (2 * GG);
  const unsigned rowoff = (8 * hu + q4) * 128 + (perm << 11);
  const unsigned pc = (4 * blk + p4) < 6 ? (4 * blk + p4) : 6;
  auto tileaddr = [&](unsigned t) AI -> unsigned {
    if constexpr (AT) { unsigned o = 56 * t + 8 * pc; return (o >> 7) * 8192 + ((((o >> 4) & 7) ^ (4 * ta)) << 4) + (o & 8) + rowoff; }
    else return blk * 8192 + (((2 * t + (p4 >> 1)) ^ (4 * blk + 2 * ta)) << 4) + 8 * (p4 & 1) + rowoff;
  };
  unsigned aA[3], aB[2];
  {
    unsigned o = (head & 1) * 8 + 8 * pc;
    unsigned ak = L_KV + (((o >> 4) ^ (4 * ta)) << 4) + (o & 8) + rowoff;
    aA[0] = LB + (GG ? L_GU + tileaddr(1) : ak);
    aA[1] = LB + (GG ? L_GU + tileaddr(2) : (ak ^ 64u));
    aA[2] = LB + L_GU + tileaddr(GG ? 3 : 0);
    aB[0] = LB + tileaddr(2 * GG); aB[1] = LB + tileaddr(2 * GG + 1);
  }
  const unsigned xrow = 32 * wr + l31;
  const unsigned aX = LB + xrow * 128 + (((2 * perm + hu) ^ ((xrow >> 1) & 7)) << 4);
  const unsigned selv = (((lane >> 4) ^ (lane >> 2)) & 1) ? 0u : 0x3f803f80u;
  const u32x4 sel4 = {selv, selv, selv, selv};
  const bf16x8 sel = __builtin_bit_cast(bf16x8, sel4);

  bf16x8 xf[28], wa[2][3], wb[8][2];
  f32x16 accA[3], accB[2];
  f32x4 csA[3], csB[2];
#pragma unroll
  for (int j = 0; j < 3; j++) { for (int i = 0; i < 16; i++) accA[j][i] = 0.f; for (int i = 0; i < 4; i++) csA[j][i] = 0.f; }
#pragma unroll
  for (int j = 0; j < 2; j++) { for (int i = 0; i < 16; i++) accB[j][i] = 0.f; for (int i = 0; i < 4; i++) csB[j][i] = 0.f; }
  u32x2 grn;

  const rsrc_t rsWD = mkrs(pwd, INSZ(1792 * XROW)), rsWO = mkrs(pwo, INSZ(448 * XROW));
  const unsigned vW2 = (lane >> 3) * XROW + (((lane & 7) ^ ((lane >> 2) & 4)) << 4) + ci * 128;
  constexpr int NS = GG ? 16 : 20;
#define RHO ((rbl + cp) & 3)
  const unsigned rho = RHO;
  auto t_of = [](int i) constexpr { return (0x54763210u >> (4 * (i >> 1))) & 7; };
  auto ksb = [&](int i) AI -> unsigned {
    if (GG) return 16 * wr + ((t_of(i) & 3) ^ rho);
    if (i < 12) { const unsigned cc = i % 6 + rho + (rho >> 1); return wr + 8 * (cc >= 6 ? cc - 6 : cc); }
    return 8 * wr + 2 * (((i - 12) >> 1) ^ rho);
  };
  auto ksi = [&](int i) AI -> unsigned { return GG ? 8 * (i & 1) + (t_of(i) & 4) : i < 12 ? 64 + 4 * (i / 6) : 112 + (i & 1); };
  auto issueW2 = [&](int i, int hf) AI {
    if (GG == 0 && i >= 12) dma(lw2, ring_area(i % RING) + hf * 1024, vW2, rsWO, ksb(i) * 14 * XROW, (16 * (i & 1) + 8 * hf) * XROW);
    else dma(lw2, ring_area(i % RING) + hf * 1024, vW2, rsWD, ksb(i) * 16 * XROW, (ksi(i) * 16 + 8 * hf) * XROW);
  };

  const unsigned aCSw = LB + L_CS + l31 * 16 + wr * 4 + GG * 2048, aCSr = LB + L_CS + l31 * 16 + GG * 2048;
  auto readX = [&](int n) AI { xf[n] = ldsr<bf16x8>(aX ^ (unsigned)((n & 3) << 5), ((n >> 2) & 3) * 16384); };
  auto readA = [&](int n, int j) AI {
    const unsigned o = ((n >> 2) & 3) * ((GG == 0 && j < 2) ? 8192 : 16384);
    wa[n & 1][j] = tr2(aA[j] ^ (unsigned)((n & 3) << 11), o, o + 512);
  };
  auto readB = [&](int n, int j) AI {
    const unsigned base = lc1_base(n >> 2);
    if (XORCOND) asm volatile("" : "+v"(aB[j]));
    wb[n & 7][j] = tr2(aB[j] ^ (((n & 3) << 11) | (base & 0x30000u)), base & 0xffffu, (base & 0xffffu) + 512);
  };
  RowC rc; bf16x8 xe; unsigned denb;
  auto rowconst = [&]() AI {
#pragma nounroll
    for (int it = 0; it < (1 << 16) && !ALL(grn[1] == E + 1); ++it) {
      __builtin_amdgcn_s_sleep(SLEEPN);
      asm volatile("buffer_load_dwordx2 %0, %1, %2, %3 offen sc1\n\ts_waitcnt vmcnt(0)" : "=v"(grn) : "v"(xrow * 8), "s"(rsWS), "s"(WS_GR + rb * 1024) : "memory");
    }
    const float den = U2F(grn[0] & 0xffff0000u);
    const unsigned nm = hh ? 0u : __builtin_amdgcn_perm(grn[0], grn[0], 0x01000100u);
    u32x4 t = {nm, nm, nm, nm}; xe = __builtin_bit_cast(bf16x8, t);
    denb = grn[0] >> 16;
    rc.r = __builtin_amdgcn_rcpf(den);
    const float k1 = -2.f * 1.4426950408889634f * 0.7978845608028654f;
    rc.K1 = k1 * rc.r; rc.K2 = (k1 * 0.044715f) * rc.r * (rc.r * rc.r); rc.Ri = den * den; rc.Lr = 1.4426950408889634f * rc.r;
  };
  WAITV(7);
  BAR(); MEMFENCE();
  STAMP(1);
  readX(0); readA(0, 0); readA(0, 1); readA(0, 2);
  SFOR(n, 28)
    SB();
    if constexpr (n == 27) {
#pragma unroll
      for (int j = 0; j < 3; j++) ldsw<unsigned>(aCSw, j * 512, hilo(csA[j][0]));
    }
    if constexpr ((n & 3) == 3) {
      if constexpr (n == 27) WAITV(0);
      else if constexpr (n == 23) asm volatile("s_waitcnt vmcnt(10)" : "+v"(grn) :: "memory");
      else if constexpr (n == 3 || n == 15 || n == 19) WAITV(11); else WAITV(10);
      { WAITL0(); BAR(); MEMFENCE(); }
      if constexpr (n == 27) STAMP(2);
    }
    constexpr int bar = (n + 1) >> 2, ph = (n + 1) & 3;
#pragma unroll
    for (int j = 0; j < 3; j++) {
      if constexpr (n < 27) { if (j == 0) readX(n + 1); readA(n + 1, j); }
      else { wa[0][j] = ldsr<bf16x8>(aCSr, j * 512); if (GG == 1 && j < 2) { readB(j, 0); readB(j, 1); } }
      SB();
      accA[j] = MFMA(wa[n & 1][j], xf[n], accA[j]);
      const int slot = 3 * ph + j;
      if (bar == 0) issueG(0, slot - 2);
      else if (bar == 6) { if (slot < 5) issueG(bar, slot); }
      else if (bar <= 6) { if (slot == 0) issueG(bar, 0); if (slot == 2) issueG(bar, 1); if (slot == 5) issueG(bar, 2); if (slot == 7) issueG(bar, 3); if (slot == 10) issueG(bar, 4); }
      else if (j < 2) {
        if constexpr (AT) dma(lw2, L_FREE + j * 1024, lane * 16, mkrs(pphi, 16 * 1568), half * 8 * 1568 + w * 2048, j * 1024);
        else issueW2(0, j);
      }
      if ((n & 3) == 0) csA[j] = MFMA16(sel, wa[n & 1][j], csA[j]);
      if (n == 25 && j == 0) rowconst();
      if (n == 14 && j == 2) asm volatile("buffer_load_dwordx2 %0, %1, %2, %3 offen sc1" : "=v"(grn) : "v"(xrow * 8), "s"(rsWS), "s"(WS_GR + rb * 1024) : "memory");
      SB();
    }
  SEND
#pragma unroll
  for (int j = 0; j < 3; j++) accA[j] = MFMA(wa[0][j], xe, accA[j]);
  asm volatile("" : "+v"(accA[0]), "+v"(accA[1]), "+v"(accA[2]));
  SB();

  const unsigned sFown = WS_FH + (rt * 32 + (AT ? 28 + 2 * half : 2 * c) + GG) * FS;
  const unsigned aWP = LB + (4 * hu + q4) * 56 + 8 * pc;
  const unsigned T12 = (E + 1) & 0xfffu, T16 = (E + 1) & 0xffffu;
  const unsigned rt2 = 2 * (8 * grp + 4 * half + rbl);
  const unsigned sF = WS_FH + rt2 * 32 * FS;
  const unsigned fc = l31 >> 1;
  const bool nd1 = GG ? (fc >> 1) == wr : (fc >= 8 && fc < 14) || fc == 14 + (wr >> 1);
  const unsigned aIW = LB + L_PHI + xrow * 64 + ((hu ^ ((xrow >> 1) & 7)) << 3);
  SmS sm;
  unsigned aWK = aWP + L_WPK, aWQ = aWP + L_FREE;
  asm volatile("" : "+v"(aWK)); if constexpr (AT) asm volatile("" : "+v"(aWQ));
  auto kstep = [&](auto s_) AI {
    constexpr int s = decltype(s_)::value;
    if constexpr (s == 0) { sm.w0 = tr2(aWK, 0, 8 * 56); sm.w1 = tr2(aWK, 16 * 56, 24 * 56); }
    if constexpr (s == 1) sm.p = featproj2(accA[0], sm.w0, sm.w1, hh);
    if constexpr (s == 2 || s == 3) {
#pragma unroll
      for (int k = 2 * s - 4; k < 2 * s - 2; k++) {
        u32x2 vv = {pk2(accA[1][4 * k], accA[1][4 * k + 1]), pk2(accA[1][4 * k + 2], accA[1][4 * k + 3])};
        if (k == 3) vv[0] = hh ? denb : vv[0];
        ldsw<u32x2>(aIW ^ (unsigned)(k << 4), L_V - L_PHI, vv);
      }
    }
    if constexpr (s == 4) sm.bx = -max16(sm.p) * rc.Lr;
    if constexpr (s == 5) sm.bn = min16(sm.p) * rc.Lr;
    if constexpr (s >= 6 && s < 22) {
      constexpr int i = s - 6;
      sm.e[i] = __builtin_amdgcn_exp2f(__builtin_fmaf(sm.p[i], rc.Lr, sm.bx)); sm.en[i] = __builtin_amdgcn_exp2f(__builtin_fmaf(sm.p[i], -rc.Lr, sm.bn));
      if constexpr (i == 0) { sm.sp = sm.e[0]; sm.sn = sm.en[0]; } else if constexpr (i < 12) { sm.sp += sm.e[i]; sm.sn += sm.en[i]; }
      else if constexpr (i == 12) { sm.tp = sm.e[12]; sm.tn = sm.en[12]; } else { sm.tp += sm.e[i]; sm.tn += sm.en[i]; }
    }
    if constexpr (s == 22) { sm.sp = __builtin_amdgcn_rcpf(xsum(sm.sp + (hh ? 0.f : sm.tp))) * rc.r; sm.sn = __builtin_amdgcn_rcpf(xsum(sm.sn + (hh ? 0.f : sm.tn))) * rc.r; }
    if constexpr (s >= 23) {
#pragma unroll
      for (int u = (s == 29 ? 6 : s - 23); u <= (s == 29 ? 7 : s - 23); u++) {
        const int k = u >> 1;
        if (u & 1) { u32x2 t = {pk2s(sm.en[4 * k] * sm.sn, sm.en[4 * k + 1] * sm.sn), pk2s(sm.en[4 * k + 2] * sm.sn, sm.en[4 * k + 3] * sm.sn)}; ldsw<u32x2>(aIW ^ (unsigned)(k << 4), 8192, t); }
        else { u32x2 t = {pk2s(sm.e[4 * k] * sm.sp, sm.e[4 * k + 1] * sm.sp), pk2s(sm.e[4 * k + 2] * sm.sp, sm.e[4 * k + 3] * sm.sp)}; ldsw<u32x2>(aIW ^ (unsigned)(k << 4), 0, t); }
      }
    }
  };
  auto qstep = [&](auto s_, f32x16& a, unsigned lh) AI {
    constexpr int s = decltype(s_)::value;
    if constexpr (s == 0) { sm.w0 = tr2(aWQ, lh * 1568, lh * 1568 + 8 * 56); sm.w1 = tr2(aWQ, lh * 1568 + 16 * 56, lh * 1568 + 24 * 56); }
    if constexpr (s == 1) sm.p = featproj2(a, sm.w0, sm.w1, hh);
    if constexpr (s == 3) sm.bx = -max16(sm.p) * rc.Lr;
    if constexpr (s == 4) sm.bn = min16(sm.p) * rc.Lr;
    if constexpr (s >= 5 && s < 21) {
      constexpr int i = (s - 5) >> 1;
      float t0, t1;
      if constexpr ((s - 5) & 1) { t0 = __builtin_fmaf(sm.p[2 * i], -rc.Lr, sm.bn); t1 = __builtin_fmaf(sm.p[2 * i + 1], -rc.Lr, sm.bn); }
      else { t0 = __builtin_fmaf(sm.p[2 * i], rc.Lr, sm.bx); t1 = __builtin_fmaf(sm.p[2 * i + 1], rc.Lr, sm.bx); }
      a[8 * ((s - 5) & 1) + i] = U2F(pk2(__builtin_amdgcn_exp2f(t0), __builtin_amdgcn_exp2f(t1)));
      if constexpr (s == 20) asm volatile("" : "+v"(a));
    }
  };
  bf16x8 ka_[4], kb_[2][4]; f32x4 kd[2];
  auto kvstep = [&](auto s_) AI {
    constexpr int s = decltype(s_)::value;
    if constexpr (s == 0) {
      const unsigned g4 = lane >> 4, r0 = 8 * g4 + q4, sw = 4 * blk + (q4 >> 1);
      unsigned b0 = LB + L_PHI + r0 * 64 + ((p4 ^ sw) << 3);
      unsigned a0 = (b0 ^ ((wr >> 1) << 5)) + (wr & 1) * 8192;
#pragma unroll
      for (int k = 0; k < 4; k++) {
        ka_[k] = __builtin_shufflevector(trd2(a0, k * 2048), trd2(a0 ^ 16u, k * 2048 + 256), 0, 1, 2, 3, 4, 5, 6, 7);
        kb_[0][k] = __builtin_shufflevector(trd2(b0, L_V - L_PHI + k * 2048), trd2(b0 ^ 16u, L_V - L_PHI + k * 2048 + 256), 0, 1, 2, 3, 4, 5, 6, 7);
        kb_[1][k] = __builtin_shufflevector(trd2(b0 ^ 32u, L_V - L_PHI + k * 2048), trd2(b0 ^ 48u, L_V - L_PHI + k * 2048 + 256), 0, 1, 2, 3, 4, 5, 6, 7);
      }
    }
    if constexpr (s == 1) {
      f32x4 z = {0.f, 0.f, 0.f, 0.f}; kd[0] = z; kd[1] = z;
#pragma unroll
      for (int k = 0; k < 4; k++) { kd[0] = MFMA16(ka_[k], kb_[0][k], kd[0]); kd[1] = MFMA16(ka_[k], kb_[1][k], kd[1]); }
    }
    if constexpr (s == 3) {
#pragma unroll
      for (int d = 0; d < 2; d++) {
        u32x4 t;
#pragma unroll
        for (int i = 0; i < 4; i++) t[i] = (F2U(kd[d][i]) & ~0xfffu) | T12;
        st16(t, rsWS, (lane >> 4) * 512 + (lane & 15) * 16, ((head * 16 + rb) * 2 + (wr & 1)) * 4096 + (wr >> 1) * 2048, WS_KVP + d * 256);
      }
    }
  };
  auto qexp2 = [&](f32x16& a0, unsigned l0, f32x16& a1, unsigned l1) AI {
    const f32x16 p0 = featproj(a0, aWQ, l0 * 1568, hh), p1 = featproj(a1, aWQ, l1 * 1568, hh);
    a0 = dualexpP(p0, rc.Lr); a1 = dualexpP(p1, rc.Lr); asm volatile("" : "+v"(a0), "+v"(a1));
  };

  unsigned fl1 = 0;
  EpiS es;
  u32x4 pv[8];
  const unsigned vKP = l31 * 16 + hu * 8192, sKP = (rb * 32 + half) * 4096 + wr * 1024 + GG * 512;
  bf16x8 cb[2];
  auto ldflag = [&]() AI { asm volatile("buffer_load_dword %0, %1, %2, %3 offen sc1" : "=v"(fl1) : "v"(nd1 ? lane * FS : 0x80000000u), "s"(rsWS), "s"(sF) : "memory"); };
  auto touch = [&](unsigned wo4) AI {
    const unsigned rw = (rbl * 8 + w + wo4) * 44 + lane;
    const bool dn = rw < 896;
    const unsigned vo = lane < 44 ? (dn ? ((rw >> 6) * 128 + 64 + (rw & 63)) : rw - 896) * XROW + ci * 128 : 0x80000000u;
    asm volatile("s_add_u32 m0, %0, %c5\n\ts_nop 0\n\tbuffer_load_dword %1, %3, 0 offen lds\n\tbuffer_load_dword %2, %4, 0 offen lds"
                 :: "s"(LB + w * 256), "v"(dn ? vo : 0x80000000u), "v"(dn ? 0x80000000u : vo), "s"(rsWD), "s"(rsWO), "i"(L_ST) : "memory", "scc");
  };
  auto stream = [&]() AI {
    __builtin_amdgcn_s_setprio(PS);
    SFOR(n, 28)
      if constexpr (n == 27) { ldsw<unsigned>(aCSw, 3 * 512, hilo(csB[0][0])); ldsw<unsigned>(aCSw, 0, hilo(csB[1][0])); WAITL0(); BAR(); MEMFENCE(); }
      else if constexpr (GG == 1) { if constexpr (n == S2N || (!AT && n == B1N)) { if constexpr (AT) WAITV(10); BAR(); MEMFENCE(); } }
      else if constexpr (AT ? n == 7 : (n & 3) == 3) { asm volatile("s_waitcnt lgkmcnt(%c0)" :: "i"(4 * DIST - 4) : "memory"); BAR(); MEMFENCE(); }
      SFOR(j, 2)
        constexpr int q = 2 * n + j;
        if constexpr (DIST == 3 && n == 0) readB(2, j);
        if constexpr (n + DIST < 28) readB(n + DIST, j);
        if constexpr (n == 27) cb[j] = ldsr<bf16x8>(aCSr, j ? 0 : 3 * 512);
        SB();
        accB[j] = MFMA(wb[n & 7][j], xf[n], accB[j]);
        if constexpr ((n & 3) == 0) csB[j] = MFMA16(sel, wb[n & 7][j], csB[j]);
        if constexpr (!AT) {

          if constexpr (GG == 1 && j == 0 && n >= B1N && n < B1N + 4) issueW2(1 + ((n - B1N) >> 1), (n - B1N) & 1);

          if constexpr (GG == 0) {
            if constexpr (j == 1 && n < 16) { epiSlice<(n >> 2) & 3, n & 3>(accA[2], rc, es); if constexpr (n == 15) st16(es.r, rsWS, vL16, sA2, 0); }
            if constexpr (j == 0 && n >= 3 && n < 7) issueW2(1 + ((n - 3) >> 1), (n - 3) & 1);
            if constexpr (j == 0 && n >= 7 && n < 27 && (n & 1)) issueW2(1 + ((n + 1) >> 2), ((n + 1) >> 1) & 1);
            if constexpr (n == 27) issueW2(8, j);
          }
        } else {
          if constexpr (GG == 1 && j == 0 && n < 10) clri(n);
          if constexpr (GG == 0 && j == 1 && n < 21) qstep(n_, accA[2], 0);
          if constexpr (GG == 0 && n >= 24) pv[q - 48] = ld16(rsWS, vKP, sKP, WS_KVP + (q - 48) * 16384);
        }
        SB();
      SEND
    SEND
    accB[0] = MFMA(cb[0], xe, accB[0]); accB[1] = MFMA(cb[1], xe, accB[1]);
    asm volatile("" : "+v"(accB[0]), "+v"(accB[1]));
    __builtin_amdgcn_s_setprio(GG ? PV : 0);
    SB();
  };
  auto pairF = [&](auto p_, const f32x16& a) AI { constexpr int p = decltype(p_)::value; epiSlice<p, 0>(a, rc, es); epiSlice<p, 1>(a, rc, es); epiSlice<p, 2>(a, rc, es); epiSlice<p, 3>(a, rc, es); };
  if constexpr (GG == 0) {
    __builtin_amdgcn_s_setprio(PV);
    if constexpr (AT) { SFOR(q, 6) clri(q); SEND }
    SFOR(s, 30)
      kstep(s_);
    SEND
    if constexpr (!AT) { touch(0); touch(4); }
    if constexpr (AT) WAITV(6);
    WAITL0(); SB(); BAR(); SB();
    STAMP(3);
    kvstep(IntC<0>{});
    readB(0, 0); readB(0, 1); readB(1, 0); readB(1, 1);
    kvstep(IntC<1>{}); kvstep(IntC<3>{});
    if constexpr (AT) { WAITV(2); stflag(E + 1, rsWS, sFown); }
    STAMP(4);
    SB();
    stream();
  } else {
    stream();
    STAMP(3);
    if constexpr (AT) {
      WAITV(0); stflag(E + 1, rsWS, sFown);
      SFOR(s, 21) qstep(s_, accA[0], 1); SEND
      SFOR(s, 21) qstep(s_, accA[1], 2); SEND
#pragma unroll
      for (int i = 0; i < 8; i++) pv[i] = ld16(rsWS, vKP, sKP, WS_KVP + i * 16384);
      SFOR(s, 21) qstep(s_, accA[2], 3); SEND
      SB(); BAR(); SB();
    } else {
      SFOR(e, 20)
        if constexpr (e < 12) pairF(IntC<e & 3>{}, accA[e >> 2]); else pairF(IntC<e & 3>{}, accB[(e >> 2) - 3]);
        if constexpr ((e & 3) == 3) st16(es.r, rsWS, vL16, sA2, (e < 12 ? 1 + (e >> 2) : 3 + (e >> 2)) * 1024);
        if constexpr (e == 0) { issueW2(3, 0); issueW2(3, 1); }
        if constexpr ((e & 3) == 2) { SB(); BAR(); SB(); issueW2(4 + (e >> 2), 0); issueW2(4 + (e >> 2), 1); }
      SEND
    }
  }

  if constexpr (AT) {
    __builtin_amdgcn_s_setprio(0);
    STAMP(5);
    waittag<16384>(pv, rsWS, vKP, sKP + WS_KVP, T12, 0xfffu);
    STAMP(6);
    {
      u32x4 o;
      f32x4 s4 = __builtin_bit_cast(f32x4, pv[0]);
#pragma unroll
      for (int i = 1; i < 8; i++) s4 += __builtin_bit_cast(f32x4, pv[i]);
      asm("" : "+v"(s4));
#pragma unroll
      for (int k = 0; k < 4; k++) {
        float sum = xsum(s4[k]);
        sum = l31 == 29 ? 1.f : sum;
        if (GG == 1) sum = wr == 3 ? 0.f : sum;
        o[k] = (pk2(sum, sum) & 0xffff0000u) | T16;
      }
      st16(o, rsWS, hh ? 0x80000000u : l31 * 16, (rb * 2 + half) * 4096 + wr * 1024 + GG * 512, WS_KVR);
    }
    const unsigned sKR = (8 * half + w) * 8192;
    {
      const f32x16 p0 = featproj(accB[0], aWQ, (4 + 2 * GG) * 1568, hh), p1 = featproj(accB[1], aWQ, (5 + 2 * GG) * 1568, hh);
      accB[0] = dualexpP(p0, rc.Lr); asm volatile("" : "+v"(accB[0]));
#pragma unroll
      for (int i = 0; i < 8; i++) pv[i] = ld16(rsWS, vL16, sKR, WS_KVR + i * 1024);
      accB[1] = dualexpP(p1, rc.Lr); asm volatile("" : "+v"(accB[1]));
    }
    STAMP(7);
    waittag<1024>(pv, rsWS, vL16, sKR + WS_KVR, T16, 0xffffu);
    STAMP(8);
#pragma unroll
    for (int i = 0; i < 4; i++) {
      u32x4 f = {__builtin_amdgcn_perm(pv[2 * i][1], pv[2 * i][0], 0x07060302u), __builtin_amdgcn_perm(pv[2 * i][3], pv[2 * i][2], 0x07060302u),
                 __builtin_amdgcn_perm(pv[2 * i + 1][1], pv[2 * i + 1][0], 0x07060302u), __builtin_amdgcn_perm(pv[2 * i + 1][3], pv[2 * i + 1][2], 0x07060302u)};
      ldsw<u32x4>(LB + w * 4096 + vL16, i * 1024, f);
    }
    WAITL0(); BAR(); MEMFENCE();
    f32x16 op[2], on[2];
    auto omm = [&](const f32x16& phi, unsigned lh, int z) AI {
#pragma unroll
      for (int i = 0; i < 16; i++) { op[z][i] = 0.f; on[z][i] = 0.f; }
      bf16x8 kf[4];
#pragma unroll
      for (int i = 0; i < 4; i++) kf[i] = ldsr<bf16x8>(LB + vL16, lh * 4096 + i * 1024);
#pragma unroll
      for (int i = 0; i < 2; i++) { op[z] = MFMA(kf[i], sub4(phi, i), op[z]); on[z] = MFMA(kf[2 + i], sub4(phi, 2 + i), on[z]); }
      SB();
    };
    auto ofin = [&](unsigned lh, int z) AI {
      const float ia = __builtin_amdgcn_rcpf(op[z][13]), ib = __builtin_amdgcn_rcpf(on[z][13]);
      const float id = __builtin_amdgcn_rcpf(__builtin_fmaf(op[z][12], ia, __builtin_fmaf(on[z][12], ib, 1e-6f)));
      const float ca = xhi(ia * id), cb2 = xhi(ib * id);
      u32x4 f0, f1;
#pragma unroll
      for (int i = 0; i < 4; i++) {
        f0[i] = pk2s(__builtin_fmaf(op[z][2 * i], ca, on[z][2 * i] * cb2), __builtin_fmaf(op[z][2 * i + 1], ca, on[z][2 * i + 1] * cb2));
        f1[i] = pk2s(__builtin_fmaf(op[z][8 + 2 * i], ca, on[z][8 + 2 * i] * cb2), __builtin_fmaf(op[z][9 + 2 * i], ca, on[z][9 + 2 * i] * cb2));
      }
      f1[2] = hh ? 0u : f1[2]; f1[3] = hh ? 0u : f1[3];
      st16(f0, rsWS, vL16, sA2, (112 + 2 * lh) * 1024); st16(f1, rsWS, vL16, sA2, (113 + 2 * lh) * 1024);
      SB();
    };
    if constexpr (GG) __builtin_amdgcn_s_setprio(1);
    omm(accA[2], GG ? 3 : 0, 0); omm(accB[0], 4 + 2 * GG, 1); ofin(GG ? 3 : 0, 0); omm(accB[1], 5 + 2 * GG, 0); ofin(4 + 2 * GG, 1);
    if constexpr (GG) { omm(accA[0], 1, 1); ofin(7, 0); omm(accA[1], 2, 0); ofin(1, 1); ofin(2, 0); } else ofin(5, 0);
    STAMP(9);
  } else {
    __builtin_amdgcn_s_setprio(0);
    STAMP(5);
    const unsigned sA = WS_A2 + rt2 * 144 * 1024;
    unsigned ln = vL16; asm volatile("" : "+v"(ln));
    const unsigned aW = lw2 + (ln & 0x200) + ((ln & 0xc0) << 1) + ((((ln >> 3) & 0x20) | ((ln >> 1) & 0x10)) ^ ((ln >> 1) & 0x40)) + ((ln >> 1) & 8);
    f32x16 acc[2][2];
#pragma unroll
    for (int i = 0; i < 4; i++)
#pragma unroll
      for (int k = 0; k < 16; k++) acc[i >> 1][i & 1][k] = 0.f;
    u32x4 af[NS][2];
    u32x4 xr;
    auto loadA = [&](int i) AI { af[i][0] = ld16(rsWS, vL16, sA + ksb(i) * 1024, ksi(i) * 1024); af[i][1] = ld16(rsWS, vL16, sA + ksb(i) * 1024, (144 + ksi(i)) * 1024); };
    const unsigned vO = (ln >> 7) * XROW + (ln & 0x70), sO = (rt2 * 32 + 8 * w) * XROW + ci * 128;
    if constexpr (GG == 0) { st16(epiP(accB[0], rc), rsWS, vL16, sA2, 4 * 1024); st16(epiP(accB[1], rc), rsWS, vL16, sA2, 5 * 1024); }
    STAMP(6);
    constexpr int MV = GG ? 7 : 9;
    bf16x8 wf[NS][2];
    auto readW = [&](int i) AI {
      const unsigned ra = ring_area(i % RING), la = aW + (ra & 0x30000u);
      wf[i][0] = tr2(la, ra & 0xffffu, (ra & 0xffffu) + 1024); wf[i][1] = tr2(la ^ 64u, ra & 0xffffu, (ra & 0xffffu) + 1024);
    };
    waitv<GG ? 7 : 4>();
    SFOR(i, GG ? 7 : 8) readW(i); SEND
    WAITV(0); stflag(E + 1, rsWS, sFown);
    if constexpr (!GG) readW(8);
    WAITL0(); SB();
    BAR();
    SB(); STAMP(11);
    if constexpr (GG) __builtin_amdgcn_s_setprio(P2G1);
    SFOR(i, MV)
      if constexpr (i == MV - 2) ldflag();
      issueW2(9 + i, 0); issueW2(9 + i, 1);
    SEND
    asm volatile("s_waitcnt vmcnt(4)" : "+v"(fl1) :: "memory");
    if (!ALL(!nd1 || fl1 == E + 1u)) poll(rsWS, lane * FS, sF, E, nd1 ? 1u : 0u);
    STAMP(7);
#pragma unroll
    for (int j = 0; j < PD; j++) loadA(j);
    asm volatile("buffer_load_dwordx4 %0, %1, %2, %3 offen" : "=v"(xr) : "v"(vO), "s"(rsX), "s"(sO) : "memory");
    SFOR(i, NS)
      asm volatile("s_waitcnt vmcnt(%c2)" : "+v"(af[i][0]), "+v"(af[i][1]) : "i"(later2(i, NS, GG)) : "memory");
      if constexpr (i + 1 >= MV && i + 1 < NS) readW(i + 1);
      if constexpr (GG == 0 && i >= 12) {
        auto stale = [&]() AI {
          unsigned m = UMAX(UMAX(UMAX(af[i][0][0], af[i][0][1]), UMAX(af[i][0][2], af[i][0][3])), UMAX(UMAX(af[i][1][0], af[i][1][1]), UMAX(af[i][1][2], af[i][1][3])));
          return ANY(m == ~0u);
        };
        if (__builtin_expect(stale(), 0)) {
          int it = 0;
#pragma nounroll
          do { __builtin_amdgcn_s_sleep(SLEEPR); rel1<true>(af[i][0], af[i][1], rsWS, vL16, sA + (ksb(i) + ksi(i)) * 1024); } while (++it < (1 << 14) && stale());
          SFOR(u, PD - 1) if constexpr (i + 1 + u < NS) rel1<false>(af[i + 1 + u][0], af[i + 1 + u][1], rsWS, vL16, sA + (ksb(i + 1 + u) + ksi(i + 1 + u)) * 1024); SEND
        }
      }
      if constexpr (i == 6) STAMP(8);
      if constexpr (i == 12) STAMP(9);
      SB();
      if constexpr (i + PD < NS) loadA(i + PD);
#pragma unroll
      for (int mm = 0; mm < 2; mm++) acc[0][mm] = MFMA(wf[i][0], __builtin_bit_cast(bf16x8, af[i][mm]), acc[0][mm]);
#pragma unroll
      for (int mm = 0; mm < 2; mm++) acc[1][mm] = MFMA(wf[i][1], __builtin_bit_cast(bf16x8, af[i][mm]), acc[1][mm]);
      if constexpr (GG == 0 && (i == 9 || i == 10)) { SB(); issueW2(i + 9, 0); issueW2(i + 9, 1); }
      SB();
    SEND
    STAMP(10);
    __builtin_amdgcn_s_setprio(0);
#pragma unroll
    for (int qd = 0; qd < 4; qd++)
#pragma unroll
      for (int k = 0; k < 4; k++) {
        const unsigned ra = ring_area(2 * qd + (k >> 1)) + (k & 1) * 1024;
        ldsw<bf16x8>(((lw2 + vL16) ^ (unsigned)(32 * (4 * (qd >> 1) + k))) + (ra & 0x30000u), ra & 0xffffu, sub4(acc[qd >> 1][qd & 1], k));
      }
    WAITL0(); BAR(); MEMFENCE();
    STAMP(12);
    const unsigned c8 = (ln >> 4) & 7, j2 = (c8 & 4) + 2 * GG + ((c8 >> 1) & 1);
    const unsigned rd = LB + ((0x51407362u >> (4 * j2)) & 7) * 16384 + (c8 & 1) * 1024 + (((8 * wr + (ln >> 7)) << 4) ^ (c8 << 5));
    asm volatile("" : "+v"(xr));
    f32x4 sum[2];
#pragma unroll
    for (int k = 0; k < 2; k++) {
      f32x4 t = {U2F(xr[2 * k] << 16), U2F(xr[2 * k] & 0xffff0000u), U2F(xr[2 * k + 1] << 16), U2F(xr[2 * k + 1] & 0xffff0000u)};
#pragma unroll
      for (int v = 0; v < 8; v++) t += ldsr<f32x4>(rd, v * 2048 + k * 512);
      sum[k] = t;
    }
    u32x4 o = {pk2(sum[0][0], sum[0][1]), pk2(sum[0][2], sum[0][3]), pk2(sum[1][0], sum[1][1]), pk2(sum[1][2], sum[1][3])};
    __builtin_amdgcn_raw_buffer_store_b128(o, mkrs(pout, 2048 * XROW), vO + sO, 0, OAUX);
    if (b == 32 && w == 0) stflag(E + 4, rsWS, WS_EPOCH);
  }
  STAMPS_OUT();
  };
  if (!attn && g) prog(BoolC<false>{}, IntC<1>{});
  else if (!attn) prog(BoolC<false>{}, IntC<0>{});
  else if (g) prog(BoolC<true>{}, IntC<1>{});
  else prog(BoolC<true>{}, IntC<0>{});
}

extern "C" void kernel_launch(void* const* d_in, const int* in_sizes, int n_in, void* d_out, int out_size, void* d_ws, size_t ws_size,
                              hipStream_t stream) {
  fused_kernel<<<256, 512, 0, stream>>>((const char*)d_in[0], (const char*)d_in[1], (const char*)d_in[5], (const char*)d_in[6], (char*)d_ws,
                                        (const char*)d_in[2], (const char*)d_in[7], (const char*)d_in[3], (char*)d_out);
}
```

```cpp
#define COOP 0
#include <hip/hip_runtime.h>
#include <hip/hip_bf16.h>
#include <utility>

#define DI __device__ __forceinline__
#define LAS __attribute__((address_space(3)))
typedef __attribute__((ext_vector_type(8))) short bf16x8;
typedef __attribute__((ext_vector_type(4))) short s16x4;
typedef __attribute__((ext_vector_type(2))) __bf16 bf2;
typedef __attribute__((ext_vector_type(2))) float f32x2;
typedef __attribute__((ext_vector_type(4))) float f32x4;
typedef __attribute__((ext_vector_type(16))) float f32x16;
typedef __attribute__((ext_vector_type(2))) unsigned u32x2;
typedef __attribute__((ext_vector_type(4))) unsigned u32x4;
typedef __amdgpu_buffer_rsrc_t rsrc_t;
#define F2U(x) __builtin_bit_cast(unsigned, (float)(x))
#define U2F(x) __builtin_bit_cast(float, (unsigned)(x))
#define ALL(x) (__builtin_amdgcn_ballot_w64(!(x)) == 0ull)
#define ANY(x) (__builtin_amdgcn_ballot_w64(x) != 0ull)
#define UMAX(a, b) __builtin_elementwise_max((unsigned)(a), (unsigned)(b))

constexpr unsigned XROW = 896, QROW = 2688, FROW = 3584;
constexpr unsigned FS = 128;
constexpr unsigned WS_EPOCH = 0;
constexpr unsigned WS_GR = 4096;
constexpr unsigned WS_KVP = 131072;
constexpr unsigned WS_A2 = WS_KVP + 16 * 16 * 8192;
constexpr unsigned WS_ST = WS_A2 + 64 * 144 * 1024;
constexpr unsigned WS_FH = WS_ST + 262144;
constexpr unsigned WS_KVR = WS_FH + 2048 * FS;
constexpr unsigned WS_END = WS_KVR + 16 * 8192;
constexpr unsigned L_GU = 65536, L_KV = 131072;
constexpr unsigned L_TAIL = L_KV + 3 * 8192;
constexpr unsigned L_WPK = L_TAIL;
constexpr unsigned L_ST = L_TAIL + 2048;
constexpr unsigned L_CS = L_TAIL + 4096;
constexpr unsigned L_PHI = L_GU + 32768;
constexpr unsigned L_V = L_KV + 16384;
constexpr unsigned L_FREE = 32768;

#define MFMA(a, b, c) __builtin_amdgcn_mfma_f32_32x32x16_bf16((a), (b), (c), 0, 0, 0)
#define MFMA16(a, b, c) __builtin_amdgcn_mfma_f32_16x16x32_bf16((a), (b), (c), 0, 0, 0)
#define AI __attribute__((always_inline))
#define BAR() __builtin_amdgcn_s_barrier()
#define WAITV(n) asm volatile("s_waitcnt vmcnt(" #n ")" ::: "memory")
#define WAITL0() asm volatile("s_waitcnt lgkmcnt(0)" ::: "memory")
#define SB() __builtin_amdgcn_sched_barrier(0)
#define MEMFENCE() asm volatile("" ::: "memory")

template <int N> DI void waitv() { asm volatile("s_waitcnt vmcnt(%c0)" :: "i"(N) : "memory"); }

#define INSZ(x) (x)
DI rsrc_t mkrs(const void* p, unsigned bytes) { return __builtin_amdgcn_make_buffer_rsrc((void*)p, (short)0, (int)bytes, 0x00020000); }
DI void dma(unsigned lds, unsigned limm, unsigned voff, rsrc_t rs, unsigned soff, unsigned simm) {
  unsigned t;
  asm volatile("s_add_u32 m0, %1, %c5\n\ts_add_u32 %0, %4, %c6\n\tbuffer_load_dwordx4 %2, %3, %0 offen lds"
               : "=&s"(t) : "s"(lds), "v"(voff), "s"(rs), "s"(soff), "i"(limm), "i"(simm) : "memory", "scc");
}
DI void dmat(unsigned lds, unsigned voff, rsrc_t rs) {
  asm volatile("s_mov_b32 m0, %0\n\ts_nop 0\n\tbuffer_load_dword %1, %2, 0 offen lds" :: "s"(lds), "v"(voff), "s"(rs) : "memory");
}
DI s16x4 trd(unsigned a) { return __builtin_amdgcn_ds_read_tr16_b64_v4i16((LAS s16x4*)(size_t)a); }
DI s16x4 trd2(unsigned a, unsigned off) { return __builtin_amdgcn_ds_read_tr16_b64_v4i16((LAS s16x4*)((LAS char*)(size_t)a + off)); }
DI bf16x8 tr2(unsigned a, unsigned off0, unsigned off1) {
  LAS char* p = (LAS char*)(size_t)a;
  s16x4 lo = __builtin_amdgcn_ds_read_tr16_b64_v4i16((LAS s16x4*)(p + off0));
  s16x4 hi = __builtin_amdgcn_ds_read_tr16_b64_v4i16((LAS s16x4*)(p + off1));
  return __builtin_shufflevector(lo, hi, 0, 1, 2, 3, 4, 5, 6, 7);
}
template <class T> DI T ldsr(unsigned a, unsigned off) { return *(LAS T*)((LAS char*)(size_t)a + off); }
template <class T> DI void ldsw(unsigned a, unsigned off, T v) { *(LAS T*)((LAS char*)(size_t)a + off) = v; }
DI unsigned pk2(float a, float b) { f32x2 f = {a, b}; return __builtin_bit_cast(unsigned, __builtin_convertvector(f, bf2)); }
DI float rbf(float a) { return U2F(pk2(a, a) << 16); }
DI float dot2(unsigned a, unsigned b, float c) { return __builtin_amdgcn_fdot2_f32_bf16(__builtin_bit_cast(bf2, a), __builtin_bit_cast(bf2, b), c, false); }
DI float xsum(float v) { auto r = __builtin_amdgcn_permlane32_swap(F2U(v), F2U(v), false, false); float a = U2F(r[0]); asm("" : "+v"(a)); return a + U2F(r[1]); }
DI float xmax(float v) { auto r = __builtin_amdgcn_permlane32_swap(F2U(v), F2U(v), false, false); return __builtin_fmaxf(U2F(r[0]), U2F(r[1])); }
DI float xmin(float v) { auto r = __builtin_amdgcn_permlane32_swap(F2U(v), F2U(v), false, false); return __builtin_fminf(U2F(r[0]), U2F(r[1])); }
DI float xhi(float v) { auto r = __builtin_amdgcn_permlane32_swap(F2U(v), F2U(v), false, false); return U2F(r[1]); }
DI void st16(u32x4 v, rsrc_t rs, unsigned voff, unsigned soff, unsigned simm) {
  unsigned t;
  asm volatile("s_add_u32 %0, %4, %c5\n\tbuffer_store_dwordx4 %1, %2, %3, %0 offen sc1\n\ts_nop 1" : "=&s"(t) : "v"(v), "v"(voff), "s"(rs), "s"(soff), "i"(simm) : "memory", "scc");
}
DI void stflag(unsigned v, rsrc_t rs, unsigned soff) {
  unsigned voff = (__builtin_amdgcn_mbcnt_hi(~0u, __builtin_amdgcn_mbcnt_lo(~0u, 0u)) == 0u) ? 0u : 0x80000000u;
  __builtin_amdgcn_raw_buffer_store_b32(v, rs, voff, soff, 16);
}
#define XORCOND (n == 24)
#define PS 0
#define PV 0
#define KEEP2 0
#define S2N 14
#define B1N 23
#define DIST 2
#define P2G1 1
#define OAUX 0
#define RECY 0
#define POFF 99
#define H1S 0
#define H0L 10
#define H0C 22
#define Q3A 1
#define SLEEPR 4
#define SLEEPN 2
DI u32x4 ld16(rsrc_t rs, unsigned voff, unsigned soff, unsigned simm) {
  u32x4 r; unsigned t;
  asm volatile("s_add_u32 %1, %4, %c5\n\tbuffer_load_dwordx4 %0, %2, %3, %1 offen sc1" : "=v"(r), "=&s"(t) : "v"(voff), "s"(rs), "s"(soff), "i"(simm) : "memory", "scc");
  return r;
}
template <unsigned STEP> DI void ld8w(u32x4 (&p)[8], rsrc_t rs, unsigned voff, unsigned soff) {
  unsigned t;
#define L1(k) "buffer_load_dwordx4 %" #k ", %9, %10, %8 offen sc1\n\ts_add_u32 %8, %8, %c12\n\t"
  asm volatile("s_mov_b32 %8, %11\n\t" L1(0) L1(1) L1(2) L1(3) L1(4) L1(5) L1(6) L1(7) "s_waitcnt vmcnt(0)"
               : "=&v"(p[0]), "=&v"(p[1]), "=&v"(p[2]), "=&v"(p[3]), "=&v"(p[4]), "=&v"(p[5]), "=&v"(p[6]), "=&v"(p[7]), "=&s"(t)
               : "v"(voff), "s"(rs), "s"(soff), "i"(STEP) : "memory", "scc");
#undef L1
}
DI bool untag(u32x4 (&p)[8], unsigned tag, unsigned mask) {
  unsigned chk = 0;
#pragma unroll
  for (int i = 0; i < 8; i++)
#pragma unroll
    for (int k = 0; k < 4; k++) { p[i][k] ^= tag; chk |= p[i][k]; }
  return ALL((chk & mask) == 0u);
}
template <unsigned STEP> DI void waittag(u32x4 (&p)[8], rsrc_t rs, unsigned voff, unsigned soff, unsigned tag, unsigned mask) {
  asm volatile("s_waitcnt vmcnt(0)" : "+v"(p[0]), "+v"(p[1]), "+v"(p[2]), "+v"(p[3]), "+v"(p[4]), "+v"(p[5]), "+v"(p[6]), "+v"(p[7]) :: "memory");
#pragma nounroll
  for (int it = 0; !untag(p, tag, mask) && it < (1 << 16); ++it) { __builtin_amdgcn_s_sleep(SLEEPN); ld8w<STEP>(p, rs, voff, soff); }
}
template <bool WAIT> DI void rel1(u32x4& a, u32x4& b, rsrc_t rs, unsigned voff, unsigned so) {
  unsigned t;
  if constexpr (WAIT) asm volatile("buffer_load_dwordx4 %0, %3, %4, %5 offen sc1\n\ts_add_u32 %2, %5, 147456\n\tbuffer_load_dwordx4 %1, %3, %4, %2 offen sc1\n\ts_waitcnt vmcnt(0)"
                                   : "+v"(a), "+v"(b), "=&s"(t) : "v"(voff), "s"(rs), "s"(so) : "memory", "scc");
  else asm volatile("buffer_load_dwordx4 %0, %3, %4, %5 offen sc1\n\ts_add_u32 %2, %5, 147456\n\tbuffer_load_dwordx4 %1, %3, %4, %2 offen sc1"
                    : "+v"(a), "+v"(b), "=&s"(t) : "v"(voff), "s"(rs), "s"(so) : "memory", "scc");
}
DI void poll(rsrc_t rs, unsigned voff, unsigned soff, unsigned E, unsigned need) {
  voff = need ? voff : 0x80000000u;
#pragma nounroll
  for (int it = 0; it < (1 << 16); ++it) {
    unsigned v;
    asm volatile("buffer_load_dword %0, %1, %2, %3 offen sc1\n\ts_waitcnt vmcnt(0)" : "=v"(v) : "v"(voff), "s"(rs), "s"(soff) : "memory");
    bool ok = (need == 0u) || ((v - E - need) <= (3u - need));
    if (ALL(ok)) break;
    __builtin_amdgcn_s_sleep(SLEEPN);
  }
}

template <bool B> struct BoolC { static constexpr bool value = B; };
template <int I> struct IntC { static constexpr int value = I; };
template <int... I, class F> DI void sfor_impl(std::integer_sequence<int, I...>, F&& f) { (f(IntC<I>{}), ...); }
template <int N, class F> DI void sfor(F&& f) { sfor_impl(std::make_integer_sequence<int, N>{}, f); }
#define SFOR(n, N) sfor<N>([&](auto n##_) AI { constexpr int n = decltype(n##_)::value;
#define SEND });
struct RowC { float r, K1, K2, Ri, Lr; };

DI unsigned hilo(float v) { float hi = rbf(v); return pk2(hi, v - hi); }
DI float max3(float a, float b, float c) { return __builtin_fmaxf(__builtin_fmaxf(a, b), c); }
DI float min3(float a, float b, float c) { return __builtin_fminf(__builtin_fminf(a, b), c); }
DI unsigned pk2s(float a, float b) { unsigned r; asm("v_cvt_pk_bf16_f32 %0, %1, %2" : "=v"(r) : "v"(a), "v"(b)); return r; }
DI f32x2 sub2(const f32x16& v, int k) { f32x2 t = {v[2 * k], v[2 * k + 1]}; return t; }
DI f32x2 exp2v(f32x2 v) { f32x2 r = {__builtin_amdgcn_exp2f(v[0]), __builtin_amdgcn_exp2f(v[1])}; return r; }
DI f32x2 dup2(float a) { f32x2 r = {a, a}; asm("" : "+v"(r)); return r; }
DI f32x2 fma2(f32x2 a, f32x2 b, f32x2 c) { return __builtin_elementwise_fma(a, b, c); }
DI u32x4 epiP(const f32x16& a, const RowC& c) {
  u32x4 r;
  const f32x2 K1 = dup2(c.K1), K2 = dup2(c.K2), Ri = dup2(c.Ri);
#pragma unroll
  for (int i = 0; i < 4; i++) {
    const f32x2 g = sub2(a, i), d = fma2(exp2v(g * fma2(g * g, K2, K1)), Ri, Ri);
    const f32x2 s = {__builtin_amdgcn_rcpf(d[0]), __builtin_amdgcn_rcpf(d[1])}, h = (g * sub2(a, 4 + i)) * s;
    r[i] = pk2(h[0], h[1]);
  }
  return r;
}
struct EpiS { float x0, x1, m0, m1; u32x4 r; };
template <int P, int PH> DI void epiSlice(const f32x16& a, const RowC& c, EpiS& s) {
  const float g0 = a[2 * P], g1 = a[2 * P + 1];
  if constexpr (PH == 0) { s.x0 = g0 * __builtin_fmaf(g0 * g0, c.K2, c.K1); s.x1 = g1 * __builtin_fmaf(g1 * g1, c.K2, c.K1); }
  if constexpr (PH == 1) { s.x0 = __builtin_amdgcn_exp2f(s.x0); s.x1 = __builtin_amdgcn_exp2f(s.x1); s.m0 = g0 * a[8 + 2 * P]; s.m1 = g1 * a[9 + 2 * P]; }
  if constexpr (PH == 2) { s.x0 = __builtin_amdgcn_rcpf(__builtin_fmaf(s.x0, c.Ri, c.Ri)); s.x1 = __builtin_amdgcn_rcpf(__builtin_fmaf(s.x1, c.Ri, c.Ri)); }
  if constexpr (PH == 3) s.r[P] = pk2s(s.m0 * s.x0, s.m1 * s.x1);
}
DI f32x16 featproj2(const f32x16& a, bf16x8 w0, bf16x8 w1, bool hh) {
  u32x4 q0, q1;
#pragma unroll
  for (int i = 0; i < 4; i++) { q0[i] = pk2(a[2 * i], a[2 * i + 1]); q1[i] = pk2(a[8 + 2 * i], a[9 + 2 * i]); }
  q1[2] = hh ? 0u : q1[2]; q1[3] = hh ? 0u : q1[3];
  f32x16 z;
#pragma unroll
  for (int i = 0; i < 16; i++) z[i] = 0.f;
  z = MFMA(w0, __builtin_bit_cast(bf16x8, q0), z);
  z = MFMA(w1, __builtin_bit_cast(bf16x8, q1), z);
  return z;
}
DI f32x16 featproj(const f32x16& a, unsigned wpaddr, unsigned off, bool hh) {
  return featproj2(a, tr2(wpaddr, off, off + 8 * 56), tr2(wpaddr, off + 16 * 56, off + 24 * 56), hh);
}
DI float max16(const f32x16& p) { float m = max3(p[0], p[1], p[2]); _Pragma("unroll") for (int i = 3; i < 15; i += 2) m = max3(m, p[i], p[i + 1]); return xmax(__builtin_fmaxf(m, p[15])); }
DI float min16(const f32x16& p) { float m = min3(p[0], p[1], p[2]); _Pragma("unroll") for (int i = 3; i < 15; i += 2) m = min3(m, p[i], p[i + 1]); return xmin(__builtin_fminf(m, p[15])); }
DI f32x16 dualexpP(const f32x16& p, float Lr) {
  const f32x2 bx = dup2(-max16(p) * Lr), bn = dup2(min16(p) * Lr), L2 = dup2(Lr);
  f32x16 o;
#pragma unroll
  for (int i = 0; i < 8; i++) {
    const f32x2 ep = exp2v(fma2(sub2(p, i), L2, bx)), en = exp2v(fma2(sub2(p, i), -L2, bn));
    o[i] = U2F(pk2(ep[0], ep[1])); o[8 + i] = U2F(pk2(en[0], en[1]));
  }
  return o;
}
struct SmS { f32x16 p, e; float en[16], bx, bn, sp, sn, tp, tn; bf16x8 w0, w1; };
DI bf16x8 sub4(const f32x16& v, int k) { f32x4 t = {v[4 * k], v[4 * k + 1], v[4 * k + 2], v[4 * k + 3]}; return __builtin_bit_cast(bf16x8, t); }

DI constexpr unsigned lc1_base(int ch) { return ch == 6 ? L_KV : ((ch & 1) ? L_GU : 0u) + (ch < 2 ? 49152u : ch < 4 ? 0u : 16384u); }
template <int CTRL, int RM> DI float dppadd(float v) { return v + U2F(__builtin_amdgcn_update_dpp(0u, F2U(v), CTRL, RM, 0xf, false)); }
DI float wavesum63(float v) {
  v = dppadd<0x111, 0xf>(v); v = dppadd<0x112, 0xf>(v); v = dppadd<0x114, 0xf>(v); v = dppadd<0x118, 0xf>(v);
  v = dppadd<0x142, 0xa>(v); return dppadd<0x143, 0xc>(v);
}

#define STAMP(i)
#define STAMPS_OUT()
#define CSTAMP(i)

#define PDV 5
constexpr int PD = PDV, RING = 9;
constexpr int later2(int i, int NS, int GG) {
  const int younger = 2 * ((i + PD <= NS ? i + PD : NS) - 1 - i);
  int extra = i < PD ? 1 : 0;
  if (GG == 0 && (i == 10 || i == 11)) extra += 2 * (i - 9);
  return younger + extra;
}
DI constexpr unsigned ring_area(int j) { return j == 0 ? L_FREE : j == 1 ? L_PHI : lc1_base(j - 2); }
constexpr int cumG1(int s) { return s < 4 ? 0 : s < 22 ? (s - 4) / 2 : (9 + (s - 22) * 5 / 4 < 48 ? 9 + (s - 22) * 5 / 4 : 48); }

#define CODE_BYTES 65536
__global__ __launch_bounds__(512, 2) void fused_kernel(const char* px_, const char* pqkv_, const char* pwg_, const char* pwu_, char* ws_, const char* pphi_,
                                                       const char* pwd_, const char* pwo_, char* pout_) {
  typedef __attribute__((ext_vector_type(8))) unsigned long long u64x8;
  u64x8 ka; unsigned long long kb;
  asm volatile("s_load_dwordx16 %0, %2, 0\n\ts_load_dwordx2 %1, %2, 64" : "=&s"(ka), "=&s"(kb) : "s"(__builtin_amdgcn_kernarg_segment_ptr()));
  __shared__ __attribute__((aligned(16))) char smem[163840];
  const unsigned LB = (unsigned)(size_t)smem;
  const unsigned tid = __builtin_amdgcn_workitem_id_x(), lane = tid & 63;
  const unsigned w = __builtin_amdgcn_readfirstlane(tid >> 6), g = w >> 2, wr = w & 3;
  const unsigned b = __builtin_amdgcn_workgroup_id_x(), xcd = b & 7, bi = b >> 3, grp = xcd >> 1, half = xcd & 1, rbl = bi & 3, cp = bi >> 2, ci = cp - 1;
  const unsigned rb = 4 * grp + rbl, c = 7 * half + ci;
  const unsigned head = 8 * half + (((half ? 0x76425310u : 0x75316420u) >> (4 * cp)) & 7);
  const bool attn = cp == 0;
  STAMP(0); CSTAMP(14);
  const unsigned wstr = attn ? QROW : FROW;
  const unsigned sG0 = half * (attn ? 448 : 1792) + (attn ? 0 : ci * 256), sX0 = rb * 128 * XROW;
  unsigned vx0, vx1, vG, vU, vKV;
  auto voffs = [&](unsigned ln) AI {
    const unsigned r8 = ln >> 3, c8 = ln & 7, sa = (r8 >> 1) & 1, lc = c8 ^ (4 * sa);
    vx0 = (16 * w + r8) * XROW + ((c8 ^ (r8 >> 1)) << 4); vx1 = (vx0 ^ 64u) + 8 * XROW;
    const unsigned cg = attn ? lc : (c8 ^ (2 * sa)), cu = attn ? (lc >= 6 ? lc - 2 : lc) : (cg ^ 4);
    vG = (8 * w + r8) * wstr + (cg << 4); vU = (8 * w + r8) * wstr + (cu << 4);
    vKV = (8 * w + r8) * QROW + 896 * (1 + (lc >> 2)) + ((lc & 3) << 4) + ((56 * head) & ~15u);
  };
  voffs(lane);
  const unsigned lw1 = LB + w * 1024, lw2 = LB + w * 2048;
#define ROT (((0x24924u >> (2 * (cp + rbl))) & 3) * 2)
  const unsigned rot = ROT;
  const unsigned srow = 128 * rb + 64 * half + 8 * cp + w;
  unsigned vrow = lane < 56 ? lane * 16 : 0x80000000u;
  asm volatile("s_waitcnt lgkmcnt(0)" : "+s"(ka), "+s"(kb), "+v"(vx0), "+v"(vx1), "+v"(vG), "+v"(vU), "+v"(vKV), "+v"(vrow));
  const char *px = (const char*)ka[0], *pqkv = (const char*)ka[1], *pwg = (const char*)ka[2], *pwu = (const char*)ka[3], *pphi = (const char*)ka[5],
             *pwd = (const char*)ka[6], *pwo = (const char*)ka[7];
  char *ws = (char*)ka[4], *pout = (char*)kb;
  const rsrc_t rsX = mkrs(px, INSZ(2048 * XROW)), rsQ = mkrs(pqkv, INSZ(448 * QROW));
  const rsrc_t rsG = mkrs(attn ? pqkv : pwg, INSZ(448 * wstr)), rsU = mkrs(attn ? pqkv + 128 : pwu, INSZ(448 * wstr - (attn ? 128 : 0)));
  auto cidx = [&](int st) AI -> unsigned { unsigned t = rot + st; return t >= 7 ? t - 7 : t; };
  auto issueA = [&](int st, int k) AI {
    const unsigned sl = st & 3, ch = cidx(st);
    if (k == 0) dma(lw2, sl * 16384, vx0, rsX, sX0 + ch * 128, 0);
    if (k == 1) dma(lw2, sl * 16384 + 1024, vx1, rsX, sX0 + ch * 128, 0);
    if (k == 2) dma(lw1, L_GU + sl * 16384, vG, rsG, sG0 + ch * 64 * wstr, 0);
    if (k == 3) dma(lw1, L_GU + sl * 16384 + 8192, vU, rsU, sG0 + ch * 64 * wstr, 0);
    if (k == 4) dma(lw1, L_KV + sl * 8192, vKV, rsQ, ch * 64 * QROW, 0);
  };
  const rsrc_t rsWS = mkrs(ws, WS_END);
  unsigned Ev, cpf; u32x4 xrw;
  asm volatile("buffer_load_dwordx4 %0, %1, %2, %3 offen" : "=v"(xrw) : "v"(vrow), "s"(rsX), "s"(srow * XROW) : "memory");
  asm volatile("buffer_load_dword %0, off, %1, 0 sc1" : "=v"(Ev) : "s"(rsWS) : "memory");
  {
    unsigned long long pc; asm volatile("s_getpc_b64 %0" : "=s"(pc));
    const unsigned vo = w == 0 && lane < 24 ? (bi * 24 + lane) * 128 : 0x80000000u;
    asm volatile("buffer_load_dword %0, %1, %2, 0 offen" : "=v"(cpf) : "v"(vo), "s"(mkrs((const void*)pc, CODE_BYTES)) : "memory");
  }
#pragma unroll
  for (int k = 0; k < 5; k++) issueA(0, k);
  asm volatile("s_waitcnt vmcnt(%c3)" : "+v"(xrw), "+v"(Ev), "+v"(cpf) : "i"(5 + COOP) : "memory");
#pragma unroll
  for (int k = 0; k < 5; k++) issueA(1, k);
  issueA(2, 0);
  const unsigned E = __builtin_amdgcn_readfirstlane(Ev);
  {
    float s1 = 0.f, s2 = 0.f;
#pragma unroll
    for (int i = 0; i < 4; i++) { s1 = dot2(xrw[i], 0x3f803f80u, s1); s2 = dot2(xrw[i], xrw[i], s2); }
    s1 = wavesum63(s1); s2 = wavesum63(s2);
    const float mean = s1 * (1.f / 448.f), var = __builtin_fmaxf(s2 * (1.f / 448.f) - mean * mean, 0.f);
    u32x2 gr = {pk2(-mean, __builtin_amdgcn_sqrtf(rbf(rbf(var) + 1e-5f))), E + 1};
    __builtin_amdgcn_raw_buffer_store_b64(gr, rsWS, lane == 63 ? 0u : 0x80000000u, WS_GR + srow * 8, 16);
  }

  auto prog = [&](auto at_, auto gg_) AI {
  constexpr bool AT = decltype(at_)::value; constexpr int GG = decltype(gg_)::value;
  constexpr unsigned lcstep = AT ? 224 : 128;
  unsigned lane;
  asm volatile("v_mbcnt_lo_u32_b32 %0, -1, 0\n\tv_mbcnt_hi_u32_b32 %0, -1, %0" : "=v"(lane));
  __builtin_assume(lane < 64);
  voffs(lane);
  auto issueL = [&](int hc) AI {
    const int st = hc >> 1; const unsigned ch = cidx(st);
    if (hc & 1) dma(lw1, lc1_base(st) + 8192, vU, rsU, sG0 + ch * 64 * wstr, lcstep);
    else dma(lw1, lc1_base(st), vG, rsG, sG0 + ch * 64 * wstr, lcstep);
  };
  auto issueG = [&](int bar, int k) AI {
    if (bar == 0) issueA(2 + k / 5, k % 5);
    else if (bar <= 3) issueA(bar + 3, k);
    else if (k < 4) issueL(4 * (bar - 4) + k);
    else if (bar == 4) dma(LB + (w & 1) * 1024, L_WPK, lane * 16, mkrs(pphi, 16 * 1568), head * 1568 + (w & 1) * 1024, 0);
    else issueL(12 + (bar - 5));
  };

  const unsigned rt = 4 * rb + wr;
  const unsigned sA2 = WS_A2 + rt * 144 * 1024 + (AT ? half * 16384 : c * 8192), vL16 = lane * 16;
  const u32x4 sent = {~0u, ~0u, ~0u, ~0u};
  auto clr = [&](int ks) AI { st16(sent, rsWS, vL16, sA2, ks * 1024); };

  auto clri = [&](int q) AI { clr(GG ? (q < 6 ? 114 + q : 118 + q) : (q < 2 ? 112 + q : 118 + q)); };

  const bool hh = lane >= 32;
  const unsigned hu = lane >> 5, blk = (lane >> 4) & 1, q4 = (lane >> 2) & 3, p4 = lane & 3, ta = (q4 >> 1) & 1, l31 = lane & 31;
  const unsigned perm = wr ^ (2 * GG);
  const unsigned rowoff = (8 * hu + q4) * 128 + (perm << 11);
  const unsigned pc = (4 * blk + p4) < 6 ? (4 * blk + p4) : 6;
  auto tileaddr = [&](unsigned t) AI -> unsigned {
    if constexpr (AT) { unsigned o = 56 * t + 8 * pc; return (o >> 7) * 8192 + ((((o >> 4) & 7) ^ (4 * ta)) << 4) + (o & 8) + rowoff; }
    else return blk * 8192 + (((2 * t + (p4 >> 1)) ^ (4 * blk + 2 * ta)) << 4) + 8 * (p4 & 1) + rowoff;
  };
  unsigned aA[3], aB[2];
  {
    unsigned o = (head & 1) * 8 + 8 * pc;
    unsigned ak = L_KV + (((o >> 4) ^ (4 * ta)) << 4) + (o & 8) + rowoff;
    aA[0] = LB + (GG ? L_GU + tileaddr(1) : ak);
    aA[1] = LB + (GG ? L_GU + tileaddr(2) : (ak ^ 64u));
    aA[2] = LB + L_GU + tileaddr(GG ? 3 : 0);
    aB[0] = LB + tileaddr(2 * GG); aB[1] = LB + tileaddr(2 * GG + 1);
  }
  const unsigned xrow = 32 * wr + l31;
  const unsigned aX = LB + xrow * 128 + (((2 * perm + hu) ^ ((xrow >> 1) & 7)) << 4);
  const unsigned selv = (((lane >> 4) ^ (lane >> 2)) & 1) ? 0u : 0x3f803f80u;
  const u32x4 sel4 = {selv, selv, selv, selv};
  const bf16x8 sel = __builtin_bit_cast(bf16x8, sel4);

  bf16x8 xf[28], wa[2][3], wb[8][2];
  f32x16 accA[3], accB[2];
  f32x4 csA[3], csB[2];
#pragma unroll
  for (int j = 0; j < 3; j++) { for (int i = 0; i < 16; i++) accA[j][i] = 0.f; for (int i = 0; i < 4; i++) csA[j][i] = 0.f; }
#pragma unroll
  for (int j = 0; j < 2; j++) { for (int i = 0; i < 16; i++) accB[j][i] = 0.f; for (int i = 0; i < 4; i++) csB[j][i] = 0.f; }
  u32x2 grn;

  const rsrc_t rsWD = mkrs(pwd, INSZ(1792 * XROW)), rsWO = mkrs(pwo, INSZ(448 * XROW));
  const unsigned vW2 = (lane >> 3) * XROW + (((lane & 7) ^ ((lane >> 2) & 4)) << 4) + ci * 128;
  constexpr int NS = GG ? 16 : 20;
#define RHO ((rbl + cp) & 3)
  const unsigned rho = RHO;
  auto t_of = [](int i) constexpr { return (0x54763210u >> (4 * (i >> 1))) & 7; };
  auto ksb = [&](int i) AI -> unsigned {
    if (GG) return 16 * wr + ((t_of(i) & 3) ^ rho);
    if (i < 12) { const unsigned cc = i % 6 + rho + (rho >> 1); return wr + 8 * (cc >= 6 ? cc - 6 : cc); }
    return 8 * wr + 2 * (((i - 12) >> 1) ^ rho);
  };
  auto ksi = [&](int i) AI -> unsigned { return GG ? 8 * (i & 1) + (t_of(i) & 4) : i < 12 ? 64 + 4 * (i / 6) : 112 + (i & 1); };
  auto issueW2 = [&](int i, int hf) AI {
    if (GG == 0 && i >= 12) dma(lw2, ring_area(i % RING) + hf * 1024, vW2, rsWO, ksb(i) * 14 * XROW, (16 * (i & 1) + 8 * hf) * XROW);
    else dma(lw2, ring_area(i % RING) + hf * 1024, vW2, rsWD, ksb(i) * 16 * XROW, (ksi(i) * 16 + 8 * hf) * XROW);
  };

  const unsigned aCSw = LB + L_CS + l31 * 16 + wr * 4 + GG * 2048, aCSr = LB + L_CS + l31 * 16 + GG * 2048;
  auto readX = [&](int n) AI { xf[n] = ldsr<bf16x8>(aX ^ (unsigned)((n & 3) << 5), ((n >> 2) & 3) * 16384); };
  auto readA = [&](int n, int j) AI {
    const unsigned o = ((n >> 2) & 3) * ((GG == 0 && j < 2) ? 8192 : 16384);
    wa[n & 1][j] = tr2(aA[j] ^ (unsigned)((n & 3) << 11), o, o + 512);
  };
  auto readB = [&](int n, int j) AI {
    const unsigned base = lc1_base(n >> 2);
    if (XORCOND) asm volatile("" : "+v"(aB[j]));
    wb[n & 7][j] = tr2(aB[j] ^ (((n & 3) << 11) | (base & 0x30000u)), base & 0xffffu, (base & 0xffffu) + 512);
  };
  RowC rc; bf16x8 xe; unsigned denb;
  auto rowconst = [&]() AI {
#pragma nounroll
    for (int it = 0; it < (1 << 16) && !ALL(grn[1] == E + 1); ++it) {
      __builtin_amdgcn_s_sleep(SLEEPN);
      asm volatile("buffer_load_dwordx2 %0, %1, %2, %3 offen sc1\n\ts_waitcnt vmcnt(0)" : "=v"(grn) : "v"(xrow * 8), "s"(rsWS), "s"(WS_GR + rb * 1024) : "memory");
    }
    const float den = U2F(grn[0] & 0xffff0000u);
    const unsigned nm = hh ? 0u : __builtin_amdgcn_perm(grn[0], grn[0], 0x01000100u);
    u32x4 t = {nm, nm, nm, nm}; xe = __builtin_bit_cast(bf16x8, t);
    denb = grn[0] >> 16;
    rc.r = __builtin_amdgcn_rcpf(den);
    const float k1 = -2.f * 1.4426950408889634f * 0.7978845608028654f;
    rc.K1 = k1 * rc.r; rc.K2 = (k1 * 0.044715f) * rc.r * (rc.r * rc.r); rc.Ri = den * den; rc.Lr = 1.4426950408889634f * rc.r;
  };
  waitv<7 + COOP>();
  BAR(); MEMFENCE();
  STAMP(1);
  readX(0); readA(0, 0); readA(0, 1); readA(0, 2);
  SFOR(n, 28)
    SB();
    if constexpr (n == 27) {
#pragma unroll
      for (int j = 0; j < 3; j++) ldsw<unsigned>(aCSw, j * 512, hilo(csA[j][0]));
    }
    if constexpr ((n & 3) == 3) {
      if constexpr (n == 27) WAITV(0);
      else if constexpr (n == 23) asm volatile("s_waitcnt vmcnt(10)" : "+v"(grn) :: "memory");
      else if constexpr (n == 3 || n == 15 || n == 19) WAITV(11); else WAITV(10);
      { WAITL0(); BAR(); MEMFENCE(); }
      if constexpr (n == 27) STAMP(2);
    }
    constexpr int bar = (n + 1) >> 2, ph = (n + 1) & 3;
#pragma unroll
    for (int j = 0; j < 3; j++) {
      if constexpr (n < 27) { if (j == 0) readX(n + 1); readA(n + 1, j); }
      else { wa[0][j] = ldsr<bf16x8>(aCSr, j * 512); if (GG == 1 && j < 2) { readB(j, 0); readB(j, 1); } }
      SB();
      accA[j] = MFMA(wa[n & 1][j], xf[n], accA[j]);
      const int slot = 3 * ph + j;
      if (bar == 0) issueG(0, slot - 2);
      else if (bar == 6) { if (slot < 5) issueG(bar, slot); }
      else if (bar <= 6) { if (slot == 0) issueG(bar, 0); if (slot == 2) issueG(bar, 1); if (slot == 5) issueG(bar, 2); if (slot == 7) issueG(bar, 3); if (slot == 10) issueG(bar, 4); }
      else if (j < 2) {
        if constexpr (AT) dma(lw2, L_FREE + j * 1024, lane * 16, mkrs(pphi, 16 * 1568), half * 8 * 1568 + w * 2048, j * 1024);
        else issueW2(0, j);
      }
      if ((n & 3) == 0) csA[j] = MFMA16(sel, wa[n & 1][j], csA[j]);
      if (n == 25 && j == 0) rowconst();
      if (n == 14 && j == 2) asm volatile("buffer_load_dwordx2 %0, %1, %2, %3 offen sc1" : "=v"(grn) : "v"(xrow * 8), "s"(rsWS), "s"(WS_GR + rb * 1024) : "memory");
      SB();
    }
  SEND
#pragma unroll
  for (int j = 0; j < 3; j++) accA[j] = MFMA(wa[0][j], xe, accA[j]);
  asm volatile("" : "+v"(accA[0]), "+v"(accA[1]), "+v"(accA[2]));
  SB();

  const unsigned sFown = WS_FH + (rt * 32 + (AT ? 28 + 2 * half : 2 * c) + GG) * FS;
  const unsigned aWP = LB + (4 * hu + q4) * 56 + 8 * pc;
  const unsigned T12 = (E + 1) & 0xfffu, T16 = (E + 1) & 0xffffu;
  const unsigned rt2 = 2 * (8 * grp + 4 * half + rbl);
  const unsigned sF = WS_FH + rt2 * 32 * FS;
  const unsigned fc = l31 >> 1;
  const bool nd1 = GG ? (fc >> 1) == wr : (fc >= 8 && fc < 14) || fc == 14 + (wr >> 1);
  const unsigned aIW = LB + L_PHI + xrow * 64 + ((hu ^ ((xrow >> 1) & 7)) << 3);
  SmS sm;
  unsigned aWK = aWP + L_WPK, aWQ = aWP + L_FREE;
  asm volatile("" : "+v"(aWK)); if constexpr (AT) asm volatile("" : "+v"(aWQ));
  auto kstep = [&](auto s_) AI {
    constexpr int s = decltype(s_)::value;
    if constexpr (s == 0) { sm.w0 = tr2(aWK, 0, 8 * 56); sm.w1 = tr2(aWK, 16 * 56, 24 * 56); }
    if constexpr (s == 1) sm.p = featproj2(accA[0], sm.w0, sm.w1, hh);
    if constexpr (s == 2 || s == 3) {
#pragma unroll
      for (int k = 2 * s - 4; k < 2 * s - 2; k++) {
        u32x2 vv = {pk2(accA[1][4 * k], accA[1][4 * k + 1]), pk2(accA[1][4 * k + 2], accA[1][4 * k + 3])};
        if (k == 3) vv[0] = hh ? denb : vv[0];
        ldsw<u32x2>(aIW ^ (unsigned)(k << 4), L_V - L_PHI, vv);
      }
    }
    if constexpr (s == 4) sm.bx = -max16(sm.p) * rc.Lr;
    if constexpr (s == 5) sm.bn = min16(sm.p) * rc.Lr;
    if constexpr (s >= 6 && s < 22) {
      constexpr int i = s - 6;
      sm.e[i] = __builtin_amdgcn_exp2f(__builtin_fmaf(sm.p[i], rc.Lr, sm.bx)); sm.en[i] = __builtin_amdgcn_exp2f(__builtin_fmaf(sm.p[i], -rc.Lr, sm.bn));
      if constexpr (i == 0) { sm.sp = sm.e[0]; sm.sn = sm.en[0]; } else if constexpr (i < 12) { sm.sp += sm.e[i]; sm.sn += sm.en[i]; }
      else if constexpr (i == 12) { sm.tp = sm.e[12]; sm.tn = sm.en[12]; } else { sm.tp += sm.e[i]; sm.tn += sm.en[i]; }
    }
    if constexpr (s == 22) { sm.sp = __builtin_amdgcn_rcpf(xsum(sm.sp + (hh ? 0.f : sm.tp))) * rc.r; sm.sn = __builtin_amdgcn_rcpf(xsum(sm.sn + (hh ? 0.f : sm.tn))) * rc.r; }
    if constexpr (s >= 23) {
#pragma unroll
      for (int u = (s == 29 ? 6 : s - 23); u <= (s == 29 ? 7 : s - 23); u++) {
        const int k = u >> 1;
        if (u & 1) { u32x2 t = {pk2s(sm.en[4 * k] * sm.sn, sm.en[4 * k + 1] * sm.sn), pk2s(sm.en[4 * k + 2] * sm.sn, sm.en[4 * k + 3] * sm.sn)}; ldsw<u32x2>(aIW ^ (unsigned)(k << 4), 8192, t); }
        else { u32x2 t = {pk2s(sm.e[4 * k] * sm.sp, sm.e[4 * k + 1] * sm.sp), pk2s(sm.e[4 * k + 2] * sm.sp, sm.e[4 * k + 3] * sm.sp)}; ldsw<u32x2>(aIW ^ (unsigned)(k << 4), 0, t); }
      }
    }
  };
  auto qstep = [&](auto s_, f32x16& a, unsigned lh) AI {
    constexpr int s = decltype(s_)::value;
    if constexpr (s == 0) { sm.w0 = tr2(aWQ, lh * 1568, lh * 1568 + 8 * 56); sm.w1 = tr2(aWQ, lh * 1568 + 16 * 56, lh * 1568 + 24 * 56); }
    if constexpr (s == 1) sm.p = featproj2(a, sm.w0, sm.w1, hh);
    if constexpr (s == 3) sm.bx = -max16(sm.p) * rc.Lr;
    if constexpr (s == 4) sm.bn = min16(sm.p) * rc.Lr;
    if constexpr (s >= 5 && s < 21) {
      constexpr int i = (s - 5) >> 1;
      float t0, t1;
      if constexpr ((s - 5) & 1) { t0 = __builtin_fmaf(sm.p[2 * i], -rc.Lr, sm.bn); t1 = __builtin_fmaf(sm.p[2 * i + 1], -rc.Lr, sm.bn); }
      else { t0 = __builtin_fmaf(sm.p[2 * i], rc.Lr, sm.bx); t1 = __builtin_fmaf(sm.p[2 * i + 1], rc.Lr, sm.bx); }
      a[8 * ((s - 5) & 1) + i] = U2F(pk2(__builtin_amdgcn_exp2f(t0), __builtin_amdgcn_exp2f(t1)));
      if constexpr (s == 20) asm volatile("" : "+v"(a));
    }
  };
  bf16x8 ka_[4], kb_[2][4]; f32x4 kd[2];
  auto kvstep = [&](auto s_) AI {
    constexpr int s = decltype(s_)::value;
    if constexpr (s == 0) {
      const unsigned g4 = lane >> 4, r0 = 8 * g4 + q4, sw = 4 * blk + (q4 >> 1);
      unsigned b0 = LB + L_PHI + r0 * 64 + ((p4 ^ sw) << 3);
      unsigned a0 = (b0 ^ ((wr >> 1) << 5)) + (wr & 1) * 8192;
#pragma unroll
      for (int k = 0; k < 4; k++) {
        ka_[k] = __builtin_shufflevector(trd2(a0, k * 2048), trd2(a0 ^ 16u, k * 2048 + 256), 0, 1, 2, 3, 4, 5, 6, 7);
        kb_[0][k] = __builtin_shufflevector(trd2(b0, L_V - L_PHI + k * 2048), trd2(b0 ^ 16u, L_V - L_PHI + k * 2048 + 256), 0, 1, 2, 3, 4, 5, 6, 7);
        kb_[1][k] = __builtin_shufflevector(trd2(b0 ^ 32u, L_V - L_PHI + k * 2048), trd2(b0 ^ 48u, L_V - L_PHI + k * 2048 + 256), 0, 1, 2, 3, 4, 5, 6, 7);
      }
    }
    if constexpr (s == 1) {
      f32x4 z = {0.f, 0.f, 0.f, 0.f}; kd[0] = z; kd[1] = z;
#pragma unroll
      for (int k = 0; k < 4; k++) { kd[0] = MFMA16(ka_[k], kb_[0][k], kd[0]); kd[1] = MFMA16(ka_[k], kb_[1][k], kd[1]); }
    }
    if constexpr (s == 3) {
#pragma unroll
      for (int d = 0; d < 2; d++) {
        u32x4 t;
#pragma unroll
        for (int i = 0; i < 4; i++) t[i] = (F2U(kd[d][i]) & ~0xfffu) | T12;
        st16(t, rsWS, (lane >> 4) * 512 + (lane & 15) * 16, ((head * 16 + rb) * 2 + (wr & 1)) * 4096 + (wr >> 1) * 2048, WS_KVP + d * 256);
      }
    }
  };
  auto qexp2 = [&](f32x16& a0, unsigned l0, f32x16& a1, unsigned l1) AI {
    const f32x16 p0 = featproj(a0, aWQ, l0 * 1568, hh), p1 = featproj(a1, aWQ, l1 * 1568, hh);
    a0 = dualexpP(p0, rc.Lr); a1 = dualexpP(p1, rc.Lr); asm volatile("" : "+v"(a0), "+v"(a1));
  };

  unsigned fl1 = 0;
  EpiS es;
  u32x4 pv[8];
  const unsigned vKP = l31 * 16 + hu * 8192, sKP = (rb * 32 + half) * 4096 + wr * 1024 + GG * 512, sKR = (8 * half + w) * 8192;
  auto hop1 = [&]() AI {
    waittag<16384>(pv, rsWS, vKP, sKP + WS_KVP, T12, 0xfffu);
    u32x4 o;
#pragma unroll
    for (int k = 0; k < 4; k++) {
      float sum = U2F(pv[0][k]);
#pragma unroll
      for (int i = 1; i < 8; i++) sum += U2F(pv[i][k]);
      sum = xsum(sum);
      sum = l31 == 29 ? 1.f : sum;
      if (GG == 1) sum = wr == 3 ? 0.f : sum;
      o[k] = (pk2s(sum, sum) & 0xffff0000u) | T16;
    }
    st16(o, rsWS, hh ? 0x80000000u : l31 * 16, (rb * 2 + half) * 4096 + wr * 1024 + GG * 512, WS_KVR);
    if (b == 0 && w == 4) stflag(E + 4, rsWS, WS_EPOCH);
    SB();
  };
  bf16x8 cb[2];
  auto ldflag = [&]() AI { asm volatile("buffer_load_dword %0, %1, %2, %3 offen sc1" : "=v"(fl1) : "v"(nd1 ? lane * FS : 0x80000000u), "s"(rsWS), "s"(sF) : "memory"); };
  auto touch = [&](unsigned wo4) AI {
    const unsigned rw = (rbl * 8 + w + wo4) * 44 + lane;
    const bool dn = rw < 896;
    const unsigned vo = lane < 44 ? (dn ? ((rw >> 6) * 128 + 64 + (rw & 63)) : rw - 896) * XROW + ci * 128 : 0x80000000u;
    asm volatile("s_add_u32 m0, %0, %c5\n\ts_nop 0\n\tbuffer_load_dword %1, %3, 0 offen lds\n\tbuffer_load_dword %2, %4, 0 offen lds"
                 :: "s"(LB + w * 256), "v"(dn ? vo : 0x80000000u), "v"(dn ? 0x80000000u : vo), "s"(rsWD), "s"(rsWO), "i"(L_ST) : "memory", "scc");
  };
  auto stream = [&]() AI {
    __builtin_amdgcn_s_setprio(PS);
    SFOR(n, 28)
      if constexpr (n == 27) { ldsw<unsigned>(aCSw, 3 * 512, hilo(csB[0][0])); ldsw<unsigned>(aCSw, 0, hilo(csB[1][0])); WAITL0(); BAR(); MEMFENCE(); }
      else if constexpr (GG == 1) { if constexpr (n == S2N || (!AT && n == B1N)) { if constexpr (AT) WAITV(10); BAR(); MEMFENCE(); } }
      else if constexpr (AT ? n == 7 : (n & 3) == 3) { asm volatile("s_waitcnt lgkmcnt(%c0)" :: "i"(4 * DIST - 4) : "memory"); BAR(); MEMFENCE(); }
      SFOR(j, 2)
        constexpr int q = 2 * n + j;
        if constexpr (DIST == 3 && n == 0) readB(2, j);
        if constexpr (n + DIST < 28) readB(n + DIST, j);
        if constexpr (n == 27) cb[j] = ldsr<bf16x8>(aCSr, j ? 0 : 3 * 512);
        SB();
        accB[j] = MFMA(wb[n & 7][j], xf[n], accB[j]);
        if constexpr ((n & 3) == 0) csB[j] = MFMA16(sel, wb[n & 7][j], csB[j]);
        if constexpr (!AT) {

          if constexpr (GG == 1 && j == 0 && n >= B1N && n < B1N + 4) issueW2(1 + ((n - B1N) >> 1), (n - B1N) & 1);

          if constexpr (GG == 0) {
            if constexpr (j == 1 && n < 16) { epiSlice<(n >> 2) & 3, n & 3>(accA[2], rc, es); if constexpr (n == 15) st16(es.r, rsWS, vL16, sA2, 0); }
            if constexpr (j == 0 && n >= 3 && n < 7) issueW2(1 + ((n - 3) >> 1), (n - 3) & 1);
            if constexpr (j == 0 && n >= 7 && n < 27 && (n & 1)) issueW2(1 + ((n + 1) >> 2), ((n + 1) >> 1) & 1);
            if constexpr (n == 27) issueW2(8, j);
          }
        } else {
          if constexpr (GG == 1 && j == 0 && n < 10) clri(n);
          if constexpr (GG == 0 && j == 1 && n < 21) qstep(n_, accA[2], 0);
          if constexpr (GG == 0 && j == 0 && n >= H0L && n < H0L + 8) pv[n - H0L] = ld16(rsWS, vKP, sKP, WS_KVP + (n - H0L) * 16384);
          if constexpr (GG == 0 && j == 1 && n == H0C) { asm volatile("" : "+v"(accB[0]), "+v"(accB[1])); STAMP(11); hop1(); STAMP(6); }
        }
        SB();
      SEND
    SEND
    accB[0] = MFMA(cb[0], xe, accB[0]); accB[1] = MFMA(cb[1], xe, accB[1]);
    asm volatile("" : "+v"(accB[0]), "+v"(accB[1]));
    __builtin_amdgcn_s_setprio(GG ? PV : 0);
    SB();
  };
  unsigned ln = vL16, aW = 0;
  auto mkaw = [&]() AI {
    asm volatile("" : "+v"(ln));
    aW = lw2 + (ln & 0x200) + ((ln & 0xc0) << 1) + ((((ln >> 3) & 0x20) | ((ln >> 1) & 0x10)) ^ ((ln >> 1) & 0x40)) + ((ln >> 1) & 8);
  };
  bf16x8 wf[NS][2];
  auto readW = [&](int i) AI {
    const unsigned ra = ring_area(i % RING), la = aW + (ra & 0x30000u);
    wf[i][0] = tr2(la, ra & 0xffffu, (ra & 0xffffu) + 1024); wf[i][1] = tr2(la ^ 64u, ra & 0xffffu, (ra & 0xffffu) + 1024);
  };
  auto pairF = [&](auto p_, const f32x16& a) AI { constexpr int p = decltype(p_)::value; epiSlice<p, 0>(a, rc, es); epiSlice<p, 1>(a, rc, es); epiSlice<p, 2>(a, rc, es); epiSlice<p, 3>(a, rc, es); };
  if constexpr (GG == 0) {
    __builtin_amdgcn_s_setprio(PV);
    if constexpr (AT) { SFOR(q, 6) clri(q); SEND }
    SFOR(s, 30)
      kstep(s_);
    SEND
    if constexpr (!AT) { touch(0); touch(4); }
    if constexpr (AT) WAITV(6);
    WAITL0(); SB(); BAR(); SB();
    STAMP(3);
    kvstep(IntC<0>{});
    readB(0, 0); readB(0, 1); readB(1, 0); readB(1, 1);
    kvstep(IntC<1>{}); kvstep(IntC<3>{});
    if constexpr (AT) { WAITV(2); stflag(E + 1, rsWS, sFown); }
    STAMP(4);
    SB();
    stream();
  } else {
    stream();
    STAMP(3);
    if constexpr (AT) {
      WAITV(0); stflag(E + 1, rsWS, sFown);
      SFOR(s, 21) qstep(s_, accA[0], 1); SEND
      SFOR(s, 21)
        if constexpr (s >= H1S && s < H1S + 8) pv[s - H1S] = ld16(rsWS, vKP, sKP, WS_KVP + (s - H1S) * 16384);
        qstep(s_, accA[1], 2);
      SEND
      STAMP(4); hop1(); STAMP(6);
      SFOR(s, Q3A) qstep(s_, accA[2], 3); SEND
      SB(); BAR(); SB();
      SFOR(s, 21 - Q3A) qstep(IntC<Q3A + s>{}, accA[2], 3); SEND
    } else {
      mkaw();
      SFOR(e, 20)
        if constexpr (e < 12) pairF(IntC<e & 3>{}, accA[e >> 2]); else pairF(IntC<e & 3>{}, accB[(e >> 2) - 3]);
        if constexpr ((e & 3) == 3) st16(es.r, rsWS, vL16, sA2, (e < 12 ? 1 + (e >> 2) : 3 + (e >> 2)) * 1024);
        if constexpr (e == 0) { issueW2(3, 0); issueW2(3, 1); }
        if constexpr ((e & 3) == 2) { SB(); BAR(); SB(); issueW2(4 + (e >> 2), 0); issueW2(4 + (e >> 2), 1); }
      SEND
    }
  }

  if constexpr (AT) {
    __builtin_amdgcn_s_setprio(0);
    STAMP(5);
    auto qexp = [&](f32x16& a, unsigned lh) AI { a = dualexpP(featproj(a, aWQ, lh * 1568, hh), rc.Lr); asm volatile("" : "+v"(a)); SB(); };
#pragma unroll
    for (int i = 0; i < 8; i++) pv[i] = ld16(rsWS, vL16, sKR, WS_KVR + i * 1024);
    qexp(accB[0], 4 + 2 * GG);
    STAMP(7);
    waittag<1024>(pv, rsWS, vL16, sKR + WS_KVR, T16, 0xffffu);
    STAMP(8);
#pragma unroll
    for (int i = 0; i < 4; i++) {
      u32x4 f = {__builtin_amdgcn_perm(pv[2 * i][1], pv[2 * i][0], 0x07060302u), __builtin_amdgcn_perm(pv[2 * i][3], pv[2 * i][2], 0x07060302u),
                 __builtin_amdgcn_perm(pv[2 * i + 1][1], pv[2 * i + 1][0], 0x07060302u), __builtin_amdgcn_perm(pv[2 * i + 1][3], pv[2 * i + 1][2], 0x07060302u)};
      ldsw<u32x4>(LB + w * 4096 + vL16, i * 1024, f);
    }
    WAITL0(); BAR(); MEMFENCE();
    f32x16 op[2], on[2];
    auto omm = [&](const f32x16& phi, unsigned lh, int z) AI {
#pragma unroll
      for (int i = 0; i < 16; i++) { op[z][i] = 0.f; on[z][i] = 0.f; }
      bf16x8 kf[4];
#pragma unroll
      for (int i = 0; i < 4; i++) kf[i] = ldsr<bf16x8>(LB + vL16, lh * 4096 + i * 1024);
#pragma unroll
      for (int i = 0; i < 2; i++) { op[z] = MFMA(kf[i], sub4(phi, i), op[z]); on[z] = MFMA(kf[2 + i], sub4(phi, 2 + i), on[z]); }
      SB();
    };
    auto ofin = [&](unsigned lh, int z) AI {
      const float ia = __builtin_amdgcn_rcpf(op[z][13]), ib = __builtin_amdgcn_rcpf(on[z][13]);
      const float id = __builtin_amdgcn_rcpf(__builtin_fmaf(op[z][12], ia, __builtin_fmaf(on[z][12], ib, 1e-6f)));
      const float ca = xhi(ia * id), cb2 = xhi(ib * id);
      const f32x2 c2 = dup2(ca), d2 = dup2(cb2);
      u32x4 f0, f1;
#pragma unroll
      for (int i = 0; i < 4; i++) {
        const f32x2 u = fma2(sub2(op[z], i), c2, sub2(on[z], i) * d2), v = fma2(sub2(op[z], 4 + i), c2, sub2(on[z], 4 + i) * d2);
        f0[i] = pk2(u[0], u[1]); f1[i] = pk2(v[0], v[1]);
      }
      f1[2] = hh ? 0u : f1[2]; f1[3] = hh ? 0u : f1[3];
      st16(f0, rsWS, vL16, sA2, (112 + 2 * lh) * 1024); st16(f1, rsWS, vL16, sA2, (113 + 2 * lh) * 1024);
      SB();
    };
    if constexpr (GG) {
      omm(accA[0], 1, 0); omm(accA[1], 2, 1); ofin(1, 0); omm(accA[2], 3, 0); ofin(2, 1); omm(accB[0], 6, 1); ofin(3, 0);
      qexp(accB[1], 7); ofin(6, 1); omm(accB[1], 7, 0); ofin(7, 0);
    } else { omm(accA[2], 0, 0); omm(accB[0], 4, 1); ofin(0, 0); qexp(accB[1], 5); ofin(4, 1); omm(accB[1], 5, 0); ofin(5, 0); }
    STAMP(9);
  } else {
    __builtin_amdgcn_s_setprio(0);
    STAMP(5);
    const unsigned sA = WS_A2 + rt2 * 144 * 1024;
    if constexpr (GG == 0) mkaw();
    f32x16 acc[2][2];
#pragma unroll
    for (int i = 0; i < 4; i++)
#pragma unroll
      for (int k = 0; k < 16; k++) acc[i >> 1][i & 1][k] = 0.f;
    u32x4 af[NS][2];
    u32x4 xr;
    auto loadA = [&](int i) AI { af[i][0] = ld16(rsWS, vL16, sA + ksb(i) * 1024, ksi(i) * 1024); af[i][1] = ld16(rsWS, vL16, sA + ksb(i) * 1024, (144 + ksi(i)) * 1024); };
    const unsigned vO = (ln >> 7) * XROW + (ln & 0x70), sO = (rt2 * 32 + 8 * w) * XROW + ci * 128;
    if constexpr (GG == 0) { st16(epiP(accB[0], rc), rsWS, vL16, sA2, 4 * 1024); st16(epiP(accB[1], rc), rsWS, vL16, sA2, 5 * 1024); }
    STAMP(6);
    constexpr int MV = GG ? 7 : 9;
    if constexpr (!(RECY && GG)) waitv<GG ? 7 : 4>();
    SFOR(i, GG ? (RECY ? 0 : 7) : 8) readW(i); SEND
    WAITV(0); stflag(E + 1, rsWS, sFown);
    if constexpr (!GG) readW(8);
    WAITL0(); SB();
    BAR();
    SB(); STAMP(11);
    if constexpr (GG) __builtin_amdgcn_s_setprio(P2G1);
    if constexpr (!(RECY && GG)) {
      SFOR(i, MV)
        if constexpr (i == MV - 2) ldflag();
        issueW2(9 + i, 0); issueW2(9 + i, 1);
      SEND
      asm volatile("s_waitcnt vmcnt(4)" : "+v"(fl1) :: "memory");
    }
    if ((RECY && GG) || !ALL(!nd1 || fl1 == E + 1u)) poll(rsWS, lane * FS, sF, E, nd1 ? 1u : 0u);
    STAMP(7);
#pragma unroll
    for (int j = 0; j < PD; j++) loadA(j);
    asm volatile("buffer_load_dwordx4 %0, %1, %2, %3 offen" : "=v"(xr) : "v"(vO), "s"(rsX), "s"(sO) : "memory");
    SFOR(i, NS)
      asm volatile("s_waitcnt vmcnt(%c2)" : "+v"(af[i][0]), "+v"(af[i][1]) : "i"(later2(i, NS, GG)) : "memory");
      if constexpr (i + 1 >= MV && i + 1 < NS) readW(i + 1);
      if constexpr (GG == 0 && i >= 12) {
        auto stale = [&]() AI {
          unsigned m = UMAX(UMAX(UMAX(af[i][0][0], af[i][0][1]), UMAX(af[i][0][2], af[i][0][3])), UMAX(UMAX(af[i][1][0], af[i][1][1]), UMAX(af[i][1][2], af[i][1][3])));
          return ANY(m == ~0u);
        };
        if (__builtin_expect(stale(), 0)) {
          int it = 0;
#pragma nounroll
          do { __builtin_amdgcn_s_sleep(SLEEPR); rel1<true>(af[i][0], af[i][1], rsWS, vL16, sA + (ksb(i) + ksi(i)) * 1024); } while (++it < (1 << 14) && stale());
          SFOR(u, PD - 1) if constexpr (i + 1 + u < NS) rel1<false>(af[i + 1 + u][0], af[i + 1 + u][1], rsWS, vL16, sA + (ksb(i + 1 + u) + ksi(i + 1 + u)) * 1024); SEND
        }
      }
      if constexpr (GG && i == POFF) __builtin_amdgcn_s_setprio(0);
      if constexpr (i == 6) STAMP(8);
      if constexpr (i == 12) STAMP(9);
      SB();
      if constexpr (i + PD < NS) loadA(i + PD);
#pragma unroll
      for (int mm = 0; mm < 2; mm++) acc[0][mm] = MFMA(wf[i][0], __builtin_bit_cast(bf16x8, af[i][mm]), acc[0][mm]);
#pragma unroll
      for (int mm = 0; mm < 2; mm++) acc[1][mm] = MFMA(wf[i][1], __builtin_bit_cast(bf16x8, af[i][mm]), acc[1][mm]);
      if constexpr (GG == 0 && (i == 9 || i == 10)) { SB(); issueW2(i + 9, 0); issueW2(i + 9, 1); }
      SB();
    SEND
    STAMP(10);
    __builtin_amdgcn_s_setprio(0);
#pragma unroll
    for (int qd = 0; qd < 4; qd++)
#pragma unroll
      for (int k = 0; k < 4; k++) {
        const unsigned ra = ring_area(2 * qd + (k >> 1)) + (k & 1) * 1024;
        ldsw<bf16x8>(((lw2 + vL16) ^ (unsigned)(32 * (4 * (qd >> 1) + k))) + (ra & 0x30000u), ra & 0xffffu, sub4(acc[qd >> 1][qd & 1], k));
      }
    WAITL0(); BAR(); MEMFENCE();
    STAMP(12);
    const unsigned c8 = (ln >> 4) & 7, j2 = (c8 & 4) + 2 * GG + ((c8 >> 1) & 1);
    const unsigned rd = LB + ((0x51407362u >> (4 * j2)) & 7) * 16384 + (c8 & 1) * 1024 + (((8 * wr + (ln >> 7)) << 4) ^ (c8 << 5));
    asm volatile("" : "+v"(xr));
    f32x4 sum[2];
#pragma unroll
    for (int k = 0; k < 2; k++) {
      f32x4 t = {U2F(xr[2 * k] << 16), U2F(xr[2 * k] & 0xffff0000u), U2F(xr[2 * k + 1] << 16), U2F(xr[2 * k + 1] & 0xffff0000u)};
#pragma unroll
      for (int v = 0; v < 8; v++) t += ldsr<f32x4>(rd, v * 2048 + k * 512);
      sum[k] = t;
    }
    u32x4 o = {pk2(sum[0][0], sum[0][1]), pk2(sum[0][2], sum[0][3]), pk2(sum[1][0], sum[1][1]), pk2(sum[1][2], sum[1][3])};
    __builtin_amdgcn_raw_buffer_store_b128(o, mkrs(pout, 2048 * XROW), vO + sO, 0, OAUX);
  }
  STAMPS_OUT();
  };
  if (!attn && g) prog(BoolC<false>{}, IntC<1>{});
  else if (!attn) prog(BoolC<false>{}, IntC<0>{});
  else if (g) prog(BoolC<true>{}, IntC<1>{});
  else prog(BoolC<true>{}, IntC<0>{});
}

extern "C" void kernel_launch(void* const* d_in, const int* in_sizes, int n_in, void* d_out, int out_size, void* d_ws, size_t ws_size,
                              hipStream_t stream) {
  fused_kernel<<<256, 512, 0, stream>>>((const char*)d_in[0], (const char*)d_in[1], (const char*)d_in[5], (const char*)d_in[6], (char*)d_ws,
                                        (const char*)d_in[2], (const char*)d_in[7], (const char*)d_in[3], (char*)d_out);
}
```

```cpp
#define ROT ((cp + 2 * rbl) % 7)
#include <hip/hip_runtime.h>
#include <hip/hip_bf16.h>
#include <utility>

#define DI __device__ __forceinline__
#define LAS __attribute__((address_space(3)))
typedef __attribute__((ext_vector_type(8))) short bf16x8;
typedef __attribute__((ext_vector_type(4))) short s16x4;
typedef __attribute__((ext_vector_type(2))) __bf16 bf2;
typedef __attribute__((ext_vector_type(2))) float f32x2;
typedef __attribute__((ext_vector_type(4))) float f32x4;
typedef __attribute__((ext_vector_type(16))) float f32x16;
typedef __attribute__((ext_vector_type(2))) unsigned u32x2;
typedef __attribute__((ext_vector_type(4))) unsigned u32x4;
typedef __amdgpu_buffer_rsrc_t rsrc_t;
#define F2U(x) __builtin_bit_cast(unsigned, (float)(x))
#define U2F(x) __builtin_bit_cast(float, (unsigned)(x))
#define ALL(x) (__builtin_amdgcn_ballot_w64(!(x)) == 0ull)
#define ANY(x) (__builtin_amdgcn_ballot_w64(x) != 0ull)
#define UMAX(a, b) __builtin_elementwise_max((unsigned)(a), (unsigned)(b))

#define NTX 0
#define NTW 0
#define NT2 0
constexpr unsigned XROW = 896, QROW = 2688, FROW = 3584;
constexpr unsigned FS = 128;
constexpr unsigned WS_EPOCH = 0;
constexpr unsigned WS_GR = 4096;
constexpr unsigned WS_KVP = 131072;
constexpr unsigned WS_A2 = WS_KVP + 16 * 16 * 8192;
constexpr unsigned WS_ST = WS_A2 + 64 * 144 * 1024;
constexpr unsigned WS_FH = WS_ST + 262144;
constexpr unsigned WS_KVR = WS_FH + 2048 * FS;
constexpr unsigned WS_END = WS_KVR + 16 * 8192;
constexpr unsigned L_GU = 65536, L_KV = 131072;
constexpr unsigned L_TAIL = L_KV + 3 * 8192;
constexpr unsigned L_WPK = L_TAIL;
constexpr unsigned L_ST = L_TAIL + 2048;
constexpr unsigned L_CS = L_TAIL + 4096;
constexpr unsigned L_PHI = L_GU + 32768;
constexpr unsigned L_V = L_KV + 16384;
constexpr unsigned L_FREE = 32768;

#define MFMA(a, b, c) __builtin_amdgcn_mfma_f32_32x32x16_bf16((a), (b), (c), 0, 0, 0)
#define MFMA16(a, b, c) __builtin_amdgcn_mfma_f32_16x16x32_bf16((a), (b), (c), 0, 0, 0)
#define AI __attribute__((always_inline))
#define BAR() __builtin_amdgcn_s_barrier()
#define WAITV(n) asm volatile("s_waitcnt vmcnt(" #n ")" ::: "memory")
#define WAITL0() asm volatile("s_waitcnt lgkmcnt(0)" ::: "memory")
#define SB() __builtin_amdgcn_sched_barrier(0)
#define MEMFENCE() asm volatile("" ::: "memory")

template <int N> DI void waitv() { asm volatile("s_waitcnt vmcnt(%c0)" :: "i"(N) : "memory"); }

#define INSZ(x) (x)
DI rsrc_t mkrs(const void* p, unsigned bytes) { return __builtin_amdgcn_make_buffer_rsrc((void*)p, (short)0, (int)bytes, 0x00020000); }
template <bool NT = false> DI void dma(unsigned lds, unsigned limm, unsigned voff, rsrc_t rs, unsigned soff, unsigned simm) {
  unsigned t;
  if constexpr (NT) asm volatile("s_add_u32 m0, %1, %c5\n\ts_add_u32 %0, %4, %c6\n\tbuffer_load_dwordx4 %2, %3, %0 offen nt lds"
               : "=&s"(t) : "s"(lds), "v"(voff), "s"(rs), "s"(soff), "i"(limm), "i"(simm) : "memory", "scc");
  else asm volatile("s_add_u32 m0, %1, %c5\n\ts_add_u32 %0, %4, %c6\n\tbuffer_load_dwordx4 %2, %3, %0 offen lds"
               : "=&s"(t) : "s"(lds), "v"(voff), "s"(rs), "s"(soff), "i"(limm), "i"(simm) : "memory", "scc");
}
DI void dmat(unsigned lds, unsigned voff, rsrc_t rs) {
  asm volatile("s_mov_b32 m0, %0\n\ts_nop 0\n\tbuffer_load_dword %1, %2, 0 offen lds" :: "s"(lds), "v"(voff), "s"(rs) : "memory");
}
DI s16x4 trd(unsigned a) { return __builtin_amdgcn_ds_read_tr16_b64_v4i16((LAS s16x4*)(size_t)a); }
DI s16x4 trd2(unsigned a, unsigned off) { return __builtin_amdgcn_ds_read_tr16_b64_v4i16((LAS s16x4*)((LAS char*)(size_t)a + off)); }
DI bf16x8 tr2(unsigned a, unsigned off0, unsigned off1) {
  LAS char* p = (LAS char*)(size_t)a;
  s16x4 lo = __builtin_amdgcn_ds_read_tr16_b64_v4i16((LAS s16x4*)(p + off0));
  s16x4 hi = __builtin_amdgcn_ds_read_tr16_b64_v4i16((LAS s16x4*)(p + off1));
  return __builtin_shufflevector(lo, hi, 0, 1, 2, 3, 4, 5, 6, 7);
}
template <class T> DI T ldsr(unsigned a, unsigned off) { return *(LAS T*)((LAS char*)(size_t)a + off); }
template <class T> DI void ldsw(unsigned a, unsigned off, T v) { *(LAS T*)((LAS char*)(size_t)a + off) = v; }
DI unsigned pk2(float a, float b) { f32x2 f = {a, b}; return __builtin_bit_cast(unsigned, __builtin_convertvector(f, bf2)); }
DI float rbf(float a) { return U2F(pk2(a, a) << 16); }
DI float dot2(unsigned a, unsigned b, float c) { return __builtin_amdgcn_fdot2_f32_bf16(__builtin_bit_cast(bf2, a), __builtin_bit_cast(bf2, b), c, false); }
DI float xsum(float v) { auto r = __builtin_amdgcn_permlane32_swap(F2U(v), F2U(v), false, false); float a = U2F(r[0]); asm("" : "+v"(a)); return a + U2F(r[1]); }
DI float xmax(float v) { auto r = __builtin_amdgcn_permlane32_swap(F2U(v), F2U(v), false, false); return __builtin_fmaxf(U2F(r[0]), U2F(r[1])); }
DI float xmin(float v) { auto r = __builtin_amdgcn_permlane32_swap(F2U(v), F2U(v), false, false); return __builtin_fminf(U2F(r[0]), U2F(r[1])); }
DI float xhi(float v) { auto r = __builtin_amdgcn_permlane32_swap(F2U(v), F2U(v), false, false); return U2F(r[1]); }
DI void st16(u32x4 v, rsrc_t rs, unsigned voff, unsigned soff, unsigned simm) {
  unsigned t;
  asm volatile("s_add_u32 %0, %4, %c5\n\tbuffer_store_dwordx4 %1, %2, %3, %0 offen sc1\n\ts_nop 1" : "=&s"(t) : "v"(v), "v"(voff), "s"(rs), "s"(soff), "i"(simm) : "memory", "scc");
}
DI void stflag(unsigned v, rsrc_t rs, unsigned soff) {
  unsigned voff = (__builtin_amdgcn_mbcnt_hi(~0u, __builtin_amdgcn_mbcnt_lo(~0u, 0u)) == 0u) ? 0u : 0x80000000u;
  __builtin_amdgcn_raw_buffer_store_b32(v, rs, voff, soff, 16);
}
#define XORCOND (n == 24)
#define PS 0
#define PV 0
#define KEEP2 0
#define S2N 14
#define B1N 23
#define DIST 2
#define P2G1 1
#define OAUX 0
#define RECY 0
#define POFF 99
#define H1S 0
#define H0L 10
#define H0C 22
#define Q3A 1
#define COOP 0
#define SLEEPR 4
#define SLEEPN 2
DI u32x4 ld16(rsrc_t rs, unsigned voff, unsigned soff, unsigned simm) {
  u32x4 r; unsigned t;
  asm volatile("s_add_u32 %1, %4, %c5\n\tbuffer_load_dwordx4 %0, %2, %3, %1 offen sc1" : "=v"(r), "=&s"(t) : "v"(voff), "s"(rs), "s"(soff), "i"(simm) : "memory", "scc");
  return r;
}
template <unsigned STEP> DI void ld8w(u32x4 (&p)[8], rsrc_t rs, unsigned voff, unsigned soff) {
  unsigned t;
#define L1(k) "buffer_load_dwordx4 %" #k ", %9, %10, %8 offen sc1\n\ts_add_u32 %8, %8, %c12\n\t"
  asm volatile("s_mov_b32 %8, %11\n\t" L1(0) L1(1) L1(2) L1(3) L1(4) L1(5) L1(6) L1(7) "s_waitcnt vmcnt(0)"
               : "=&v"(p[0]), "=&v"(p[1]), "=&v"(p[2]), "=&v"(p[3]), "=&v"(p[4]), "=&v"(p[5]), "=&v"(p[6]), "=&v"(p[7]), "=&s"(t)
               : "v"(voff), "s"(rs), "s"(soff), "i"(STEP) : "memory", "scc");
#undef L1
}
DI bool untag(u32x4 (&p)[8], unsigned tag, unsigned mask) {
  unsigned chk = 0;
#pragma unroll
  for (int i = 0; i < 8; i++)
#pragma unroll
    for (int k = 0; k < 4; k++) { p[i][k] ^= tag; chk |= p[i][k]; }
  return ALL((chk & mask) == 0u);
}
template <unsigned STEP> DI void waittag(u32x4 (&p)[8], rsrc_t rs, unsigned voff, unsigned soff, unsigned tag, unsigned mask) {
  asm volatile("s_waitcnt vmcnt(0)" : "+v"(p[0]), "+v"(p[1]), "+v"(p[2]), "+v"(p[3]), "+v"(p[4]), "+v"(p[5]), "+v"(p[6]), "+v"(p[7]) :: "memory");
#pragma nounroll
  for (int it = 0; !untag(p, tag, mask) && it < (1 << 16); ++it) { __builtin_amdgcn_s_sleep(SLEEPN); ld8w<STEP>(p, rs, voff, soff); }
}
template <bool WAIT> DI void rel1(u32x4& a, u32x4& b, rsrc_t rs, unsigned voff, unsigned so) {
  unsigned t;
  if constexpr (WAIT) asm volatile("buffer_load_dwordx4 %0, %3, %4, %5 offen sc1\n\ts_add_u32 %2, %5, 147456\n\tbuffer_load_dwordx4 %1, %3, %4, %2 offen sc1\n\ts_waitcnt vmcnt(0)"
                                   : "+v"(a), "+v"(b), "=&s"(t) : "v"(voff), "s"(rs), "s"(so) : "memory", "scc");
  else asm volatile("buffer_load_dwordx4 %0, %3, %4, %5 offen sc1\n\ts_add_u32 %2, %5, 147456\n\tbuffer_load_dwordx4 %1, %3, %4, %2 offen sc1"
                    : "+v"(a), "+v"(b), "=&s"(t) : "v"(voff), "s"(rs), "s"(so) : "memory", "scc");
}
DI void poll(rsrc_t rs, unsigned voff, unsigned soff, unsigned E, unsigned need) {
  voff = need ? voff : 0x80000000u;
#pragma nounroll
  for (int it = 0; it < (1 << 16); ++it) {
    unsigned v;
    asm volatile("buffer_load_dword %0, %1, %2, %3 offen sc1\n\ts_waitcnt vmcnt(0)" : "=v"(v) : "v"(voff), "s"(rs), "s"(soff) : "memory");
    bool ok = (need == 0u) || ((v - E - need) <= (3u - need));
    if (ALL(ok)) break;
    __builtin_amdgcn_s_sleep(SLEEPN);
  }
}

template <bool B> struct BoolC { static constexpr bool value = B; };
template <int I> struct IntC { static constexpr int value = I; };
template <int... I, class F> DI void sfor_impl(std::integer_sequence<int, I...>, F&& f) { (f(IntC<I>{}), ...); }
template <int N, class F> DI void sfor(F&& f) { sfor_impl(std::make_integer_sequence<int, N>{}, f); }
#define SFOR(n, N) sfor<N>([&](auto n##_) AI { constexpr int n = decltype(n##_)::value;
#define SEND });
struct RowC { float r, K1, K2, Ri, Lr; };

DI unsigned hilo(float v) { float hi = rbf(v); return pk2(hi, v - hi); }
DI float max3(float a, float b, float c) { return __builtin_fmaxf(__builtin_fmaxf(a, b), c); }
DI float min3(float a, float b, float c) { return __builtin_fminf(__builtin_fminf(a, b), c); }
DI unsigned pk2s(float a, float b) { unsigned r; asm("v_cvt_pk_bf16_f32 %0, %1, %2" : "=v"(r) : "v"(a), "v"(b)); return r; }
DI f32x2 sub2(const f32x16& v, int k) { f32x2 t = {v[2 * k], v[2 * k + 1]}; return t; }
DI f32x2 exp2v(f32x2 v) { f32x2 r = {__builtin_amdgcn_exp2f(v[0]), __builtin_amdgcn_exp2f(v[1])}; return r; }
DI f32x2 dup2(float a) { f32x2 r = {a, a}; asm("" : "+v"(r)); return r; }
DI f32x2 fma2(f32x2 a, f32x2 b, f32x2 c) { return __builtin_elementwise_fma(a, b, c); }
DI u32x4 epiP(const f32x16& a, const RowC& c) {
  u32x4 r;
  const f32x2 K1 = dup2(c.K1), K2 = dup2(c.K2), Ri = dup2(c.Ri);
#pragma unroll
  for (int i = 0; i < 4; i++) {
    const f32x2 g = sub2(a, i), d = fma2(exp2v(g * fma2(g * g, K2, K1)), Ri, Ri);
    const f32x2 s = {__builtin_amdgcn_rcpf(d[0]), __builtin_amdgcn_rcpf(d[1])}, h = (g * sub2(a, 4 + i)) * s;
    r[i] = pk2(h[0], h[1]);
  }
  return r;
}
struct EpiS { float x0, x1, m0, m1; u32x4 r; };
template <int P, int PH> DI void epiSlice(const f32x16& a, const RowC& c, EpiS& s) {
  const float g0 = a[2 * P], g1 = a[2 * P + 1];
  if constexpr (PH == 0) { s.x0 = g0 * __builtin_fmaf(g0 * g0, c.K2, c.K1); s.x1 = g1 * __builtin_fmaf(g1 * g1, c.K2, c.K1); }
  if constexpr (PH == 1) { s.x0 = __builtin_amdgcn_exp2f(s.x0); s.x1 = __builtin_amdgcn_exp2f(s.x1); s.m0 = g0 * a[8 + 2 * P]; s.m1 = g1 * a[9 + 2 * P]; }
  if constexpr (PH == 2) { s.x0 = __builtin_amdgcn_rcpf(__builtin_fmaf(s.x0, c.Ri, c.Ri)); s.x1 = __builtin_amdgcn_rcpf(__builtin_fmaf(s.x1, c.Ri, c.Ri)); }
  if constexpr (PH == 3) s.r[P] = pk2s(s.m0 * s.x0, s.m1 * s.x1);
}
DI f32x16 featproj2(const f32x16& a, bf16x8 w0, bf16x8 w1, bool hh) {
  u32x4 q0, q1;
#pragma unroll
  for (int i = 0; i < 4; i++) { q0[i] = pk2(a[2 * i], a[2 * i + 1]); q1[i] = pk2(a[8 + 2 * i], a[9 + 2 * i]); }
  q1[2] = hh ? 0u : q1[2]; q1[3] = hh ? 0u : q1[3];
  f32x16 z;
#pragma unroll
  for (int i = 0; i < 16; i++) z[i] = 0.f;
  z = MFMA(w0, __builtin_bit_cast(bf16x8, q0), z);
  z = MFMA(w1, __builtin_bit_cast(bf16x8, q1), z);
  return z;
}
DI f32x16 featproj(const f32x16& a, unsigned wpaddr, unsigned off, bool hh) {
  return featproj2(a, tr2(wpaddr, off, off + 8 * 56), tr2(wpaddr, off + 16 * 56, off + 24 * 56), hh);
}
DI float max16(const f32x16& p) { float m = max3(p[0], p[1], p[2]); _Pragma("unroll") for (int i = 3; i < 15; i += 2) m = max3(m, p[i], p[i + 1]); return xmax(__builtin_fmaxf(m, p[15])); }
DI float min16(const f32x16& p) { float m = min3(p[0], p[1], p[2]); _Pragma("unroll") for (int i = 3; i < 15; i += 2) m = min3(m, p[i], p[i + 1]); return xmin(__builtin_fminf(m, p[15])); }
DI f32x16 dualexpP(const f32x16& p, float Lr) {
  const f32x2 bx = dup2(-max16(p) * Lr), bn = dup2(min16(p) * Lr), L2 = dup2(Lr);
  f32x16 o;
#pragma unroll
  for (int i = 0; i < 8; i++) {
    const f32x2 ep = exp2v(fma2(sub2(p, i), L2, bx)), en = exp2v(fma2(sub2(p, i), -L2, bn));
    o[i] = U2F(pk2(ep[0], ep[1])); o[8 + i] = U2F(pk2(en[0], en[1]));
  }
  return o;
}
struct SmS { f32x16 p, e; float en[16], bx, bn, sp, sn, tp, tn; bf16x8 w0, w1; };
DI bf16x8 sub4(const f32x16& v, int k) { f32x4 t = {v[4 * k], v[4 * k + 1], v[4 * k + 2], v[4 * k + 3]}; return __builtin_bit_cast(bf16x8, t); }

DI constexpr unsigned lc1_base(int ch) { return ch == 6 ? L_KV : ((ch & 1) ? L_GU : 0u) + (ch < 2 ? 49152u : ch < 4 ? 0u : 16384u); }
template <int CTRL, int RM> DI float dppadd(float v) { return v + U2F(__builtin_amdgcn_update_dpp(0u, F2U(v), CTRL, RM, 0xf, false)); }
DI float wavesum63(float v) {
  v = dppadd<0x111, 0xf>(v); v = dppadd<0x112, 0xf>(v); v = dppadd<0x114, 0xf>(v); v = dppadd<0x118, 0xf>(v);
  v = dppadd<0x142, 0xa>(v); return dppadd<0x143, 0xc>(v);
}

#define STAMP(i)
#define STAMPS_OUT()
#define CSTAMP(i)

#define PDV 5
constexpr int PD = PDV, RING = 9;
constexpr int later2(int i, int NS, int GG) {
  const int younger = 2 * ((i + PD <= NS ? i + PD : NS) - 1 - i);
  int extra = i < PD ? 1 : 0;
  if (GG == 0 && (i == 10 || i == 11)) extra += 2 * (i - 9);
  return younger + extra;
}
DI constexpr unsigned ring_area(int j) { return j == 0 ? L_FREE : j == 1 ? L_PHI : lc1_base(j - 2); }
constexpr int cumG1(int s) { return s < 4 ? 0 : s < 22 ? (s - 4) / 2 : (9 + (s - 22) * 5 / 4 < 48 ? 9 + (s - 22) * 5 / 4 : 48); }

#define CODE_BYTES 65536
__global__ __launch_bounds__(512, 2) void fused_kernel(const char* px_, const char* pqkv_, const char* pwg_, const char* pwu_, char* ws_, const char* pphi_,
                                                       const char* pwd_, const char* pwo_, char* pout_) {
  typedef __attribute__((ext_vector_type(8))) unsigned long long u64x8;
  u64x8 ka; unsigned long long kb;
  asm volatile("s_load_dwordx16 %0, %2, 0\n\ts_load_dwordx2 %1, %2, 64" : "=&s"(ka), "=&s"(kb) : "s"(__builtin_amdgcn_kernarg_segment_ptr()));
  __shared__ __attribute__((aligned(16))) char smem[163840];
  const unsigned LB = (unsigned)(size_t)smem;
  const unsigned tid = __builtin_amdgcn_workitem_id_x(), lane = tid & 63;
  const unsigned w = __builtin_amdgcn_readfirstlane(tid >> 6), g = w >> 2, wr = w & 3;
  const unsigned b = __builtin_amdgcn_workgroup_id_x(), xcd = b & 7, bi = b >> 3, grp = xcd >> 1, half = xcd & 1, rbl = bi & 3, cp = bi >> 2, ci = cp - 1;
  const unsigned rb = 4 * grp + rbl, c = 7 * half + ci;
  const unsigned head = 8 * half + (((half ? 0x76425310u : 0x75316420u) >> (4 * cp)) & 7);
  const bool attn = cp == 0;
  STAMP(0); CSTAMP(14);
  const unsigned wstr = attn ? QROW : FROW;
  const unsigned sG0 = half * (attn ? 448 : 1792) + (attn ? 0 : ci * 256), sX0 = rb * 128 * XROW;
  unsigned vx0, vx1, vG, vU, vKV;
  auto voffs = [&](unsigned ln) AI {
    const unsigned r8 = ln >> 3, c8 = ln & 7, sa = (r8 >> 1) & 1, lc = c8 ^ (4 * sa);
    vx0 = (16 * w + r8) * XROW + ((c8 ^ (r8 >> 1)) << 4); vx1 = (vx0 ^ 64u) + 8 * XROW;
    const unsigned cg = attn ? lc : (c8 ^ (2 * sa)), cu = attn ? (lc >= 6 ? lc - 2 : lc) : (cg ^ 4);
    vG = (8 * w + r8) * wstr + (cg << 4); vU = (8 * w + r8) * wstr + (cu << 4);
    vKV = (8 * w + r8) * QROW + 896 * (1 + (lc >> 2)) + ((lc & 3) << 4) + ((56 * head) & ~15u);
  };
  voffs(lane);
  const unsigned lw1 = LB + w * 1024, lw2 = LB + w * 2048;
  const unsigned rot = ROT;
  const unsigned srow = 128 * rb + 64 * half + 8 * cp + w;
  unsigned vrow = lane < 56 ? lane * 16 : 0x80000000u;
  asm volatile("s_waitcnt lgkmcnt(0)" : "+s"(ka), "+s"(kb), "+v"(vx0), "+v"(vx1), "+v"(vG), "+v"(vU), "+v"(vKV), "+v"(vrow));
  const char *px = (const char*)ka[0], *pqkv = (const char*)ka[1], *pwg = (const char*)ka[2], *pwu = (const char*)ka[3], *pphi = (const char*)ka[5],
             *pwd = (const char*)ka[6], *pwo = (const char*)ka[7];
  char *ws = (char*)ka[4], *pout = (char*)kb;
  const rsrc_t rsX = mkrs(px, INSZ(2048 * XROW)), rsQ = mkrs(pqkv, INSZ(448 * QROW));
  const rsrc_t rsG = mkrs(attn ? pqkv : pwg, INSZ(448 * wstr)), rsU = mkrs(attn ? pqkv + 128 : pwu, INSZ(448 * wstr - (attn ? 128 : 0)));
  auto cidx = [&](int st) AI -> unsigned { unsigned t = rot + st; return t >= 7 ? t - 7 : t; };
  auto issueA = [&](int st, int k) AI {
    const unsigned sl = st & 3, ch = cidx(st);
    if (k == 0) dma<NTX>(lw2, sl * 16384, vx0, rsX, sX0 + ch * 128, 0);
    if (k == 1) dma<NTX>(lw2, sl * 16384 + 1024, vx1, rsX, sX0 + ch * 128, 0);
    if (k == 2) dma<NTW>(lw1, L_GU + sl * 16384, vG, rsG, sG0 + ch * 64 * wstr, 0);
    if (k == 3) dma<NTW>(lw1, L_GU + sl * 16384 + 8192, vU, rsU, sG0 + ch * 64 * wstr, 0);
    if (k == 4) dma<NTW>(lw1, L_KV + sl * 8192, vKV, rsQ, ch * 64 * QROW, 0);
  };
  const rsrc_t rsWS = mkrs(ws, WS_END);
  unsigned Ev, cpf; u32x4 xrw;
  asm volatile("buffer_load_dwordx4 %0, %1, %2, %3 offen" : "=v"(xrw) : "v"(vrow), "s"(rsX), "s"(srow * XROW) : "memory");
  asm volatile("buffer_load_dword %0, off, %1, 0 sc1" : "=v"(Ev) : "s"(rsWS) : "memory");
  {
    unsigned long long pc; asm volatile("s_getpc_b64 %0" : "=s"(pc));
    const unsigned vo = w == 0 && lane < 24 ? (bi * 24 + lane) * 128 : 0x80000000u;
    asm volatile("buffer_load_dword %0, %1, %2, 0 offen" : "=v"(cpf) : "v"(vo), "s"(mkrs((const void*)pc, CODE_BYTES)) : "memory");
  }
#pragma unroll
  for (int k = 0; k < 5; k++) issueA(0, k);
  asm volatile("s_waitcnt vmcnt(%c3)" : "+v"(xrw), "+v"(Ev), "+v"(cpf) : "i"(5 + COOP) : "memory");
#pragma unroll
  for (int k = 0; k < 5; k++) issueA(1, k);
  issueA(2, 0);
  const unsigned E = __builtin_amdgcn_readfirstlane(Ev);
  {
    float s1 = 0.f, s2 = 0.f;
#pragma unroll
    for (int i = 0; i < 4; i++) { s1 = dot2(xrw[i], 0x3f803f80u, s1); s2 = dot2(xrw[i], xrw[i], s2); }
    s1 = wavesum63(s1); s2 = wavesum63(s2);
    const float mean = s1 * (1.f / 448.f), var = __builtin_fmaxf(s2 * (1.f / 448.f) - mean * mean, 0.f);
    u32x2 gr = {pk2(-mean, __builtin_amdgcn_sqrtf(rbf(rbf(var) + 1e-5f))), E + 1};
    __builtin_amdgcn_raw_buffer_store_b64(gr, rsWS, lane == 63 ? 0u : 0x80000000u, WS_GR + srow * 8, 16);
  }

  auto prog = [&](auto at_, auto gg_) AI {
  constexpr bool AT = decltype(at_)::value; constexpr int GG = decltype(gg_)::value;
  constexpr unsigned lcstep = AT ? 224 : 128;
  unsigned lane;
  asm volatile("v_mbcnt_lo_u32_b32 %0, -1, 0\n\tv_mbcnt_hi_u32_b32 %0, -1, %0" : "=v"(lane));
  __builtin_assume(lane < 64);
  voffs(lane);
  auto issueL = [&](int hc) AI {
    const int st = hc >> 1; const unsigned ch = cidx(st);
    if (hc & 1) dma<NTW>(lw1, lc1_base(st) + 8192, vU, rsU, sG0 + ch * 64 * wstr, lcstep);
    else dma<NTW>(lw1, lc1_base(st), vG, rsG, sG0 + ch * 64 * wstr, lcstep);
  };
  auto issueG = [&](int bar, int k) AI {
    if (bar == 0) issueA(2 + k / 5, k % 5);
    else if (bar <= 3) issueA(bar + 3, k);
    else if (k < 4) issueL(4 * (bar - 4) + k);
    else if (bar == 4) dma(LB + (w & 1) * 1024, L_WPK, lane * 16, mkrs(pphi, 16 * 1568), head * 1568 + (w & 1) * 1024, 0);
    else issueL(12 + (bar - 5));
  };

  const unsigned rt = 4 * rb + wr;
  const unsigned sA2 = WS_A2 + rt * 144 * 1024 + (AT ? half * 16384 : c * 8192), vL16 = lane * 16;
  const u32x4 sent = {~0u, ~0u, ~0u, ~0u};
  auto clr = [&](int ks) AI { st16(sent, rsWS, vL16, sA2, ks * 1024); };

  auto clri = [&](int q) AI { clr(GG ? (q < 6 ? 114 + q : 118 + q) : (q < 2 ? 112 + q : 118 + q)); };

  const bool hh = lane >= 32;
  const unsigned hu = lane >> 5, blk = (lane >> 4) & 1, q4 = (lane >> 2) & 3, p4 = lane & 3, ta = (q4 >> 1) & 1, l31 = lane & 31;
  const unsigned perm = wr ^ (2 * GG);
  const unsigned rowoff = (8 * hu + q4) * 128 + (perm << 11);
  const unsigned pc = (4 * blk + p4) < 6 ? (4 * blk + p4) : 6;
  auto tileaddr = [&](unsigned t) AI -> unsigned {
    if constexpr (AT) { unsigned o = 56 * t + 8 * pc; return (o >> 7) * 8192 + ((((o >> 4) & 7) ^ (4 * ta)) << 4) + (o & 8) + rowoff; }
    else return blk * 8192 + (((2 * t + (p4 >> 1)) ^ (4 * blk + 2 * ta)) << 4) + 8 * (p4 & 1) + rowoff;
  };
  unsigned aA[3], aB[2];
  {
    unsigned o = (head & 1) * 8 + 8 * pc;
    unsigned ak = L_KV + (((o >> 4) ^ (4 * ta)) << 4) + (o & 8) + rowoff;
    aA[0] = LB + (GG ? L_GU + tileaddr(1) : ak);
    aA[1] = LB + (GG ? L_GU + tileaddr(2) : (ak ^ 64u));
    aA[2] = LB + L_GU + tileaddr(GG ? 3 : 0);
    aB[0] = LB + tileaddr(2 * GG); aB[1] = LB + tileaddr(2 * GG + 1);
  }
  const unsigned xrow = 32 * wr + l31;
  const unsigned aX = LB + xrow * 128 + (((2 * perm + hu) ^ ((xrow >> 1) & 7)) << 4);
  const unsigned selv = (((lane >> 4) ^ (lane >> 2)) & 1) ? 0u : 0x3f803f80u;
  const u32x4 sel4 = {selv, selv, selv, selv};
  const bf16x8 sel = __builtin_bit_cast(bf16x8, sel4);

  bf16x8 xf[28], wa[2][3], wb[8][2];
  f32x16 accA[3], accB[2];
  f32x4 csA[3], csB[2];
#pragma unroll
  for (int j = 0; j < 3; j++) { for (int i = 0; i < 16; i++) accA[j][i] = 0.f; for (int i = 0; i < 4; i++) csA[j][i] = 0.f; }
#pragma unroll
  for (int j = 0; j < 2; j++) { for (int i = 0; i < 16; i++) accB[j][i] = 0.f; for (int i = 0; i < 4; i++) csB[j][i] = 0.f; }
  u32x2 grn;

  const rsrc_t rsWD = mkrs(pwd, INSZ(1792 * XROW)), rsWO = mkrs(pwo, INSZ(448 * XROW));
  const unsigned vW2 = (lane >> 3) * XROW + (((lane & 7) ^ ((lane >> 2) & 4)) << 4) + ci * 128;
  constexpr int NS = GG ? 16 : 20;
#define RHO ((rbl + cp) & 3)
  const unsigned rho = RHO;
  auto t_of = [](int i) constexpr { return (0x54763210u >> (4 * (i >> 1))) & 7; };
  auto ksb = [&](int i) AI -> unsigned {
    if (GG) return 16 * wr + ((t_of(i) & 3) ^ rho);
    if (i < 12) { const unsigned cc = i % 6 + rho + (rho >> 1); return wr + 8 * (cc >= 6 ? cc - 6 : cc); }
    return 8 * wr + 2 * (((i - 12) >> 1) ^ rho);
  };
  auto ksi = [&](int i) AI -> unsigned { return GG ? 8 * (i & 1) + (t_of(i) & 4) : i < 12 ? 64 + 4 * (i / 6) : 112 + (i & 1); };
  auto issueW2 = [&](int i, int hf) AI {
    if (GG == 0 && i >= 12) dma<NT2>(lw2, ring_area(i % RING) + hf * 1024, vW2, rsWO, ksb(i) * 14 * XROW, (16 * (i & 1) + 8 * hf) * XROW);
    else dma<NT2>(lw2, ring_area(i % RING) + hf * 1024, vW2, rsWD, ksb(i) * 16 * XROW, (ksi(i) * 16 + 8 * hf) * XROW);
  };

  const unsigned aCSw = LB + L_CS + l31 * 16 + wr * 4 + GG * 2048, aCSr = LB + L_CS + l31 * 16 + GG * 2048;
  auto readX = [&](int n) AI { xf[n] = ldsr<bf16x8>(aX ^ (unsigned)((n & 3) << 5), ((n >> 2) & 3) * 16384); };
  auto readA = [&](int n, int j) AI {
    const unsigned o = ((n >> 2) & 3) * ((GG == 0 && j < 2) ? 8192 : 16384);
    wa[n & 1][j] = tr2(aA[j] ^ (unsigned)((n & 3) << 11), o, o + 512);
  };
  auto readB = [&](int n, int j) AI {
    const unsigned base = lc1_base(n >> 2);
    if (XORCOND) asm volatile("" : "+v"(aB[j]));
    wb[n & 7][j] = tr2(aB[j] ^ (((n & 3) << 11) | (base & 0x30000u)), base & 0xffffu, (base & 0xffffu) + 512);
  };
  RowC rc; bf16x8 xe; unsigned denb;
  auto rowconst = [&]() AI {
#pragma nounroll
    for (int it = 0; it < (1 << 16) && !ALL(grn[1] == E + 1); ++it) {
      __builtin_amdgcn_s_sleep(SLEEPN);
      asm volatile("buffer_load_dwordx2 %0, %1, %2, %3 offen sc1\n\ts_waitcnt vmcnt(0)" : "=v"(grn) : "v"(xrow * 8), "s"(rsWS), "s"(WS_GR + rb * 1024) : "memory");
    }
    const float den = U2F(grn[0] & 0xffff0000u);
    const unsigned nm = hh ? 0u : __builtin_amdgcn_perm(grn[0], grn[0], 0x01000100u);
    u32x4 t = {nm, nm, nm, nm}; xe = __builtin_bit_cast(bf16x8, t);
    denb = grn[0] >> 16;
    rc.r = __builtin_amdgcn_rcpf(den);
    const float k1 = -2.f * 1.4426950408889634f * 0.7978845608028654f;
    rc.K1 = k1 * rc.r; rc.K2 = (k1 * 0.044715f) * rc.r * (rc.r * rc.r); rc.Ri = den * den; rc.Lr = 1.4426950408889634f * rc.r;
  };
  waitv<7 + COOP>();
  BAR(); MEMFENCE();
  STAMP(1);
  readX(0); readA(0, 0); readA(0, 1); readA(0, 2);
  SFOR(n, 28)
    SB();
    if constexpr (n == 27) {
#pragma unroll
      for (int j = 0; j < 3; j++) ldsw<unsigned>(aCSw, j * 512, hilo(csA[j][0]));
    }
    if constexpr ((n & 3) == 3) {
      if constexpr (n == 27) WAITV(0);
      else if constexpr (n == 23) asm volatile("s_waitcnt vmcnt(10)" : "+v"(grn) :: "memory");
      else if constexpr (n == 3 || n == 15 || n == 19) WAITV(11); else WAITV(10);
      { WAITL0(); BAR(); MEMFENCE(); }
      if constexpr (n == 27) STAMP(2);
    }
    constexpr int bar = (n + 1) >> 2, ph = (n + 1) & 3;
#pragma unroll
    for (int j = 0; j < 3; j++) {
      if constexpr (n < 27) { if (j == 0) readX(n + 1); readA(n + 1, j); }
      else { wa[0][j] = ldsr<bf16x8>(aCSr, j * 512); if (GG == 1 && j < 2) { readB(j, 0); readB(j, 1); } }
      SB();
      accA[j] = MFMA(wa[n & 1][j], xf[n], accA[j]);
      const int slot = 3 * ph + j;
      if (bar == 0) issueG(0, slot - 2);
      else if (bar == 6) { if (slot < 5) issueG(bar, slot); }
      else if (bar <= 6) { if (slot == 0) issueG(bar, 0); if (slot == 2) issueG(bar, 1); if (slot == 5) issueG(bar, 2); if (slot == 7) issueG(bar, 3); if (slot == 10) issueG(bar, 4); }
      else if (j < 2) {
        if constexpr (AT) dma(lw2, L_FREE + j * 1024, lane * 16, mkrs(pphi, 16 * 1568), half * 8 * 1568 + w * 2048, j * 1024);
        else issueW2(0, j);
      }
      if ((n & 3) == 0) csA[j] = MFMA16(sel, wa[n & 1][j], csA[j]);
      if (n == 25 && j == 0) rowconst();
      if (n == 14 && j == 2) asm volatile("buffer_load_dwordx2 %0, %1, %2, %3 offen sc1" : "=v"(grn) : "v"(xrow * 8), "s"(rsWS), "s"(WS_GR + rb * 1024) : "memory");
      SB();
    }
  SEND
#pragma unroll
  for (int j = 0; j < 3; j++) accA[j] = MFMA(wa[0][j], xe, accA[j]);
  asm volatile("" : "+v"(accA[0]), "+v"(accA[1]), "+v"(accA[2]));
  SB();

  const unsigned sFown = WS_FH + (rt * 32 + (AT ? 28 + 2 * half : 2 * c) + GG) * FS;
  const unsigned aWP = LB + (4 * hu + q4) * 56 + 8 * pc;
  const unsigned T12 = (E + 1) & 0xfffu, T16 = (E + 1) & 0xffffu;
  const unsigned rt2 = 2 * (8 * grp + 4 * half + rbl);
  const unsigned sF = WS_FH + rt2 * 32 * FS;
  const unsigned fc = l31 >> 1;
  const bool nd1 = GG ? (fc >> 1) == wr : (fc >= 8 && fc < 14) || fc == 14 + (wr >> 1);
  const unsigned aIW = LB + L_PHI + xrow * 64 + ((hu ^ ((xrow >> 1) & 7)) << 3);
  SmS sm;
  unsigned aWK = aWP + L_WPK, aWQ = aWP + L_FREE;
  asm volatile("" : "+v"(aWK)); if constexpr (AT) asm volatile("" : "+v"(aWQ));
  auto kstep = [&](auto s_) AI {
    constexpr int s = decltype(s_)::value;
    if constexpr (s == 0) { sm.w0 = tr2(aWK, 0, 8 * 56); sm.w1 = tr2(aWK, 16 * 56, 24 * 56); }
    if constexpr (s == 1) sm.p = featproj2(accA[0], sm.w0, sm.w1, hh);
    if constexpr (s == 2 || s == 3) {
#pragma unroll
      for (int k = 2 * s - 4; k < 2 * s - 2; k++) {
        u32x2 vv = {pk2(accA[1][4 * k], accA[1][4 * k + 1]), pk2(accA[1][4 * k + 2], accA[1][4 * k + 3])};
        if (k == 3) vv[0] = hh ? denb : vv[0];
        ldsw<u32x2>(aIW ^ (unsigned)(k << 4), L_V - L_PHI, vv);
      }
    }
    if constexpr (s == 4) sm.bx = -max16(sm.p) * rc.Lr;
    if constexpr (s == 5) sm.bn = min16(sm.p) * rc.Lr;
    if constexpr (s >= 6 && s < 22) {
      constexpr int i = s - 6;
      sm.e[i] = __builtin_amdgcn_exp2f(__builtin_fmaf(sm.p[i], rc.Lr, sm.bx)); sm.en[i] = __builtin_amdgcn_exp2f(__builtin_fmaf(sm.p[i], -rc.Lr, sm.bn));
      if constexpr (i == 0) { sm.sp = sm.e[0]; sm.sn = sm.en[0]; } else if constexpr (i < 12) { sm.sp += sm.e[i]; sm.sn += sm.en[i]; }
      else if constexpr (i == 12) { sm.tp = sm.e[12]; sm.tn = sm.en[12]; } else { sm.tp += sm.e[i]; sm.tn += sm.en[i]; }
    }
    if constexpr (s == 22) { sm.sp = __builtin_amdgcn_rcpf(xsum(sm.sp + (hh ? 0.f : sm.tp))) * rc.r; sm.sn = __builtin_amdgcn_rcpf(xsum(sm.sn + (hh ? 0.f : sm.tn))) * rc.r; }
    if constexpr (s >= 23) {
#pragma unroll
      for (int u = (s == 29 ? 6 : s - 23); u <= (s == 29 ? 7 : s - 23); u++) {
        const int k = u >> 1;
        if (u & 1) { u32x2 t = {pk2s(sm.en[4 * k] * sm.sn, sm.en[4 * k + 1] * sm.sn), pk2s(sm.en[4 * k + 2] * sm.sn, sm.en[4 * k + 3] * sm.sn)}; ldsw<u32x2>(aIW ^ (unsigned)(k << 4), 8192, t); }
        else { u32x2 t = {pk2s(sm.e[4 * k] * sm.sp, sm.e[4 * k + 1] * sm.sp), pk2s(sm.e[4 * k + 2] * sm.sp, sm.e[4 * k + 3] * sm.sp)}; ldsw<u32x2>(aIW ^ (unsigned)(k << 4), 0, t); }
      }
    }
  };
  auto qstep = [&](auto s_, f32x16& a, unsigned lh) AI {
    constexpr int s = decltype(s_)::value;
    if constexpr (s == 0) { sm.w0 = tr2(aWQ, lh * 1568, lh * 1568 + 8 * 56); sm.w1 = tr2(aWQ, lh * 1568 + 16 * 56, lh * 1568 + 24 * 56); }
    if constexpr (s == 1) sm.p = featproj2(a, sm.w0, sm.w1, hh);
    if constexpr (s == 3) sm.bx = -max16(sm.p) * rc.Lr;
    if constexpr (s == 4) sm.bn = min16(sm.p) * rc.Lr;
    if constexpr (s >= 5 && s < 21) {
      constexpr int i = (s - 5) >> 1;
      float t0, t1;
      if constexpr ((s - 5) & 1) { t0 = __builtin_fmaf(sm.p[2 * i], -rc.Lr, sm.bn); t1 = __builtin_fmaf(sm.p[2 * i + 1], -rc.Lr, sm.bn); }
      else { t0 = __builtin_fmaf(sm.p[2 * i], rc.Lr, sm.bx); t1 = __builtin_fmaf(sm.p[2 * i + 1], rc.Lr, sm.bx); }
      a[8 * ((s - 5) & 1) + i] = U2F(pk2(__builtin_amdgcn_exp2f(t0), __builtin_amdgcn_exp2f(t1)));
      if constexpr (s == 20) asm volatile("" : "+v"(a));
    }
  };
  bf16x8 ka_[4], kb_[2][4]; f32x4 kd[2];
  auto kvstep = [&](auto s_) AI {
    constexpr int s = decltype(s_)::value;
    if constexpr (s == 0) {
      const unsigned g4 = lane >> 4, r0 = 8 * g4 + q4, sw = 4 * blk + (q4 >> 1);
      unsigned b0 = LB + L_PHI + r0 * 64 + ((p4 ^ sw) << 3);
      unsigned a0 = (b0 ^ ((wr >> 1) << 5)) + (wr & 1) * 8192;
#pragma unroll
      for (int k = 0; k < 4; k++) {
        ka_[k] = __builtin_shufflevector(trd2(a0, k * 2048), trd2(a0 ^ 16u, k * 2048 + 256), 0, 1, 2, 3, 4, 5, 6, 7);
        kb_[0][k] = __builtin_shufflevector(trd2(b0, L_V - L_PHI + k * 2048), trd2(b0 ^ 16u, L_V - L_PHI + k * 2048 + 256), 0, 1, 2, 3, 4, 5, 6, 7);
        kb_[1][k] = __builtin_shufflevector(trd2(b0 ^ 32u, L_V - L_PHI + k * 2048), trd2(b0 ^ 48u, L_V - L_PHI + k * 2048 + 256), 0, 1, 2, 3, 4, 5, 6, 7);
      }
    }
    if constexpr (s == 1) {
      f32x4 z = {0.f, 0.f, 0.f, 0.f}; kd[0] = z; kd[1] = z;
#pragma unroll
      for (int k = 0; k < 4; k++) { kd[0] = MFMA16(ka_[k], kb_[0][k], kd[0]); kd[1] = MFMA16(ka_[k], kb_[1][k], kd[1]); }
    }
    if constexpr (s == 3) {
#pragma unroll
      for (int d = 0; d < 2; d++) {
        u32x4 t;
#pragma unroll
        for (int i = 0; i < 4; i++) t[i] = (F2U(kd[d][i]) & ~0xfffu) | T12;
        st16(t, rsWS, (lane >> 4) * 512 + (lane & 15) * 16, ((head * 16 + rb) * 2 + (wr & 1)) * 4096 + (wr >> 1) * 2048, WS_KVP + d * 256);
      }
    }
  };
  auto qexp2 = [&](f32x16& a0, unsigned l0, f32x16& a1, unsigned l1) AI {
    const f32x16 p0 = featproj(a0, aWQ, l0 * 1568, hh), p1 = featproj(a1, aWQ, l1 * 1568, hh);
    a0 = dualexpP(p0, rc.Lr); a1 = dualexpP(p1, rc.Lr); asm volatile("" : "+v"(a0), "+v"(a1));
  };

  unsigned fl1 = 0;
  EpiS es;
  u32x4 pv[8];
  const unsigned vKP = l31 * 16 + hu * 8192, sKP = (rb * 32 + half) * 4096 + wr * 1024 + GG * 512, sKR = (8 * half + w) * 8192;
  auto hop1 = [&]() AI {
    waittag<16384>(pv, rsWS, vKP, sKP + WS_KVP, T12, 0xfffu);
    u32x4 o;
#pragma unroll
    for (int k = 0; k < 4; k++) {
      float sum = U2F(pv[0][k]);
#pragma unroll
      for (int i = 1; i < 8; i++) sum += U2F(pv[i][k]);
      sum = xsum(sum);
      sum = l31 == 29 ? 1.f : sum;
      if (GG == 1) sum = wr == 3 ? 0.f : sum;
      o[k] = (pk2s(sum, sum) & 0xffff0000u) | T16;
    }
    st16(o, rsWS, hh ? 0x80000000u : l31 * 16, (rb * 2 + half) * 4096 + wr * 1024 + GG * 512, WS_KVR);
    if (b == 0 && w == 4) stflag(E + 4, rsWS, WS_EPOCH);
    SB();
  };
  bf16x8 cb[2];
  auto ldflag = [&]() AI { asm volatile("buffer_load_dword %0, %1, %2, %3 offen sc1" : "=v"(fl1) : "v"(nd1 ? lane * FS : 0x80000000u), "s"(rsWS), "s"(sF) : "memory"); };
  auto touch = [&](unsigned wo4) AI {
    const unsigned rw = (rbl * 8 + w + wo4) * 44 + lane;
    const bool dn = rw < 896;
    const unsigned vo = lane < 44 ? (dn ? ((rw >> 6) * 128 + 64 + (rw & 63)) : rw - 896) * XROW + ci * 128 : 0x80000000u;
    asm volatile("s_add_u32 m0, %0, %c5\n\ts_nop 0\n\tbuffer_load_dword %1, %3, 0 offen lds\n\tbuffer_load_dword %2, %4, 0 offen lds"
                 :: "s"(LB + w * 256), "v"(dn ? vo : 0x80000000u), "v"(dn ? 0x80000000u : vo), "s"(rsWD), "s"(rsWO), "i"(L_ST) : "memory", "scc");
  };
  auto stream = [&]() AI {
    __builtin_amdgcn_s_setprio(PS);
    SFOR(n, 28)
      if constexpr (n == 27) { ldsw<unsigned>(aCSw, 3 * 512, hilo(csB[0][0])); ldsw<unsigned>(aCSw, 0, hilo(csB[1][0])); WAITL0(); BAR(); MEMFENCE(); }
      else if constexpr (GG == 1) { if constexpr (n == S2N || (!AT && n == B1N)) { if constexpr (AT) WAITV(10); BAR(); MEMFENCE(); } }
      else if constexpr (AT ? n == 7 : (n & 3) == 3) { asm volatile("s_waitcnt lgkmcnt(%c0)" :: "i"(4 * DIST - 4) : "memory"); BAR(); MEMFENCE(); }
      SFOR(j, 2)
        constexpr int q = 2 * n + j;
        if constexpr (DIST == 3 && n == 0) readB(2, j);
        if constexpr (n + DIST < 28) readB(n + DIST, j);
        if constexpr (n == 27) cb[j] = ldsr<bf16x8>(aCSr, j ? 0 : 3 * 512);
        SB();
        accB[j] = MFMA(wb[n & 7][j], xf[n], accB[j]);
        if constexpr ((n & 3) == 0) csB[j] = MFMA16(sel, wb[n & 7][j], csB[j]);
        if constexpr (!AT) {

          if constexpr (GG == 1 && j == 0 && n >= B1N && n < B1N + 4) issueW2(1 + ((n - B1N) >> 1), (n - B1N) & 1);

          if constexpr (GG == 0) {
            if constexpr (j == 1 && n < 16) { epiSlice<(n >> 2) & 3, n & 3>(accA[2], rc, es); if constexpr (n == 15) st16(es.r, rsWS, vL16, sA2, 0); }
            if constexpr (j == 0 && n >= 3 && n < 7) issueW2(1 + ((n - 3) >> 1), (n - 3) & 1);
            if constexpr (j == 0 && n >= 7 && n < 27 && (n & 1)) issueW2(1 + ((n + 1) >> 2), ((n + 1) >> 1) & 1);
            if constexpr (n == 27) issueW2(8, j);
          }
        } else {
          if constexpr (GG == 1 && j == 0 && n < 10) clri(n);
          if constexpr (GG == 0 && j == 1 && n < 21) qstep(n_, accA[2], 0);
          if constexpr (GG == 0 && j == 0 && n >= H0L && n < H0L + 8) pv[n - H0L] = ld16(rsWS, vKP, sKP, WS_KVP + (n - H0L) * 16384);
          if constexpr (GG == 0 && j == 1 && n == H0C) { asm volatile("" : "+v"(accB[0]), "+v"(accB[1])); STAMP(11); hop1(); STAMP(6); }
        }
        SB();
      SEND
    SEND
    accB[0] = MFMA(cb[0], xe, accB[0]); accB[1] = MFMA(cb[1], xe, accB[1]);
    asm volatile("" : "+v"(accB[0]), "+v"(accB[1]));
    __builtin_amdgcn_s_setprio(GG ? PV : 0);
    SB();
  };
  unsigned ln = vL16, aW = 0;
  auto mkaw = [&]() AI {
    asm volatile("" : "+v"(ln));
    aW = lw2 + (ln & 0x200) + ((ln & 0xc0) << 1) + ((((ln >> 3) & 0x20) | ((ln >> 1) & 0x10)) ^ ((ln >> 1) & 0x40)) + ((ln >> 1) & 8);
  };
  bf16x8 wf[NS][2];
  auto readW = [&](int i) AI {
    const unsigned ra = ring_area(i % RING), la = aW + (ra & 0x30000u);
    wf[i][0] = tr2(la, ra & 0xffffu, (ra & 0xffffu) + 1024); wf[i][1] = tr2(la ^ 64u, ra & 0xffffu, (ra & 0xffffu) + 1024);
  };
  auto pairF = [&](auto p_, const f32x16& a) AI { constexpr int p = decltype(p_)::value; epiSlice<p, 0>(a, rc, es); epiSlice<p, 1>(a, rc, es); epiSlice<p, 2>(a, rc, es); epiSlice<p, 3>(a, rc, es); };
  if constexpr (GG == 0) {
    __builtin_amdgcn_s_setprio(PV);
    if constexpr (AT) { SFOR(q, 6) clri(q); SEND }
    SFOR(s, 30)
      kstep(s_);
    SEND
    if constexpr (!AT) { touch(0); touch(4); }
    if constexpr (AT) WAITV(6);
    WAITL0(); SB(); BAR(); SB();
    STAMP(3);
    kvstep(IntC<0>{});
    readB(0, 0); readB(0, 1); readB(1, 0); readB(1, 1);
    kvstep(IntC<1>{}); kvstep(IntC<3>{});
    if constexpr (AT) { WAITV(2); stflag(E + 1, rsWS, sFown); }
    STAMP(4);
    SB();
    stream();
  } else {
    stream();
    STAMP(3);
    if constexpr (AT) {
      WAITV(0); stflag(E + 1, rsWS, sFown);
      SFOR(s, 21) qstep(s_, accA[0], 1); SEND
      SFOR(s, 21)
        if constexpr (s >= H1S && s < H1S + 8) pv[s - H1S] = ld16(rsWS, vKP, sKP, WS_KVP + (s - H1S) * 16384);
        qstep(s_, accA[1], 2);
      SEND
      STAMP(4); hop1(); STAMP(6);
      SFOR(s, Q3A) qstep(s_, accA[2], 3); SEND
      SB(); BAR(); SB();
      SFOR(s, 21 - Q3A) qstep(IntC<Q3A + s>{}, accA[2], 3); SEND
    } else {
      mkaw();
      SFOR(e, 20)
        if constexpr (e < 12) pairF(IntC<e & 3>{}, accA[e >> 2]); else pairF(IntC<e & 3>{}, accB[(e >> 2) - 3]);
        if constexpr ((e & 3) == 3) st16(es.r, rsWS, vL16, sA2, (e < 12 ? 1 + (e >> 2) : 3 + (e >> 2)) * 1024);
        if constexpr (e == 0) { issueW2(3, 0); issueW2(3, 1); }
        if constexpr ((e & 3) == 2) { SB(); BAR(); SB(); issueW2(4 + (e >> 2), 0); issueW2(4 + (e >> 2), 1); }
      SEND
    }
  }

  if constexpr (AT) {
    __builtin_amdgcn_s_setprio(0);
    STAMP(5);
    auto qexp = [&](f32x16& a, unsigned lh) AI { a = dualexpP(featproj(a, aWQ, lh * 1568, hh), rc.Lr); asm volatile("" : "+v"(a)); SB(); };
#pragma unroll
    for (int i = 0; i < 8; i++) pv[i] = ld16(rsWS, vL16, sKR, WS_KVR + i * 1024);
    qexp(accB[0], 4 + 2 * GG);
    STAMP(7);
    waittag<1024>(pv, rsWS, vL16, sKR + WS_KVR, T16, 0xffffu);
    STAMP(8);
#pragma unroll
    for (int i = 0; i < 4; i++) {
      u32x4 f = {__builtin_amdgcn_perm(pv[2 * i][1], pv[2 * i][0], 0x07060302u), __builtin_amdgcn_perm(pv[2 * i][3], pv[2 * i][2], 0x07060302u),
                 __builtin_amdgcn_perm(pv[2 * i + 1][1], pv[2 * i + 1][0], 0x07060302u), __builtin_amdgcn_perm(pv[2 * i + 1][3], pv[2 * i + 1][2], 0x07060302u)};
      ldsw<u32x4>(LB + w * 4096 + vL16, i * 1024, f);
    }
    WAITL0(); BAR(); MEMFENCE();
    f32x16 op[2], on[2];
    auto omm = [&](const f32x16& phi, unsigned lh, int z) AI {
#pragma unroll
      for (int i = 0; i < 16; i++) { op[z][i] = 0.f; on[z][i] = 0.f; }
      bf16x8 kf[4];
#pragma unroll
      for (int i = 0; i < 4; i++) kf[i] = ldsr<bf16x8>(LB + vL16, lh * 4096 + i * 1024);
#pragma unroll
      for (int i = 0; i < 2; i++) { op[z] = MFMA(kf[i], sub4(phi, i), op[z]); on[z] = MFMA(kf[2 + i], sub4(phi, 2 + i), on[z]); }
      SB();
    };
    auto ofin = [&](unsigned lh, int z) AI {
      const float ia = __builtin_amdgcn_rcpf(op[z][13]), ib = __builtin_amdgcn_rcpf(on[z][13]);
      const float id = __builtin_amdgcn_rcpf(__builtin_fmaf(op[z][12], ia, __builtin_fmaf(on[z][12], ib, 1e-6f)));
      const float ca = xhi(ia * id), cb2 = xhi(ib * id);
      const f32x2 c2 = dup2(ca), d2 = dup2(cb2);
      u32x4 f0, f1;
#pragma unroll
      for (int i = 0; i < 4; i++) {
        const f32x2 u = fma2(sub2(op[z], i), c2, sub2(on[z], i) * d2), v = fma2(sub2(op[z], 4 + i), c2, sub2(on[z], 4 + i) * d2);
        f0[i] = pk2(u[0], u[1]); f1[i] = pk2(v[0], v[1]);
      }
      f1[2] = hh ? 0u : f1[2]; f1[3] = hh ? 0u : f1[3];
      st16(f0, rsWS, vL16, sA2, (112 + 2 * lh) * 1024); st16(f1, rsWS, vL16, sA2, (113 + 2 * lh) * 1024);
      SB();
    };
    if constexpr (GG) {
      omm(accA[0], 1, 0); omm(accA[1], 2, 1); ofin(1, 0); omm(accA[2], 3, 0); ofin(2, 1); omm(accB[0], 6, 1); ofin(3, 0);
      qexp(accB[1], 7); ofin(6, 1); omm(accB[1], 7, 0); ofin(7, 0);
    } else { omm(accA[2], 0, 0); omm(accB[0], 4, 1); ofin(0, 0); qexp(accB[1], 5); ofin(4, 1); omm(accB[1], 5, 0); ofin(5, 0); }
    STAMP(9);
  } else {
    __builtin_amdgcn_s_setprio(0);
    STAMP(5);
    const unsigned sA = WS_A2 + rt2 * 144 * 1024;
    if constexpr (GG == 0) mkaw();
    f32x16 acc[2][2];
#pragma unroll
    for (int i = 0; i < 4; i++)
#pragma unroll
      for (int k = 0; k < 16; k++) acc[i >> 1][i & 1][k] = 0.f;
    u32x4 af[NS][2];
    u32x4 xr;
    auto loadA = [&](int i) AI { af[i][0] = ld16(rsWS, vL16, sA + ksb(i) * 1024, ksi(i) * 1024); af[i][1] = ld16(rsWS, vL16, sA + ksb(i) * 1024, (144 + ksi(i)) * 1024); };
    const unsigned vO = (ln >> 7) * XROW + (ln & 0x70), sO = (rt2 * 32 + 8 * w) * XROW + ci * 128;
    if constexpr (GG == 0) { st16(epiP(accB[0], rc), rsWS, vL16, sA2, 4 * 1024); st16(epiP(accB[1], rc), rsWS, vL16, sA2, 5 * 1024); }
    STAMP(6);
    constexpr int MV = GG ? 7 : 9;
    if constexpr (!(RECY && GG)) waitv<GG ? 7 : 4>();
    SFOR(i, GG ? (RECY ? 0 : 7) : 8) readW(i); SEND
    WAITV(0); stflag(E + 1, rsWS, sFown);
    if constexpr (!GG) readW(8);
    WAITL0(); SB();
    BAR();
    SB(); STAMP(11);
    if constexpr (GG) __builtin_amdgcn_s_setprio(P2G1);
    if constexpr (!(RECY && GG)) {
      SFOR(i, MV)
        if constexpr (i == MV - 2) ldflag();
        issueW2(9 + i, 0); issueW2(9 + i, 1);
      SEND
      asm volatile("s_waitcnt vmcnt(4)" : "+v"(fl1) :: "memory");
    }
    if ((RECY && GG) || !ALL(!nd1 || fl1 == E + 1u)) poll(rsWS, lane * FS, sF, E, nd1 ? 1u : 0u);
    STAMP(7);
#pragma unroll
    for (int j = 0; j < PD; j++) loadA(j);
    asm volatile("buffer_load_dwordx4 %0, %1, %2, %3 offen" : "=v"(xr) : "v"(vO), "s"(rsX), "s"(sO) : "memory");
    SFOR(i, NS)
      asm volatile("s_waitcnt vmcnt(%c2)" : "+v"(af[i][0]), "+v"(af[i][1]) : "i"(later2(i, NS, GG)) : "memory");
      if constexpr (i + 1 >= MV && i + 1 < NS) readW(i + 1);
      if constexpr (GG == 0 && i >= 12) {
        auto stale = [&]() AI {
          unsigned m = UMAX(UMAX(UMAX(af[i][0][0], af[i][0][1]), UMAX(af[i][0][2], af[i][0][3])), UMAX(UMAX(af[i][1][0], af[i][1][1]), UMAX(af[i][1][2], af[i][1][3])));
          return ANY(m == ~0u);
        };
        if (__builtin_expect(stale(), 0)) {
          int it = 0;
#pragma nounroll
          do { __builtin_amdgcn_s_sleep(SLEEPR); rel1<true>(af[i][0], af[i][1], rsWS, vL16, sA + (ksb(i) + ksi(i)) * 1024); } while (++it < (1 << 14) && stale());
          SFOR(u, PD - 1) if constexpr (i + 1 + u < NS) rel1<false>(af[i + 1 + u][0], af[i + 1 + u][1], rsWS, vL16, sA + (ksb(i + 1 + u) + ksi(i + 1 + u)) * 1024); SEND
        }
      }
      if constexpr (GG && i == POFF) __builtin_amdgcn_s_setprio(0);
      if constexpr (i == 6) STAMP(8);
      if constexpr (i == 12) STAMP(9);
      SB();
      if constexpr (i + PD < NS) loadA(i + PD);
#pragma unroll
      for (int mm = 0; mm < 2; mm++) acc[0][mm] = MFMA(wf[i][0], __builtin_bit_cast(bf16x8, af[i][mm]), acc[0][mm]);
#pragma unroll
      for (int mm = 0; mm < 2; mm++) acc[1][mm] = MFMA(wf[i][1], __builtin_bit_cast(bf16x8, af[i][mm]), acc[1][mm]);
      if constexpr (GG == 0 && (i == 9 || i == 10)) { SB(); issueW2(i + 9, 0); issueW2(i + 9, 1); }
      SB();
    SEND
    STAMP(10);
    __builtin_amdgcn_s_setprio(0);
#pragma unroll
    for (int qd = 0; qd < 4; qd++)
#pragma unroll
      for (int k = 0; k < 4; k++) {
        const unsigned ra = ring_area(2 * qd + (k >> 1)) + (k & 1) * 1024;
        ldsw<bf16x8>(((lw2 + vL16) ^ (unsigned)(32 * (4 * (qd >> 1) + k))) + (ra & 0x30000u), ra & 0xffffu, sub4(acc[qd >> 1][qd & 1], k));
      }
    WAITL0(); BAR(); MEMFENCE();
    STAMP(12);
    const unsigned c8 = (ln >> 4) & 7, j2 = (c8 & 4) + 2 * GG + ((c8 >> 1) & 1);
    const unsigned rd = LB + ((0x51407362u >> (4 * j2)) & 7) * 16384 + (c8 & 1) * 1024 + (((8 * wr + (ln >> 7)) << 4) ^ (c8 << 5));
    asm volatile("" : "+v"(xr));
    f32x4 sum[2];
#pragma unroll
    for (int k = 0; k < 2; k++) {
      f32x4 t = {U2F(xr[2 * k] << 16), U2F(xr[2 * k] & 0xffff0000u), U2F(xr[2 * k + 1] << 16), U2F(xr[2 * k + 1] & 0xffff0000u)};
#pragma unroll
      for (int v = 0; v < 8; v++) t += ldsr<f32x4>(rd, v * 2048 + k * 512);
      sum[k] = t;
    }
    u32x4 o = {pk2(sum[0][0], sum[0][1]), pk2(sum[0][2], sum[0][3]), pk2(sum[1][0], sum[1][1]), pk2(sum[1][2], sum[1][3])};
    __builtin_amdgcn_raw_buffer_store_b128(o, mkrs(pout, 2048 * XROW), vO + sO, 0, OAUX);
  }
  STAMPS_OUT();
  };
  if (!attn && g) prog(BoolC<false>{}, IntC<1>{});
  else if (!attn) prog(BoolC<false>{}, IntC<0>{});
  else if (g) prog(BoolC<true>{}, IntC<1>{});
  else prog(BoolC<true>{}, IntC<0>{});
}

extern "C" void kernel_launch(void* const* d_in, const int* in_sizes, int n_in, void* d_out, int out_size, void* d_ws, size_t ws_size,
                              hipStream_t stream) {
  fused_kernel<<<256, 512, 0, stream>>>((const char*)d_in[0], (const char*)d_in[1], (const char*)d_in[5], (const char*)d_in[6], (char*)d_ws,
                                        (const char*)d_in[2], (const char*)d_in[7], (const char*)d_in[3], (char*)d_out);
}
```

```cpp
#define TEARLY 0
#include <hip/hip_runtime.h>
#include <hip/hip_bf16.h>
#include <utility>

#define DI __device__ __forceinline__
#define LAS __attribute__((address_space(3)))
typedef __attribute__((ext_vector_type(8))) short bf16x8;
typedef __attribute__((ext_vector_type(4))) short s16x4;
typedef __attribute__((ext_vector_type(2))) __bf16 bf2;
typedef __attribute__((ext_vector_type(2))) float f32x2;
typedef __attribute__((ext_vector_type(4))) float f32x4;
typedef __attribute__((ext_vector_type(16))) float f32x16;
typedef __attribute__((ext_vector_type(2))) unsigned u32x2;
typedef __attribute__((ext_vector_type(4))) unsigned u32x4;
typedef __amdgpu_buffer_rsrc_t rsrc_t;
#define F2U(x) __builtin_bit_cast(unsigned, (float)(x))
#define U2F(x) __builtin_bit_cast(float, (unsigned)(x))
#define ALL(x) (__builtin_amdgcn_ballot_w64(!(x)) == 0ull)
#define ANY(x) (__builtin_amdgcn_ballot_w64(x) != 0ull)
#define UMAX(a, b) __builtin_elementwise_max((unsigned)(a), (unsigned)(b))

#define NTX 0
#define NTW 0
#define NT2 0
constexpr unsigned XROW = 896, QROW = 2688, FROW = 3584;
constexpr unsigned FS = 128;
constexpr unsigned WS_EPOCH = 0;
constexpr unsigned WS_GR = 4096;
constexpr unsigned WS_KVP = 131072;
constexpr unsigned WS_A2 = WS_KVP + 16 * 16 * 8192;
constexpr unsigned WS_ST = WS_A2 + 64 * 144 * 1024;
constexpr unsigned WS_FH = WS_ST + 262144;
constexpr unsigned WS_KVR = WS_FH + 2048 * FS;
constexpr unsigned WS_END = WS_KVR + 16 * 8192;
constexpr unsigned L_GU = 65536, L_KV = 131072;
constexpr unsigned L_TAIL = L_KV + 3 * 8192;
constexpr unsigned L_WPK = L_TAIL;
constexpr unsigned L_ST = L_TAIL + 2048;
constexpr unsigned L_CS = L_TAIL + 4096;
constexpr unsigned L_PHI = L_GU + 32768;
constexpr unsigned L_V = L_KV + 16384;
constexpr unsigned L_FREE = 32768;

#define MFMA(a, b, c) __builtin_amdgcn_mfma_f32_32x32x16_bf16((a), (b), (c), 0, 0, 0)
#define MFMA16(a, b, c) __builtin_amdgcn_mfma_f32_16x16x32_bf16((a), (b), (c), 0, 0, 0)
#define AI __attribute__((always_inline))
#define BAR() __builtin_amdgcn_s_barrier()
#define WAITV(n) asm volatile("s_waitcnt vmcnt(" #n ")" ::: "memory")
#define WAITL0() asm volatile("s_waitcnt lgkmcnt(0)" ::: "memory")
#define SB() __builtin_amdgcn_sched_barrier(0)
#define MEMFENCE() asm volatile("" ::: "memory")

template <int N> DI void waitv() { asm volatile("s_waitcnt vmcnt(%c0)" :: "i"(N) : "memory"); }

#define INSZ(x) (x)
DI rsrc_t mkrs(const void* p, unsigned bytes) { return __builtin_amdgcn_make_buffer_rsrc((void*)p, (short)0, (int)bytes, 0x00020000); }
template <bool NT = false> DI void dma(unsigned lds, unsigned limm, unsigned voff, rsrc_t rs, unsigned soff, unsigned simm) {
  unsigned t;
  if constexpr (NT) asm volatile("s_add_u32 m0, %1, %c5\n\ts_add_u32 %0, %4, %c6\n\tbuffer_load_dwordx4 %2, %3, %0 offen nt lds"
               : "=&s"(t) : "s"(lds), "v"(voff), "s"(rs), "s"(soff), "i"(limm), "i"(simm) : "memory", "scc");
  else asm volatile("s_add_u32 m0, %1, %c5\n\ts_add_u32 %0, %4, %c6\n\tbuffer_load_dwordx4 %2, %3, %0 offen lds"
               : "=&s"(t) : "s"(lds), "v"(voff), "s"(rs), "s"(soff), "i"(limm), "i"(simm) : "memory", "scc");
}
DI void dmat(unsigned lds, unsigned voff, rsrc_t rs) {
  asm volatile("s_mov_b32 m0, %0\n\ts_nop 0\n\tbuffer_load_dword %1, %2, 0 offen lds" :: "s"(lds), "v"(voff), "s"(rs) : "memory");
}
DI s16x4 trd(unsigned a) { return __builtin_amdgcn_ds_read_tr16_b64_v4i16((LAS s16x4*)(size_t)a); }
DI s16x4 trd2(unsigned a, unsigned off) { return __builtin_amdgcn_ds_read_tr16_b64_v4i16((LAS s16x4*)((LAS char*)(size_t)a + off)); }
DI bf16x8 tr2(unsigned a, unsigned off0, unsigned off1) {
  LAS char* p = (LAS char*)(size_t)a;
  s16x4 lo = __builtin_amdgcn_ds_read_tr16_b64_v4i16((LAS s16x4*)(p + off0));
  s16x4 hi = __builtin_amdgcn_ds_read_tr16_b64_v4i16((LAS s16x4*)(p + off1));
  return __builtin_shufflevector(lo, hi, 0, 1, 2, 3, 4, 5, 6, 7);
}
template <class T> DI T ldsr(unsigned a, unsigned off) { return *(LAS T*)((LAS char*)(size_t)a + off); }
template <class T> DI void ldsw(unsigned a, unsigned off, T v) { *(LAS T*)((LAS char*)(size_t)a + off) = v; }
DI unsigned pk2(float a, float b) { f32x2 f = {a, b}; return __builtin_bit_cast(unsigned, __builtin_convertvector(f, bf2)); }
DI float rbf(float a) { return U2F(pk2(a, a) << 16); }
DI float dot2(unsigned a, unsigned b, float c) { return __builtin_amdgcn_fdot2_f32_bf16(__builtin_bit_cast(bf2, a), __builtin_bit_cast(bf2, b), c, false); }
DI float xsum(float v) { auto r = __builtin_amdgcn_permlane32_swap(F2U(v), F2U(v), false, false); float a = U2F(r[0]); asm("" : "+v"(a)); return a + U2F(r[1]); }
DI float xmax(float v) { auto r = __builtin_amdgcn_permlane32_swap(F2U(v), F2U(v), false, false); return __builtin_fmaxf(U2F(r[0]), U2F(r[1])); }
DI float xmin(float v) { auto r = __builtin_amdgcn_permlane32_swap(F2U(v), F2U(v), false, false); return __builtin_fminf(U2F(r[0]), U2F(r[1])); }
DI float xhi(float v) { auto r = __builtin_amdgcn_permlane32_swap(F2U(v), F2U(v), false, false); return U2F(r[1]); }
#define STB "sc1"
DI void st16(u32x4 v, rsrc_t rs, unsigned voff, unsigned soff, unsigned simm) {
  unsigned t;
  asm volatile("s_add_u32 %0, %4, %c5\n\tbuffer_store_dwordx4 %1, %2, %3, %0 offen " STB "\n\ts_nop 1" : "=&s"(t) : "v"(v), "v"(voff), "s"(rs), "s"(soff), "i"(simm) : "memory", "scc");
}
DI void stflag(unsigned v, rsrc_t rs, unsigned soff) {
  unsigned voff = (__builtin_amdgcn_mbcnt_hi(~0u, __builtin_amdgcn_mbcnt_lo(~0u, 0u)) == 0u) ? 0u : 0x80000000u;
  __builtin_amdgcn_raw_buffer_store_b32(v, rs, voff, soff, 16);
}
#define XORCOND (n == 24)
#define PS 0
#define PV 0
#define KEEP2 0
#define S2N 14
#define B1N 23
#define DIST 2
#define P2G1 1
#define OAUX 0
#define RECY 0
#define POFF 99
#define H1S 0
#define H0L 14
#define H0C 22
#define Q3A 1
#define COOP 0
#define PON -2
#define XN 20
#define BAN 18
#define BBN 21
#define SL0 8
#define SLN 2
#define SLD 3
#define NSL 48
#define H1C 16
#define OV vL16
#define CV vL16
#define Q6E 0
#define SLEEPR 4
#define SLEEPN 2
DI u32x4 ld16(rsrc_t rs, unsigned voff, unsigned soff, unsigned simm) {
  u32x4 r; unsigned t;
  asm volatile("s_add_u32 %1, %4, %c5\n\tbuffer_load_dwordx4 %0, %2, %3, %1 offen sc1" : "=v"(r), "=&s"(t) : "v"(voff), "s"(rs), "s"(soff), "i"(simm) : "memory", "scc");
  return r;
}
template <unsigned STEP> DI void ld8w(u32x4 (&p)[8], rsrc_t rs, unsigned voff, unsigned soff) {
  unsigned t;
#define L1(k) "buffer_load_dwordx4 %" #k ", %9, %10, %8 offen sc1\n\ts_add_u32 %8, %8, %c12\n\t"
  asm volatile("s_mov_b32 %8, %11\n\t" L1(0) L1(1) L1(2) L1(3) L1(4) L1(5) L1(6) L1(7) "s_waitcnt vmcnt(0)"
               : "=&v"(p[0]), "=&v"(p[1]), "=&v"(p[2]), "=&v"(p[3]), "=&v"(p[4]), "=&v"(p[5]), "=&v"(p[6]), "=&v"(p[7]), "=&s"(t)
               : "v"(voff), "s"(rs), "s"(soff), "i"(STEP) : "memory", "scc");
#undef L1
}
DI bool untag(u32x4 (&p)[8], unsigned tag, unsigned mask) {
  unsigned chk = 0;
#pragma unroll
  for (int i = 0; i < 8; i++)
#pragma unroll
    for (int k = 0; k < 4; k++) { p[i][k] ^= tag; chk |= p[i][k]; }
  return ALL((chk & mask) == 0u);
}
template <unsigned STEP> DI void waittag(u32x4 (&p)[8], rsrc_t rs, unsigned voff, unsigned soff, unsigned tag, unsigned mask) {
  asm volatile("s_waitcnt vmcnt(0)" : "+v"(p[0]), "+v"(p[1]), "+v"(p[2]), "+v"(p[3]), "+v"(p[4]), "+v"(p[5]), "+v"(p[6]), "+v"(p[7]) :: "memory");
#pragma nounroll
  for (int it = 0; !untag(p, tag, mask) && it < (1 << 16); ++it) { __builtin_amdgcn_s_sleep(SLEEPN); ld8w<STEP>(p, rs, voff, soff); }
}
template <bool WAIT> DI void rel1(u32x4& a, u32x4& b, rsrc_t rs, unsigned voff, unsigned so) {
  unsigned t;
  if constexpr (WAIT) asm volatile("buffer_load_dwordx4 %0, %3, %4, %5 offen sc1\n\ts_add_u32 %2, %5, 147456\n\tbuffer_load_dwordx4 %1, %3, %4, %2 offen sc1\n\ts_waitcnt vmcnt(0)"
                                   : "+v"(a), "+v"(b), "=&s"(t) : "v"(voff), "s"(rs), "s"(so) : "memory", "scc");
  else asm volatile("buffer_load_dwordx4 %0, %3, %4, %5 offen sc1\n\ts_add_u32 %2, %5, 147456\n\tbuffer_load_dwordx4 %1, %3, %4, %2 offen sc1"
                    : "+v"(a), "+v"(b), "=&s"(t) : "v"(voff), "s"(rs), "s"(so) : "memory", "scc");
}
DI void poll(rsrc_t rs, unsigned voff, unsigned soff, unsigned E, unsigned need) {
  voff = need ? voff : 0x80000000u;
#pragma nounroll
  for (int it = 0; it < (1 << 16); ++it) {
    unsigned v;
    asm volatile("buffer_load_dword %0, %1, %2, %3 offen sc1\n\ts_waitcnt vmcnt(0)" : "=v"(v) : "v"(voff), "s"(rs), "s"(soff) : "memory");
    bool ok = (need == 0u) || ((v - E - need) <= (3u - need));
    if (ALL(ok)) break;
    __builtin_amdgcn_s_sleep(SLEEPN);
  }
}

template <bool B> struct BoolC { static constexpr bool value = B; };
template <int I> struct IntC { static constexpr int value = I; };
template <int... I, class F> DI void sfor_impl(std::integer_sequence<int, I...>, F&& f) { (f(IntC<I>{}), ...); }
template <int N, class F> DI void sfor(F&& f) { sfor_impl(std::make_integer_sequence<int, N>{}, f); }
#define SFOR(n, N) sfor<N>([&](auto n##_) AI { constexpr int n = decltype(n##_)::value;
#define SEND });
struct RowC { float r, K1, K2, Ri, Lr; };

DI unsigned hilo(float v) { float hi = rbf(v); return pk2(hi, v - hi); }
DI float max3(float a, float b, float c) { return __builtin_fmaxf(__builtin_fmaxf(a, b), c); }
DI float min3(float a, float b, float c) { return __builtin_fminf(__builtin_fminf(a, b), c); }
DI unsigned pk2s(float a, float b) { unsigned r; asm("v_cvt_pk_bf16_f32 %0, %1, %2" : "=v"(r) : "v"(a), "v"(b)); return r; }
DI f32x2 sub2(const f32x16& v, int k) { f32x2 t = {v[2 * k], v[2 * k + 1]}; return t; }
DI f32x2 exp2v(f32x2 v) { f32x2 r = {__builtin_amdgcn_exp2f(v[0]), __builtin_amdgcn_exp2f(v[1])}; return r; }
DI f32x2 dup2(float a) { f32x2 r = {a, a}; asm("" : "+v"(r)); return r; }
DI f32x2 fma2(f32x2 a, f32x2 b, f32x2 c) { return __builtin_elementwise_fma(a, b, c); }
DI u32x4 epiP(const f32x16& a, const RowC& c) {
  u32x4 r;
  const f32x2 K1 = dup2(c.K1), K2 = dup2(c.K2), Ri = dup2(c.Ri);
#pragma unroll
  for (int i = 0; i < 4; i++) {
    const f32x2 g = sub2(a, i), d = fma2(exp2v(g * fma2(g * g, K2, K1)), Ri, Ri);
    const f32x2 s = {__builtin_amdgcn_rcpf(d[0]), __builtin_amdgcn_rcpf(d[1])}, h = (g * sub2(a, 4 + i)) * s;
    r[i] = pk2(h[0], h[1]);
  }
  return r;
}
struct EpiS { float x0, x1, m0, m1; u32x4 r; };
template <int P, int PH> DI void epiSlice(const f32x16& a, const RowC& c, EpiS& s) {
  const float g0 = a[2 * P], g1 = a[2 * P + 1];
  if constexpr (PH == 0) { s.x0 = g0 * __builtin_fmaf(g0 * g0, c.K2, c.K1); s.x1 = g1 * __builtin_fmaf(g1 * g1, c.K2, c.K1); }
  if constexpr (PH == 1) { s.x0 = __builtin_amdgcn_exp2f(s.x0); s.x1 = __builtin_amdgcn_exp2f(s.x1); s.m0 = g0 * a[8 + 2 * P]; s.m1 = g1 * a[9 + 2 * P]; }
  if constexpr (PH == 2) { s.x0 = __builtin_amdgcn_rcpf(__builtin_fmaf(s.x0, c.Ri, c.Ri)); s.x1 = __builtin_amdgcn_rcpf(__builtin_fmaf(s.x1, c.Ri, c.Ri)); }
  if constexpr (PH == 3) s.r[P] = pk2s(s.m0 * s.x0, s.m1 * s.x1);
}
DI f32x16 featproj2(const f32x16& a, bf16x8 w0, bf16x8 w1, bool hh) {
  u32x4 q0, q1;
#pragma unroll
  for (int i = 0; i < 4; i++) { q0[i] = pk2(a[2 * i], a[2 * i + 1]); q1[i] = pk2(a[8 + 2 * i], a[9 + 2 * i]); }
  q1[2] = hh ? 0u : q1[2]; q1[3] = hh ? 0u : q1[3];
  f32x16 z;
#pragma unroll
  for (int i = 0; i < 16; i++) z[i] = 0.f;
  z = MFMA(w0, __builtin_bit_cast(bf16x8, q0), z);
  z = MFMA(w1, __builtin_bit_cast(bf16x8, q1), z);
  return z;
}
DI f32x16 featproj(const f32x16& a, unsigned wpaddr, unsigned off, bool hh) {
  return featproj2(a, tr2(wpaddr, off, off + 8 * 56), tr2(wpaddr, off + 16 * 56, off + 24 * 56), hh);
}
DI float max16(const f32x16& p) { float m = max3(p[0], p[1], p[2]); _Pragma("unroll") for (int i = 3; i < 15; i += 2) m = max3(m, p[i], p[i + 1]); return xmax(__builtin_fmaxf(m, p[15])); }
DI float min16(const f32x16& p) { float m = min3(p[0], p[1], p[2]); _Pragma("unroll") for (int i = 3; i < 15; i += 2) m = min3(m, p[i], p[i + 1]); return xmin(__builtin_fminf(m, p[15])); }
DI f32x16 dualexpP(const f32x16& p, float Lr) {
  const f32x2 bx = dup2(-max16(p) * Lr), bn = dup2(min16(p) * Lr), L2 = dup2(Lr);
  f32x16 o;
#pragma unroll
  for (int i = 0; i < 8; i++) {
    const f32x2 ep = exp2v(fma2(sub2(p, i), L2, bx)), en = exp2v(fma2(sub2(p, i), -L2, bn));
    o[i] = U2F(pk2(ep[0], ep[1])); o[8 + i] = U2F(pk2(en[0], en[1]));
  }
  return o;
}
struct SmS { f32x16 p, e; float en[16], bx, bn, sp, sn, tp, tn; bf16x8 w0, w1; };
DI bf16x8 sub4(const f32x16& v, int k) { f32x4 t = {v[4 * k], v[4 * k + 1], v[4 * k + 2], v[4 * k + 3]}; return __builtin_bit_cast(bf16x8, t); }

DI constexpr unsigned lc1_base(int ch) { return ch == 6 ? L_KV : ((ch & 1) ? L_GU : 0u) + (ch < 2 ? 49152u : ch < 4 ? 0u : 16384u); }
template <int CTRL, int RM> DI float dppadd(float v) { return v + U2F(__builtin_amdgcn_update_dpp(0u, F2U(v), CTRL, RM, 0xf, false)); }
DI float wavesum63(float v) {
  v = dppadd<0x111, 0xf>(v); v = dppadd<0x112, 0xf>(v); v = dppadd<0x114, 0xf>(v); v = dppadd<0x118, 0xf>(v);
  v = dppadd<0x142, 0xa>(v); return dppadd<0x143, 0xc>(v);
}

#define STAMP(i)
#define STAMPS_OUT()
#define CSTAMP(i)

#define PDV 5
constexpr int PD = PDV, RING = 9;
constexpr int later2(int i, int NS, int GG) {
  const int younger = 2 * ((i + PD <= NS ? i + PD : NS) - 1 - i);
  int extra = i < PD ? 1 : 0;
  if (GG == 0 && (i == 10 || i == 11)) extra += 2 * (i - 9);
  return younger + extra;
}
DI constexpr unsigned ring_area(int j) { return j == 0 ? L_FREE : j == 1 ? L_PHI : lc1_base(j - 2); }
constexpr int cumG1(int s) { return s < 4 ? 0 : s < 22 ? (s - 4) / 2 : (9 + (s - 22) * 5 / 4 < 48 ? 9 + (s - 22) * 5 / 4 : 48); }

#define CODE_BYTES 65536
__global__ __launch_bounds__(512, 2) void fused_kernel(const char* px_, const char* pqkv_, const char* pwg_, const char* pwu_, char* ws_, const char* pphi_,
                                                       const char* pwd_, const char* pwo_, char* pout_) {
  typedef __attribute__((ext_vector_type(8))) unsigned long long u64x8;
  u64x8 ka; unsigned long long kb;
  asm volatile("s_load_dwordx16 %0, %2, 0\n\ts_load_dwordx2 %1, %2, 64" : "=&s"(ka), "=&s"(kb) : "s"(__builtin_amdgcn_kernarg_segment_ptr()));
  __shared__ __attribute__((aligned(16))) char smem[163840];
  const unsigned LB = (unsigned)(size_t)smem;
  const unsigned tid = __builtin_amdgcn_workitem_id_x(), lane = tid & 63;
  const unsigned w = __builtin_amdgcn_readfirstlane(tid >> 6), g = w >> 2, wr = w & 3;
  const unsigned b = __builtin_amdgcn_workgroup_id_x(), xcd = b & 7, bi = b >> 3, grp = xcd >> 1, half = xcd & 1, rbl = bi & 3, cp = bi >> 2, ci = cp - 1;
  const unsigned rb = 4 * grp + rbl, c = 7 * half + ci;
  const unsigned head = 8 * half + (((half ? 0x76425310u : 0x75316420u) >> (4 * cp)) & 7);
  const bool attn = cp == 0;
  STAMP(0); CSTAMP(14);
  const unsigned wstr = attn ? QROW : FROW;
  const unsigned sG0 = half * (attn ? 448 : 1792) + (attn ? 0 : ci * 256), sX0 = rb * 128 * XROW;
  unsigned vx0, vx1, vG, vU, vKV;
  auto voffs = [&](unsigned ln) AI {
    const unsigned r8 = ln >> 3, c8 = ln & 7, sa = (r8 >> 1) & 1, lc = c8 ^ (4 * sa);
    vx0 = (16 * w + r8) * XROW + ((c8 ^ (r8 >> 1)) << 4); vx1 = (vx0 ^ 64u) + 8 * XROW;
    const unsigned cg = attn ? lc : (c8 ^ (2 * sa)), cu = attn ? (lc >= 6 ? lc - 2 : lc) : (cg ^ 4);
    vG = (8 * w + r8) * wstr + (cg << 4); vU = (8 * w + r8) * wstr + (cu << 4);
    vKV = (8 * w + r8) * QROW + 896 * (1 + (lc >> 2)) + ((lc & 3) << 4) + ((56 * head) & ~15u);
  };
  voffs(lane);
  const unsigned lw1 = LB + w * 1024, lw2 = LB + w * 2048;
#define ROT (((0x24924u >> (2 * (cp + rbl))) & 3) * 2)
  const unsigned rot = ROT;
  const unsigned srow = 128 * rb + 64 * half + 8 * cp + w;
  unsigned vrow = lane < 56 ? lane * 16 : 0x80000000u;
  asm volatile("s_waitcnt lgkmcnt(0)" : "+s"(ka), "+s"(kb), "+v"(vx0), "+v"(vx1), "+v"(vG), "+v"(vU), "+v"(vKV), "+v"(vrow));
  const char *px = (const char*)ka[0], *pqkv = (const char*)ka[1], *pwg = (const char*)ka[2], *pwu = (const char*)ka[3], *pphi = (const char*)ka[5],
             *pwd = (const char*)ka[6], *pwo = (const char*)ka[7];
  char *ws = (char*)ka[4], *pout = (char*)kb;
  const rsrc_t rsX = mkrs(px, INSZ(2048 * XROW)), rsQ = mkrs(pqkv, INSZ(448 * QROW));
  const rsrc_t rsG = mkrs(attn ? pqkv : pwg, INSZ(448 * wstr)), rsU = mkrs(attn ? pqkv + 128 : pwu, INSZ(448 * wstr - (attn ? 128 : 0)));
  auto cidx = [&](int st) AI -> unsigned { unsigned t = rot + st; return t >= 7 ? t - 7 : t; };
  auto issueA = [&](int st, int k) AI {
    const unsigned sl = st & 3, ch = cidx(st);
    if (k == 0) dma<NTX>(lw2, sl * 16384, vx0, rsX, sX0 + ch * 128, 0);
    if (k == 1) dma<NTX>(lw2, sl * 16384 + 1024, vx1, rsX, sX0 + ch * 128, 0);
    if (k == 2) dma<NTW>(lw1, L_GU + sl * 16384, vG, rsG, sG0 + ch * 64 * wstr, 0);
    if (k == 3) dma<NTW>(lw1, L_GU + sl * 16384 + 8192, vU, rsU, sG0 + ch * 64 * wstr, 0);
    if (k == 4) dma<NTW>(lw1, L_KV + sl * 8192, vKV, rsQ, ch * 64 * QROW, 0);
  };
  const rsrc_t rsWS = mkrs(ws, WS_END);
  unsigned Ev, cpf; u32x4 xrw;
  asm volatile("buffer_load_dwordx4 %0, %1, %2, %3 offen" : "=v"(xrw) : "v"(vrow), "s"(rsX), "s"(srow * XROW) : "memory");
  asm volatile("buffer_load_dword %0, off, %1, 0 sc1" : "=v"(Ev) : "s"(rsWS) : "memory");
  {
    unsigned long long pc; asm volatile("s_getpc_b64 %0" : "=s"(pc));
    const unsigned vo = w == 0 && lane < 24 ? (bi * 24 + lane) * 128 : 0x80000000u;
    asm volatile("buffer_load_dword %0, %1, %2, 0 offen" : "=v"(cpf) : "v"(vo), "s"(mkrs((const void*)pc, CODE_BYTES)) : "memory");
  }
#pragma unroll
  for (int k = 0; k < 5; k++) issueA(0, k);
  asm volatile("s_waitcnt vmcnt(%c3)" : "+v"(xrw), "+v"(Ev), "+v"(cpf) : "i"(5 + COOP) : "memory");
#pragma unroll
  for (int k = 0; k < 5; k++) issueA(1, k);
  issueA(2, 0);
  const unsigned E = __builtin_amdgcn_readfirstlane(Ev);
  {
    float s1 = 0.f, s2 = 0.f;
#pragma unroll
    for (int i = 0; i < 4; i++) { s1 = dot2(xrw[i], 0x3f803f80u, s1); s2 = dot2(xrw[i], xrw[i], s2); }
    s1 = wavesum63(s1); s2 = wavesum63(s2);
    const float mean = s1 * (1.f / 448.f), var = __builtin_fmaxf(s2 * (1.f / 448.f) - mean * mean, 0.f);
    u32x2 gr = {pk2(-mean, __builtin_amdgcn_sqrtf(rbf(rbf(var) + 1e-5f))), E + 1};
    __builtin_amdgcn_raw_buffer_store_b64(gr, rsWS, lane == 63 ? 0u : 0x80000000u, WS_GR + srow * 8, 16);
  }

  auto prog = [&](auto at_, auto gg_) AI {
  constexpr bool AT = decltype(at_)::value; constexpr int GG = decltype(gg_)::value;
  constexpr unsigned lcstep = AT ? 224 : 128;
  unsigned lane;
  asm volatile("v_mbcnt_lo_u32_b32 %0, -1, 0\n\tv_mbcnt_hi_u32_b32 %0, -1, %0" : "=v"(lane));
  __builtin_assume(lane < 64);
  voffs(lane);
  auto issueL = [&](int hc) AI {
    const int st = hc >> 1; const unsigned ch = cidx(st);
    if (hc & 1) dma<NTW>(lw1, lc1_base(st) + 8192, vU, rsU, sG0 + ch * 64 * wstr, lcstep);
    else dma<NTW>(lw1, lc1_base(st), vG, rsG, sG0 + ch * 64 * wstr, lcstep);
  };
  auto issueG = [&](int bar, int k) AI {
    if (bar == 0) issueA(2 + k / 5, k % 5);
    else if (bar <= 3) issueA(bar + 3, k);
    else if (k < 4) issueL(4 * (bar - 4) + k);
    else if (bar == 4) dma(LB + (w & 1) * 1024, L_WPK, lane * 16, mkrs(pphi, 16 * 1568), head * 1568 + (w & 1) * 1024, 0);
    else issueL(12 + (bar - 5));
  };

  const unsigned rt = 4 * rb + wr;
  const unsigned sA2 = WS_A2 + rt * 144 * 1024 + (AT ? half * 16384 : c * 8192), vL16 = lane * 16;
  const u32x4 sent = {~0u, ~0u, ~0u, ~0u};
  auto clr = [&](int ks) AI { st16(sent, rsWS, CV, sA2, ks * 1024); };

  auto clri = [&](int q) AI { clr(GG ? (q < 6 ? 114 + q : 120 + q) : (q < 2 ? 112 + q : 118 + q)); };

  const bool hh = lane >= 32;
  const unsigned hu = lane >> 5, blk = (lane >> 4) & 1, q4 = (lane >> 2) & 3, p4 = lane & 3, ta = (q4 >> 1) & 1, l31 = lane & 31;
  const unsigned perm = wr ^ (2 * GG);
  const unsigned rowoff = (8 * hu + q4) * 128 + (perm << 11);
  const unsigned pc = (4 * blk + p4) < 6 ? (4 * blk + p4) : 6;
  auto tileaddr = [&](unsigned t) AI -> unsigned {
    if constexpr (AT) { unsigned o = 56 * t + 8 * pc; return (o >> 7) * 8192 + ((((o >> 4) & 7) ^ (4 * ta)) << 4) + (o & 8) + rowoff; }
    else return blk * 8192 + (((2 * t + (p4 >> 1)) ^ (4 * blk + 2 * ta)) << 4) + 8 * (p4 & 1) + rowoff;
  };
  constexpr int NT = !AT ? 2 : GG ? 1 : 3, TB0 = GG ? 4 - NT : 0;
  unsigned aA[3], aB[NT];
  {
    unsigned o = (head & 1) * 8 + 8 * pc;
    unsigned ak = L_KV + (((o >> 4) ^ (4 * ta)) << 4) + (o & 8) + rowoff;
    aA[0] = LB + (GG ? L_GU + tileaddr(1) : ak);
    aA[1] = LB + (GG ? L_GU + tileaddr(2) : (ak ^ 64u));
    aA[2] = LB + L_GU + tileaddr(GG ? 3 : 0);
#pragma unroll
    for (int j = 0; j < NT; j++) aB[j] = LB + tileaddr(TB0 + j);
  }
  const unsigned xrow = 32 * wr + l31;
  const unsigned aX = LB + xrow * 128 + (((2 * perm + hu) ^ ((xrow >> 1) & 7)) << 4);
  const unsigned selv = (((lane >> 4) ^ (lane >> 2)) & 1) ? 0u : 0x3f803f80u;
  const u32x4 sel4 = {selv, selv, selv, selv};
  const bf16x8 sel = __builtin_bit_cast(bf16x8, sel4);

  bf16x8 xf[28], wa[2][3], wq[8];
  f32x16 accA[3], accB[NT];
  f32x4 csA[3], csB[NT];
#pragma unroll
  for (int j = 0; j < 3; j++) { for (int i = 0; i < 16; i++) accA[j][i] = 0.f; for (int i = 0; i < 4; i++) csA[j][i] = 0.f; }
#pragma unroll
  for (int j = 0; j < NT; j++) { for (int i = 0; i < 16; i++) accB[j][i] = 0.f; for (int i = 0; i < 4; i++) csB[j][i] = 0.f; }
  u32x2 grn;

  const rsrc_t rsWD = mkrs(pwd, INSZ(1792 * XROW)), rsWO = mkrs(pwo, INSZ(448 * XROW));
  const unsigned vW2 = (lane >> 3) * XROW + (((lane & 7) ^ ((lane >> 2) & 4)) << 4) + ci * 128;
  constexpr int NS = GG ? 16 : 20;
#define RHO ((rbl + cp) & 3)
  const unsigned rho = RHO;
  auto t_of = [](int i) constexpr { return (0x54763210u >> (4 * (i >> 1))) & 7; };
  auto ksb = [&](int i) AI -> unsigned {
    if (GG) return 16 * wr + ((t_of(i) & 3) ^ rho);
    if (i < 12) { const unsigned cc = i % 6 + rho + (rho >> 1); return wr + 8 * (cc >= 6 ? cc - 6 : cc); }
    return 8 * wr + 2 * (((i - 12) >> 1) ^ rho);
  };
  auto ksi = [&](int i) AI -> unsigned { return GG ? 8 * (i & 1) + (t_of(i) & 4) : i < 12 ? 64 + 4 * (i / 6) : 112 + (i & 1); };
  auto issueW2 = [&](int i, int hf) AI {
    if (GG == 0 && i >= 12) dma<NT2>(lw2, ring_area(i % RING) + hf * 1024, vW2, rsWO, ksb(i) * 14 * XROW, (16 * (i & 1) + 8 * hf) * XROW);
    else dma<NT2>(lw2, ring_area(i % RING) + hf * 1024, vW2, rsWD, ksb(i) * 16 * XROW, (ksi(i) * 16 + 8 * hf) * XROW);
  };

  const unsigned aCSw = LB + L_CS + l31 * 16 + wr * 4 + GG * 2048, aCSr = LB + L_CS + l31 * 16 + GG * 2048;
  auto readX = [&](int n) AI { xf[n] = ldsr<bf16x8>(aX ^ (unsigned)((n & 3) << 5), ((n >> 2) & 3) * 16384); };
  auto readA = [&](int n, int j) AI {
    const unsigned o = ((n >> 2) & 3) * ((GG == 0 && j < 2) ? 8192 : 16384);
    wa[n & 1][j] = tr2(aA[j] ^ (unsigned)((n & 3) << 11), o, o + 512);
  };
  auto readB = [&](int q) AI {
    const int n = q / NT, j = q % NT;
    const unsigned base = lc1_base(n >> 2);
    if ((NT == 3 && n < XN) ? (n & 3) == 0 : n == 24) asm volatile("" : "+v"(aB[j]));
    wq[q & 7] = tr2(aB[j] ^ (((n & 3) << 11) | (base & 0x30000u)), base & 0xffffu, (base & 0xffffu) + 512);
  };
  RowC rc; unsigned nm, denb;
  auto mkxe = [&]() AI { asm volatile("" : "+v"(nm)); u32x4 t = {nm, nm, nm, nm}; return __builtin_bit_cast(bf16x8, t); };
  auto rowconst = [&]() AI {
#pragma nounroll
    for (int it = 0; it < (1 << 16) && !ALL(grn[1] == E + 1); ++it) {
      __builtin_amdgcn_s_sleep(SLEEPN);
      asm volatile("buffer_load_dwordx2 %0, %1, %2, %3 offen sc1\n\ts_waitcnt vmcnt(0)" : "=v"(grn) : "v"(xrow * 8), "s"(rsWS), "s"(WS_GR + rb * 1024) : "memory");
    }
    const float den = U2F(grn[0] & 0xffff0000u);
    nm = hh ? 0u : __builtin_amdgcn_perm(grn[0], grn[0], 0x01000100u);
    denb = grn[0] >> 16;
    rc.r = __builtin_amdgcn_rcpf(den);
    const float k1 = -2.f * 1.4426950408889634f * 0.7978845608028654f;
    rc.K1 = k1 * rc.r; rc.K2 = (k1 * 0.044715f) * rc.r * (rc.r * rc.r); rc.Ri = den * den; rc.Lr = 1.4426950408889634f * rc.r;
  };
  waitv<7 + COOP>();
  BAR(); MEMFENCE();
  STAMP(1);
  readX(0); readA(0, 0); readA(0, 1); readA(0, 2);
  SFOR(n, 28)
    SB();
    if constexpr (n == 27) {
#pragma unroll
      for (int j = 0; j < 3; j++) ldsw<unsigned>(aCSw, j * 512, hilo(csA[j][0]));
    }
    if constexpr ((n & 3) == 3) {
      if constexpr (n == 27) WAITV(0);
      else if constexpr (n == 23) asm volatile("s_waitcnt vmcnt(10)" : "+v"(grn) :: "memory");
      else if constexpr (n == 3 || n == 15 || n == 19) WAITV(11); else WAITV(10);
      { WAITL0(); BAR(); MEMFENCE(); }
      if constexpr (n == 27) STAMP(2);
    }
    constexpr int bar = (n + 1) >> 2, ph = (n + 1) & 3;
#pragma unroll
    for (int j = 0; j < 3; j++) {
      if constexpr (n < 27) { if (j == 0) readX(n + 1); readA(n + 1, j); }
      else { wa[0][j] = ldsr<bf16x8>(aCSr, j * 512); if (GG == 1 && j < 2) { readB(2 * j); readB(2 * j + 1); } }
      SB();
      accA[j] = MFMA(wa[n & 1][j], xf[n], accA[j]);
      const int slot = 3 * ph + j;
      if (bar == 0) issueG(0, slot - 2);
      else if (bar == 6) { if (slot < 5) issueG(bar, slot); }
      else if (bar <= 6) { if (slot == 0) issueG(bar, 0); if (slot == 2) issueG(bar, 1); if (slot == 5) issueG(bar, 2); if (slot == 7) issueG(bar, 3); if (slot == 10) issueG(bar, 4); }
      else if (j < 2) {
        if constexpr (AT) dma(lw2, L_FREE + j * 1024, lane * 16, mkrs(pphi, 16 * 1568), half * 8 * 1568 + w * 2048, j * 1024);
        else issueW2(0, j);
      }
      if ((n & 3) == 0) csA[j] = MFMA16(sel, wa[n & 1][j], csA[j]);
      if (n == 25 && j == 0) rowconst();
      if (n == 14 && j == 2) asm volatile("buffer_load_dwordx2 %0, %1, %2, %3 offen sc1" : "=v"(grn) : "v"(xrow * 8), "s"(rsWS), "s"(WS_GR + rb * 1024) : "memory");
      SB();
    }
  SEND
  { const bf16x8 xe = mkxe();
#pragma unroll
  for (int j = 0; j < 3; j++) accA[j] = MFMA(wa[0][j], xe, accA[j]); }
  asm volatile("" : "+v"(accA[0]), "+v"(accA[1]), "+v"(accA[2]));
  SB();

  const unsigned sFown = WS_FH + (rt * 32 + (AT ? 28 + 2 * half : 2 * c) + GG) * FS;
  const unsigned aWP = LB + (4 * hu + q4) * 56 + 8 * pc;
  const unsigned T12 = (E + 1) & 0xfffu, T16 = (E + 1) & 0xffffu;
  const unsigned rt2 = 2 * (8 * grp + 4 * half + rbl);
  const unsigned sF = WS_FH + rt2 * 32 * FS;
  const unsigned fc = l31 >> 1;
  const bool nd1 = GG ? (fc >> 1) == wr : (fc >= 8 && fc < 14) || fc == 14 + (wr >> 1);
  const unsigned aIW = LB + L_PHI + xrow * 64 + ((hu ^ ((xrow >> 1) & 7)) << 3);
  SmS sm;
  unsigned aWK = aWP + L_WPK, aWQ = aWP + L_FREE;
  asm volatile("" : "+v"(aWK)); if constexpr (AT) asm volatile("" : "+v"(aWQ));
  auto kstep = [&](auto s_) AI {
    constexpr int s = decltype(s_)::value;
    if constexpr (s == 0) { sm.w0 = tr2(aWK, 0, 8 * 56); sm.w1 = tr2(aWK, 16 * 56, 24 * 56); }
    if constexpr (s == 1) sm.p = featproj2(accA[0], sm.w0, sm.w1, hh);
    if constexpr (s == 2 || s == 3) {
#pragma unroll
      for (int k = 2 * s - 4; k < 2 * s - 2; k++) {
        u32x2 vv = {pk2(accA[1][4 * k], accA[1][4 * k + 1]), pk2(accA[1][4 * k + 2], accA[1][4 * k + 3])};
        if (k == 3) vv[0] = hh ? denb : vv[0];
        ldsw<u32x2>(aIW ^ (unsigned)(k << 4), L_V - L_PHI, vv);
      }
    }
    if constexpr (s == 4) sm.bx = -max16(sm.p) * rc.Lr;
    if constexpr (s == 5) sm.bn = min16(sm.p) * rc.Lr;
    if constexpr (s >= 6 && s < 22) {
      constexpr int i = s - 6;
      sm.e[i] = __builtin_amdgcn_exp2f(__builtin_fmaf(sm.p[i], rc.Lr, sm.bx)); sm.en[i] = __builtin_amdgcn_exp2f(__builtin_fmaf(sm.p[i], -rc.Lr, sm.bn));
      if constexpr (i == 0) { sm.sp = sm.e[0]; sm.sn = sm.en[0]; } else if constexpr (i < 12) { sm.sp += sm.e[i]; sm.sn += sm.en[i]; }
      else if constexpr (i == 12) { sm.tp = sm.e[12]; sm.tn = sm.en[12]; } else { sm.tp += sm.e[i]; sm.tn += sm.en[i]; }
    }
    if constexpr (s == 22) { sm.sp = __builtin_amdgcn_rcpf(xsum(sm.sp + (hh ? 0.f : sm.tp))) * rc.r; sm.sn = __builtin_amdgcn_rcpf(xsum(sm.sn + (hh ? 0.f : sm.tn))) * rc.r; }
    if constexpr (s >= 23) {
#pragma unroll
      for (int u = (s == 29 ? 6 : s - 23); u <= (s == 29 ? 7 : s - 23); u++) {
        const int k = u >> 1;
        if (u & 1) { u32x2 t = {pk2s(sm.en[4 * k] * sm.sn, sm.en[4 * k + 1] * sm.sn), pk2s(sm.en[4 * k + 2] * sm.sn, sm.en[4 * k + 3] * sm.sn)}; ldsw<u32x2>(aIW ^ (unsigned)(k << 4), 8192, t); }
        else { u32x2 t = {pk2s(sm.e[4 * k] * sm.sp, sm.e[4 * k + 1] * sm.sp), pk2s(sm.e[4 * k + 2] * sm.sp, sm.e[4 * k + 3] * sm.sp)}; ldsw<u32x2>(aIW ^ (unsigned)(k << 4), 0, t); }
      }
    }
  };
  auto qstep = [&](auto s_, f32x16& a, unsigned lh) AI {
    constexpr int s = decltype(s_)::value;
    if constexpr (s == 0) { sm.w0 = tr2(aWQ, lh * 1568, lh * 1568 + 8 * 56); sm.w1 = tr2(aWQ, lh * 1568 + 16 * 56, lh * 1568 + 24 * 56); }
    if constexpr (s == 1) sm.p = featproj2(a, sm.w0, sm.w1, hh);
    if constexpr (s == 3) sm.bx = -max16(sm.p) * rc.Lr;
    if constexpr (s == 4) sm.bn = min16(sm.p) * rc.Lr;
    if constexpr (s >= 5 && s < 21) {
      constexpr int i = (s - 5) >> 1;
      float t0, t1;
      if constexpr ((s - 5) & 1) { t0 = __builtin_fmaf(sm.p[2 * i], -rc.Lr, sm.bn); t1 = __builtin_fmaf(sm.p[2 * i + 1], -rc.Lr, sm.bn); }
      else { t0 = __builtin_fmaf(sm.p[2 * i], rc.Lr, sm.bx); t1 = __builtin_fmaf(sm.p[2 * i + 1], rc.Lr, sm.bx); }
      a[8 * ((s - 5) & 1) + i] = U2F(pk2(__builtin_amdgcn_exp2f(t0), __builtin_amdgcn_exp2f(t1)));
      if constexpr (s == 20) asm volatile("" : "+v"(a));
    }
  };
  bf16x8 ka_[4], kb_[2][4]; f32x4 kd[2];
  auto kvstep = [&](auto s_) AI {
    constexpr int s = decltype(s_)::value;
    if constexpr (s == 0) {
      const unsigned g4 = lane >> 4, r0 = 8 * g4 + q4, sw = 4 * blk + (q4 >> 1);
      unsigned b0 = LB + L_PHI + r0 * 64 + ((p4 ^ sw) << 3);
      unsigned a0 = (b0 ^ ((wr >> 1) << 5)) + (wr & 1) * 8192;
#pragma unroll
      for (int k = 0; k < 4; k++) {
        ka_[k] = __builtin_shufflevector(trd2(a0, k * 2048), trd2(a0 ^ 16u, k * 2048 + 256), 0, 1, 2, 3, 4, 5, 6, 7);
        kb_[0][k] = __builtin_shufflevector(trd2(b0, L_V - L_PHI + k * 2048), trd2(b0 ^ 16u, L_V - L_PHI + k * 2048 + 256), 0, 1, 2, 3, 4, 5, 6, 7);
        kb_[1][k] = __builtin_shufflevector(trd2(b0 ^ 32u, L_V - L_PHI + k * 2048), trd2(b0 ^ 48u, L_V - L_PHI + k * 2048 + 256), 0, 1, 2, 3, 4, 5, 6, 7);
      }
    }
    if constexpr (s == 1) {
      f32x4 z = {0.f, 0.f, 0.f, 0.f}; kd[0] = z; kd[1] = z;
#pragma unroll
      for (int k = 0; k < 4; k++) { kd[0] = MFMA16(ka_[k], kb_[0][k], kd[0]); kd[1] = MFMA16(ka_[k], kb_[1][k], kd[1]); }
    }
    if constexpr (s == 3) {
#pragma unroll
      for (int d = 0; d < 2; d++) {
        u32x4 t;
#pragma unroll
        for (int i = 0; i < 4; i++) t[i] = (F2U(kd[d][i]) & ~0xfffu) | T12;
        st16(t, rsWS, (lane >> 4) * 512 + (lane & 15) * 16, ((head * 16 + rb) * 2 + (wr & 1)) * 4096 + (wr >> 1) * 2048, WS_KVP + d * 256);
      }
    }
  };
  auto qexp2 = [&](f32x16& a0, unsigned l0, f32x16& a1, unsigned l1) AI {
    const f32x16 p0 = featproj(a0, aWQ, l0 * 1568, hh), p1 = featproj(a1, aWQ, l1 * 1568, hh);
    a0 = dualexpP(p0, rc.Lr); a1 = dualexpP(p1, rc.Lr); asm volatile("" : "+v"(a0), "+v"(a1));
  };

  unsigned fl1 = 0;
  EpiS es;
  u32x4 pv[8];
  const unsigned vKP = l31 * 16 + hu * 8192, sKP = (rb * 32 + half) * 4096 + wr * 1024 + GG * 512, sKR = (8 * half + w) * 8192;
  auto hop1 = [&]() AI {
    waittag<16384>(pv, rsWS, vKP, sKP + WS_KVP, T12, 0xfffu);
    u32x4 o;
#pragma unroll
    for (int k = 0; k < 4; k++) {
      float sum = U2F(pv[0][k]);
#pragma unroll
      for (int i = 1; i < 8; i++) sum += U2F(pv[i][k]);
      sum = xsum(sum);
      sum = l31 == 29 ? 1.f : sum;
      if (GG == 1) sum = wr == 3 ? 0.f : sum;
      o[k] = (pk2s(sum, sum) & 0xffff0000u) | T16;
    }
    st16(o, rsWS, hh ? 0x80000000u : l31 * 16, (rb * 2 + half) * 4096 + wr * 1024 + GG * 512, WS_KVR);
    if (b == 0 && w == 4) stflag(E + 4, rsWS, WS_EPOCH);
    SB();
  };
  bf16x8 cb[NT];
  auto ldflag = [&]() AI { asm volatile("buffer_load_dword %0, %1, %2, %3 offen sc1" : "=v"(fl1) : "v"(nd1 ? lane * FS : 0x80000000u), "s"(rsWS), "s"(sF) : "memory"); };
  auto touch = [&](unsigned wo4) AI {
    const unsigned rw = (rbl * 8 + w + wo4) * 44 + lane;
    const bool dn = rw < 896;
    const unsigned vo = lane < 44 ? (dn ? ((rw >> 6) * 128 + 64 + (rw & 63)) : rw - 896) * XROW + ci * 128 : 0x80000000u;
    asm volatile("s_add_u32 m0, %0, %c5\n\ts_nop 0\n\tbuffer_load_dword %1, %3, 0 offen lds\n\tbuffer_load_dword %2, %4, 0 offen lds"
                 :: "s"(LB + w * 256), "v"(dn ? vo : 0x80000000u), "v"(dn ? 0x80000000u : vo), "s"(rsWD), "s"(rsWO), "i"(L_ST) : "memory", "scc");
  };
  auto stream = [&]() AI {
    SFOR(q, 28 * NT)
      constexpr int n = q / NT, j = q % NT;
      if constexpr (j == 0) {
        if constexpr (n == 27) {
#pragma unroll
          for (int t = 0; t < NT; t++) ldsw<unsigned>(aCSw, ((3 + t) & 3) * 512, hilo(csB[t][0]));
          if constexpr (AT && GG) WAITV(8);
          WAITL0(); BAR(); MEMFENCE();
        }
        else if constexpr (!AT && GG && (n == S2N || n == B1N)) { BAR(); MEMFENCE(); }
        else if constexpr (!AT && !GG && (n & 3) == 3) { asm volatile("s_waitcnt lgkmcnt(4)" ::: "memory"); BAR(); MEMFENCE(); }
      }
      if constexpr (q + 4 < 28 * NT) readB(q + 4);
      if constexpr (n == 27) cb[j] = ldsr<bf16x8>(aCSr, ((3 + j) & 3) * 512);
      SB();
      accB[j] = MFMA(wq[q & 7], xf[n], accB[j]);
      if constexpr ((n & 3) == 1) csB[j] = MFMA16(sel, wq[q & 7], csB[j]);
      if constexpr (!AT) {
        if constexpr (GG == 1 && j == 0 && n >= B1N && n < B1N + 4) issueW2(1 + ((n - B1N) >> 1), (n - B1N) & 1);
        if constexpr (GG == 0) {
          if constexpr (j == 1 && n < 16) { epiSlice<(n >> 2) & 3, n & 3>(accA[2], rc, es); if constexpr (n == 15) st16(es.r, rsWS, vL16, sA2, 0); }
          if constexpr (j == 0 && n >= 3 && n < 7) issueW2(1 + ((n - 3) >> 1), (n - 3) & 1);
          if constexpr (j == 0 && n >= 7 && n < 27 && (n & 1)) issueW2(1 + ((n + 1) >> 2), ((n + 1) >> 1) & 1);
          if constexpr (n == 27) issueW2(8, j);
        }
      } else {
        if constexpr (GG == 1 && n < 16 && (n & 1) == 0) clri(n >> 1);
        if constexpr (GG == 0 && j < 2 && n >= 1 && 2 * (n - 1) + j < 21) qstep(IntC<2 * (n - 1) + j>{}, accA[2], 0);
        if constexpr (GG == 0 && q >= 3 * H0L && q < 3 * H0L + 8) pv[q - 3 * H0L] = ld16(rsWS, vKP, sKP, WS_KVP + (q - 3 * H0L) * 16384);
        if constexpr (GG == 0 && j == 2 && n == H0C) { asm volatile("" : "+v"(accB[0]), "+v"(accB[1]), "+v"(accB[NT - 1]), "+v"(csB[0]), "+v"(csB[1]), "+v"(csB[NT - 1])); STAMP(11); hop1(); STAMP(6); }
      }
      SB();
    SEND
    const bf16x8 xe = mkxe();
#pragma unroll
    for (int t = 0; t < NT; t++) { accB[t] = MFMA(cb[t], xe, accB[t]); asm volatile("" : "+v"(accB[t])); }
    SB();
  };
  unsigned ln = vL16, aW = 0;
  auto mkaw = [&]() AI {
    asm volatile("" : "+v"(ln));
    aW = lw2 + (ln & 0x200) + ((ln & 0xc0) << 1) + ((((ln >> 3) & 0x20) | ((ln >> 1) & 0x10)) ^ ((ln >> 1) & 0x40)) + ((ln >> 1) & 8);
  };
  bf16x8 wf[NS][2];
  auto readW = [&](int i) AI {
    const unsigned ra = ring_area(i % RING), la = aW + (ra & 0x30000u);
    wf[i][0] = tr2(la, ra & 0xffffu, (ra & 0xffffu) + 1024); wf[i][1] = tr2(la ^ 64u, ra & 0xffffu, (ra & 0xffffu) + 1024);
  };
  auto pairF = [&](auto p_, const f32x16& a) AI { constexpr int p = decltype(p_)::value; epiSlice<p, 0>(a, rc, es); epiSlice<p, 1>(a, rc, es); epiSlice<p, 2>(a, rc, es); epiSlice<p, 3>(a, rc, es); };
  if constexpr (GG == 0) {
    if constexpr (AT) { SFOR(q, 8) clri(q); SEND }
    SFOR(s, 30)
      kstep(s_);
      if constexpr (!AT && s == 1 && TEARLY) {
        const unsigned rw = (rbl * 4 + wr) * 56 + lane;
        dmat(LB + L_ST + w * 256, lane < 56 ? ((rw >> 6) * 128 + (rw & 63)) * XROW + ci * 128 : 0x80000000u, rsWD);
        if (TEARLY == 2) { touch(0); touch(4); }
      }
    SEND
    if constexpr (!AT && TEARLY != 2) { touch(0); touch(4); }
    if constexpr (AT) WAITV(8);
    WAITL0(); SB(); BAR(); SB();
    STAMP(3);
    kvstep(IntC<0>{});
    readB(0); readB(1); readB(2); readB(3);
    kvstep(IntC<1>{}); kvstep(IntC<3>{});
    if constexpr (AT) { WAITV(2); stflag(E + 1, rsWS, sFown); }
    STAMP(4);
    SB();
    stream();
    if constexpr (!AT) { st16(epiP(accB[0], rc), rsWS, vL16, sA2, 4 * 1024); st16(epiP(accB[1 % NT], rc), rsWS, vL16, sA2, 5 * 1024); }
  } else {
    stream();
    STAMP(3);
    if constexpr (AT) {
      SFOR(s, 21) qstep(s_, accA[0], 1); SEND
      WAITV(0); stflag(E + 1, rsWS, sFown);
      SFOR(s, 21) qstep(s_, accA[1], 2); SEND
      SFOR(s, 21)
        if constexpr (s < 8) pv[s] = ld16(rsWS, vKP, sKP, WS_KVP + s * 16384);
        qstep(s_, accA[2], 3);
      SEND
      STAMP(4); hop1(); STAMP(6);
      SB(); BAR(); SB();
    } else {
      mkaw();
      SFOR(e, 20)
        if constexpr (e < 12) pairF(IntC<e & 3>{}, accA[e >> 2]); else pairF(IntC<e & 3>{}, accB[((e >> 2) - 3) % NT]);
        if constexpr ((e & 3) == 3) st16(es.r, rsWS, vL16, sA2, (e < 12 ? 1 + (e >> 2) : 3 + (e >> 2)) * 1024);
        if constexpr (e == 0) { issueW2(3, 0); issueW2(3, 1); }
        if constexpr ((e & 3) == 2) { SB(); BAR(); SB(); issueW2(4 + (e >> 2), 0); issueW2(4 + (e >> 2), 1); }
      SEND
    }
  }

  if constexpr (AT) {
    STAMP(5);
    auto qexp = [&](f32x16& a, unsigned lh) AI { a = dualexpP(featproj(a, aWQ, lh * 1568, hh), rc.Lr); asm volatile("" : "+v"(a)); SB(); };
    if constexpr (GG == 0) qexp(accB[0], 4);
#pragma unroll
    for (int i = 0; i < 8; i++) pv[i] = ld16(rsWS, vL16, sKR, WS_KVR + i * 1024);
    if constexpr (GG == 0) { qexp(accB[1 % NT], 5); qexp(accB[NT - 1], 6); } else qexp(accB[0], 7);
    STAMP(7);
    waittag<1024>(pv, rsWS, vL16, sKR + WS_KVR, T16, 0xffffu);
    STAMP(8);
#pragma unroll
    for (int i = 0; i < 4; i++) {
      u32x4 f = {__builtin_amdgcn_perm(pv[2 * i][1], pv[2 * i][0], 0x07060302u), __builtin_amdgcn_perm(pv[2 * i][3], pv[2 * i][2], 0x07060302u),
                 __builtin_amdgcn_perm(pv[2 * i + 1][1], pv[2 * i + 1][0], 0x07060302u), __builtin_amdgcn_perm(pv[2 * i + 1][3], pv[2 * i + 1][2], 0x07060302u)};
      ldsw<u32x4>(LB + w * 4096 + vL16, i * 1024, f);
    }
    WAITL0(); BAR(); MEMFENCE();
    STAMP(10);
    f32x16 op[4], on[4];
    auto omm = [&](const f32x16& phi, unsigned lh, int z) AI {
#pragma unroll
      for (int i = 0; i < 16; i++) { op[z][i] = 0.f; on[z][i] = 0.f; }
      bf16x8 kf[4];
#pragma unroll
      for (int i = 0; i < 4; i++) kf[i] = ldsr<bf16x8>(LB + vL16, lh * 4096 + i * 1024);
#pragma unroll
      for (int i = 0; i < 2; i++) { op[z] = MFMA(kf[i], sub4(phi, i), op[z]); on[z] = MFMA(kf[2 + i], sub4(phi, 2 + i), on[z]); }
    };
    auto ofin = [&](unsigned lh, int z) AI {
      const float ia = __builtin_amdgcn_rcpf(op[z][13]), ib = __builtin_amdgcn_rcpf(on[z][13]);
      const float id = __builtin_amdgcn_rcpf(__builtin_fmaf(op[z][12], ia, __builtin_fmaf(on[z][12], ib, 1e-6f)));
      const float ca = xhi(ia * id), cb2 = xhi(ib * id);
      const f32x2 c2 = dup2(ca), d2 = dup2(cb2);
      u32x4 f0, f1;
#pragma unroll
      for (int i = 0; i < 4; i++) {
        const f32x2 u = fma2(sub2(op[z], i), c2, sub2(on[z], i) * d2), v = fma2(sub2(op[z], 4 + i), c2, sub2(on[z], 4 + i) * d2);
        f0[i] = pk2(u[0], u[1]); f1[i] = pk2(v[0], v[1]);
      }
      f1[2] = hh ? 0u : f1[2]; f1[3] = hh ? 0u : f1[3];
      st16(f0, rsWS, OV, sA2, (112 + 2 * lh) * 1024); st16(f1, rsWS, OV, sA2, (113 + 2 * lh) * 1024);
      SB();
    };
    if constexpr (GG) { omm(accA[0], 1, 0); omm(accA[1], 2, 1); SB(); ofin(1, 0); omm(accA[2], 3, 2); omm(accB[0], 7, 3); SB(); ofin(2, 1); ofin(3, 2); ofin(7, 3); }
    else { omm(accA[2], 0, 0); omm(accB[0], 4, 1); SB(); ofin(0, 0); omm(accB[1 % NT], 5, 2); omm(accB[NT - 1], 6, 3); SB(); ofin(4, 1); ofin(5, 2); ofin(6, 3); }
    STAMP(9);
  } else {
    __builtin_amdgcn_s_setprio(0);
    STAMP(5);
    const unsigned sA = WS_A2 + rt2 * 144 * 1024;
    if constexpr (GG == 0) mkaw();
    f32x16 acc[2][2];
#pragma unroll
    for (int i = 0; i < 4; i++)
#pragma unroll
      for (int k = 0; k < 16; k++) acc[i >> 1][i & 1][k] = 0.f;
    u32x4 af[NS][2];
    u32x4 xr;
    auto loadA = [&](int i) AI { af[i][0] = ld16(rsWS, vL16, sA + ksb(i) * 1024, ksi(i) * 1024); af[i][1] = ld16(rsWS, vL16, sA + ksb(i) * 1024, (144 + ksi(i)) * 1024); };
    const unsigned vO = (ln >> 7) * XROW + (ln & 0x70), sO = (rt2 * 32 + 8 * w) * XROW + ci * 128;
    STAMP(6);
    constexpr int MV = GG ? 7 : 9;
    if constexpr (!(RECY && GG)) waitv<GG ? 7 : 4>();
    SFOR(i, GG ? (RECY ? 0 : 7) : 8) readW(i); SEND
    WAITV(0); stflag(E + 1, rsWS, sFown);
    if constexpr (!GG) readW(8);
    WAITL0(); SB();
    BAR();
    SB(); STAMP(11);
    if constexpr (GG && PON == -2) __builtin_amdgcn_s_setprio(1);
    if constexpr (!(RECY && GG)) {
      SFOR(i, MV)
        if constexpr (i == MV - 2) ldflag();
        issueW2(9 + i, 0); issueW2(9 + i, 1);
      SEND
      asm volatile("s_waitcnt vmcnt(4)" : "+v"(fl1) :: "memory");
    }
    if ((RECY && GG) || !ALL(!nd1 || fl1 == E + 1u)) poll(rsWS, lane * FS, sF, E, nd1 ? 1u : 0u);
    STAMP(7);
    if constexpr (GG && PON == -1) __builtin_amdgcn_s_setprio(1);
#pragma unroll
    for (int j = 0; j < PD; j++) loadA(j);
    asm volatile("buffer_load_dwordx4 %0, %1, %2, %3 offen" : "=v"(xr) : "v"(vO), "s"(rsX), "s"(sO) : "memory");
    SFOR(i, NS)
      asm volatile("s_waitcnt vmcnt(%c2)" : "+v"(af[i][0]), "+v"(af[i][1]) : "i"(later2(i, NS, GG)) : "memory");
      if constexpr (i + 1 >= MV && i + 1 < NS) readW(i + 1);
      if constexpr (GG == 0 && i >= 12) {
        auto stale = [&]() AI {
          unsigned m = UMAX(UMAX(UMAX(af[i][0][0], af[i][0][1]), UMAX(af[i][0][2], af[i][0][3])), UMAX(UMAX(af[i][1][0], af[i][1][1]), UMAX(af[i][1][2], af[i][1][3])));
          return ANY(m == ~0u);
        };
        if (__builtin_expect(stale(), 0)) {
          int it = 0;
#pragma nounroll
          do { __builtin_amdgcn_s_sleep(SLEEPR); rel1<true>(af[i][0], af[i][1], rsWS, vL16, sA + (ksb(i) + ksi(i)) * 1024); } while (++it < (1 << 14) && stale());
          SFOR(u, PD - 1) if constexpr (i + 1 + u < NS) rel1<false>(af[i + 1 + u][0], af[i + 1 + u][1], rsWS, vL16, sA + (ksb(i + 1 + u) + ksi(i + 1 + u)) * 1024); SEND
        }
      }
      if constexpr (GG && i == POFF) __builtin_amdgcn_s_setprio(0);
      if constexpr (GG && i == PON) __builtin_amdgcn_s_setprio(1);
      if constexpr (GG && PON == -3) __builtin_amdgcn_s_setprio(i & 1);
      if constexpr (i == 6) STAMP(8);
      if constexpr (i == 12) STAMP(9);
      SB();
      if constexpr (i + PD < NS) loadA(i + PD);
#pragma unroll
      for (int mm = 0; mm < 2; mm++) acc[0][mm] = MFMA(wf[i][0], __builtin_bit_cast(bf16x8, af[i][mm]), acc[0][mm]);
#pragma unroll
      for (int mm = 0; mm < 2; mm++) acc[1][mm] = MFMA(wf[i][1], __builtin_bit_cast(bf16x8, af[i][mm]), acc[1][mm]);
      if constexpr (GG == 0 && (i == 9 || i == 10)) { SB(); issueW2(i + 9, 0); issueW2(i + 9, 1); }
      SB();
    SEND
    STAMP(10);
    __builtin_amdgcn_s_setprio(0);
#pragma unroll
    for (int qd = 0; qd < 4; qd++)
#pragma unroll
      for (int k = 0; k < 4; k++) {
        const unsigned ra = ring_area(2 * qd + (k >> 1)) + (k & 1) * 1024;
        ldsw<bf16x8>(((lw2 + vL16) ^ (unsigned)(32 * (4 * (qd >> 1) + k))) + (ra & 0x30000u), ra & 0xffffu, sub4(acc[qd >> 1][qd & 1], k));
      }
    WAITL0(); BAR(); MEMFENCE();
    STAMP(12);
    const unsigned c8 = (ln >> 4) & 7, j2 = (c8 & 4) + 2 * GG + ((c8 >> 1) & 1);
    const unsigned rd = LB + ((0x51407362u >> (4 * j2)) & 7) * 16384 + (c8 & 1) * 1024 + (((8 * wr + (ln >> 7)) << 4) ^ (c8 << 5));
    asm volatile("" : "+v"(xr));
    f32x4 sum[2];
#pragma unroll
    for (int k = 0; k < 2; k++) {
      f32x4 t = {U2F(xr[2 * k] << 16), U2F(xr[2 * k] & 0xffff0000u), U2F(xr[2 * k + 1] << 16), U2F(xr[2 * k + 1] & 0xffff0000u)};
#pragma unroll
      for (int v = 0; v < 8; v++) t += ldsr<f32x4>(rd, v * 2048 + k * 512);
      sum[k] = t;
    }
    u32x4 o = {pk2(sum[0][0], sum[0][1]), pk2(sum[0][2], sum[0][3]), pk2(sum[1][0], sum[1][1]), pk2(sum[1][2], sum[1][3])};
    __builtin_amdgcn_raw_buffer_store_b128(o, mkrs(pout, 2048 * XROW), vO + sO, 0, OAUX);
  }
  STAMPS_OUT();
  };
  if (!attn && g) prog(BoolC<false>{}, IntC<1>{});
  else if (!attn) prog(BoolC<false>{}, IntC<0>{});
  else if (g) prog(BoolC<true>{}, IntC<1>{});
  else prog(BoolC<true>{}, IntC<0>{});
}

extern "C" void kernel_launch(void* const* d_in, const int* in_sizes, int n_in, void* d_out, int out_size, void* d_ws, size_t ws_size,
                              hipStream_t stream) {
  fused_kernel<<<256, 512, 0, stream>>>((const char*)d_in[0], (const char*)d_in[1], (const char*)d_in[5], (const char*)d_in[6], (char*)d_ws,
                                        (const char*)d_in[2], (const char*)d_in[7], (const char*)d_in[3], (char*)d_out);
}
```

```cpp
#include <hip/hip_runtime.h>
#include <hip/hip_bf16.h>
#include <utility>

#define DI __device__ __forceinline__
#define LAS __attribute__((address_space(3)))
typedef __attribute__((ext_vector_type(8))) short bf16x8;
typedef __attribute__((ext_vector_type(4))) short s16x4;
typedef __attribute__((ext_vector_type(2))) __bf16 bf2;
typedef __attribute__((ext_vector_type(2))) float f32x2;
typedef __attribute__((ext_vector_type(4))) float f32x4;
typedef __attribute__((ext_vector_type(16))) float f32x16;
typedef __attribute__((ext_vector_type(2))) unsigned u32x2;
typedef __attribute__((ext_vector_type(4))) unsigned u32x4;
typedef __amdgpu_buffer_rsrc_t rsrc_t;
#define F2U(x) __builtin_bit_cast(unsigned, (float)(x))
#define U2F(x) __builtin_bit_cast(float, (unsigned)(x))
#define ALL(x) (__builtin_amdgcn_ballot_w64(!(x)) == 0ull)
#define ANY(x) (__builtin_amdgcn_ballot_w64(x) != 0ull)
#define UMAX(a, b) __builtin_elementwise_max((unsigned)(a), (unsigned)(b))

#define NTX 0
#define NTW 0
#define NT2 0
constexpr unsigned XROW = 896, QROW = 2688, FROW = 3584;
constexpr unsigned FS = 128;
constexpr unsigned WS_EPOCH = 0;
constexpr unsigned WS_GR = 4096;
constexpr unsigned WS_KVP = 131072;
constexpr unsigned WS_A2 = WS_KVP + 16 * 16 * 8192;
constexpr unsigned WS_ST = WS_A2 + 64 * 144 * 1024;
constexpr unsigned WS_FH = WS_ST + 262144;
constexpr unsigned WS_KVR = WS_FH + 2048 * FS;
constexpr unsigned WS_END = WS_KVR + 16 * 8192;
constexpr unsigned L_GU = 65536, L_KV = 131072;
constexpr unsigned L_TAIL = L_KV + 3 * 8192;
constexpr unsigned L_WPK = L_TAIL;
constexpr unsigned L_ST = L_TAIL + 2048;
constexpr unsigned L_CS = L_TAIL + 4096;
constexpr unsigned L_PHI = L_GU + 32768;
constexpr unsigned L_V = L_KV + 16384;
constexpr unsigned L_FREE = 32768;

#define MFMA(a, b, c) __builtin_amdgcn_mfma_f32_32x32x16_bf16((a), (b), (c), 0, 0, 0)
#define MFMA16(a, b, c) __builtin_amdgcn_mfma_f32_16x16x32_bf16((a), (b), (c), 0, 0, 0)
#define AI __attribute__((always_inline))
#define BAR() __builtin_amdgcn_s_barrier()
#define WAITV(n) asm volatile("s_waitcnt vmcnt(" #n ")" ::: "memory")
#define WAITL0() asm volatile("s_waitcnt lgkmcnt(0)" ::: "memory")
#define SB() __builtin_amdgcn_sched_barrier(0)
#define MEMFENCE() asm volatile("" ::: "memory")

template <int N> DI void waitv() { asm volatile("s_waitcnt vmcnt(%c0)" :: "i"(N) : "memory"); }

#define INSZ(x) (x)
DI rsrc_t mkrs(const void* p, unsigned bytes) { return __builtin_amdgcn_make_buffer_rsrc((void*)p, (short)0, (int)bytes, 0x00020000); }
template <bool NT = false> DI void dma(unsigned lds, unsigned limm, unsigned voff, rsrc_t rs, unsigned soff, unsigned simm) {
  unsigned t;
  if constexpr (NT) asm volatile("s_add_u32 m0, %1, %c5\n\ts_add_u32 %0, %4, %c6\n\tbuffer_load_dwordx4 %2, %3, %0 offen nt lds"
               : "=&s"(t) : "s"(lds), "v"(voff), "s"(rs), "s"(soff), "i"(limm), "i"(simm) : "memory", "scc");
  else asm volatile("s_add_u32 m0, %1, %c5\n\ts_add_u32 %0, %4, %c6\n\tbuffer_load_dwordx4 %2, %3, %0 offen lds"
               : "=&s"(t) : "s"(lds), "v"(voff), "s"(rs), "s"(soff), "i"(limm), "i"(simm) : "memory", "scc");
}
DI void dmat(unsigned lds, unsigned voff, rsrc_t rs) {
  asm volatile("s_mov_b32 m0, %0\n\ts_nop 0\n\tbuffer_load_dword %1, %2, 0 offen lds" :: "s"(lds), "v"(voff), "s"(rs) : "memory");
}
DI s16x4 trd(unsigned a) { return __builtin_amdgcn_ds_read_tr16_b64_v4i16((LAS s16x4*)(size_t)a); }
DI s16x4 trd2(unsigned a, unsigned off) { return __builtin_amdgcn_ds_read_tr16_b64_v4i16((LAS s16x4*)((LAS char*)(size_t)a + off)); }
DI bf16x8 tr2(unsigned a, unsigned off0, unsigned off1) {
  LAS char* p = (LAS char*)(size_t)a;
  s16x4 lo = __builtin_amdgcn_ds_read_tr16_b64_v4i16((LAS s16x4*)(p + off0));
  s16x4 hi = __builtin_amdgcn_ds_read_tr16_b64_v4i16((LAS s16x4*)(p + off1));
  return __builtin_shufflevector(lo, hi, 0, 1, 2, 3, 4, 5, 6, 7);
}
template <class T> DI T ldsr(unsigned a, unsigned off) { return *(LAS T*)((LAS char*)(size_t)a + off); }
template <class T> DI void ldsw(unsigned a, unsigned off, T v) { *(LAS T*)((LAS char*)(size_t)a + off) = v; }
DI unsigned pk2(float a, float b) { f32x2 f = {a, b}; return __builtin_bit_cast(unsigned, __builtin_convertvector(f, bf2)); }
DI float rbf(float a) { return U2F(pk2(a, a) << 16); }
DI float dot2(unsigned a, unsigned b, float c) { return __builtin_amdgcn_fdot2_f32_bf16(__builtin_bit_cast(bf2, a), __builtin_bit_cast(bf2, b), c, false); }
DI float xsum(float v) { auto r = __builtin_amdgcn_permlane32_swap(F2U(v), F2U(v), false, false); float a = U2F(r[0]); asm("" : "+v"(a)); return a + U2F(r[1]); }
DI float xmax(float v) { auto r = __builtin_amdgcn_permlane32_swap(F2U(v), F2U(v), false, false); return __builtin_fmaxf(U2F(r[0]), U2F(r[1])); }
DI float xmin(float v) { auto r = __builtin_amdgcn_permlane32_swap(F2U(v), F2U(v), false, false); return __builtin_fminf(U2F(r[0]), U2F(r[1])); }
DI float xhi(float v) { auto r = __builtin_amdgcn_permlane32_swap(F2U(v), F2U(v), false, false); return U2F(r[1]); }
#define STB "sc1"
DI void st16(u32x4 v, rsrc_t rs, unsigned voff, unsigned soff, unsigned simm) {
  unsigned t;
  asm volatile("s_add_u32 %0, %4, %c5\n\tbuffer_store_dwordx4 %1, %2, %3, %0 offen " STB "\n\ts_nop 1" : "=&s"(t) : "v"(v), "v"(voff), "s"(rs), "s"(soff), "i"(simm) : "memory", "scc");
}
DI void stflag(unsigned v, rsrc_t rs, unsigned soff) {
  unsigned voff = (__builtin_amdgcn_mbcnt_hi(~0u, __builtin_amdgcn_mbcnt_lo(~0u, 0u)) == 0u) ? 0u : 0x80000000u;
  __builtin_amdgcn_raw_buffer_store_b32(v, rs, voff, soff, 16);
}
#define XORCOND (n == 24)
#define PS 0
#define PV 0
#define KEEP2 0
#define S2N 14
#define B1N 23
#define DIST 2
#define P2G1 1
#define OAUX 0
#define RECY 0
#define POFF 99
#define H1S 0
#define H0L 14
#define H0C 22
#define Q3A 1
#define COOP 0
#define PON -2
#define XN 20
#define BAN 18
#define BBN 21
#define SL0 8
#define SLN 2
#define SLD 3
#define NSL 48
#define H1C 16
#define OV vL16
#define CV vL16
#define Q6E 0
#define TEARLY 0
#define NOCS 0
#define TPOS 0
#define SLEEPR 4
#define SLEEPN 2
DI u32x4 ld16(rsrc_t rs, unsigned voff, unsigned soff, unsigned simm) {
  u32x4 r; unsigned t;
  asm volatile("s_add_u32 %1, %4, %c5\n\tbuffer_load_dwordx4 %0, %2, %3, %1 offen sc1" : "=v"(r), "=&s"(t) : "v"(voff), "s"(rs), "s"(soff), "i"(simm) : "memory", "scc");
  return r;
}
template <unsigned STEP> DI void ld8w(u32x4 (&p)[8], rsrc_t rs, unsigned voff, unsigned soff) {
  unsigned t;
#define L1(k) "buffer_load_dwordx4 %" #k ", %9, %10, %8 offen sc1\n\ts_add_u32 %8, %8, %c12\n\t"
  asm volatile("s_mov_b32 %8, %11\n\t" L1(0) L1(1) L1(2) L1(3) L1(4) L1(5) L1(6) L1(7) "s_waitcnt vmcnt(0)"
               : "=&v"(p[0]), "=&v"(p[1]), "=&v"(p[2]), "=&v"(p[3]), "=&v"(p[4]), "=&v"(p[5]), "=&v"(p[6]), "=&v"(p[7]), "=&s"(t)
               : "v"(voff), "s"(rs), "s"(soff), "i"(STEP) : "memory", "scc");
#undef L1
}
DI bool untag(u32x4 (&p)[8], unsigned tag, unsigned mask) {
  unsigned chk = 0;
#pragma unroll
  for (int i = 0; i < 8; i++)
#pragma unroll
    for (int k = 0; k < 4; k++) { p[i][k] ^= tag; chk |= p[i][k]; }
  return ALL((chk & mask) == 0u);
}
template <unsigned STEP> DI void waittag(u32x4 (&p)[8], rsrc_t rs, unsigned voff, unsigned soff, unsigned tag, unsigned mask) {
  asm volatile("s_waitcnt vmcnt(0)" : "+v"(p[0]), "+v"(p[1]), "+v"(p[2]), "+v"(p[3]), "+v"(p[4]), "+v"(p[5]), "+v"(p[6]), "+v"(p[7]) :: "memory");
#pragma nounroll
  for (int it = 0; !untag(p, tag, mask) && it < (1 << 16); ++it) { __builtin_amdgcn_s_sleep(SLEEPN); ld8w<STEP>(p, rs, voff, soff); }
}
template <bool WAIT> DI void rel1(u32x4& a, u32x4& b, rsrc_t rs, unsigned voff, unsigned so) {
  unsigned t;
  if constexpr (WAIT) asm volatile("buffer_load_dwordx4 %0, %3, %4, %5 offen sc1\n\ts_add_u32 %2, %5, 147456\n\tbuffer_load_dwordx4 %1, %3, %4, %2 offen sc1\n\ts_waitcnt vmcnt(0)"
                                   : "+v"(a), "+v"(b), "=&s"(t) : "v"(voff), "s"(rs), "s"(so) : "memory", "scc");
  else asm volatile("buffer_load_dwordx4 %0, %3, %4, %5 offen sc1\n\ts_add_u32 %2, %5, 147456\n\tbuffer_load_dwordx4 %1, %3, %4, %2 offen sc1"
                    : "+v"(a), "+v"(b), "=&s"(t) : "v"(voff), "s"(rs), "s"(so) : "memory", "scc");
}
DI void poll(rsrc_t rs, unsigned voff, unsigned soff, unsigned E, unsigned need) {
  voff = need ? voff : 0x80000000u;
#pragma nounroll
  for (int it = 0; it < (1 << 16); ++it) {
    unsigned v;
    asm volatile("buffer_load_dword %0, %1, %2, %3 offen sc1\n\ts_waitcnt vmcnt(0)" : "=v"(v) : "v"(voff), "s"(rs), "s"(soff) : "memory");
    bool ok = (need == 0u) || ((v - E - need) <= (3u - need));
    if (ALL(ok)) break;
    __builtin_amdgcn_s_sleep(SLEEPN);
  }
}

template <bool B> struct BoolC { static constexpr bool value = B; };
template <int I> struct IntC { static constexpr int value = I; };
template <int... I, class F> DI void sfor_impl(std::integer_sequence<int, I...>, F&& f) { (f(IntC<I>{}), ...); }
template <int N, class F> DI void sfor(F&& f) { sfor_impl(std::make_integer_sequence<int, N>{}, f); }
#define SFOR(n, N) sfor<N>([&](auto n##_) AI { constexpr int n = decltype(n##_)::value;
#define SEND });
struct RowC { float r, K1, K2, Ri, Lr; };

DI unsigned hilo(float v) { float hi = rbf(v); return pk2(hi, v - hi); }
DI float max3(float a, float b, float c) { return __builtin_fmaxf(__builtin_fmaxf(a, b), c); }
DI float min3(float a, float b, float c) { return __builtin_fminf(__builtin_fminf(a, b), c); }
DI unsigned pk2s(float a, float b) { unsigned r; asm("v_cvt_pk_bf16_f32 %0, %1, %2" : "=v"(r) : "v"(a), "v"(b)); return r; }
DI f32x2 sub2(const f32x16& v, int k) { f32x2 t = {v[2 * k], v[2 * k + 1]}; return t; }
DI f32x2 exp2v(f32x2 v) { f32x2 r = {__builtin_amdgcn_exp2f(v[0]), __builtin_amdgcn_exp2f(v[1])}; return r; }
DI f32x2 dup2(float a) { f32x2 r = {a, a}; asm("" : "+v"(r)); return r; }
DI f32x2 fma2(f32x2 a, f32x2 b, f32x2 c) { return __builtin_elementwise_fma(a, b, c); }
DI u32x4 epiP(const f32x16& a, const RowC& c) {
  u32x4 r;
  const f32x2 K1 = dup2(c.K1), K2 = dup2(c.K2), Ri = dup2(c.Ri);
#pragma unroll
  for (int i = 0; i < 4; i++) {
    const f32x2 g = sub2(a, i), d = fma2(exp2v(g * fma2(g * g, K2, K1)), Ri, Ri);
    const f32x2 s = {__builtin_amdgcn_rcpf(d[0]), __builtin_amdgcn_rcpf(d[1])}, h = (g * sub2(a, 4 + i)) * s;
    r[i] = pk2(h[0], h[1]);
  }
  return r;
}
struct EpiS { float x0, x1, m0, m1; u32x4 r; };
template <int P, int PH> DI void epiSlice(const f32x16& a, const RowC& c, EpiS& s) {
  const float g0 = a[2 * P], g1 = a[2 * P + 1];
  if constexpr (PH == 0) { s.x0 = g0 * __builtin_fmaf(g0 * g0, c.K2, c.K1); s.x1 = g1 * __builtin_fmaf(g1 * g1, c.K2, c.K1); }
  if constexpr (PH == 1) { s.x0 = __builtin_amdgcn_exp2f(s.x0); s.x1 = __builtin_amdgcn_exp2f(s.x1); s.m0 = g0 * a[8 + 2 * P]; s.m1 = g1 * a[9 + 2 * P]; }
  if constexpr (PH == 2) { s.x0 = __builtin_amdgcn_rcpf(__builtin_fmaf(s.x0, c.Ri, c.Ri)); s.x1 = __builtin_amdgcn_rcpf(__builtin_fmaf(s.x1, c.Ri, c.Ri)); }
  if constexpr (PH == 3) s.r[P] = pk2s(s.m0 * s.x0, s.m1 * s.x1);
}
DI f32x16 featproj2(const f32x16& a, bf16x8 w0, bf16x8 w1, bool hh) {
  u32x4 q0, q1;
#pragma unroll
  for (int i = 0; i < 4; i++) { q0[i] = pk2(a[2 * i], a[2 * i + 1]); q1[i] = pk2(a[8 + 2 * i], a[9 + 2 * i]); }
  q1[2] = hh ? 0u : q1[2]; q1[3] = hh ? 0u : q1[3];
  f32x16 z;
#pragma unroll
  for (int i = 0; i < 16; i++) z[i] = 0.f;
  z = MFMA(w0, __builtin_bit_cast(bf16x8, q0), z);
  z = MFMA(w1, __builtin_bit_cast(bf16x8, q1), z);
  return z;
}
DI f32x16 featproj(const f32x16& a, unsigned wpaddr, unsigned off, bool hh) {
  return featproj2(a, tr2(wpaddr, off, off + 8 * 56), tr2(wpaddr, off + 16 * 56, off + 24 * 56), hh);
}
DI float max16(const f32x16& p) { float m = max3(p[0], p[1], p[2]); _Pragma("unroll") for (int i = 3; i < 15; i += 2) m = max3(m, p[i], p[i + 1]); return xmax(__builtin_fmaxf(m, p[15])); }
DI float min16(const f32x16& p) { float m = min3(p[0], p[1], p[2]); _Pragma("unroll") for (int i = 3; i < 15; i += 2) m = min3(m, p[i], p[i + 1]); return xmin(__builtin_fminf(m, p[15])); }
DI f32x16 dualexpP(const f32x16& p, float Lr) {
  const f32x2 bx = dup2(-max16(p) * Lr), bn = dup2(min16(p) * Lr), L2 = dup2(Lr);
  f32x16 o;
#pragma unroll
  for (int i = 0; i < 8; i++) {
    const f32x2 ep = exp2v(fma2(sub2(p, i), L2, bx)), en = exp2v(fma2(sub2(p, i), -L2, bn));
    o[i] = U2F(pk2(ep[0], ep[1])); o[8 + i] = U2F(pk2(en[0], en[1]));
  }
  return o;
}
struct SmS { f32x16 p, e; float en[16], bx, bn, sp, sn, tp, tn; bf16x8 w0, w1; };
DI bf16x8 sub4(const f32x16& v, int k) { f32x4 t = {v[4 * k], v[4 * k + 1], v[4 * k + 2], v[4 * k + 3]}; return __builtin_bit_cast(bf16x8, t); }

DI constexpr unsigned lc1_base(int ch) { return ch == 6 ? L_KV : ((ch & 1) ? L_GU : 0u) + (ch < 2 ? 49152u : ch < 4 ? 0u : 16384u); }
template <int CTRL, int RM> DI float dppadd(float v) { return v + U2F(__builtin_amdgcn_update_dpp(0u, F2U(v), CTRL, RM, 0xf, false)); }
DI float wavesum63(float v) {
  v = dppadd<0x111, 0xf>(v); v = dppadd<0x112, 0xf>(v); v = dppadd<0x114, 0xf>(v); v = dppadd<0x118, 0xf>(v);
  v = dppadd<0x142, 0xa>(v); return dppadd<0x143, 0xc>(v);
}

#define STAMP(i)
#define STAMPS_OUT()
#define CSTAMP(i)

#define PDV 5
constexpr int PD = PDV, RING = 9;
constexpr int later2(int i, int NS, int GG) {
  const int younger = 2 * ((i + PD <= NS ? i + PD : NS) - 1 - i);
  int extra = i < PD ? 1 : 0;
  if (GG == 0 && (i == 10 || i == 11)) extra += 2 * (i - 9);
  return younger + extra;
}
DI constexpr unsigned ring_area(int j) { return j == 0 ? L_FREE : j == 1 ? L_PHI : lc1_base(j - 2); }
constexpr int cumG1(int s) { return s < 4 ? 0 : s < 22 ? (s - 4) / 2 : (9 + (s - 22) * 5 / 4 < 48 ? 9 + (s - 22) * 5 / 4 : 48); }

#define CODE_BYTES 65536
__global__ __launch_bounds__(512, 2) void fused_kernel(const char* px_, const char* pqkv_, const char* pwg_, const char* pwu_, char* ws_, const char* pphi_,
                                                       const char* pwd_, const char* pwo_, char* pout_) {
  typedef __attribute__((ext_vector_type(8))) unsigned long long u64x8;
  u64x8 ka; unsigned long long kb;
  asm volatile("s_load_dwordx16 %0, %2, 0\n\ts_load_dwordx2 %1, %2, 64" : "=&s"(ka), "=&s"(kb) : "s"(__builtin_amdgcn_kernarg_segment_ptr()));
  __shared__ __attribute__((aligned(16))) char smem[163840];
  const unsigned LB = (unsigned)(size_t)smem;
  const unsigned tid = __builtin_amdgcn_workitem_id_x(), lane = tid & 63;
  const unsigned w = __builtin_amdgcn_readfirstlane(tid >> 6), g = w >> 2, wr = w & 3;
  const unsigned b = __builtin_amdgcn_workgroup_id_x(), xcd = b & 7, bi = b >> 3, grp = xcd >> 1, half = xcd & 1, rbl = bi & 3, cp = bi >> 2, ci = cp - 1;
  const unsigned rb = 4 * grp + rbl, c = 7 * half + ci;
  const unsigned head = 8 * half + (((half ? 0x76425310u : 0x75316420u) >> (4 * cp)) & 7);
  const bool attn = cp == 0;
  STAMP(0); CSTAMP(14);
  const unsigned wstr = attn ? QROW : FROW;
  const unsigned sG0 = half * (attn ? 448 : 1792) + (attn ? 0 : ci * 256), sX0 = rb * 128 * XROW;
  unsigned vx0, vx1, vG, vU, vKV;
  auto voffs = [&](unsigned ln) AI {
    const unsigned r8 = ln >> 3, c8 = ln & 7, sa = (r8 >> 1) & 1, lc = c8 ^ (4 * sa);
    vx0 = (16 * w + r8) * XROW + ((c8 ^ (r8 >> 1)) << 4); vx1 = (vx0 ^ 64u) + 8 * XROW;
    const unsigned cg = attn ? lc : (c8 ^ (2 * sa)), cu = attn ? (lc >= 6 ? lc - 2 : lc) : (cg ^ 4);
    vG = (8 * w + r8) * wstr + (cg << 4); vU = (8 * w + r8) * wstr + (cu << 4);
    vKV = (8 * w + r8) * QROW + 896 * (1 + (lc >> 2)) + ((lc & 3) << 4) + ((56 * head) & ~15u);
  };
  voffs(lane);
  const unsigned lw1 = LB + w * 1024, lw2 = LB + w * 2048;
#define ROT (((0x24924u >> (2 * (cp + rbl))) & 3) * 2)
  const unsigned rot = ROT;
  const unsigned srow = 128 * rb + 64 * half + 8 * cp + w;
  unsigned vrow = lane < 56 ? lane * 16 : 0x80000000u;
  asm volatile("s_waitcnt lgkmcnt(0)" : "+s"(ka), "+s"(kb), "+v"(vx0), "+v"(vx1), "+v"(vG), "+v"(vU), "+v"(vKV), "+v"(vrow));
  const char *px = (const char*)ka[0], *pqkv = (const char*)ka[1], *pwg = (const char*)ka[2], *pwu = (const char*)ka[3], *pphi = (const char*)ka[5],
             *pwd = (const char*)ka[6], *pwo = (const char*)ka[7];
  char *ws = (char*)ka[4], *pout = (char*)kb;
  const rsrc_t rsX = mkrs(px, INSZ(2048 * XROW)), rsQ = mkrs(pqkv, INSZ(448 * QROW));
  const rsrc_t rsG = mkrs(attn ? pqkv : pwg, INSZ(448 * wstr)), rsU = mkrs(attn ? pqkv + 128 : pwu, INSZ(448 * wstr - (attn ? 128 : 0)));
  auto cidx = [&](int st) AI -> unsigned { unsigned t = rot + st; return t >= 7 ? t - 7 : t; };
  auto issueA = [&](int st, int k) AI {
    const unsigned sl = st & 3, ch = cidx(st);
    if (k == 0) dma<NTX>(lw2, sl * 16384, vx0, rsX, sX0 + ch * 128, 0);
    if (k == 1) dma<NTX>(lw2, sl * 16384 + 1024, vx1, rsX, sX0 + ch * 128, 0);
    if (k == 2) dma<NTW>(lw1, L_GU + sl * 16384, vG, rsG, sG0 + ch * 64 * wstr, 0);
    if (k == 3) dma<NTW>(lw1, L_GU + sl * 16384 + 8192, vU, rsU, sG0 + ch * 64 * wstr, 0);
    if (k == 4) dma<NTW>(lw1, L_KV + sl * 8192, vKV, rsQ, ch * 64 * QROW, 0);
  };
  const rsrc_t rsWS = mkrs(ws, WS_END);
  unsigned Ev, cpf; u32x4 xrw;
  asm volatile("buffer_load_dwordx4 %0, %1, %2, %3 offen" : "=v"(xrw) : "v"(vrow), "s"(rsX), "s"(srow * XROW) : "memory");
  asm volatile("buffer_load_dword %0, off, %1, 0 sc1" : "=v"(Ev) : "s"(rsWS) : "memory");
  {
    unsigned long long pc; asm volatile("s_getpc_b64 %0" : "=s"(pc));
    const unsigned vo = w == 0 && lane < 24 ? (bi * 24 + lane) * 128 : 0x80000000u;
    asm volatile("buffer_load_dword %0, %1, %2, 0 offen" : "=v"(cpf) : "v"(vo), "s"(mkrs((const void*)pc, CODE_BYTES)) : "memory");
  }
#pragma unroll
  for (int k = 0; k < 5; k++) issueA(0, k);
  asm volatile("s_waitcnt vmcnt(%c3)" : "+v"(xrw), "+v"(Ev), "+v"(cpf) : "i"(5 + COOP) : "memory");
#pragma unroll
  for (int k = 0; k < 5; k++) issueA(1, k);
  issueA(2, 0);
  const unsigned E = __builtin_amdgcn_readfirstlane(Ev);
  {
    float s1 = 0.f, s2 = 0.f;
#pragma unroll
    for (int i = 0; i < 4; i++) { s1 = dot2(xrw[i], 0x3f803f80u, s1); s2 = dot2(xrw[i], xrw[i], s2); }
    s1 = wavesum63(s1); s2 = wavesum63(s2);
    const float mean = s1 * (1.f / 448.f), var = __builtin_fmaxf(s2 * (1.f / 448.f) - mean * mean, 0.f);
    u32x2 gr = {pk2(-mean, __builtin_amdgcn_sqrtf(rbf(rbf(var) + 1e-5f))), E + 1};
    __builtin_amdgcn_raw_buffer_store_b64(gr, rsWS, lane == 63 ? 0u : 0x80000000u, WS_GR + srow * 8, 16);
  }

  auto prog = [&](auto at_, auto gg_) AI {
  constexpr bool AT = decltype(at_)::value; constexpr int GG = decltype(gg_)::value;
  constexpr unsigned lcstep = AT ? 224 : 128;
  unsigned lane;
  asm volatile("v_mbcnt_lo_u32_b32 %0, -1, 0\n\tv_mbcnt_hi_u32_b32 %0, -1, %0" : "=v"(lane));
  __builtin_assume(lane < 64);
  voffs(lane);
  auto issueL = [&](int hc) AI {
    const int st = hc >> 1; const unsigned ch = cidx(st);
    if (hc & 1) dma<NTW>(lw1, lc1_base(st) + 8192, vU, rsU, sG0 + ch * 64 * wstr, lcstep);
    else dma<NTW>(lw1, lc1_base(st), vG, rsG, sG0 + ch * 64 * wstr, lcstep);
  };
  auto issueG = [&](int bar, int k) AI {
    if (bar == 0) issueA(2 + k / 5, k % 5);
    else if (bar <= 3) issueA(bar + 3, k);
    else if (k < 4) issueL(4 * (bar - 4) + k);
    else if (bar == 4) dma(LB + (w & 1) * 1024, L_WPK, lane * 16, mkrs(pphi, 16 * 1568), head * 1568 + (w & 1) * 1024, 0);
    else issueL(12 + (bar - 5));
  };

  const unsigned rt = 4 * rb + wr;
  const unsigned sA2 = WS_A2 + rt * 144 * 1024 + (AT ? half * 16384 : c * 8192), vL16 = lane * 16;
  const u32x4 sent = {~0u, ~0u, ~0u, ~0u};
  auto clr = [&](int ks) AI { st16(sent, rsWS, CV, sA2, ks * 1024); };

  auto clri = [&](int q) AI { clr(GG ? (q < 6 ? 114 + q : 120 + q) : (q < 2 ? 112 + q : 118 + q)); };

  const bool hh = lane >= 32;
  const unsigned hu = lane >> 5, blk = (lane >> 4) & 1, q4 = (lane >> 2) & 3, p4 = lane & 3, ta = (q4 >> 1) & 1, l31 = lane & 31;
  const unsigned perm = wr ^ (2 * GG);
  const unsigned rowoff = (8 * hu + q4) * 128 + (perm << 11);
  const unsigned pc = (4 * blk + p4) < 6 ? (4 * blk + p4) : 6;
  auto tileaddr = [&](unsigned t) AI -> unsigned {
    if constexpr (AT) { unsigned o = 56 * t + 8 * pc; return (o >> 7) * 8192 + ((((o >> 4) & 7) ^ (4 * ta)) << 4) + (o & 8) + rowoff; }
    else return blk * 8192 + (((2 * t + (p4 >> 1)) ^ (4 * blk + 2 * ta)) << 4) + 8 * (p4 & 1) + rowoff;
  };
  constexpr int NT = !AT ? 2 : GG ? 1 : 3, TB0 = GG ? 4 - NT : 0;
  unsigned aA[3], aB[NT];
  {
    unsigned o = (head & 1) * 8 + 8 * pc;
    unsigned ak = L_KV + (((o >> 4) ^ (4 * ta)) << 4) + (o & 8) + rowoff;
    aA[0] = LB + (GG ? L_GU + tileaddr(1) : ak);
    aA[1] = LB + (GG ? L_GU + tileaddr(2) : (ak ^ 64u));
    aA[2] = LB + L_GU + tileaddr(GG ? 3 : 0);
#pragma unroll
    for (int j = 0; j < NT; j++) aB[j] = LB + tileaddr(TB0 + j);
  }
  const unsigned xrow = 32 * wr + l31;
  const unsigned aX = LB + xrow * 128 + (((2 * perm + hu) ^ ((xrow >> 1) & 7)) << 4);
  const unsigned selv = (((lane >> 4) ^ (lane >> 2)) & 1) ? 0u : 0x3f803f80u;
  const u32x4 sel4 = {selv, selv, selv, selv};
  const bf16x8 sel = __builtin_bit_cast(bf16x8, sel4);

  bf16x8 xf[28], wa[2][3], wq[8];
  f32x16 accA[3], accB[NT];
  f32x4 csA[3], csB[NT];
#pragma unroll
  for (int j = 0; j < 3; j++) { for (int i = 0; i < 16; i++) accA[j][i] = 0.f; for (int i = 0; i < 4; i++) csA[j][i] = 0.f; }
#pragma unroll
  for (int j = 0; j < NT; j++) { for (int i = 0; i < 16; i++) accB[j][i] = 0.f; for (int i = 0; i < 4; i++) csB[j][i] = 0.f; }
  u32x2 grn;

  const rsrc_t rsWD = mkrs(pwd, INSZ(1792 * XROW)), rsWO = mkrs(pwo, INSZ(448 * XROW));
  const unsigned vW2 = (lane >> 3) * XROW + (((lane & 7) ^ ((lane >> 2) & 4)) << 4) + ci * 128;
  constexpr int NS = GG ? 16 : 20;
#define RHO ((rbl + cp) & 3)
  const unsigned rho = RHO;
  auto t_of = [](int i) constexpr { return (0x54763210u >> (4 * (i >> 1))) & 7; };
  auto ksb = [&](int i) AI -> unsigned {
    if (GG) return 16 * wr + ((t_of(i) & 3) ^ rho);
    if (i < 12) { const unsigned cc = i % 6 + rho + (rho >> 1); return wr + 8 * (cc >= 6 ? cc - 6 : cc); }
    return 8 * wr + 2 * (((i - 12) >> 1) ^ rho);
  };
  auto ksi = [&](int i) AI -> unsigned { return GG ? 8 * (i & 1) + (t_of(i) & 4) : i < 12 ? 64 + 4 * (i / 6) : 112 + (i & 1); };
  auto issueW2 = [&](int i, int hf) AI {
    if (GG == 0 && i >= 12) dma<NT2>(lw2, ring_area(i % RING) + hf * 1024, vW2, rsWO, ksb(i) * 14 * XROW, (16 * (i & 1) + 8 * hf) * XROW);
    else dma<NT2>(lw2, ring_area(i % RING) + hf * 1024, vW2, rsWD, ksb(i) * 16 * XROW, (ksi(i) * 16 + 8 * hf) * XROW);
  };

  const unsigned aCSw = LB + L_CS + l31 * 16 + wr * 4 + GG * 2048, aCSr = LB + L_CS + l31 * 16 + GG * 2048;
  auto readX = [&](int n) AI { xf[n] = ldsr<bf16x8>(aX ^ (unsigned)((n & 3) << 5), ((n >> 2) & 3) * 16384); };
  auto readA = [&](int n, int j) AI {
    const unsigned o = ((n >> 2) & 3) * ((GG == 0 && j < 2) ? 8192 : 16384);
    wa[n & 1][j] = tr2(aA[j] ^ (unsigned)((n & 3) << 11), o, o + 512);
  };
  auto readB = [&](int q) AI {
    const int n = q / NT, j = q % NT;
    const unsigned base = lc1_base(n >> 2);
    if ((NT == 3 && n < XN) ? (n & 3) == 0 : n == 24) asm volatile("" : "+v"(aB[j]));
    wq[q & 7] = tr2(aB[j] ^ (((n & 3) << 11) | (base & 0x30000u)), base & 0xffffu, (base & 0xffffu) + 512);
  };
  RowC rc; unsigned nm, denb;
  auto mkxe = [&]() AI { asm volatile("" : "+v"(nm)); u32x4 t = {nm, nm, nm, nm}; return __builtin_bit_cast(bf16x8, t); };
  auto rowconst = [&]() AI {
#pragma nounroll
    for (int it = 0; it < (1 << 16) && !ALL(grn[1] == E + 1); ++it) {
      __builtin_amdgcn_s_sleep(SLEEPN);
      asm volatile("buffer_load_dwordx2 %0, %1, %2, %3 offen sc1\n\ts_waitcnt vmcnt(0)" : "=v"(grn) : "v"(xrow * 8), "s"(rsWS), "s"(WS_GR + rb * 1024) : "memory");
    }
    const float den = U2F(grn[0] & 0xffff0000u);
    nm = hh ? 0u : __builtin_amdgcn_perm(grn[0], grn[0], 0x01000100u);
    denb = grn[0] >> 16;
    rc.r = __builtin_amdgcn_rcpf(den);
    const float k1 = -2.f * 1.4426950408889634f * 0.7978845608028654f;
    rc.K1 = k1 * rc.r; rc.K2 = (k1 * 0.044715f) * rc.r * (rc.r * rc.r); rc.Ri = den * den; rc.Lr = 1.4426950408889634f * rc.r;
  };
  waitv<7 + COOP>();
  BAR(); MEMFENCE();
  STAMP(1);
  readX(0); readA(0, 0); readA(0, 1); readA(0, 2);
  SFOR(n, 28)
    SB();
    if constexpr (n == 27) {
#pragma unroll
      for (int j = 0; j < 3; j++) ldsw<unsigned>(aCSw, j * 512, hilo(csA[j][0]));
    }
    if constexpr ((n & 3) == 3) {
      if constexpr (n == 27) WAITV(0);
      else if constexpr (n == 23) asm volatile("s_waitcnt vmcnt(10)" : "+v"(grn) :: "memory");
      else if constexpr (n == 3 || n == 15 || n == 19) WAITV(11); else WAITV(10);
      { WAITL0(); BAR(); MEMFENCE(); }
      if constexpr (n == 27) STAMP(2);
    }
    constexpr int bar = (n + 1) >> 2, ph = (n + 1) & 3;
#pragma unroll
    for (int j = 0; j < 3; j++) {
      if constexpr (n < 27) { if (j == 0) readX(n + 1); readA(n + 1, j); }
      else { wa[0][j] = ldsr<bf16x8>(aCSr, j * 512); if (GG == 1 && j < 2) { readB(2 * j); readB(2 * j + 1); } }
      SB();
      accA[j] = MFMA(wa[n & 1][j], xf[n], accA[j]);
      const int slot = 3 * ph + j;
      if (bar == 0) issueG(0, slot - 2);
      else if (bar == 6) { if (slot < 5) issueG(bar, slot); }
      else if (bar <= 6) { if (slot == 0) issueG(bar, 0); if (slot == 2) issueG(bar, 1); if (slot == 5) issueG(bar, 2); if (slot == 7) issueG(bar, 3); if (slot == 10) issueG(bar, 4); }
      else if (j < 2) {
        if constexpr (AT) dma(lw2, L_FREE + j * 1024, lane * 16, mkrs(pphi, 16 * 1568), half * 8 * 1568 + w * 2048, j * 1024);
        else issueW2(0, j);
      }
      if (!NOCS && (n & 3) == 0) csA[j] = MFMA16(sel, wa[n & 1][j], csA[j]);
      if (n == 25 && j == 0) rowconst();
      if (n == 14 && j == 2) asm volatile("buffer_load_dwordx2 %0, %1, %2, %3 offen sc1" : "=v"(grn) : "v"(xrow * 8), "s"(rsWS), "s"(WS_GR + rb * 1024) : "memory");
      SB();
    }
  SEND
  { const bf16x8 xe = mkxe();
#pragma unroll
  for (int j = 0; j < 3; j++) accA[j] = MFMA(wa[0][j], xe, accA[j]); }
  asm volatile("" : "+v"(accA[0]), "+v"(accA[1]), "+v"(accA[2]));
  SB();

  const unsigned sFown = WS_FH + (rt * 32 + (AT ? 28 + 2 * half : 2 * c) + GG) * FS;
  const unsigned aWP = LB + (4 * hu + q4) * 56 + 8 * pc;
  const unsigned T12 = (E + 1) & 0xfffu, T16 = (E + 1) & 0xffffu;
  const unsigned rt2 = 2 * (8 * grp + 4 * half + rbl);
  const unsigned sF = WS_FH + rt2 * 32 * FS;
  const unsigned fc = l31 >> 1;
  const bool nd1 = GG ? (fc >> 1) == wr : (fc >= 8 && fc < 14) || fc == 14 + (wr >> 1);
  const unsigned aIW = LB + L_PHI + xrow * 64 + ((hu ^ ((xrow >> 1) & 7)) << 3);
  SmS sm;
  unsigned aWK = aWP + L_WPK, aWQ = aWP + L_FREE;
  asm volatile("" : "+v"(aWK)); if constexpr (AT) asm volatile("" : "+v"(aWQ));
  auto kstep = [&](auto s_) AI {
    constexpr int s = decltype(s_)::value;
    if constexpr (s == 0) { sm.w0 = tr2(aWK, 0, 8 * 56); sm.w1 = tr2(aWK, 16 * 56, 24 * 56); }
    if constexpr (s == 1) sm.p = featproj2(accA[0], sm.w0, sm.w1, hh);
    if constexpr (s == 2 || s == 3) {
#pragma unroll
      for (int k = 2 * s - 4; k < 2 * s - 2; k++) {
        u32x2 vv = {pk2(accA[1][4 * k], accA[1][4 * k + 1]), pk2(accA[1][4 * k + 2], accA[1][4 * k + 3])};
        if (k == 3) vv[0] = hh ? denb : vv[0];
        ldsw<u32x2>(aIW ^ (unsigned)(k << 4), L_V - L_PHI, vv);
      }
    }
    if constexpr (s == 4) sm.bx = -max16(sm.p) * rc.Lr;
    if constexpr (s == 5) sm.bn = min16(sm.p) * rc.Lr;
    if constexpr (s >= 6 && s < 22) {
      constexpr int i = s - 6;
      sm.e[i] = __builtin_amdgcn_exp2f(__builtin_fmaf(sm.p[i], rc.Lr, sm.bx)); sm.en[i] = __builtin_amdgcn_exp2f(__builtin_fmaf(sm.p[i], -rc.Lr, sm.bn));
      if constexpr (i == 0) { sm.sp = sm.e[0]; sm.sn = sm.en[0]; } else if constexpr (i < 12) { sm.sp += sm.e[i]; sm.sn += sm.en[i]; }
      else if constexpr (i == 12) { sm.tp = sm.e[12]; sm.tn = sm.en[12]; } else { sm.tp += sm.e[i]; sm.tn += sm.en[i]; }
    }
    if constexpr (s == 22) { sm.sp = __builtin_amdgcn_rcpf(xsum(sm.sp + (hh ? 0.f : sm.tp))) * rc.r; sm.sn = __builtin_amdgcn_rcpf(xsum(sm.sn + (hh ? 0.f : sm.tn))) * rc.r; }
    if constexpr (s >= 23) {
#pragma unroll
      for (int u = (s == 29 ? 6 : s - 23); u <= (s == 29 ? 7 : s - 23); u++) {
        const int k = u >> 1;
        if (u & 1) { u32x2 t = {pk2s(sm.en[4 * k] * sm.sn, sm.en[4 * k + 1] * sm.sn), pk2s(sm.en[4 * k + 2] * sm.sn, sm.en[4 * k + 3] * sm.sn)}; ldsw<u32x2>(aIW ^ (unsigned)(k << 4), 8192, t); }
        else { u32x2 t = {pk2s(sm.e[4 * k] * sm.sp, sm.e[4 * k + 1] * sm.sp), pk2s(sm.e[4 * k + 2] * sm.sp, sm.e[4 * k + 3] * sm.sp)}; ldsw<u32x2>(aIW ^ (unsigned)(k << 4), 0, t); }
      }
    }
  };
  auto qstep = [&](auto s_, f32x16& a, unsigned lh) AI {
    constexpr int s = decltype(s_)::value;
    if constexpr (s == 0) { sm.w0 = tr2(aWQ, lh * 1568, lh * 1568 + 8 * 56); sm.w1 = tr2(aWQ, lh * 1568 + 16 * 56, lh * 1568 + 24 * 56); }
    if constexpr (s == 1) sm.p = featproj2(a, sm.w0, sm.w1, hh);
    if constexpr (s == 3) sm.bx = -max16(sm.p) * rc.Lr;
    if constexpr (s == 4) sm.bn = min16(sm.p) * rc.Lr;
    if constexpr (s >= 5 && s < 21) {
      constexpr int i = (s - 5) >> 1;
      float t0, t1;
      if constexpr ((s - 5) & 1) { t0 = __builtin_fmaf(sm.p[2 * i], -rc.Lr, sm.bn); t1 = __builtin_fmaf(sm.p[2 * i + 1], -rc.Lr, sm.bn); }
      else { t0 = __builtin_fmaf(sm.p[2 * i], rc.Lr, sm.bx); t1 = __builtin_fmaf(sm.p[2 * i + 1], rc.Lr, sm.bx); }
      a[8 * ((s - 5) & 1) + i] = U2F(pk2(__builtin_amdgcn_exp2f(t0), __builtin_amdgcn_exp2f(t1)));
      if constexpr (s == 20) asm volatile("" : "+v"(a));
    }
  };
  bf16x8 ka_[4], kb_[2][4]; f32x4 kd[2];
  auto kvstep = [&](auto s_) AI {
    constexpr int s = decltype(s_)::value;
    if constexpr (s == 0) {
      const unsigned g4 = lane >> 4, r0 = 8 * g4 + q4, sw = 4 * blk + (q4 >> 1);
      unsigned b0 = LB + L_PHI + r0 * 64 + ((p4 ^ sw) << 3);
      unsigned a0 = (b0 ^ ((wr >> 1) << 5)) + (wr & 1) * 8192;
#pragma unroll
      for (int k = 0; k < 4; k++) {
        ka_[k] = __builtin_shufflevector(trd2(a0, k * 2048), trd2(a0 ^ 16u, k * 2048 + 256), 0, 1, 2, 3, 4, 5, 6, 7);
        kb_[0][k] = __builtin_shufflevector(trd2(b0, L_V - L_PHI + k * 2048), trd2(b0 ^ 16u, L_V - L_PHI + k * 2048 + 256), 0, 1, 2, 3, 4, 5, 6, 7);
        kb_[1][k] = __builtin_shufflevector(trd2(b0 ^ 32u, L_V - L_PHI + k * 2048), trd2(b0 ^ 48u, L_V - L_PHI + k * 2048 + 256), 0, 1, 2, 3, 4, 5, 6, 7);
      }
    }
    if constexpr (s == 1) {
      f32x4 z = {0.f, 0.f, 0.f, 0.f}; kd[0] = z; kd[1] = z;
#pragma unroll
      for (int k = 0; k < 4; k++) { kd[0] = MFMA16(ka_[k], kb_[0][k], kd[0]); kd[1] = MFMA16(ka_[k], kb_[1][k], kd[1]); }
    }
    if constexpr (s == 3) {
#pragma unroll
      for (int d = 0; d < 2; d++) {
        u32x4 t;
#pragma unroll
        for (int i = 0; i < 4; i++) t[i] = (F2U(kd[d][i]) & ~0xfffu) | T12;
        st16(t, rsWS, (lane >> 4) * 512 + (lane & 15) * 16, ((head * 16 + rb) * 2 + (wr & 1)) * 4096 + (wr >> 1) * 2048, WS_KVP + d * 256);
      }
    }
  };
  auto qexp2 = [&](f32x16& a0, unsigned l0, f32x16& a1, unsigned l1) AI {
    const f32x16 p0 = featproj(a0, aWQ, l0 * 1568, hh), p1 = featproj(a1, aWQ, l1 * 1568, hh);
    a0 = dualexpP(p0, rc.Lr); a1 = dualexpP(p1, rc.Lr); asm volatile("" : "+v"(a0), "+v"(a1));
  };

  unsigned fl1 = 0;
  EpiS es;
  u32x4 pv[8];
  const unsigned vKP = l31 * 16 + hu * 8192, sKP = (rb * 32 + half) * 4096 + wr * 1024 + GG * 512, sKR = (8 * half + w) * 8192;
  auto hop1 = [&]() AI {
    waittag<16384>(pv, rsWS, vKP, sKP + WS_KVP, T12, 0xfffu);
    u32x4 o;
#pragma unroll
    for (int k = 0; k < 4; k++) {
      float sum = U2F(pv[0][k]);
#pragma unroll
      for (int i = 1; i < 8; i++) sum += U2F(pv[i][k]);
      sum = xsum(sum);
      sum = l31 == 29 ? 1.f : sum;
      if (GG == 1) sum = wr == 3 ? 0.f : sum;
      o[k] = (pk2s(sum, sum) & 0xffff0000u) | T16;
    }
    st16(o, rsWS, hh ? 0x80000000u : l31 * 16, (rb * 2 + half) * 4096 + wr * 1024 + GG * 512, WS_KVR);
    if (b == 0 && w == 4) stflag(E + 4, rsWS, WS_EPOCH);
    SB();
  };
  bf16x8 cb[NT];
  auto ldflag = [&]() AI { asm volatile("buffer_load_dword %0, %1, %2, %3 offen sc1" : "=v"(fl1) : "v"(nd1 ? lane * FS : 0x80000000u), "s"(rsWS), "s"(sF) : "memory"); };
  auto touch = [&](unsigned wo4) AI {
    const unsigned rw = (rbl * 8 + w + wo4) * 44 + lane;
    const bool dn = rw < 896;
    const unsigned vo = lane < 44 ? (dn ? ((rw >> 6) * 128 + 64 + (rw & 63)) : rw - 896) * XROW + ci * 128 : 0x80000000u;
    asm volatile("s_add_u32 m0, %0, %c5\n\ts_nop 0\n\tbuffer_load_dword %1, %3, 0 offen lds\n\tbuffer_load_dword %2, %4, 0 offen lds"
                 :: "s"(LB + w * 256), "v"(dn ? vo : 0x80000000u), "v"(dn ? 0x80000000u : vo), "s"(rsWD), "s"(rsWO), "i"(L_ST) : "memory", "scc");
  };
  auto stream = [&]() AI {
    SFOR(q, 28 * NT)
      constexpr int n = q / NT, j = q % NT;
      if constexpr (j == 0) {
        if constexpr (n == 27) {
#pragma unroll
          for (int t = 0; t < NT; t++) ldsw<unsigned>(aCSw, ((3 + t) & 3) * 512, hilo(csB[t][0]));
          if constexpr (AT && GG) WAITV(8);
          WAITL0(); BAR(); MEMFENCE();
        }
        else if constexpr (!AT && GG && (n == S2N || n == B1N)) { BAR(); MEMFENCE(); }
        else if constexpr (!AT && !GG && (n & 3) == 3) { asm volatile("s_waitcnt lgkmcnt(4)" ::: "memory"); BAR(); MEMFENCE(); }
      }
      if constexpr (q + 4 < 28 * NT) readB(q + 4);
      if constexpr (n == 27) cb[j] = ldsr<bf16x8>(aCSr, ((3 + j) & 3) * 512);
      SB();
      accB[j] = MFMA(wq[q & 7], xf[n], accB[j]);
      if constexpr (!NOCS && (n & 3) == 1) csB[j] = MFMA16(sel, wq[q & 7], csB[j]);
      if constexpr (!AT) {
        if constexpr (GG == 1 && j == 0 && n >= B1N && n < B1N + 4) issueW2(1 + ((n - B1N) >> 1), (n - B1N) & 1);
        if constexpr (GG == 0) {
          if constexpr (j == 1 && n < 16) { epiSlice<(n >> 2) & 3, n & 3>(accA[2], rc, es); if constexpr (n == 15) st16(es.r, rsWS, vL16, sA2, 0); }
          if constexpr (j == 0 && n >= 3 && n < 7) issueW2(1 + ((n - 3) >> 1), (n - 3) & 1);
          if constexpr (j == 0 && n >= 7 && n < 27 && (n & 1)) issueW2(1 + ((n + 1) >> 2), ((n + 1) >> 1) & 1);
          if constexpr (n == 27) issueW2(8, j);
        }
      } else {
        if constexpr (GG == 1 && n < 16 && (n & 1) == 0) clri(n >> 1);
        if constexpr (GG == 0 && j < 2 && n >= 1 && 2 * (n - 1) + j < 21) qstep(IntC<2 * (n - 1) + j>{}, accA[2], 0);
        if constexpr (GG == 0 && q >= 3 * H0L && q < 3 * H0L + 8) pv[q - 3 * H0L] = ld16(rsWS, vKP, sKP, WS_KVP + (q - 3 * H0L) * 16384);
        if constexpr (GG == 0 && j == 2 && n == H0C) { asm volatile("" : "+v"(accB[0]), "+v"(accB[1]), "+v"(accB[NT - 1]), "+v"(csB[0]), "+v"(csB[1]), "+v"(csB[NT - 1])); STAMP(11); hop1(); STAMP(6); }
      }
      SB();
    SEND
    const bf16x8 xe = mkxe();
#pragma unroll
    for (int t = 0; t < NT; t++) { accB[t] = MFMA(cb[t], xe, accB[t]); asm volatile("" : "+v"(accB[t])); }
    SB();
  };
  unsigned ln = vL16, aW = 0;
  auto mkaw = [&]() AI {
    asm volatile("" : "+v"(ln));
    aW = lw2 + (ln & 0x200) + ((ln & 0xc0) << 1) + ((((ln >> 3) & 0x20) | ((ln >> 1) & 0x10)) ^ ((ln >> 1) & 0x40)) + ((ln >> 1) & 8);
  };
  bf16x8 wf[NS][2];
  auto readW = [&](int i) AI {
    const unsigned ra = ring_area(i % RING), la = aW + (ra & 0x30000u);
    wf[i][0] = tr2(la, ra & 0xffffu, (ra & 0xffffu) + 1024); wf[i][1] = tr2(la ^ 64u, ra & 0xffffu, (ra & 0xffffu) + 1024);
  };
  auto pairF = [&](auto p_, const f32x16& a) AI { constexpr int p = decltype(p_)::value; epiSlice<p, 0>(a, rc, es); epiSlice<p, 1>(a, rc, es); epiSlice<p, 2>(a, rc, es); epiSlice<p, 3>(a, rc, es); };
  if constexpr (GG == 0) {
    if constexpr (AT) { SFOR(q, 8) clri(q); SEND }
    SFOR(s, 30)
      kstep(s_);
      if constexpr (!AT && s == 1 && TEARLY) {
        const unsigned rw = (rbl * 4 + wr) * 56 + lane;
        dmat(LB + L_ST + w * 256, lane < 56 ? ((rw >> 6) * 128 + (rw & 63)) * XROW + ci * 128 : 0x80000000u, rsWD);
        if (TEARLY == 2) { touch(0); touch(4); }
      }
    SEND
    if constexpr (!AT && TPOS == 0) { touch(0); touch(4); }
    if constexpr (AT) WAITV(8);
    WAITL0(); SB(); BAR(); SB();
    STAMP(3);
    kvstep(IntC<0>{});
    readB(0); readB(1); readB(2); readB(3);
    kvstep(IntC<1>{}); kvstep(IntC<3>{});
    if constexpr (AT) { WAITV(2); stflag(E + 1, rsWS, sFown); }
    if constexpr (!AT && TPOS == 1) { touch(0); touch(4); }
    STAMP(4);
    SB();
    stream();
    if constexpr (!AT) { st16(epiP(accB[0], rc), rsWS, vL16, sA2, 4 * 1024); st16(epiP(accB[1 % NT], rc), rsWS, vL16, sA2, 5 * 1024); }
  } else {
    stream();
    STAMP(3);
    if constexpr (AT) {
      SFOR(s, 21) qstep(s_, accA[0], 1); SEND
      WAITV(0); stflag(E + 1, rsWS, sFown);
      SFOR(s, 21) qstep(s_, accA[1], 2); SEND
      SFOR(s, 21)
        if constexpr (s < 8) pv[s] = ld16(rsWS, vKP, sKP, WS_KVP + s * 16384);
        qstep(s_, accA[2], 3);
      SEND
      STAMP(4); hop1(); STAMP(6);
      SB(); BAR(); SB();
    } else {
      mkaw();
      if constexpr (TPOS == 2) { touch(0); touch(0xfffffffcu); }
      SFOR(e, 20)
        if constexpr (e < 12) pairF(IntC<e & 3>{}, accA[e >> 2]); else pairF(IntC<e & 3>{}, accB[((e >> 2) - 3) % NT]);
        if constexpr ((e & 3) == 3) st16(es.r, rsWS, vL16, sA2, (e < 12 ? 1 + (e >> 2) : 3 + (e >> 2)) * 1024);
        if constexpr (e == 0) { issueW2(3, 0); issueW2(3, 1); }
        if constexpr ((e & 3) == 2) { SB(); BAR(); SB(); issueW2(4 + (e >> 2), 0); issueW2(4 + (e >> 2), 1); }
      SEND
    }
  }

  if constexpr (AT) {
    STAMP(5);
    auto qexp = [&](f32x16& a, unsigned lh) AI { a = dualexpP(featproj(a, aWQ, lh * 1568, hh), rc.Lr); asm volatile("" : "+v"(a)); SB(); };
    if constexpr (GG == 0) qexp(accB[0], 4);
#pragma unroll
    for (int i = 0; i < 8; i++) pv[i] = ld16(rsWS, vL16, sKR, WS_KVR + i * 1024);
    if constexpr (GG == 0) { qexp(accB[1 % NT], 5); qexp(accB[NT - 1], 6); } else qexp(accB[0], 7);
    STAMP(7);
    waittag<1024>(pv, rsWS, vL16, sKR + WS_KVR, T16, 0xffffu);
    STAMP(8);
#pragma unroll
    for (int i = 0; i < 4; i++) {
      u32x4 f = {__builtin_amdgcn_perm(pv[2 * i][1], pv[2 * i][0], 0x07060302u), __builtin_amdgcn_perm(pv[2 * i][3], pv[2 * i][2], 0x07060302u),
                 __builtin_amdgcn_perm(pv[2 * i + 1][1], pv[2 * i + 1][0], 0x07060302u), __builtin_amdgcn_perm(pv[2 * i + 1][3], pv[2 * i + 1][2], 0x07060302u)};
      ldsw<u32x4>(LB + w * 4096 + vL16, i * 1024, f);
    }
    WAITL0(); BAR(); MEMFENCE();
    STAMP(10);
    f32x16 op[4], on[4];
    auto omm = [&](const f32x16& phi, unsigned lh, int z) AI {
#pragma unroll
      for (int i = 0; i < 16; i++) { op[z][i] = 0.f; on[z][i] = 0.f; }
      bf16x8 kf[4];
#pragma unroll
      for (int i = 0; i < 4; i++) kf[i] = ldsr<bf16x8>(LB + vL16, lh * 4096 + i * 1024);
#pragma unroll
      for (int i = 0; i < 2; i++) { op[z] = MFMA(kf[i], sub4(phi, i), op[z]); on[z] = MFMA(kf[2 + i], sub4(phi, 2 + i), on[z]); }
    };
    auto ofin = [&](unsigned lh, int z) AI {
      const float ia = __builtin_amdgcn_rcpf(op[z][13]), ib = __builtin_amdgcn_rcpf(on[z][13]);
      const float id = __builtin_amdgcn_rcpf(__builtin_fmaf(op[z][12], ia, __builtin_fmaf(on[z][12], ib, 1e-6f)));
      const float ca = xhi(ia * id), cb2 = xhi(ib * id);
      const f32x2 c2 = dup2(ca), d2 = dup2(cb2);
      u32x4 f0, f1;
#pragma unroll
      for (int i = 0; i < 4; i++) {
        const f32x2 u = fma2(sub2(op[z], i), c2, sub2(on[z], i) * d2), v = fma2(sub2(op[z], 4 + i), c2, sub2(on[z], 4 + i) * d2);
        f0[i] = pk2(u[0], u[1]); f1[i] = pk2(v[0], v[1]);
      }
      f1[2] = hh ? 0u : f1[2]; f1[3] = hh ? 0u : f1[3];
      st16(f0, rsWS, OV, sA2, (112 + 2 * lh) * 1024); st16(f1, rsWS, OV, sA2, (113 + 2 * lh) * 1024);
      SB();
    };
    if constexpr (GG) { omm(accA[0], 1, 0); omm(accA[1], 2, 1); SB(); ofin(1, 0); omm(accA[2], 3, 2); omm(accB[0], 7, 3); SB(); ofin(2, 1); ofin(3, 2); ofin(7, 3); }
    else { omm(accA[2], 0, 0); omm(accB[0], 4, 1); SB(); ofin(0, 0); omm(accB[1 % NT], 5, 2); omm(accB[NT - 1], 6, 3); SB(); ofin(4, 1); ofin(5, 2); ofin(6, 3); }
    STAMP(9);
  } else {
    __builtin_amdgcn_s_setprio(0);
    STAMP(5);
    const unsigned sA = WS_A2 + rt2 * 144 * 1024;
    if constexpr (GG == 0) mkaw();
    f32x16 acc[2][2];
#pragma unroll
    for (int i = 0; i < 4; i++)
#pragma unroll
      for (int k = 0; k < 16; k++) acc[i >> 1][i & 1][k] = 0.f;
    u32x4 af[NS][2];
    u32x4 xr;
    auto loadA = [&](int i) AI { af[i][0] = ld16(rsWS, vL16, sA + ksb(i) * 1024, ksi(i) * 1024); af[i][1] = ld16(rsWS, vL16, sA + ksb(i) * 1024, (144 + ksi(i)) * 1024); };
    const unsigned vO = (ln >> 7) * XROW + (ln & 0x70), sO = (rt2 * 32 + 8 * w) * XROW + ci * 128;
    STAMP(6);
    constexpr int MV = GG ? 7 : 9;
    if constexpr (!(RECY && GG)) waitv<GG ? 7 : 4>();
    SFOR(i, GG ? (RECY ? 0 : 7) : 8) readW(i); SEND
    WAITV(0); stflag(E + 1, rsWS, sFown);
    if constexpr (!GG) readW(8);
    WAITL0(); SB();
    BAR();
    SB(); STAMP(11);
    if constexpr (GG && PON == -2) __builtin_amdgcn_s_setprio(1);
    if constexpr (!(RECY && GG)) {
      SFOR(i, MV)
        if constexpr (i == MV - 2) ldflag();
        issueW2(9 + i, 0); issueW2(9 + i, 1);
      SEND
      asm volatile("s_waitcnt vmcnt(4)" : "+v"(fl1) :: "memory");
    }
    if ((RECY && GG) || !ALL(!nd1 || fl1 == E + 1u)) poll(rsWS, lane * FS, sF, E, nd1 ? 1u : 0u);
    STAMP(7);
    if constexpr (GG && PON == -1) __builtin_amdgcn_s_setprio(1);
#pragma unroll
    for (int j = 0; j < PD; j++) loadA(j);
    asm volatile("buffer_load_dwordx4 %0, %1, %2, %3 offen" : "=v"(xr) : "v"(vO), "s"(rsX), "s"(sO) : "memory");
    SFOR(i, NS)
      asm volatile("s_waitcnt vmcnt(%c2)" : "+v"(af[i][0]), "+v"(af[i][1]) : "i"(later2(i, NS, GG)) : "memory");
      if constexpr (i + 1 >= MV && i + 1 < NS) readW(i + 1);
      if constexpr (GG == 0 && i >= 12) {
        auto stale = [&]() AI {
          unsigned m = UMAX(UMAX(UMAX(af[i][0][0], af[i][0][1]), UMAX(af[i][0][2], af[i][0][3])), UMAX(UMAX(af[i][1][0], af[i][1][1]), UMAX(af[i][1][2], af[i][1][3])));
          return ANY(m == ~0u);
        };
        if (__builtin_expect(stale(), 0)) {
          int it = 0;
#pragma nounroll
          do { __builtin_amdgcn_s_sleep(SLEEPR); rel1<true>(af[i][0], af[i][1], rsWS, vL16, sA + (ksb(i) + ksi(i)) * 1024); } while (++it < (1 << 14) && stale());
          SFOR(u, PD - 1) if constexpr (i + 1 + u < NS) rel1<false>(af[i + 1 + u][0], af[i + 1 + u][1], rsWS, vL16, sA + (ksb(i + 1 + u) + ksi(i + 1 + u)) * 1024); SEND
        }
      }
      if constexpr (GG && i == POFF) __builtin_amdgcn_s_setprio(0);
      if constexpr (GG && i == PON) __builtin_amdgcn_s_setprio(1);
      if constexpr (GG && PON == -3) __builtin_amdgcn_s_setprio(i & 1);
      if constexpr (i == 6) STAMP(8);
      if constexpr (i == 12) STAMP(9);
      SB();
      if constexpr (i + PD < NS) loadA(i + PD);
#pragma unroll
      for (int mm = 0; mm < 2; mm++) acc[0][mm] = MFMA(wf[i][0], __builtin_bit_cast(bf16x8, af[i][mm]), acc[0][mm]);
#pragma unroll
      for (int mm = 0; mm < 2; mm++) acc[1][mm] = MFMA(wf[i][1], __builtin_bit_cast(bf16x8, af[i][mm]), acc[1][mm]);
      if constexpr (GG == 0 && (i == 9 || i == 10)) { SB(); issueW2(i + 9, 0); issueW2(i + 9, 1); }
      SB();
    SEND
    STAMP(10);
    __builtin_amdgcn_s_setprio(0);
#pragma unroll
    for (int qd = 0; qd < 4; qd++)
#pragma unroll
      for (int k = 0; k < 4; k++) {
        const unsigned ra = ring_area(2 * qd + (k >> 1)) + (k & 1) * 1024;
        ldsw<bf16x8>(((lw2 + vL16) ^ (unsigned)(32 * (4 * (qd >> 1) + k))) + (ra & 0x30000u), ra & 0xffffu, sub4(acc[qd >> 1][qd & 1], k));
      }
    WAITL0(); BAR(); MEMFENCE();
    STAMP(12);
    const unsigned c8 = (ln >> 4) & 7, j2 = (c8 & 4) + 2 * GG + ((c8 >> 1) & 1);
    const unsigned rd = LB + ((0x51407362u >> (4 * j2)) & 7) * 16384 + (c8 & 1) * 1024 + (((8 * wr + (ln >> 7)) << 4) ^ (c8 << 5));
    asm volatile("" : "+v"(xr));
    f32x4 sum[2];
#pragma unroll
    for (int k = 0; k < 2; k++) {
      f32x4 t = {U2F(xr[2 * k] << 16), U2F(xr[2 * k] & 0xffff0000u), U2F(xr[2 * k + 1] << 16), U2F(xr[2 * k + 1] & 0xffff0000u)};
#pragma unroll
      for (int v = 0; v < 8; v++) t += ldsr<f32x4>(rd, v * 2048 + k * 512);
      sum[k] = t;
    }
    u32x4 o = {pk2(sum[0][0], sum[0][1]), pk2(sum[0][2], sum[0][3]), pk2(sum[1][0], sum[1][1]), pk2(sum[1][2], sum[1][3])};
    __builtin_amdgcn_raw_buffer_store_b128(o, mkrs(pout, 2048 * XROW), vO + sO, 0, OAUX);
  }
  STAMPS_OUT();
  };
  if (!attn && g) prog(BoolC<false>{}, IntC<1>{});
  else if (!attn) prog(BoolC<false>{}, IntC<0>{});
  else if (g) prog(BoolC<true>{}, IntC<1>{});
  else prog(BoolC<true>{}, IntC<0>{});
}

extern "C" void kernel_launch(void* const* d_in, const int* in_sizes, int n_in, void* d_out, int out_size, void* d_ws, size_t ws_size,
                              hipStream_t stream) {
  fused_kernel<<<256, 512, 0, stream>>>((const char*)d_in[0], (const char*)d_in[1], (const char*)d_in[5], (const char*)d_in[6], (char*)d_ws,
                                        (const char*)d_in[2], (const char*)d_in[7], (const char*)d_in[3], (char*)d_out);
}
```

```cpp
#include <hip/hip_runtime.h>
#include <hip/hip_bf16.h>
#include <utility>

#define DI __device__ __forceinline__
#define LAS __attribute__((address_space(3)))
typedef __attribute__((ext_vector_type(8))) short bf16x8;
typedef __attribute__((ext_vector_type(4))) short s16x4;
typedef __attribute__((ext_vector_type(2))) __bf16 bf2;
typedef __attribute__((ext_vector_type(2))) float f32x2;
typedef __attribute__((ext_vector_type(4))) float f32x4;
typedef __attribute__((ext_vector_type(16))) float f32x16;
typedef __attribute__((ext_vector_type(2))) unsigned u32x2;
typedef __attribute__((ext_vector_type(4))) unsigned u32x4;
typedef __amdgpu_buffer_rsrc_t rsrc_t;
#define F2U(x) __builtin_bit_cast(unsigned, (float)(x))
#define U2F(x) __builtin_bit_cast(float, (unsigned)(x))
#define ALL(x) (__builtin_amdgcn_ballot_w64(!(x)) == 0ull)
#define ANY(x) (__builtin_amdgcn_ballot_w64(x) != 0ull)
#define UMAX(a, b) __builtin_elementwise_max((unsigned)(a), (unsigned)(b))

#define NTX 0
#define NTW 0
#define NT2 0
constexpr unsigned XROW = 896, QROW = 2688, FROW = 3584;
constexpr unsigned FS = 128;
constexpr unsigned WS_EPOCH = 0;
constexpr unsigned WS_GR = 4096;
constexpr unsigned WS_KVP = 131072;
constexpr unsigned WS_A2 = WS_KVP + 16 * 16 * 8192;
constexpr unsigned WS_ST = WS_A2 + 64 * 144 * 1024;
constexpr unsigned WS_FH = WS_ST + 262144;
constexpr unsigned WS_KVR = WS_FH + 2048 * FS;
constexpr unsigned WS_END = WS_KVR + 16 * 8192;
constexpr unsigned L_GU = 65536, L_KV = 131072;
constexpr unsigned L_TAIL = L_KV + 3 * 8192;
constexpr unsigned L_WPK = L_TAIL;
constexpr unsigned L_ST = L_TAIL + 2048;
constexpr unsigned L_CS = L_TAIL + 4096;
constexpr unsigned L_PHI = L_GU + 32768;
constexpr unsigned L_V = L_KV + 16384;
constexpr unsigned L_FREE = 32768;

#define MFMA(a, b, c) __builtin_amdgcn_mfma_f32_32x32x16_bf16((a), (b), (c), 0, 0, 0)
#define MFMA16(a, b, c) __builtin_amdgcn_mfma_f32_16x16x32_bf16((a), (b), (c), 0, 0, 0)
#define AI __attribute__((always_inline))
#define BAR() __builtin_amdgcn_s_barrier()
#define WAITV(n) asm volatile("s_waitcnt vmcnt(" #n ")" ::: "memory")
#define WAITL0() asm volatile("s_waitcnt lgkmcnt(0)" ::: "memory")
#define SB() __builtin_amdgcn_sched_barrier(0)
#define MEMFENCE() asm volatile("" ::: "memory")

template <int N> DI void waitv() { asm volatile("s_waitcnt vmcnt(%c0)" :: "i"(N) : "memory"); }

#define INSZ(x) (x)
DI rsrc_t mkrs(const void* p, unsigned bytes) { return __builtin_amdgcn_make_buffer_rsrc((void*)p, (short)0, (int)bytes, 0x00020000); }
template <bool NT = false> DI void dma(unsigned lds, unsigned limm, unsigned voff, rsrc_t rs, unsigned soff, unsigned simm) {
  unsigned t;
  if constexpr (NT) asm volatile("s_add_u32 m0, %1, %c5\n\ts_add_u32 %0, %4, %c6\n\tbuffer_load_dwordx4 %2, %3, %0 offen nt lds"
               : "=&s"(t) : "s"(lds), "v"(voff), "s"(rs), "s"(soff), "i"(limm), "i"(simm) : "memory", "scc");
  else asm volatile("s_add_u32 m0, %1, %c5\n\ts_add_u32 %0, %4, %c6\n\tbuffer_load_dwordx4 %2, %3, %0 offen lds"
               : "=&s"(t) : "s"(lds), "v"(voff), "s"(rs), "s"(soff), "i"(limm), "i"(simm) : "memory", "scc");
}
DI void dmat(unsigned lds, unsigned voff, rsrc_t rs) {
  asm volatile("s_mov_b32 m0, %0\n\ts_nop 0\n\tbuffer_load_dword %1, %2, 0 offen lds" :: "s"(lds), "v"(voff), "s"(rs) : "memory");
}
DI s16x4 trd(unsigned a) { return __builtin_amdgcn_ds_read_tr16_b64_v4i16((LAS s16x4*)(size_t)a); }
DI s16x4 trd2(unsigned a, unsigned off) { return __builtin_amdgcn_ds_read_tr16_b64_v4i16((LAS s16x4*)((LAS char*)(size_t)a + off)); }
DI bf16x8 tr2(unsigned a, unsigned off0, unsigned off1) {
  LAS char* p = (LAS char*)(size_t)a;
  s16x4 lo = __builtin_amdgcn_ds_read_tr16_b64_v4i16((LAS s16x4*)(p + off0));
  s16x4 hi = __builtin_amdgcn_ds_read_tr16_b64_v4i16((LAS s16x4*)(p + off1));
  return __builtin_shufflevector(lo, hi, 0, 1, 2, 3, 4, 5, 6, 7);
}
template <class T> DI T ldsr(unsigned a, unsigned off) { return *(LAS T*)((LAS char*)(size_t)a + off); }
template <class T> DI void ldsw(unsigned a, unsigned off, T v) { *(LAS T*)((LAS char*)(size_t)a + off) = v; }
DI unsigned pk2(float a, float b) { f32x2 f = {a, b}; return __builtin_bit_cast(unsigned, __builtin_convertvector(f, bf2)); }
DI float rbf(float a) { return U2F(pk2(a, a) << 16); }
DI float dot2(unsigned a, unsigned b, float c) { return __builtin_amdgcn_fdot2_f32_bf16(__builtin_bit_cast(bf2, a), __builtin_bit_cast(bf2, b), c, false); }
DI float xsum(float v) { auto r = __builtin_amdgcn_permlane32_swap(F2U(v), F2U(v), false, false); float a = U2F(r[0]); asm("" : "+v"(a)); return a + U2F(r[1]); }
DI float xmax(float v) { auto r = __builtin_amdgcn_permlane32_swap(F2U(v), F2U(v), false, false); return __builtin_fmaxf(U2F(r[0]), U2F(r[1])); }
DI float xmin(float v) { auto r = __builtin_amdgcn_permlane32_swap(F2U(v), F2U(v), false, false); return __builtin_fminf(U2F(r[0]), U2F(r[1])); }
DI float xhi(float v) { auto r = __builtin_amdgcn_permlane32_swap(F2U(v), F2U(v), false, false); return U2F(r[1]); }
#define STB "sc1"
DI void st16(u32x4 v, rsrc_t rs, unsigned voff, unsigned soff, unsigned simm) {
  unsigned t;
  asm volatile("s_add_u32 %0, %4, %c5\n\tbuffer_store_dwordx4 %1, %2, %3, %0 offen " STB "\n\ts_nop 1" : "=&s"(t) : "v"(v), "v"(voff), "s"(rs), "s"(soff), "i"(simm) : "memory", "scc");
}
DI void stflag(unsigned v, rsrc_t rs, unsigned soff) {
  unsigned voff = (__builtin_amdgcn_mbcnt_hi(~0u, __builtin_amdgcn_mbcnt_lo(~0u, 0u)) == 0u) ? 0u : 0x80000000u;
  __builtin_amdgcn_raw_buffer_store_b32(v, rs, voff, soff, 16);
}
#define XORCOND (n == 24)
#define PS 0
#define PV 0
#define KEEP2 0
#define S2N 14
#define B1N 23
#define DIST 2
#define P2G1 1
#define OAUX 0
#define RECY 0
#define POFF 99
#define H1S 0
#define H0L 14
#define H0C 22
#define Q3A 1
#define COOP 0
#define PON -2
#define XN 20
#define BAN 18
#define BBN 21
#define SL0 8
#define SLN 2
#define SLD 3
#define NSL 48
#define H1C 16
#define OV vL16
#define CV vL16
#define Q6E 0
#define TEARLY 0
#define NOCS 0
#define TPOS 0
#define PRE 2
#define LREC 0
#define SLEEPR 4
#define SLEEPN 2
DI u32x4 ld16(rsrc_t rs, unsigned voff, unsigned soff, unsigned simm) {
  u32x4 r; unsigned t;
  asm volatile("s_add_u32 %1, %4, %c5\n\tbuffer_load_dwordx4 %0, %2, %3, %1 offen sc1" : "=v"(r), "=&s"(t) : "v"(voff), "s"(rs), "s"(soff), "i"(simm) : "memory", "scc");
  return r;
}
template <unsigned STEP> DI void ld8w(u32x4 (&p)[8], rsrc_t rs, unsigned voff, unsigned soff) {
  unsigned t;
#define L1(k) "buffer_load_dwordx4 %" #k ", %9, %10, %8 offen sc1\n\ts_add_u32 %8, %8, %c12\n\t"
  asm volatile("s_mov_b32 %8, %11\n\t" L1(0) L1(1) L1(2) L1(3) L1(4) L1(5) L1(6) L1(7) "s_waitcnt vmcnt(0)"
               : "=&v"(p[0]), "=&v"(p[1]), "=&v"(p[2]), "=&v"(p[3]), "=&v"(p[4]), "=&v"(p[5]), "=&v"(p[6]), "=&v"(p[7]), "=&s"(t)
               : "v"(voff), "s"(rs), "s"(soff), "i"(STEP) : "memory", "scc");
#undef L1
}
DI bool untag(u32x4 (&p)[8], unsigned tag, unsigned mask) {
  unsigned chk = 0;
#pragma unroll
  for (int i = 0; i < 8; i++)
#pragma unroll
    for (int k = 0; k < 4; k++) { p[i][k] ^= tag; chk |= p[i][k]; }
  return ALL((chk & mask) == 0u);
}
template <unsigned STEP> DI void waittag(u32x4 (&p)[8], rsrc_t rs, unsigned voff, unsigned soff, unsigned tag, unsigned mask) {
  asm volatile("s_waitcnt vmcnt(0)" : "+v"(p[0]), "+v"(p[1]), "+v"(p[2]), "+v"(p[3]), "+v"(p[4]), "+v"(p[5]), "+v"(p[6]), "+v"(p[7]) :: "memory");
#pragma nounroll
  for (int it = 0; !untag(p, tag, mask) && it < (1 << 16); ++it) { __builtin_amdgcn_s_sleep(SLEEPN); ld8w<STEP>(p, rs, voff, soff); }
}
template <bool WAIT> DI void rel1(u32x4& a, u32x4& b, rsrc_t rs, unsigned voff, unsigned so) {
  unsigned t;
  if constexpr (WAIT) asm volatile("buffer_load_dwordx4 %0, %3, %4, %5 offen sc1\n\ts_add_u32 %2, %5, 147456\n\tbuffer_load_dwordx4 %1, %3, %4, %2 offen sc1\n\ts_waitcnt vmcnt(0)"
                                   : "+v"(a), "+v"(b), "=&s"(t) : "v"(voff), "s"(rs), "s"(so) : "memory", "scc");
  else asm volatile("buffer_load_dwordx4 %0, %3, %4, %5 offen sc1\n\ts_add_u32 %2, %5, 147456\n\tbuffer_load_dwordx4 %1, %3, %4, %2 offen sc1"
                    : "+v"(a), "+v"(b), "=&s"(t) : "v"(voff), "s"(rs), "s"(so) : "memory", "scc");
}
DI void poll(rsrc_t rs, unsigned voff, unsigned soff, unsigned E, unsigned need) {
  voff = need ? voff : 0x80000000u;
#pragma nounroll
  for (int it = 0; it < (1 << 16); ++it) {
    unsigned v;
    asm volatile("buffer_load_dword %0, %1, %2, %3 offen sc1\n\ts_waitcnt vmcnt(0)" : "=v"(v) : "v"(voff), "s"(rs), "s"(soff) : "memory");
    bool ok = (need == 0u) || ((v - E - need) <= (3u - need));
    if (ALL(ok)) break;
    __builtin_amdgcn_s_sleep(SLEEPN);
  }
}

template <bool B> struct BoolC { static constexpr bool value = B; };
template <int I> struct IntC { static constexpr int value = I; };
template <int... I, class F> DI void sfor_impl(std::integer_sequence<int, I...>, F&& f) { (f(IntC<I>{}), ...); }
template <int N, class F> DI void sfor(F&& f) { sfor_impl(std::make_integer_sequence<int, N>{}, f); }
#define SFOR(n, N) sfor<N>([&](auto n##_) AI { constexpr int n = decltype(n##_)::value;
#define SEND });
struct RowC { float r, K1, K2, Ri, Lr; };

DI unsigned hilo(float v) { float hi = rbf(v); return pk2(hi, v - hi); }
DI float max3(float a, float b, float c) { return __builtin_fmaxf(__builtin_fmaxf(a, b), c); }
DI float min3(float a, float b, float c) { return __builtin_fminf(__builtin_fminf(a, b), c); }
DI unsigned pk2s(float a, float b) { unsigned r; asm("v_cvt_pk_bf16_f32 %0, %1, %2" : "=v"(r) : "v"(a), "v"(b)); return r; }
DI f32x2 sub2(const f32x16& v, int k) { f32x2 t = {v[2 * k], v[2 * k + 1]}; return t; }
DI f32x2 exp2v(f32x2 v) { f32x2 r = {__builtin_amdgcn_exp2f(v[0]), __builtin_amdgcn_exp2f(v[1])}; return r; }
DI f32x2 dup2(float a) { f32x2 r = {a, a}; asm("" : "+v"(r)); return r; }
DI f32x2 fma2(f32x2 a, f32x2 b, f32x2 c) { return __builtin_elementwise_fma(a, b, c); }
DI u32x4 epiP(const f32x16& a, const RowC& c) {
  u32x4 r;
  const f32x2 K1 = dup2(c.K1), K2 = dup2(c.K2), Ri = dup2(c.Ri);
#pragma unroll
  for (int i = 0; i < 4; i++) {
    const f32x2 g = sub2(a, i), d = fma2(exp2v(g * fma2(g * g, K2, K1)), Ri, Ri);
    const f32x2 s = {__builtin_amdgcn_rcpf(d[0]), __builtin_amdgcn_rcpf(d[1])}, h = (g * sub2(a, 4 + i)) * s;
    r[i] = pk2(h[0], h[1]);
  }
  return r;
}
struct EpiS { float x0, x1, m0, m1; u32x4 r; };
template <int P, int PH> DI void epiSlice(const f32x16& a, const RowC& c, EpiS& s) {
  const float g0 = a[2 * P], g1 = a[2 * P + 1];
  if constexpr (PH == 0) { s.x0 = g0 * __builtin_fmaf(g0 * g0, c.K2, c.K1); s.x1 = g1 * __builtin_fmaf(g1 * g1, c.K2, c.K1); }
  if constexpr (PH == 1) { s.x0 = __builtin_amdgcn_exp2f(s.x0); s.x1 = __builtin_amdgcn_exp2f(s.x1); s.m0 = g0 * a[8 + 2 * P]; s.m1 = g1 * a[9 + 2 * P]; }
  if constexpr (PH == 2) { s.x0 = __builtin_amdgcn_rcpf(__builtin_fmaf(s.x0, c.Ri, c.Ri)); s.x1 = __builtin_amdgcn_rcpf(__builtin_fmaf(s.x1, c.Ri, c.Ri)); }
  if constexpr (PH == 3) s.r[P] = pk2s(s.m0 * s.x0, s.m1 * s.x1);
}
DI f32x16 featproj2(const f32x16& a, bf16x8 w0, bf16x8 w1, bool hh) {
  u32x4 q0, q1;
#pragma unroll
  for (int i = 0; i < 4; i++) { q0[i] = pk2(a[2 * i], a[2 * i + 1]); q1[i] = pk2(a[8 + 2 * i], a[9 + 2 * i]); }
  q1[2] = hh ? 0u : q1[2]; q1[3] = hh ? 0u : q1[3];
  f32x16 z;
#pragma unroll
  for (int i = 0; i < 16; i++) z[i] = 0.f;
  z = MFMA(w0, __builtin_bit_cast(bf16x8, q0), z);
  z = MFMA(w1, __builtin_bit_cast(bf16x8, q1), z);
  return z;
}
DI f32x16 featproj(const f32x16& a, unsigned wpaddr, unsigned off, bool hh) {
  return featproj2(a, tr2(wpaddr, off, off + 8 * 56), tr2(wpaddr, off + 16 * 56, off + 24 * 56), hh);
}
DI float max16(const f32x16& p) { float m = max3(p[0], p[1], p[2]); _Pragma("unroll") for (int i = 3; i < 15; i += 2) m = max3(m, p[i], p[i + 1]); return xmax(__builtin_fmaxf(m, p[15])); }
DI float min16(const f32x16& p) { float m = min3(p[0], p[1], p[2]); _Pragma("unroll") for (int i = 3; i < 15; i += 2) m = min3(m, p[i], p[i + 1]); return xmin(__builtin_fminf(m, p[15])); }
DI f32x16 dualexpP(const f32x16& p, float Lr) {
  const f32x2 bx = dup2(-max16(p) * Lr), bn = dup2(min16(p) * Lr), L2 = dup2(Lr);
  f32x16 o;
#pragma unroll
  for (int i = 0; i < 8; i++) {
    const f32x2 ep = exp2v(fma2(sub2(p, i), L2, bx)), en = exp2v(fma2(sub2(p, i), -L2, bn));
    o[i] = U2F(pk2(ep[0], ep[1])); o[8 + i] = U2F(pk2(en[0], en[1]));
  }
  return o;
}
struct SmS { f32x16 p, e; float en[16], bx, bn, sp, sn, tp, tn; bf16x8 w0, w1; };
DI bf16x8 sub4(const f32x16& v, int k) { f32x4 t = {v[4 * k], v[4 * k + 1], v[4 * k + 2], v[4 * k + 3]}; return __builtin_bit_cast(bf16x8, t); }

DI constexpr unsigned lc1_base(int ch) { return ch == 6 ? L_KV : ((ch & 1) ? L_GU : 0u) + (ch < 2 ? 49152u : ch < 4 ? 0u : 16384u); }
template <int CTRL, int RM> DI float dppadd(float v) { return v + U2F(__builtin_amdgcn_update_dpp(0u, F2U(v), CTRL, RM, 0xf, false)); }
DI float wavesum63(float v) {
  v = dppadd<0x111, 0xf>(v); v = dppadd<0x112, 0xf>(v); v = dppadd<0x114, 0xf>(v); v = dppadd<0x118, 0xf>(v);
  v = dppadd<0x142, 0xa>(v); return dppadd<0x143, 0xc>(v);
}

#define STAMP(i)
#define STAMPS_OUT()
#define CSTAMP(i)

#define PDV 5
constexpr int PD = PDV, RING = 9;
constexpr int later2(int i, int NS, int GG) {
  const int younger = 2 * ((i + PD <= NS ? i + PD : NS) - 1 - i);
  int extra = i < PD ? 1 : 0;
  if (GG == 0 && (i == 10 || i == 11)) extra += 2 * (i - 9);
  return younger + extra;
}
DI constexpr unsigned ring_area(int j) { return j == 0 ? L_FREE : j == 1 ? L_PHI : lc1_base(j - 2); }
constexpr int cumG1(int s) { return s < 4 ? 0 : s < 22 ? (s - 4) / 2 : (9 + (s - 22) * 5 / 4 < 48 ? 9 + (s - 22) * 5 / 4 : 48); }

#define CODE_BYTES 65536
__global__ __launch_bounds__(512, 2) void fused_kernel(const char* px_, const char* pqkv_, const char* pwg_, const char* pwu_, char* ws_, const char* pphi_,
                                                       const char* pwd_, const char* pwo_, char* pout_) {
  typedef __attribute__((ext_vector_type(8))) unsigned long long u64x8;
  u64x8 ka; unsigned long long kb;
  asm volatile("s_load_dwordx16 %0, %2, 0\n\ts_load_dwordx2 %1, %2, 64" : "=&s"(ka), "=&s"(kb) : "s"(__builtin_amdgcn_kernarg_segment_ptr()));
  __shared__ __attribute__((aligned(16))) char smem[163840];
  const unsigned LB = (unsigned)(size_t)smem;
  const unsigned tid = __builtin_amdgcn_workitem_id_x(), lane = tid & 63;
  const unsigned w = __builtin_amdgcn_readfirstlane(tid >> 6), g = w >> 2, wr = w & 3;
  const unsigned b = __builtin_amdgcn_workgroup_id_x(), xcd = b & 7, bi = b >> 3, grp = xcd >> 1, half = xcd & 1, rbl = bi & 3, cp = bi >> 2, ci = cp - 1;
  const unsigned rb = 4 * grp + rbl, c = 7 * half + ci;
  const unsigned head = 8 * half + (((half ? 0x76425310u : 0x75316420u) >> (4 * cp)) & 7);
  const bool attn = cp == 0;
  STAMP(0); CSTAMP(14);
  const unsigned wstr = attn ? QROW : FROW;
  const unsigned sG0 = half * (attn ? 448 : 1792) + (attn ? 0 : ci * 256), sX0 = rb * 128 * XROW;
  unsigned vx0, vx1, vG, vU, vKV;
  auto voffs = [&](unsigned ln) AI {
    const unsigned r8 = ln >> 3, c8 = ln & 7, sa = (r8 >> 1) & 1, lc = c8 ^ (4 * sa);
    vx0 = (16 * w + r8) * XROW + ((c8 ^ (r8 >> 1)) << 4); vx1 = (vx0 ^ 64u) + 8 * XROW;
    const unsigned cg = attn ? lc : (c8 ^ (2 * sa)), cu = attn ? (lc >= 6 ? lc - 2 : lc) : (cg ^ 4);
    vG = (8 * w + r8) * wstr + (cg << 4); vU = (8 * w + r8) * wstr + (cu << 4);
    vKV = (8 * w + r8) * QROW + 896 * (1 + (lc >> 2)) + ((lc & 3) << 4) + ((56 * head) & ~15u);
  };
  voffs(lane);
  const unsigned lw1 = LB + w * 1024, lw2 = LB + w * 2048;
#define ROT (((0x24924u >> (2 * (cp + rbl))) & 3) * 2)
  const unsigned rot = ROT;
  const unsigned srow = 128 * rb + 64 * half + 8 * cp + w;
  unsigned vrow = lane < 56 ? lane * 16 : 0x80000000u;
  asm volatile("s_waitcnt lgkmcnt(0)" : "+s"(ka), "+s"(kb), "+v"(vx0), "+v"(vx1), "+v"(vG), "+v"(vU), "+v"(vKV), "+v"(vrow));
  const char *px = (const char*)ka[0], *pqkv = (const char*)ka[1], *pwg = (const char*)ka[2], *pwu = (const char*)ka[3], *pphi = (const char*)ka[5],
             *pwd = (const char*)ka[6], *pwo = (const char*)ka[7];
  char *ws = (char*)ka[4], *pout = (char*)kb;
  const rsrc_t rsX = mkrs(px, INSZ(2048 * XROW)), rsQ = mkrs(pqkv, INSZ(448 * QROW));
  const rsrc_t rsG = mkrs(attn ? pqkv : pwg, INSZ(448 * wstr)), rsU = mkrs(attn ? pqkv + 128 : pwu, INSZ(448 * wstr - (attn ? 128 : 0)));
  auto cidx = [&](int st) AI -> unsigned { unsigned t = rot + st; return t >= 7 ? t - 7 : t; };
  auto issueA = [&](int st, int k) AI {
    const unsigned sl = st & 3, ch = cidx(st);
    if (k == 0) dma<NTX>(lw2, sl * 16384, vx0, rsX, sX0 + ch * 128, 0);
    if (k == 1) dma<NTX>(lw2, sl * 16384 + 1024, vx1, rsX, sX0 + ch * 128, 0);
    if (k == 2) dma<NTW>(lw1, L_GU + sl * 16384, vG, rsG, sG0 + ch * 64 * wstr, 0);
    if (k == 3) dma<NTW>(lw1, L_GU + sl * 16384 + 8192, vU, rsU, sG0 + ch * 64 * wstr, 0);
    if (k == 4) dma<NTW>(lw1, L_KV + sl * 8192, vKV, rsQ, ch * 64 * QROW, 0);
  };
  const rsrc_t rsWS = mkrs(ws, WS_END);
  unsigned Ev, cpf; u32x4 xrw;
  asm volatile("buffer_load_dwordx4 %0, %1, %2, %3 offen" : "=v"(xrw) : "v"(vrow), "s"(rsX), "s"(srow * XROW) : "memory");
  asm volatile("buffer_load_dword %0, off, %1, 0 sc1" : "=v"(Ev) : "s"(rsWS) : "memory");
  {
    unsigned long long pc; asm volatile("s_getpc_b64 %0" : "=s"(pc));
    const unsigned vo = w == 0 && lane < 24 ? (bi * 24 + lane) * 128 : 0x80000000u;
    asm volatile("buffer_load_dword %0, %1, %2, 0 offen" : "=v"(cpf) : "v"(vo), "s"(mkrs((const void*)pc, CODE_BYTES)) : "memory");
  }
#pragma unroll
  for (int k = 0; k < 5; k++) issueA(0, k);
  asm volatile("s_waitcnt vmcnt(%c3)" : "+v"(xrw), "+v"(Ev), "+v"(cpf) : "i"(5 + COOP) : "memory");
#pragma unroll
  for (int k = 0; k < 5; k++) issueA(1, k);
  issueA(2, 0);
  const unsigned E = __builtin_amdgcn_readfirstlane(Ev);
  {
    float s1 = 0.f, s2 = 0.f;
#pragma unroll
    for (int i = 0; i < 4; i++) { s1 = dot2(xrw[i], 0x3f803f80u, s1); s2 = dot2(xrw[i], xrw[i], s2); }
    s1 = wavesum63(s1); s2 = wavesum63(s2);
    const float mean = s1 * (1.f / 448.f), var = __builtin_fmaxf(s2 * (1.f / 448.f) - mean * mean, 0.f);
    u32x2 gr = {pk2(-mean, __builtin_amdgcn_sqrtf(rbf(rbf(var) + 1e-5f))), E + 1};
    __builtin_amdgcn_raw_buffer_store_b64(gr, rsWS, lane == 63 ? 0u : 0x80000000u, WS_GR + srow * 8, 16);
  }

  auto prog = [&](auto at_, auto gg_) AI {
  constexpr bool AT = decltype(at_)::value; constexpr int GG = decltype(gg_)::value;
  constexpr unsigned lcstep = AT ? 224 : 128;
  unsigned lane;
  asm volatile("v_mbcnt_lo_u32_b32 %0, -1, 0\n\tv_mbcnt_hi_u32_b32 %0, -1, %0" : "=v"(lane));
  __builtin_assume(lane < 64);
  voffs(lane);
  auto issueL = [&](int hc) AI {
    const int st = hc >> 1; const unsigned ch = cidx(st);
    if (hc & 1) dma<NTW>(lw1, lc1_base(st) + 8192, vU, rsU, sG0 + ch * 64 * wstr, lcstep);
    else dma<NTW>(lw1, lc1_base(st), vG, rsG, sG0 + ch * 64 * wstr, lcstep);
  };
  auto issueG = [&](int bar, int k) AI {
    if (bar == 0) issueA(2 + k / 5, k % 5);
    else if (bar <= 3) issueA(bar + 3, k);
    else if (k < 4) issueL(4 * (bar - 4) + k);
    else if (bar == 4) dma(LB + (w & 1) * 1024, L_WPK, lane * 16, mkrs(pphi, 16 * 1568), head * 1568 + (w & 1) * 1024, 0);
    else issueL(12 + (bar - 5));
  };

  const unsigned rt = 4 * rb + wr;
  const unsigned sA2 = WS_A2 + rt * 144 * 1024 + (AT ? half * 16384 : c * 8192), vL16 = lane * 16;
  const u32x4 sent = {~0u, ~0u, ~0u, ~0u};
  auto clr = [&](int ks) AI { st16(sent, rsWS, CV, sA2, ks * 1024); };

  auto clri = [&](int q) AI { clr(GG ? (q < 6 ? 114 + q : 120 + q) : (q < 2 ? 112 + q : 118 + q)); };

  const bool hh = lane >= 32;
  const unsigned hu = lane >> 5, blk = (lane >> 4) & 1, q4 = (lane >> 2) & 3, p4 = lane & 3, ta = (q4 >> 1) & 1, l31 = lane & 31;
  const unsigned perm = wr ^ (2 * GG);
  const unsigned rowoff = (8 * hu + q4) * 128 + (perm << 11);
  const unsigned pc = (4 * blk + p4) < 6 ? (4 * blk + p4) : 6;
  auto tileaddr = [&](unsigned t) AI -> unsigned {
    if constexpr (AT) { unsigned o = 56 * t + 8 * pc; return (o >> 7) * 8192 + ((((o >> 4) & 7) ^ (4 * ta)) << 4) + (o & 8) + rowoff; }
    else return blk * 8192 + (((2 * t + (p4 >> 1)) ^ (4 * blk + 2 * ta)) << 4) + 8 * (p4 & 1) + rowoff;
  };
  constexpr int NT = !AT ? 2 : GG ? 1 : 3, TB0 = GG ? 4 - NT : 0;
  unsigned aA[3], aB[NT];
  {
    unsigned o = (head & 1) * 8 + 8 * pc;
    unsigned ak = L_KV + (((o >> 4) ^ (4 * ta)) << 4) + (o & 8) + rowoff;
    aA[0] = LB + (GG ? L_GU + tileaddr(1) : ak);
    aA[1] = LB + (GG ? L_GU + tileaddr(2) : (ak ^ 64u));
    aA[2] = LB + L_GU + tileaddr(GG ? 3 : 0);
#pragma unroll
    for (int j = 0; j < NT; j++) aB[j] = LB + tileaddr(TB0 + j);
  }
  const unsigned xrow = 32 * wr + l31;
  const unsigned aX = LB + xrow * 128 + (((2 * perm + hu) ^ ((xrow >> 1) & 7)) << 4);
  const unsigned selv = (((lane >> 4) ^ (lane >> 2)) & 1) ? 0u : 0x3f803f80u;
  const u32x4 sel4 = {selv, selv, selv, selv};
  const bf16x8 sel = __builtin_bit_cast(bf16x8, sel4);

  bf16x8 xf[28], wa[2][3], wq[8];
  f32x16 accA[3], accB[NT];
  f32x4 csA[3], csB[NT];
#pragma unroll
  for (int j = 0; j < 3; j++) { for (int i = 0; i < 16; i++) accA[j][i] = 0.f; for (int i = 0; i < 4; i++) csA[j][i] = 0.f; }
#pragma unroll
  for (int j = 0; j < NT; j++) { for (int i = 0; i < 16; i++) accB[j][i] = 0.f; for (int i = 0; i < 4; i++) csB[j][i] = 0.f; }
  u32x2 grn;

  const rsrc_t rsWD = mkrs(pwd, INSZ(1792 * XROW)), rsWO = mkrs(pwo, INSZ(448 * XROW));
  const unsigned vW2 = (lane >> 3) * XROW + (((lane & 7) ^ ((lane >> 2) & 4)) << 4) + ci * 128;
  constexpr int NS = GG ? 16 : 20;
#define RHO ((rbl + cp) & 3)
  const unsigned rho = RHO;
  auto t_of = [](int i) constexpr { return (0x54763210u >> (4 * (i >> 1))) & 7; };
  auto ksb = [&](int i) AI -> unsigned {
    if (GG) return 16 * wr + ((t_of(i) & 3) ^ rho);
    if (i < 12) { const unsigned cc = i % 6 + rho + (rho >> 1); return wr + 8 * (cc >= 6 ? cc - 6 : cc); }
    return 8 * wr + 2 * (((i - 12) >> 1) ^ rho);
  };
  auto ksi = [&](int i) AI -> unsigned { return GG ? 8 * (i & 1) + (t_of(i) & 4) : i < 12 ? 64 + 4 * (i / 6) : 112 + (i & 1); };
  auto issueW2 = [&](int i, int hf) AI {
    if (GG == 0 && i >= 12) dma<NT2>(lw2, ring_area(i % RING) + hf * 1024, vW2, rsWO, ksb(i) * 14 * XROW, (16 * (i & 1) + 8 * hf) * XROW);
    else dma<NT2>(lw2, ring_area(i % RING) + hf * 1024, vW2, rsWD, ksb(i) * 16 * XROW, (ksi(i) * 16 + 8 * hf) * XROW);
  };

  const unsigned aCSw = LB + L_CS + l31 * 16 + wr * 4 + GG * 2048, aCSr = LB + L_CS + l31 * 16 + GG * 2048;
  auto readX = [&](int n) AI { xf[n] = ldsr<bf16x8>(aX ^ (unsigned)((n & 3) << 5), ((n >> 2) & 3) * 16384); };
  auto readA = [&](int n, int j) AI {
    const unsigned o = ((n >> 2) & 3) * ((GG == 0 && j < 2) ? 8192 : 16384);
    wa[n & 1][j] = tr2(aA[j] ^ (unsigned)((n & 3) << 11), o, o + 512);
  };
  auto readB = [&](int q) AI {
    const int n = q / NT, j = q % NT;
    const unsigned base = lc1_base(n >> 2);
    if ((NT == 3 && n < XN) ? (n & 3) == 0 : n == 24) asm volatile("" : "+v"(aB[j]));
    wq[q & 7] = tr2(aB[j] ^ (((n & 3) << 11) | (base & 0x30000u)), base & 0xffffu, (base & 0xffffu) + 512);
  };
  RowC rc; unsigned nm, denb;
  auto mkxe = [&]() AI { asm volatile("" : "+v"(nm)); u32x4 t = {nm, nm, nm, nm}; return __builtin_bit_cast(bf16x8, t); };
  auto rowconst = [&]() AI {
#pragma nounroll
    for (int it = 0; it < (1 << 16) && !ALL(grn[1] == E + 1); ++it) {
      __builtin_amdgcn_s_sleep(SLEEPN);
      asm volatile("buffer_load_dwordx2 %0, %1, %2, %3 offen sc1\n\ts_waitcnt vmcnt(0)" : "=v"(grn) : "v"(xrow * 8), "s"(rsWS), "s"(WS_GR + rb * 1024) : "memory");
    }
    const float den = U2F(grn[0] & 0xffff0000u);
    nm = hh ? 0u : __builtin_amdgcn_perm(grn[0], grn[0], 0x01000100u);
    denb = grn[0] >> 16;
    rc.r = __builtin_amdgcn_rcpf(den);
    const float k1 = -2.f * 1.4426950408889634f * 0.7978845608028654f;
    rc.K1 = k1 * rc.r; rc.K2 = (k1 * 0.044715f) * rc.r * (rc.r * rc.r); rc.Ri = den * den; rc.Lr = 1.4426950408889634f * rc.r;
  };
  waitv<7 + COOP>();
  BAR(); MEMFENCE();
  STAMP(1);
  readX(0); readA(0, 0); readA(0, 1); readA(0, 2);
  SFOR(n, 28)
    SB();
    if constexpr (n == 27) {
#pragma unroll
      for (int j = 0; j < 3; j++) ldsw<unsigned>(aCSw, j * 512, hilo(csA[j][0]));
    }
    if constexpr ((n & 3) == 3) {
      if constexpr (n == 27) WAITV(0);
      else if constexpr (n == 23) asm volatile("s_waitcnt vmcnt(10)" : "+v"(grn) :: "memory");
      else if constexpr (n == 3 || n == 15 || n == 19) WAITV(11); else WAITV(10);
      { WAITL0(); BAR(); MEMFENCE(); }
      if constexpr (n == 27) STAMP(2);
    }
    constexpr int bar = (n + 1) >> 2, ph = (n + 1) & 3;
#pragma unroll
    for (int j = 0; j < 3; j++) {
      if constexpr (n < 27) { if (j == 0) readX(n + 1); readA(n + 1, j); }
      else { wa[0][j] = ldsr<bf16x8>(aCSr, j * 512); if (GG == 1 && j < 2) { readB(2 * j); readB(2 * j + 1); } }
      SB();
      accA[j] = MFMA(wa[n & 1][j], xf[n], accA[j]);
      const int slot = 3 * ph + j;
      if (bar == 0) issueG(0, slot - 2);
      else if (bar == 6) { if (slot < 5) issueG(bar, slot); }
      else if (bar <= 6) { if (slot == 0) issueG(bar, 0); if (slot == 2) issueG(bar, 1); if (slot == 5) issueG(bar, 2); if (slot == 7) issueG(bar, 3); if (slot == 10) issueG(bar, 4); }
      else if (j < 2) {
        if constexpr (AT) dma(lw2, L_FREE + j * 1024, lane * 16, mkrs(pphi, 16 * 1568), half * 8 * 1568 + w * 2048, j * 1024);
        else issueW2(0, j);
      }
      if (!NOCS && (n & 3) == 0) csA[j] = MFMA16(sel, wa[n & 1][j], csA[j]);
      if (n == 25 && j == 0) rowconst();
      if (n == 14 && j == 2) asm volatile("buffer_load_dwordx2 %0, %1, %2, %3 offen sc1" : "=v"(grn) : "v"(xrow * 8), "s"(rsWS), "s"(WS_GR + rb * 1024) : "memory");
      SB();
    }
  SEND
  { const bf16x8 xe = mkxe();
#pragma unroll
  for (int j = 0; j < 3; j++) accA[j] = MFMA(wa[0][j], xe, accA[j]); }
  asm volatile("" : "+v"(accA[0]), "+v"(accA[1]), "+v"(accA[2]));
  SB();

  const unsigned sFown = WS_FH + (rt * 32 + (AT ? 28 + 2 * half : 2 * c) + GG) * FS;
  const unsigned aWP = LB + (4 * hu + q4) * 56 + 8 * pc;
  const unsigned T12 = (E + 1) & 0xfffu, T16 = (E + 1) & 0xffffu;
  const unsigned rt2 = 2 * (8 * grp + 4 * half + rbl);
  const unsigned sF = WS_FH + rt2 * 32 * FS;
  const unsigned fc = l31 >> 1;
  const bool nd1 = GG ? (fc >> 1) == wr : (fc >= 8 && fc < 14) || fc == 14 + (wr >> 1);
  const unsigned aIW = LB + L_PHI + xrow * 64 + ((hu ^ ((xrow >> 1) & 7)) << 3);
  SmS sm;
  unsigned aWK = aWP + L_WPK, aWQ = aWP + L_FREE;
  asm volatile("" : "+v"(aWK)); if constexpr (AT) asm volatile("" : "+v"(aWQ));
  auto kstep = [&](auto s_) AI {
    constexpr int s = decltype(s_)::value;
    if constexpr (s == 0) { sm.w0 = tr2(aWK, 0, 8 * 56); sm.w1 = tr2(aWK, 16 * 56, 24 * 56); }
    if constexpr (s == 1) sm.p = featproj2(accA[0], sm.w0, sm.w1, hh);
    if constexpr (s == 2 || s == 3) {
#pragma unroll
      for (int k = 2 * s - 4; k < 2 * s - 2; k++) {
        u32x2 vv = {pk2(accA[1][4 * k], accA[1][4 * k + 1]), pk2(accA[1][4 * k + 2], accA[1][4 * k + 3])};
        if (k == 3) vv[0] = hh ? denb : vv[0];
        ldsw<u32x2>(aIW ^ (unsigned)(k << 4), L_V - L_PHI, vv);
      }
    }
    if constexpr (s == 4) sm.bx = -max16(sm.p) * rc.Lr;
    if constexpr (s == 5) sm.bn = min16(sm.p) * rc.Lr;
    if constexpr (s >= 6 && s < 22) {
      constexpr int i = s - 6;
      sm.e[i] = __builtin_amdgcn_exp2f(__builtin_fmaf(sm.p[i], rc.Lr, sm.bx)); sm.en[i] = __builtin_amdgcn_exp2f(__builtin_fmaf(sm.p[i], -rc.Lr, sm.bn));
      if constexpr (i == 0) { sm.sp = sm.e[0]; sm.sn = sm.en[0]; } else if constexpr (i < 12) { sm.sp += sm.e[i]; sm.sn += sm.en[i]; }
      else if constexpr (i == 12) { sm.tp = sm.e[12]; sm.tn = sm.en[12]; } else { sm.tp += sm.e[i]; sm.tn += sm.en[i]; }
    }
    if constexpr (s == 22) { sm.sp = __builtin_amdgcn_rcpf(xsum(sm.sp + (hh ? 0.f : sm.tp))) * rc.r; sm.sn = __builtin_amdgcn_rcpf(xsum(sm.sn + (hh ? 0.f : sm.tn))) * rc.r; }
    if constexpr (s >= 23) {
#pragma unroll
      for (int u = (s == 29 ? 6 : s - 23); u <= (s == 29 ? 7 : s - 23); u++) {
        const int k = u >> 1;
        if (u & 1) { u32x2 t = {pk2s(sm.en[4 * k] * sm.sn, sm.en[4 * k + 1] * sm.sn), pk2s(sm.en[4 * k + 2] * sm.sn, sm.en[4 * k + 3] * sm.sn)}; ldsw<u32x2>(aIW ^ (unsigned)(k << 4), 8192, t); }
        else { u32x2 t = {pk2s(sm.e[4 * k] * sm.sp, sm.e[4 * k + 1] * sm.sp), pk2s(sm.e[4 * k + 2] * sm.sp, sm.e[4 * k + 3] * sm.sp)}; ldsw<u32x2>(aIW ^ (unsigned)(k << 4), 0, t); }
      }
    }
  };
  auto qstep = [&](auto s_, f32x16& a, unsigned lh) AI {
    constexpr int s = decltype(s_)::value;
    if constexpr (s == 0) { sm.w0 = tr2(aWQ, lh * 1568, lh * 1568 + 8 * 56); sm.w1 = tr2(aWQ, lh * 1568 + 16 * 56, lh * 1568 + 24 * 56); }
    if constexpr (s == 1) sm.p = featproj2(a, sm.w0, sm.w1, hh);
    if constexpr (s == 3) sm.bx = -max16(sm.p) * rc.Lr;
    if constexpr (s == 4) sm.bn = min16(sm.p) * rc.Lr;
    if constexpr (s >= 5 && s < 21) {
      constexpr int i = (s - 5) >> 1;
      float t0, t1;
      if constexpr ((s - 5) & 1) { t0 = __builtin_fmaf(sm.p[2 * i], -rc.Lr, sm.bn); t1 = __builtin_fmaf(sm.p[2 * i + 1], -rc.Lr, sm.bn); }
      else { t0 = __builtin_fmaf(sm.p[2 * i], rc.Lr, sm.bx); t1 = __builtin_fmaf(sm.p[2 * i + 1], rc.Lr, sm.bx); }
      a[8 * ((s - 5) & 1) + i] = U2F(pk2(__builtin_amdgcn_exp2f(t0), __builtin_amdgcn_exp2f(t1)));
      if constexpr (s == 20) asm volatile("" : "+v"(a));
    }
  };
  bf16x8 ka_[4], kb_[2][4]; f32x4 kd[2];
  auto kvstep = [&](auto s_) AI {
    constexpr int s = decltype(s_)::value;
    if constexpr (s == 0) {
      const unsigned g4 = lane >> 4, r0 = 8 * g4 + q4, sw = 4 * blk + (q4 >> 1);
      unsigned b0 = LB + L_PHI + r0 * 64 + ((p4 ^ sw) << 3);
      unsigned a0 = (b0 ^ ((wr >> 1) << 5)) + (wr & 1) * 8192;
#pragma unroll
      for (int k = 0; k < 4; k++) {
        ka_[k] = __builtin_shufflevector(trd2(a0, k * 2048), trd2(a0 ^ 16u, k * 2048 + 256), 0, 1, 2, 3, 4, 5, 6, 7);
        kb_[0][k] = __builtin_shufflevector(trd2(b0, L_V - L_PHI + k * 2048), trd2(b0 ^ 16u, L_V - L_PHI + k * 2048 + 256), 0, 1, 2, 3, 4, 5, 6, 7);
        kb_[1][k] = __builtin_shufflevector(trd2(b0 ^ 32u, L_V - L_PHI + k * 2048), trd2(b0 ^ 48u, L_V - L_PHI + k * 2048 + 256), 0, 1, 2, 3, 4, 5, 6, 7);
      }
    }
    if constexpr (s == 1) {
      f32x4 z = {0.f, 0.f, 0.f, 0.f}; kd[0] = z; kd[1] = z;
#pragma unroll
      for (int k = 0; k < 4; k++) { kd[0] = MFMA16(ka_[k], kb_[0][k], kd[0]); kd[1] = MFMA16(ka_[k], kb_[1][k], kd[1]); }
    }
    if constexpr (s == 3) {
#pragma unroll
      for (int d = 0; d < 2; d++) {
        u32x4 t;
#pragma unroll
        for (int i = 0; i < 4; i++) t[i] = (F2U(kd[d][i]) & ~0xfffu) | T12;
        st16(t, rsWS, (lane >> 4) * 512 + (lane & 15) * 16, ((head * 16 + rb) * 2 + (wr & 1)) * 4096 + (wr >> 1) * 2048, WS_KVP + d * 256);
      }
    }
  };
  auto qexp2 = [&](f32x16& a0, unsigned l0, f32x16& a1, unsigned l1) AI {
    const f32x16 p0 = featproj(a0, aWQ, l0 * 1568, hh), p1 = featproj(a1, aWQ, l1 * 1568, hh);
    a0 = dualexpP(p0, rc.Lr); a1 = dualexpP(p1, rc.Lr); asm volatile("" : "+v"(a0), "+v"(a1));
  };

  unsigned fl1 = 0;
  EpiS es;
  u32x4 pv[8];
  const unsigned vKP = l31 * 16 + hu * 8192, sKP = (rb * 32 + half) * 4096 + wr * 1024 + GG * 512, sKR = (8 * half + w) * 8192;
  auto hop1 = [&]() AI {
    waittag<16384>(pv, rsWS, vKP, sKP + WS_KVP, T12, 0xfffu);
    u32x4 o;
#pragma unroll
    for (int k = 0; k < 4; k++) {
      float sum = U2F(pv[0][k]);
#pragma unroll
      for (int i = 1; i < 8; i++) sum += U2F(pv[i][k]);
      sum = xsum(sum);
      sum = l31 == 29 ? 1.f : sum;
      if (GG == 1) sum = wr == 3 ? 0.f : sum;
      o[k] = (pk2s(sum, sum) & 0xffff0000u) | T16;
    }
    st16(o, rsWS, hh ? 0x80000000u : l31 * 16, (rb * 2 + half) * 4096 + wr * 1024 + GG * 512, WS_KVR);
    if (b == 0 && w == 4) stflag(E + 4, rsWS, WS_EPOCH);
    SB();
  };
  bf16x8 cb[NT];
  auto ldflag = [&]() AI { asm volatile("buffer_load_dword %0, %1, %2, %3 offen sc1" : "=v"(fl1) : "v"(nd1 ? lane * FS : 0x80000000u), "s"(rsWS), "s"(sF) : "memory"); };
  auto touch = [&](unsigned wo4) AI {
    const unsigned rw = (rbl * 8 + w + wo4) * 44 + lane;
    const bool dn = rw < 896;
    const unsigned vo = lane < 44 ? (dn ? ((rw >> 6) * 128 + 64 + (rw & 63)) : rw - 896) * XROW + ci * 128 : 0x80000000u;
    asm volatile("s_add_u32 m0, %0, %c5\n\ts_nop 0\n\tbuffer_load_dword %1, %3, 0 offen lds\n\tbuffer_load_dword %2, %4, 0 offen lds"
                 :: "s"(LB + w * 256), "v"(dn ? vo : 0x80000000u), "v"(dn ? 0x80000000u : vo), "s"(rsWD), "s"(rsWO), "i"(L_ST) : "memory", "scc");
  };
  auto stream = [&]() AI {
    SFOR(q, 28 * NT)
      constexpr int n = q / NT, j = q % NT;
      if constexpr (j == 0) {
        if constexpr (n == 27) {
#pragma unroll
          for (int t = 0; t < NT; t++) ldsw<unsigned>(aCSw, ((3 + t) & 3) * 512, hilo(csB[t][0]));
          if constexpr (AT && GG) WAITV(8);
          WAITL0(); BAR(); MEMFENCE();
        }
        else if constexpr (!AT && GG && (n == S2N || n == B1N)) { BAR(); MEMFENCE(); }
        else if constexpr (!AT && !GG && (n & 3) == 3) { asm volatile("s_waitcnt lgkmcnt(4)" ::: "memory"); BAR(); MEMFENCE(); }
      }
      if constexpr (q + 4 < 28 * NT) readB(q + 4);
      if constexpr (n == 27) cb[j] = ldsr<bf16x8>(aCSr, ((3 + j) & 3) * 512);
      SB();
      accB[j] = MFMA(wq[q & 7], xf[n], accB[j]);
      if constexpr (!NOCS && (n & 3) == 1) csB[j] = MFMA16(sel, wq[q & 7], csB[j]);
      if constexpr (!AT) {
        if constexpr (GG == 1 && j == 0 && n >= B1N && n < B1N + 4) issueW2(1 + ((n - B1N) >> 1), (n - B1N) & 1);
        if constexpr (GG == 0) {
          if constexpr (j == 1 && n < 16) { epiSlice<(n >> 2) & 3, n & 3>(accA[2], rc, es); if constexpr (n == 15) st16(es.r, rsWS, vL16, sA2, 0); }
          if constexpr (j == 0 && n >= 3 && n < 7) issueW2(1 + ((n - 3) >> 1), (n - 3) & 1);
          if constexpr (j == 0 && n >= 7 && n < 27 && (n & 1)) issueW2(1 + ((n + 1) >> 2), ((n + 1) >> 1) & 1);
          if constexpr (n == 27) issueW2(8, j);
        }
      } else {
        if constexpr (GG == 1 && n < 16 && (n & 1) == 0) clri(n >> 1);
        if constexpr (GG == 0 && j < 2 && n >= 1 && 2 * (n - 1) + j < 21) qstep(IntC<2 * (n - 1) + j>{}, accA[2], 0);
        if constexpr (GG == 0 && q >= 3 * H0L && q < 3 * H0L + 8) pv[q - 3 * H0L] = ld16(rsWS, vKP, sKP, WS_KVP + (q - 3 * H0L) * 16384);
        if constexpr (GG == 0 && j == 2 && n == H0C) { asm volatile("" : "+v"(accB[0]), "+v"(accB[1]), "+v"(accB[NT - 1]), "+v"(csB[0]), "+v"(csB[1]), "+v"(csB[NT - 1])); STAMP(11); hop1(); STAMP(6); }
      }
      SB();
    SEND
    const bf16x8 xe = mkxe();
#pragma unroll
    for (int t = 0; t < NT; t++) { accB[t] = MFMA(cb[t], xe, accB[t]); asm volatile("" : "+v"(accB[t])); }
    SB();
  };
  unsigned ln = vL16, aW = 0;
  auto mkaw = [&]() AI {
    asm volatile("" : "+v"(ln));
    aW = lw2 + (ln & 0x200) + ((ln & 0xc0) << 1) + ((((ln >> 3) & 0x20) | ((ln >> 1) & 0x10)) ^ ((ln >> 1) & 0x40)) + ((ln >> 1) & 8);
  };
  bf16x8 wf[NS][2];
  auto readW = [&](int i) AI {
    const unsigned ra = ring_area(i % RING), la = aW + (ra & 0x30000u);
    wf[i][0] = tr2(la, ra & 0xffffu, (ra & 0xffffu) + 1024); wf[i][1] = tr2(la ^ 64u, ra & 0xffffu, (ra & 0xffffu) + 1024);
  };
  auto pairF = [&](auto p_, const f32x16& a) AI { constexpr int p = decltype(p_)::value; epiSlice<p, 0>(a, rc, es); epiSlice<p, 1>(a, rc, es); epiSlice<p, 2>(a, rc, es); epiSlice<p, 3>(a, rc, es); };
  if constexpr (GG == 0) {
    if constexpr (AT) { SFOR(q, 8) clri(q); SEND }
    SFOR(s, 30)
      kstep(s_);
      if constexpr (!AT && s == 1 && TEARLY) {
        const unsigned rw = (rbl * 4 + wr) * 56 + lane;
        dmat(LB + L_ST + w * 256, lane < 56 ? ((rw >> 6) * 128 + (rw & 63)) * XROW + ci * 128 : 0x80000000u, rsWD);
        if (TEARLY == 2) { touch(0); touch(4); }
      }
    SEND
    if constexpr (!AT && TPOS == 0) { touch(0); touch(4); }
    if constexpr (AT) WAITV(8);
    WAITL0(); SB(); BAR(); SB();
    STAMP(3);
    kvstep(IntC<0>{});
    readB(0); readB(1); readB(2); readB(3);
    kvstep(IntC<1>{}); kvstep(IntC<3>{});
    if constexpr (AT) { WAITV(2); stflag(E + 1, rsWS, sFown); }
    if constexpr (!AT && TPOS == 1) { touch(0); touch(4); }
    STAMP(4);
    SB();
    stream();
    if constexpr (!AT) { st16(epiP(accB[0], rc), rsWS, vL16, sA2, 4 * 1024); st16(epiP(accB[1 % NT], rc), rsWS, vL16, sA2, 5 * 1024); }
  } else {
    stream();
    STAMP(3);
    if constexpr (AT) {
      SFOR(s, 21) qstep(s_, accA[0], 1); SEND
      WAITV(0); stflag(E + 1, rsWS, sFown);
      SFOR(s, 21) qstep(s_, accA[1], 2); SEND
      SFOR(s, 21)
        if constexpr (s < 8) pv[s] = ld16(rsWS, vKP, sKP, WS_KVP + s * 16384);
        qstep(s_, accA[2], 3);
      SEND
      STAMP(4); hop1(); STAMP(6);
      SB(); BAR(); SB();
    } else {
      mkaw();
      if constexpr (TPOS == 2) { touch(0); touch(0xfffffffcu); }
      SFOR(e, 20)
        if constexpr (LREC && e == 16) { WAITV(10); SFOR(i, LREC) readW(i); SEND }
        if constexpr (e < 12) pairF(IntC<e & 3>{}, accA[e >> 2]); else pairF(IntC<e & 3>{}, accB[((e >> 2) - 3) % NT]);
        if constexpr ((e & 3) == 3) st16(es.r, rsWS, vL16, sA2, (e < 12 ? 1 + (e >> 2) : 3 + (e >> 2)) * 1024);
        if constexpr (e == 0) { issueW2(3, 0); issueW2(3, 1); }
        if constexpr (LREC && e == 16) { WAITL0(); SB(); SFOR(i, LREC) issueW2(9 + i, 0); issueW2(9 + i, 1); SEND }
        if constexpr ((e & 3) == 2) { SB(); BAR(); SB(); issueW2(4 + (e >> 2), 0); issueW2(4 + (e >> 2), 1); }
      SEND
    }
  }

  if constexpr (AT) {
    STAMP(5);
    auto qexp = [&](f32x16& a, unsigned lh) AI { a = dualexpP(featproj(a, aWQ, lh * 1568, hh), rc.Lr); asm volatile("" : "+v"(a)); SB(); };
    if constexpr (GG == 0) qexp(accB[0], 4);
#pragma unroll
    for (int i = 0; i < 8; i++) pv[i] = ld16(rsWS, vL16, sKR, WS_KVR + i * 1024);
    if constexpr (GG == 0) { qexp(accB[1 % NT], 5); qexp(accB[NT - 1], 6); } else qexp(accB[0], 7);
    STAMP(7);
    waittag<1024>(pv, rsWS, vL16, sKR + WS_KVR, T16, 0xffffu);
    STAMP(8);
#pragma unroll
    for (int i = 0; i < 4; i++) {
      u32x4 f = {__builtin_amdgcn_perm(pv[2 * i][1], pv[2 * i][0], 0x07060302u), __builtin_amdgcn_perm(pv[2 * i][3], pv[2 * i][2], 0x07060302u),
                 __builtin_amdgcn_perm(pv[2 * i + 1][1], pv[2 * i + 1][0], 0x07060302u), __builtin_amdgcn_perm(pv[2 * i + 1][3], pv[2 * i + 1][2], 0x07060302u)};
      ldsw<u32x4>(LB + w * 4096 + vL16, i * 1024, f);
    }
    WAITL0(); BAR(); MEMFENCE();
    STAMP(10);
    f32x16 op[4], on[4];
    auto omm = [&](const f32x16& phi, unsigned lh, int z) AI {
#pragma unroll
      for (int i = 0; i < 16; i++) { op[z][i] = 0.f; on[z][i] = 0.f; }
      bf16x8 kf[4];
#pragma unroll
      for (int i = 0; i < 4; i++) kf[i] = ldsr<bf16x8>(LB + vL16, lh * 4096 + i * 1024);
#pragma unroll
      for (int i = 0; i < 2; i++) { op[z] = MFMA(kf[i], sub4(phi, i), op[z]); on[z] = MFMA(kf[2 + i], sub4(phi, 2 + i), on[z]); }
    };
    auto ofin = [&](unsigned lh, int z) AI {
      const float ia = __builtin_amdgcn_rcpf(op[z][13]), ib = __builtin_amdgcn_rcpf(on[z][13]);
      const float id = __builtin_amdgcn_rcpf(__builtin_fmaf(op[z][12], ia, __builtin_fmaf(on[z][12], ib, 1e-6f)));
      const float ca = xhi(ia * id), cb2 = xhi(ib * id);
      const f32x2 c2 = dup2(ca), d2 = dup2(cb2);
      u32x4 f0, f1;
#pragma unroll
      for (int i = 0; i < 4; i++) {
        const f32x2 u = fma2(sub2(op[z], i), c2, sub2(on[z], i) * d2), v = fma2(sub2(op[z], 4 + i), c2, sub2(on[z], 4 + i) * d2);
        f0[i] = pk2(u[0], u[1]); f1[i] = pk2(v[0], v[1]);
      }
      f1[2] = hh ? 0u : f1[2]; f1[3] = hh ? 0u : f1[3];
      st16(f0, rsWS, OV, sA2, (112 + 2 * lh) * 1024); st16(f1, rsWS, OV, sA2, (113 + 2 * lh) * 1024);
      SB();
    };
    if constexpr (GG) { omm(accA[0], 1, 0); omm(accA[1], 2, 1); SB(); ofin(1, 0); omm(accA[2], 3, 2); omm(accB[0], 7, 3); SB(); ofin(2, 1); ofin(3, 2); ofin(7, 3); }
    else { omm(accA[2], 0, 0); omm(accB[0], 4, 1); SB(); ofin(0, 0); omm(accB[1 % NT], 5, 2); omm(accB[NT - 1], 6, 3); SB(); ofin(4, 1); ofin(5, 2); ofin(6, 3); }
    STAMP(9);
  } else {
    __builtin_amdgcn_s_setprio(0);
    STAMP(5);
    const unsigned sA = WS_A2 + rt2 * 144 * 1024;
    if constexpr (GG == 0) mkaw();
    f32x16 acc[2][2];
#pragma unroll
    for (int i = 0; i < 4; i++)
#pragma unroll
      for (int k = 0; k < 16; k++) acc[i >> 1][i & 1][k] = 0.f;
    u32x4 af[NS][2];
    u32x4 xr;
    auto loadA = [&](int i) AI { af[i][0] = ld16(rsWS, vL16, sA + ksb(i) * 1024, ksi(i) * 1024); af[i][1] = ld16(rsWS, vL16, sA + ksb(i) * 1024, (144 + ksi(i)) * 1024); };
    const unsigned vO = (ln >> 7) * XROW + (ln & 0x70), sO = (rt2 * 32 + 8 * w) * XROW + ci * 128;
    STAMP(6);
    constexpr int MV = GG ? 7 : 9;
    constexpr int R0 = GG ? LREC : 0;
    waitv<GG ? 7 + (LREC ? 2 * LREC - 7 + 3 : 0) : 4>();
    SFOR(i, (GG ? 7 : 8) - R0) readW(R0 + i); SEND
    WAITL0(); SB();
    SFOR(i, PRE) issueW2(9 + R0 + i, 0); issueW2(9 + R0 + i, 1); SEND
    waitv<2 * PRE>(); stflag(E + 1, rsWS, sFown);
    if constexpr (!GG) readW(8);
    WAITL0(); SB();
    BAR();
    SB(); STAMP(11);
    if constexpr (GG && PON == -2) __builtin_amdgcn_s_setprio(1);
    constexpr int NB = MV - R0 - PRE, FP = NB >= 2 ? NB - 2 : 0;
    SFOR(i, NB + 1)
      if constexpr (i == FP) ldflag();
      if constexpr (i < NB) { issueW2(9 + R0 + PRE + i, 0); issueW2(9 + R0 + PRE + i, 1); }
    SEND
    asm volatile("s_waitcnt vmcnt(%c1)" : "+v"(fl1) : "i"(2 * (NB - FP)) : "memory");
    if (!ALL(!nd1 || fl1 == E + 1u)) poll(rsWS, lane * FS, sF, E, nd1 ? 1u : 0u);
    STAMP(7);
    if constexpr (GG && PON == -1) __builtin_amdgcn_s_setprio(1);
#pragma unroll
    for (int j = 0; j < PD; j++) loadA(j);
    asm volatile("buffer_load_dwordx4 %0, %1, %2, %3 offen" : "=v"(xr) : "v"(vO), "s"(rsX), "s"(sO) : "memory");
    SFOR(i, NS)
      asm volatile("s_waitcnt vmcnt(%c2)" : "+v"(af[i][0]), "+v"(af[i][1]) : "i"(later2(i, NS, GG)) : "memory");
      if constexpr (i + 1 >= MV && i + 1 < NS) readW(i + 1);
      if constexpr (GG == 0 && i >= 12) {
        auto stale = [&]() AI {
          unsigned m = UMAX(UMAX(UMAX(af[i][0][0], af[i][0][1]), UMAX(af[i][0][2], af[i][0][3])), UMAX(UMAX(af[i][1][0], af[i][1][1]), UMAX(af[i][1][2], af[i][1][3])));
          return ANY(m == ~0u);
        };
        if (__builtin_expect(stale(), 0)) {
          int it = 0;
#pragma nounroll
          do { __builtin_amdgcn_s_sleep(SLEEPR); rel1<true>(af[i][0], af[i][1], rsWS, vL16, sA + (ksb(i) + ksi(i)) * 1024); } while (++it < (1 << 14) && stale());
          SFOR(u, PD - 1) if constexpr (i + 1 + u < NS) rel1<false>(af[i + 1 + u][0], af[i + 1 + u][1], rsWS, vL16, sA + (ksb(i + 1 + u) + ksi(i + 1 + u)) * 1024); SEND
        }
      }
      if constexpr (GG && i == POFF) __builtin_amdgcn_s_setprio(0);
      if constexpr (GG && i == PON) __builtin_amdgcn_s_setprio(1);
      if constexpr (GG && PON == -3) __builtin_amdgcn_s_setprio(i & 1);
      if constexpr (i == 6) STAMP(8);
      if constexpr (i == 12) STAMP(9);
      SB();
      if constexpr (i + PD < NS) loadA(i + PD);
#pragma unroll
      for (int mm = 0; mm < 2; mm++) acc[0][mm] = MFMA(wf[i][0], __builtin_bit_cast(bf16x8, af[i][mm]), acc[0][mm]);
#pragma unroll
      for (int mm = 0; mm < 2; mm++) acc[1][mm] = MFMA(wf[i][1], __builtin_bit_cast(bf16x8, af[i][mm]), acc[1][mm]);
      if constexpr (GG == 0 && (i == 9 || i == 10)) { SB(); issueW2(i + 9, 0); issueW2(i + 9, 1); }
      SB();
    SEND
    STAMP(10);
    __builtin_amdgcn_s_setprio(0);
#pragma unroll
    for (int qd = 0; qd < 4; qd++)
#pragma unroll
      for (int k = 0; k < 4; k++) {
        const unsigned ra = ring_area(2 * qd + (k >> 1)) + (k & 1) * 1024;
        ldsw<bf16x8>(((lw2 + vL16) ^ (unsigned)(32 * (4 * (qd >> 1) + k))) + (ra & 0x30000u), ra & 0xffffu, sub4(acc[qd >> 1][qd & 1], k));
      }
    WAITL0(); BAR(); MEMFENCE();
    STAMP(12);
    const unsigned c8 = (ln >> 4) & 7, j2 = (c8 & 4) + 2 * GG + ((c8 >> 1) & 1);
    const unsigned rd = LB + ((0x51407362u >> (4 * j2)) & 7) * 16384 + (c8 & 1) * 1024 + (((8 * wr + (ln >> 7)) << 4) ^ (c8 << 5));
    asm volatile("" : "+v"(xr));
    f32x4 sum[2];
#pragma unroll
    for (int k = 0; k < 2; k++) {
      f32x4 t = {U2F(xr[2 * k] << 16), U2F(xr[2 * k] & 0xffff0000u), U2F(xr[2 * k + 1] << 16), U2F(xr[2 * k + 1] & 0xffff0000u)};
#pragma unroll
      for (int v = 0; v < 8; v++) t += ldsr<f32x4>(rd, v * 2048 + k * 512);
      sum[k] = t;
    }
    u32x4 o = {pk2(sum[0][0], sum[0][1]), pk2(sum[0][2], sum[0][3]), pk2(sum[1][0], sum[1][1]), pk2(sum[1][2], sum[1][3])};
    __builtin_amdgcn_raw_buffer_store_b128(o, mkrs(pout, 2048 * XROW), vO + sO, 0, OAUX);
  }
  STAMPS_OUT();
  };
  if (!attn && g) prog(BoolC<false>{}, IntC<1>{});
  else if (!attn) prog(BoolC<false>{}, IntC<0>{});
  else if (g) prog(BoolC<true>{}, IntC<1>{});
  else prog(BoolC<true>{}, IntC<0>{});
}

extern "C" void kernel_launch(void* const* d_in, const int* in_sizes, int n_in, void* d_out, int out_size, void* d_ws, size_t ws_size,
                              hipStream_t stream) {
  fused_kernel<<<256, 512, 0, stream>>>((const char*)d_in[0], (const char*)d_in[1], (const char*)d_in[5], (const char*)d_in[6], (char*)d_ws,
                                        (const char*)d_in[2], (const char*)d_in[7], (const char*)d_in[3], (char*)d_out);
}
```

```cpp
#define XS 2
#define PDV 6
#include <hip/hip_runtime.h>
#include <hip/hip_bf16.h>
#include <utility>

#define DI __device__ __forceinline__
#define LAS __attribute__((address_space(3)))
typedef __attribute__((ext_vector_type(8))) short bf16x8;
typedef __attribute__((ext_vector_type(4))) short s16x4;
typedef __attribute__((ext_vector_type(2))) __bf16 bf2;
typedef __attribute__((ext_vector_type(2))) float f32x2;
typedef __attribute__((ext_vector_type(4))) float f32x4;
typedef __attribute__((ext_vector_type(16))) float f32x16;
typedef __attribute__((ext_vector_type(2))) unsigned u32x2;
typedef __attribute__((ext_vector_type(4))) unsigned u32x4;
typedef __amdgpu_buffer_rsrc_t rsrc_t;
#define F2U(x) __builtin_bit_cast(unsigned, (float)(x))
#define U2F(x) __builtin_bit_cast(float, (unsigned)(x))
#define ALL(x) (__builtin_amdgcn_ballot_w64(!(x)) == 0ull)
#define ANY(x) (__builtin_amdgcn_ballot_w64(x) != 0ull)
#define UMAX(a, b) __builtin_elementwise_max((unsigned)(a), (unsigned)(b))

#define NTX 0
#define NTW 0
#define NT2 0
constexpr unsigned XROW = 896, QROW = 2688, FROW = 3584;
constexpr unsigned FS = 128;
constexpr unsigned WS_EPOCH = 0;
constexpr unsigned WS_GR = 4096;
constexpr unsigned WS_KVP = 131072;
constexpr unsigned WS_A2 = WS_KVP + 16 * 16 * 8192;
constexpr unsigned WS_ST = WS_A2 + 64 * 144 * 1024;
constexpr unsigned WS_FH = WS_ST + 262144;
constexpr unsigned WS_KVR = WS_FH + 2048 * FS;
constexpr unsigned WS_END = WS_KVR + 16 * 8192;
constexpr unsigned L_GU = 65536, L_KV = 131072;
constexpr unsigned L_TAIL = L_KV + 3 * 8192;
constexpr unsigned L_WPK = L_TAIL;
constexpr unsigned L_ST = L_TAIL + 2048;
constexpr unsigned L_CS = L_TAIL + 4096;
constexpr unsigned L_PHI = L_GU + 32768;
constexpr unsigned L_V = L_KV + 16384;
constexpr unsigned L_FREE = 32768;

#define MFMA(a, b, c) __builtin_amdgcn_mfma_f32_32x32x16_bf16((a), (b), (c), 0, 0, 0)
#define MFMA16(a, b, c) __builtin_amdgcn_mfma_f32_16x16x32_bf16((a), (b), (c), 0, 0, 0)
#define AI __attribute__((always_inline))
#define BAR() __builtin_amdgcn_s_barrier()
#define WAITV(n) asm volatile("s_waitcnt vmcnt(" #n ")" ::: "memory")
#define WAITL0() asm volatile("s_waitcnt lgkmcnt(0)" ::: "memory")
#define SB() __builtin_amdgcn_sched_barrier(0)
#define MEMFENCE() asm volatile("" ::: "memory")

template <int N> DI void waitv() { asm volatile("s_waitcnt vmcnt(%c0)" :: "i"(N) : "memory"); }

#define INSZ(x) (x)
DI rsrc_t mkrs(const void* p, unsigned bytes) { return __builtin_amdgcn_make_buffer_rsrc((void*)p, (short)0, (int)bytes, 0x00020000); }
template <bool NT = false> DI void dma(unsigned lds, unsigned limm, unsigned voff, rsrc_t rs, unsigned soff, unsigned simm) {
  unsigned t;
  if constexpr (NT) asm volatile("s_add_u32 m0, %1, %c5\n\ts_add_u32 %0, %4, %c6\n\tbuffer_load_dwordx4 %2, %3, %0 offen nt lds"
               : "=&s"(t) : "s"(lds), "v"(voff), "s"(rs), "s"(soff), "i"(limm), "i"(simm) : "memory", "scc");
  else asm volatile("s_add_u32 m0, %1, %c5\n\ts_add_u32 %0, %4, %c6\n\tbuffer_load_dwordx4 %2, %3, %0 offen lds"
               : "=&s"(t) : "s"(lds), "v"(voff), "s"(rs), "s"(soff), "i"(limm), "i"(simm) : "memory", "scc");
}
DI void dmat(unsigned lds, unsigned voff, rsrc_t rs) {
  asm volatile("s_mov_b32 m0, %0\n\ts_nop 0\n\tbuffer_load_dword %1, %2, 0 offen lds" :: "s"(lds), "v"(voff), "s"(rs) : "memory");
}
DI s16x4 trd(unsigned a) { return __builtin_amdgcn_ds_read_tr16_b64_v4i16((LAS s16x4*)(size_t)a); }
DI s16x4 trd2(unsigned a, unsigned off) { return __builtin_amdgcn_ds_read_tr16_b64_v4i16((LAS s16x4*)((LAS char*)(size_t)a + off)); }
DI bf16x8 tr2(unsigned a, unsigned off0, unsigned off1) {
  LAS char* p = (LAS char*)(size_t)a;
  s16x4 lo = __builtin_amdgcn_ds_read_tr16_b64_v4i16((LAS s16x4*)(p + off0));
  s16x4 hi = __builtin_amdgcn_ds_read_tr16_b64_v4i16((LAS s16x4*)(p + off1));
  return __builtin_shufflevector(lo, hi, 0, 1, 2, 3, 4, 5, 6, 7);
}
template <class T> DI T ldsr(unsigned a, unsigned off) { return *(LAS T*)((LAS char*)(size_t)a + off); }
template <class T> DI void ldsw(unsigned a, unsigned off, T v) { *(LAS T*)((LAS char*)(size_t)a + off) = v; }
DI unsigned pk2(float a, float b) { f32x2 f = {a, b}; return __builtin_bit_cast(unsigned, __builtin_convertvector(f, bf2)); }
DI float rbf(float a) { return U2F(pk2(a, a) << 16); }
DI float dot2(unsigned a, unsigned b, float c) { return __builtin_amdgcn_fdot2_f32_bf16(__builtin_bit_cast(bf2, a), __builtin_bit_cast(bf2, b), c, false); }
DI float xsum(float v) { auto r = __builtin_amdgcn_permlane32_swap(F2U(v), F2U(v), false, false); float a = U2F(r[0]); asm("" : "+v"(a)); return a + U2F(r[1]); }
DI float xmax(float v) { auto r = __builtin_amdgcn_permlane32_swap(F2U(v), F2U(v), false, false); return __builtin_fmaxf(U2F(r[0]), U2F(r[1])); }
DI float xmin(float v) { auto r = __builtin_amdgcn_permlane32_swap(F2U(v), F2U(v), false, false); return __builtin_fminf(U2F(r[0]), U2F(r[1])); }
DI float xhi(float v) { auto r = __builtin_amdgcn_permlane32_swap(F2U(v), F2U(v), false, false); return U2F(r[1]); }
#define STB "sc1"
DI void st16(u32x4 v, rsrc_t rs, unsigned voff, unsigned soff, unsigned simm) {
  unsigned t;
  asm volatile("s_add_u32 %0, %4, %c5\n\tbuffer_store_dwordx4 %1, %2, %3, %0 offen " STB "\n\ts_nop 1" : "=&s"(t) : "v"(v), "v"(voff), "s"(rs), "s"(soff), "i"(simm) : "memory", "scc");
}
DI void stflag(unsigned v, rsrc_t rs, unsigned soff) {
  unsigned voff = (__builtin_amdgcn_mbcnt_hi(~0u, __builtin_amdgcn_mbcnt_lo(~0u, 0u)) == 0u) ? 0u : 0x80000000u;
  __builtin_amdgcn_raw_buffer_store_b32(v, rs, voff, soff, 16);
}
#define XORCOND (n == 24)
#define PS 0
#define PV 0
#define KEEP2 0
#define S2N 14
#define B1N 23
#define DIST 2
#define P2G1 1
#define OAUX 0
#define RECY 0
#define POFF 99
#define H1S 0
#define H0L 14
#define H0C 22
#define Q3A 1
#define COOP 0
#define PON -2
#define XN 20
#define BAN 18
#define BBN 21
#define SL0 8
#define SLN 2
#define SLD 3
#define NSL 48
#define H1C 16
#define OV vL16
#define CV vL16
#define Q6E 0
#define TEARLY 0
#define NOCS 0
#define TPOS 0
#define PRE 0
#define LREC 0
#define SLEEPR 4
#define SLEEPN 2
DI u32x4 ld16(rsrc_t rs, unsigned voff, unsigned soff, unsigned simm) {
  u32x4 r; unsigned t;
  asm volatile("s_add_u32 %1, %4, %c5\n\tbuffer_load_dwordx4 %0, %2, %3, %1 offen sc1" : "=v"(r), "=&s"(t) : "v"(voff), "s"(rs), "s"(soff), "i"(simm) : "memory", "scc");
  return r;
}
template <unsigned STEP> DI void ld8w(u32x4 (&p)[8], rsrc_t rs, unsigned voff, unsigned soff) {
  unsigned t;
#define L1(k) "buffer_load_dwordx4 %" #k ", %9, %10, %8 offen sc1\n\ts_add_u32 %8, %8, %c12\n\t"
  asm volatile("s_mov_b32 %8, %11\n\t" L1(0) L1(1) L1(2) L1(3) L1(4) L1(5) L1(6) L1(7) "s_waitcnt vmcnt(0)"
               : "=&v"(p[0]), "=&v"(p[1]), "=&v"(p[2]), "=&v"(p[3]), "=&v"(p[4]), "=&v"(p[5]), "=&v"(p[6]), "=&v"(p[7]), "=&s"(t)
               : "v"(voff), "s"(rs), "s"(soff), "i"(STEP) : "memory", "scc");
#undef L1
}
DI bool untag(u32x4 (&p)[8], unsigned tag, unsigned mask) {
  unsigned chk = 0;
#pragma unroll
  for (int i = 0; i < 8; i++)
#pragma unroll
    for (int k = 0; k < 4; k++) { p[i][k] ^= tag; chk |= p[i][k]; }
  return ALL((chk & mask) == 0u);
}
template <unsigned STEP> DI void waittag(u32x4 (&p)[8], rsrc_t rs, unsigned voff, unsigned soff, unsigned tag, unsigned mask) {
  asm volatile("s_waitcnt vmcnt(0)" : "+v"(p[0]), "+v"(p[1]), "+v"(p[2]), "+v"(p[3]), "+v"(p[4]), "+v"(p[5]), "+v"(p[6]), "+v"(p[7]) :: "memory");
#pragma nounroll
  for (int it = 0; !untag(p, tag, mask) && it < (1 << 16); ++it) { __builtin_amdgcn_s_sleep(SLEEPN); ld8w<STEP>(p, rs, voff, soff); }
}
template <bool WAIT> DI void rel1(u32x4& a, u32x4& b, rsrc_t rs, unsigned voff, unsigned so) {
  unsigned t;
  if constexpr (WAIT) asm volatile("buffer_load_dwordx4 %0, %3, %4, %5 offen sc1\n\ts_add_u32 %2, %5, 147456\n\tbuffer_load_dwordx4 %1, %3, %4, %2 offen sc1\n\ts_waitcnt vmcnt(0)"
                                   : "+v"(a), "+v"(b), "=&s"(t) : "v"(voff), "s"(rs), "s"(so) : "memory", "scc");
  else asm volatile("buffer_load_dwordx4 %0, %3, %4, %5 offen sc1\n\ts_add_u32 %2, %5, 147456\n\tbuffer_load_dwordx4 %1, %3, %4, %2 offen sc1"
                    : "+v"(a), "+v"(b), "=&s"(t) : "v"(voff), "s"(rs), "s"(so) : "memory", "scc");
}
DI void poll(rsrc_t rs, unsigned voff, unsigned soff, unsigned E, unsigned need) {
  voff = need ? voff : 0x80000000u;
#pragma nounroll
  for (int it = 0; it < (1 << 16); ++it) {
    unsigned v;
    asm volatile("buffer_load_dword %0, %1, %2, %3 offen sc1\n\ts_waitcnt vmcnt(0)" : "=v"(v) : "v"(voff), "s"(rs), "s"(soff) : "memory");
    bool ok = (need == 0u) || ((v - E - need) <= (3u - need));
    if (ALL(ok)) break;
    __builtin_amdgcn_s_sleep(SLEEPN);
  }
}

template <bool B> struct BoolC { static constexpr bool value = B; };
template <int I> struct IntC { static constexpr int value = I; };
template <int... I, class F> DI void sfor_impl(std::integer_sequence<int, I...>, F&& f) { (f(IntC<I>{}), ...); }
template <int N, class F> DI void sfor(F&& f) { sfor_impl(std::make_integer_sequence<int, N>{}, f); }
#define SFOR(n, N) sfor<N>([&](auto n##_) AI { constexpr int n = decltype(n##_)::value;
#define SEND });
struct RowC { float r, K1, K2, Ri, Lr; };

DI unsigned hilo(float v) { float hi = rbf(v); return pk2(hi, v - hi); }
DI float max3(float a, float b, float c) { return __builtin_fmaxf(__builtin_fmaxf(a, b), c); }
DI float min3(float a, float b, float c) { return __builtin_fminf(__builtin_fminf(a, b), c); }
DI unsigned pk2s(float a, float b) { unsigned r; asm("v_cvt_pk_bf16_f32 %0, %1, %2" : "=v"(r) : "v"(a), "v"(b)); return r; }
DI f32x2 sub2(const f32x16& v, int k) { f32x2 t = {v[2 * k], v[2 * k + 1]}; return t; }
DI f32x2 exp2v(f32x2 v) { f32x2 r = {__builtin_amdgcn_exp2f(v[0]), __builtin_amdgcn_exp2f(v[1])}; return r; }
DI f32x2 dup2(float a) { f32x2 r = {a, a}; asm("" : "+v"(r)); return r; }
DI f32x2 fma2(f32x2 a, f32x2 b, f32x2 c) { return __builtin_elementwise_fma(a, b, c); }
DI u32x4 epiP(const f32x16& a, const RowC& c) {
  u32x4 r;
  const f32x2 K1 = dup2(c.K1), K2 = dup2(c.K2), Ri = dup2(c.Ri);
#pragma unroll
  for (int i = 0; i < 4; i++) {
    const f32x2 g = sub2(a, i), d = fma2(exp2v(g * fma2(g * g, K2, K1)), Ri, Ri);
    const f32x2 s = {__builtin_amdgcn_rcpf(d[0]), __builtin_amdgcn_rcpf(d[1])}, h = (g * sub2(a, 4 + i)) * s;
    r[i] = pk2(h[0], h[1]);
  }
  return r;
}
struct EpiS { float x0, x1, m0, m1; u32x4 r; };
template <int P, int PH> DI void epiSlice(const f32x16& a, const RowC& c, EpiS& s) {
  const float g0 = a[2 * P], g1 = a[2 * P + 1];
  if constexpr (PH == 0) { s.x0 = g0 * __builtin_fmaf(g0 * g0, c.K2, c.K1); s.x1 = g1 * __builtin_fmaf(g1 * g1, c.K2, c.K1); }
  if constexpr (PH == 1) { s.x0 = __builtin_amdgcn_exp2f(s.x0); s.x1 = __builtin_amdgcn_exp2f(s.x1); s.m0 = g0 * a[8 + 2 * P]; s.m1 = g1 * a[9 + 2 * P]; }
  if constexpr (PH == 2) { s.x0 = __builtin_amdgcn_rcpf(__builtin_fmaf(s.x0, c.Ri, c.Ri)); s.x1 = __builtin_amdgcn_rcpf(__builtin_fmaf(s.x1, c.Ri, c.Ri)); }
  if constexpr (PH == 3) s.r[P] = pk2s(s.m0 * s.x0, s.m1 * s.x1);
}
DI f32x16 featproj2(const f32x16& a, bf16x8 w0, bf16x8 w1, bool hh) {
  u32x4 q0, q1;
#pragma unroll
  for (int i = 0; i < 4; i++) { q0[i] = pk2(a[2 * i], a[2 * i + 1]); q1[i] = pk2(a[8 + 2 * i], a[9 + 2 * i]); }
  q1[2] = hh ? 0u : q1[2]; q1[3] = hh ? 0u : q1[3];
  f32x16 z;
#pragma unroll
  for (int i = 0; i < 16; i++) z[i] = 0.f;
  z = MFMA(w0, __builtin_bit_cast(bf16x8, q0), z);
  z = MFMA(w1, __builtin_bit_cast(bf16x8, q1), z);
  return z;
}
DI f32x16 featproj(const f32x16& a, unsigned wpaddr, unsigned off, bool hh) {
  return featproj2(a, tr2(wpaddr, off, off + 8 * 56), tr2(wpaddr, off + 16 * 56, off + 24 * 56), hh);
}
DI float max16(const f32x16& p) { float m = max3(p[0], p[1], p[2]); _Pragma("unroll") for (int i = 3; i < 15; i += 2) m = max3(m, p[i], p[i + 1]); return xmax(__builtin_fmaxf(m, p[15])); }
DI float min16(const f32x16& p) { float m = min3(p[0], p[1], p[2]); _Pragma("unroll") for (int i = 3; i < 15; i += 2) m = min3(m, p[i], p[i + 1]); return xmin(__builtin_fminf(m, p[15])); }
DI f32x16 dualexpP(const f32x16& p, float Lr) {
  const f32x2 bx = dup2(-max16(p) * Lr), bn = dup2(min16(p) * Lr), L2 = dup2(Lr);
  f32x16 o;
#pragma unroll
  for (int i = 0; i < 8; i++) {
    const f32x2 ep = exp2v(fma2(sub2(p, i), L2, bx)), en = exp2v(fma2(sub2(p, i), -L2, bn));
    o[i] = U2F(pk2(ep[0], ep[1])); o[8 + i] = U2F(pk2(en[0], en[1]));
  }
  return o;
}
struct SmS { f32x16 p, e; float en[16], bx, bn, sp, sn, tp, tn; bf16x8 w0, w1; };
DI bf16x8 sub4(const f32x16& v, int k) { f32x4 t = {v[4 * k], v[4 * k + 1], v[4 * k + 2], v[4 * k + 3]}; return __builtin_bit_cast(bf16x8, t); }

DI constexpr unsigned lc1_base(int ch) { return ch == 6 ? L_KV : ((ch & 1) ? L_GU : 0u) + (ch < 2 ? 49152u : ch < 4 ? 0u : 16384u); }
template <int CTRL, int RM> DI float dppadd(float v) { return v + U2F(__builtin_amdgcn_update_dpp(0u, F2U(v), CTRL, RM, 0xf, false)); }
DI float wavesum63(float v) {
  v = dppadd<0x111, 0xf>(v); v = dppadd<0x112, 0xf>(v); v = dppadd<0x114, 0xf>(v); v = dppadd<0x118, 0xf>(v);
  v = dppadd<0x142, 0xa>(v); return dppadd<0x143, 0xc>(v);
}

#define STAMP(i)
#define STAMPS_OUT()
#define CSTAMP(i)

constexpr int PD = PDV, RING = 9;
constexpr int later2(int i, int NS, int GG) {
  const int younger = 2 * ((i + PD <= NS ? i + PD : NS) - 1 - i);
  int extra = i < PD ? 1 : 0;
  if (NS == 20 && (i == 10 || i == 11)) extra += 2 * (i - 9);
  return younger + extra;
}
DI constexpr unsigned ring_area(int j) { return j == 0 ? L_FREE : j == 1 ? L_PHI : lc1_base(j - 2); }
constexpr int cumG1(int s) { return s < 4 ? 0 : s < 22 ? (s - 4) / 2 : (9 + (s - 22) * 5 / 4 < 48 ? 9 + (s - 22) * 5 / 4 : 48); }

#define CODE_BYTES 65536
__global__ __launch_bounds__(512, 2) void fused_kernel(const char* px_, const char* pqkv_, const char* pwg_, const char* pwu_, char* ws_, const char* pphi_,
                                                       const char* pwd_, const char* pwo_, char* pout_) {
  typedef __attribute__((ext_vector_type(8))) unsigned long long u64x8;
  u64x8 ka; unsigned long long kb;
  asm volatile("s_load_dwordx16 %0, %2, 0\n\ts_load_dwordx2 %1, %2, 64" : "=&s"(ka), "=&s"(kb) : "s"(__builtin_amdgcn_kernarg_segment_ptr()));
  __shared__ __attribute__((aligned(16))) char smem[163840];
  const unsigned LB = (unsigned)(size_t)smem;
  const unsigned tid = __builtin_amdgcn_workitem_id_x(), lane = tid & 63;
  const unsigned w = __builtin_amdgcn_readfirstlane(tid >> 6), g = w >> 2, wr = w & 3;
  const unsigned b = __builtin_amdgcn_workgroup_id_x(), xcd = b & 7, bi = b >> 3, grp = xcd >> 1, half = xcd & 1, rbl = bi & 3, cp = bi >> 2, ci = cp - 1;
  const unsigned rb = 4 * grp + rbl, c = 7 * half + ci;
  const unsigned head = 8 * half + (((half ? 0x76425310u : 0x75316420u) >> (4 * cp)) & 7);
  const bool attn = cp == 0;
  STAMP(0); CSTAMP(14);
  const unsigned wstr = attn ? QROW : FROW;
  const unsigned sG0 = half * (attn ? 448 : 1792) + (attn ? 0 : ci * 256), sX0 = rb * 128 * XROW;
  unsigned vx0, vx1, vG, vU, vKV;
  auto voffs = [&](unsigned ln) AI {
    const unsigned r8 = ln >> 3, c8 = ln & 7, sa = (r8 >> 1) & 1, lc = c8 ^ (4 * sa);
    vx0 = (16 * w + r8) * XROW + ((c8 ^ (r8 >> 1)) << 4); vx1 = (vx0 ^ 64u) + 8 * XROW;
    const unsigned cg = attn ? lc : (c8 ^ (2 * sa)), cu = attn ? (lc >= 6 ? lc - 2 : lc) : (cg ^ 4);
    vG = (8 * w + r8) * wstr + (cg << 4); vU = (8 * w + r8) * wstr + (cu << 4);
    vKV = (8 * w + r8) * QROW + 896 * (1 + (lc >> 2)) + ((lc & 3) << 4) + ((56 * head) & ~15u);
  };
  voffs(lane);
  const unsigned lw1 = LB + w * 1024, lw2 = LB + w * 2048;
#define ROT (((0x24924u >> (2 * (cp + rbl))) & 3) * 2)
  const unsigned rot = ROT;
  const unsigned srow = 128 * rb + 64 * half + 8 * cp + w;
  unsigned vrow = lane < 56 ? lane * 16 : 0x80000000u;
  asm volatile("s_waitcnt lgkmcnt(0)" : "+s"(ka), "+s"(kb), "+v"(vx0), "+v"(vx1), "+v"(vG), "+v"(vU), "+v"(vKV), "+v"(vrow));
  const char *px = (const char*)ka[0], *pqkv = (const char*)ka[1], *pwg = (const char*)ka[2], *pwu = (const char*)ka[3], *pphi = (const char*)ka[5],
             *pwd = (const char*)ka[6], *pwo = (const char*)ka[7];
  char *ws = (char*)ka[4], *pout = (char*)kb;
  const rsrc_t rsX = mkrs(px, INSZ(2048 * XROW)), rsQ = mkrs(pqkv, INSZ(448 * QROW));
  const rsrc_t rsG = mkrs(attn ? pqkv : pwg, INSZ(448 * wstr)), rsU = mkrs(attn ? pqkv + 128 : pwu, INSZ(448 * wstr - (attn ? 128 : 0)));
  auto cidx = [&](int st) AI -> unsigned { unsigned t = rot + st; return t >= 7 ? t - 7 : t; };
  auto issueA = [&](int st, int k) AI {
    const unsigned sl = st & 3, ch = cidx(st);
    if (k == 0) dma<NTX>(lw2, sl * 16384, vx0, rsX, sX0 + ch * 128, 0);
    if (k == 1) dma<NTX>(lw2, sl * 16384 + 1024, vx1, rsX, sX0 + ch * 128, 0);
    if (k == 2) dma<NTW>(lw1, L_GU + sl * 16384, vG, rsG, sG0 + ch * 64 * wstr, 0);
    if (k == 3) dma<NTW>(lw1, L_GU + sl * 16384 + 8192, vU, rsU, sG0 + ch * 64 * wstr, 0);
    if (k == 4) dma<NTW>(lw1, L_KV + sl * 8192, vKV, rsQ, ch * 64 * QROW, 0);
  };
  const rsrc_t rsWS = mkrs(ws, WS_END);
  unsigned Ev, cpf; u32x4 xrw;
  asm volatile("buffer_load_dwordx4 %0, %1, %2, %3 offen" : "=v"(xrw) : "v"(vrow), "s"(rsX), "s"(srow * XROW) : "memory");
  asm volatile("buffer_load_dword %0, off, %1, 0 sc1" : "=v"(Ev) : "s"(rsWS) : "memory");
  {
    unsigned long long pc; asm volatile("s_getpc_b64 %0" : "=s"(pc));
    const unsigned vo = w == 0 && lane < 24 ? (bi * 24 + lane) * 128 : 0x80000000u;
    asm volatile("buffer_load_dword %0, %1, %2, 0 offen" : "=v"(cpf) : "v"(vo), "s"(mkrs((const void*)pc, CODE_BYTES)) : "memory");
  }
#pragma unroll
  for (int k = 0; k < 5; k++) issueA(0, k);
  asm volatile("s_waitcnt vmcnt(%c3)" : "+v"(xrw), "+v"(Ev), "+v"(cpf) : "i"(5 + COOP) : "memory");
#pragma unroll
  for (int k = 0; k < 5; k++) issueA(1, k);
  issueA(2, 0);
  const unsigned E = __builtin_amdgcn_readfirstlane(Ev);
  {
    float s1 = 0.f, s2 = 0.f;
#pragma unroll
    for (int i = 0; i < 4; i++) { s1 = dot2(xrw[i], 0x3f803f80u, s1); s2 = dot2(xrw[i], xrw[i], s2); }
    s1 = wavesum63(s1); s2 = wavesum63(s2);
    const float mean = s1 * (1.f / 448.f), var = __builtin_fmaxf(s2 * (1.f / 448.f) - mean * mean, 0.f);
    u32x2 gr = {pk2(-mean, __builtin_amdgcn_sqrtf(rbf(rbf(var) + 1e-5f))), E + 1};
    __builtin_amdgcn_raw_buffer_store_b64(gr, rsWS, lane == 63 ? 0u : 0x80000000u, WS_GR + srow * 8, 16);
  }

  auto prog = [&](auto at_, auto gg_) AI {
  constexpr bool AT = decltype(at_)::value; constexpr int GG = decltype(gg_)::value;
  constexpr unsigned lcstep = AT ? 224 : 128;
  unsigned lane;
  asm volatile("v_mbcnt_lo_u32_b32 %0, -1, 0\n\tv_mbcnt_hi_u32_b32 %0, -1, %0" : "=v"(lane));
  __builtin_assume(lane < 64);
  voffs(lane);
  auto issueL = [&](int hc) AI {
    const int st = hc >> 1; const unsigned ch = cidx(st);
    if (hc & 1) dma<NTW>(lw1, lc1_base(st) + 8192, vU, rsU, sG0 + ch * 64 * wstr, lcstep);
    else dma<NTW>(lw1, lc1_base(st), vG, rsG, sG0 + ch * 64 * wstr, lcstep);
  };
  auto issueG = [&](int bar, int k) AI {
    if (bar == 0) issueA(2 + k / 5, k % 5);
    else if (bar <= 3) issueA(bar + 3, k);
    else if (k < 4) issueL(4 * (bar - 4) + k);
    else if (bar == 4) dma(LB + (w & 1) * 1024, L_WPK, lane * 16, mkrs(pphi, 16 * 1568), head * 1568 + (w & 1) * 1024, 0);
    else issueL(12 + (bar - 5));
  };

  const unsigned rt = 4 * rb + wr;
  const unsigned sA2 = WS_A2 + rt * 144 * 1024 + (AT ? half * 16384 : c * 8192), vL16 = lane * 16;
  const u32x4 sent = {~0u, ~0u, ~0u, ~0u};
  auto clr = [&](int ks) AI { st16(sent, rsWS, CV, sA2, ks * 1024); };

  auto clri = [&](int q) AI { clr(GG ? (q < 6 ? 114 + q : 120 + q) : (q < 2 ? 112 + q : 118 + q)); };

  const bool hh = lane >= 32;
  const unsigned hu = lane >> 5, blk = (lane >> 4) & 1, q4 = (lane >> 2) & 3, p4 = lane & 3, ta = (q4 >> 1) & 1, l31 = lane & 31;
  const unsigned perm = wr ^ (2 * GG);
  const unsigned rowoff = (8 * hu + q4) * 128 + (perm << 11);
  const unsigned pc = (4 * blk + p4) < 6 ? (4 * blk + p4) : 6;
  auto tileaddr = [&](unsigned t) AI -> unsigned {
    if constexpr (AT) { unsigned o = 56 * t + 8 * pc; return (o >> 7) * 8192 + ((((o >> 4) & 7) ^ (4 * ta)) << 4) + (o & 8) + rowoff; }
    else return blk * 8192 + (((2 * t + (p4 >> 1)) ^ (4 * blk + 2 * ta)) << 4) + 8 * (p4 & 1) + rowoff;
  };
  constexpr int NT = !AT ? 2 : GG ? 1 : 3, TB0 = GG ? 4 - NT : 0;
  unsigned aA[3], aB[NT];
  {
    unsigned o = (head & 1) * 8 + 8 * pc;
    unsigned ak = L_KV + (((o >> 4) ^ (4 * ta)) << 4) + (o & 8) + rowoff;
    aA[0] = LB + (GG ? L_GU + tileaddr(1) : ak);
    aA[1] = LB + (GG ? L_GU + tileaddr(2) : (ak ^ 64u));
    aA[2] = LB + L_GU + tileaddr(GG ? 3 : 0);
#pragma unroll
    for (int j = 0; j < NT; j++) aB[j] = LB + tileaddr(TB0 + j);
  }
  const unsigned xrow = 32 * wr + l31;
  const unsigned aX = LB + xrow * 128 + (((2 * perm + hu) ^ ((xrow >> 1) & 7)) << 4);
  const unsigned selv = (((lane >> 4) ^ (lane >> 2)) & 1) ? 0u : 0x3f803f80u;
  const u32x4 sel4 = {selv, selv, selv, selv};
  const bf16x8 sel = __builtin_bit_cast(bf16x8, sel4);

  bf16x8 xf[28], wa[2][3], wq[8];
  f32x16 accA[3], accB[NT];
  f32x4 csA[3], csB[NT];
#pragma unroll
  for (int j = 0; j < 3; j++) { for (int i = 0; i < 16; i++) accA[j][i] = 0.f; for (int i = 0; i < 4; i++) csA[j][i] = 0.f; }
#pragma unroll
  for (int j = 0; j < NT; j++) { for (int i = 0; i < 16; i++) accB[j][i] = 0.f; for (int i = 0; i < 4; i++) csB[j][i] = 0.f; }
  u32x2 grn;

  const rsrc_t rsWD = mkrs(pwd, INSZ(1792 * XROW)), rsWO = mkrs(pwo, INSZ(448 * XROW));
  const unsigned vW2 = (lane >> 3) * XROW + (((lane & 7) ^ ((lane >> 2) & 4)) << 4) + ci * 128;
  constexpr int NS = GG ? 16 + XS : 20 - XS, NC = 6 - XS / 2, NO = 2 * NC;
#define RHO ((rbl + cp) & 3)
  const unsigned rho = RHO;
  auto t_of = [](int i) constexpr { return (0x54763210u >> (4 * ((i >> 1) & 7))) & 7; };
  auto ksb = [&](int i) AI -> unsigned {
    if (GG) return i < 16 ? 16 * wr + ((t_of(i) & 3) ^ rho) : wr;
    if (i < NO) { const unsigned cc = i % NC + rho + (XS ? 0 : rho >> 1); return wr + 8 * (cc >= NC ? cc - NC : cc); }
    return 8 * wr + 2 * (((i - NO) >> 1) ^ rho);
  };
  auto ksi = [&](int i) AI -> unsigned { return GG ? (i < 16 ? 8 * (i & 1) + (t_of(i) & 4) : 4 * i) : i < NO ? 64 + 4 * XS + 4 * (i / NC) : 112 + (i & 1); };
  auto issueW2 = [&](int i, int hf) AI {
    if (GG == 0 && i >= NO) dma<NT2>(lw2, ring_area(i % RING) + hf * 1024, vW2, rsWO, ksb(i) * 14 * XROW, (16 * (i & 1) + 8 * hf) * XROW);
    else dma<NT2>(lw2, ring_area(i % RING) + hf * 1024, vW2, rsWD, ksb(i) * 16 * XROW, (ksi(i) * 16 + 8 * hf) * XROW);
  };

  const unsigned aCSw = LB + L_CS + l31 * 16 + wr * 4 + GG * 2048, aCSr = LB + L_CS + l31 * 16 + GG * 2048;
  auto readX = [&](int n) AI { xf[n] = ldsr<bf16x8>(aX ^ (unsigned)((n & 3) << 5), ((n >> 2) & 3) * 16384); };
  auto readA = [&](int n, int j) AI {
    const unsigned o = ((n >> 2) & 3) * ((GG == 0 && j < 2) ? 8192 : 16384);
    wa[n & 1][j] = tr2(aA[j] ^ (unsigned)((n & 3) << 11), o, o + 512);
  };
  auto readB = [&](int q) AI {
    const int n = q / NT, j = q % NT;
    const unsigned base = lc1_base(n >> 2);
    if ((NT == 3 && n < XN) ? (n & 3) == 0 : n == 24) asm volatile("" : "+v"(aB[j]));
    wq[q & 7] = tr2(aB[j] ^ (((n & 3) << 11) | (base & 0x30000u)), base & 0xffffu, (base & 0xffffu) + 512);
  };
  RowC rc; unsigned nm, denb;
  auto mkxe = [&]() AI { asm volatile("" : "+v"(nm)); u32x4 t = {nm, nm, nm, nm}; return __builtin_bit_cast(bf16x8, t); };
  auto rowconst = [&]() AI {
#pragma nounroll
    for (int it = 0; it < (1 << 16) && !ALL(grn[1] == E + 1); ++it) {
      __builtin_amdgcn_s_sleep(SLEEPN);
      asm volatile("buffer_load_dwordx2 %0, %1, %2, %3 offen sc1\n\ts_waitcnt vmcnt(0)" : "=v"(grn) : "v"(xrow * 8), "s"(rsWS), "s"(WS_GR + rb * 1024) : "memory");
    }
    const float den = U2F(grn[0] & 0xffff0000u);
    nm = hh ? 0u : __builtin_amdgcn_perm(grn[0], grn[0], 0x01000100u);
    denb = grn[0] >> 16;
    rc.r = __builtin_amdgcn_rcpf(den);
    const float k1 = -2.f * 1.4426950408889634f * 0.7978845608028654f;
    rc.K1 = k1 * rc.r; rc.K2 = (k1 * 0.044715f) * rc.r * (rc.r * rc.r); rc.Ri = den * den; rc.Lr = 1.4426950408889634f * rc.r;
  };
  waitv<7 + COOP>();
  BAR(); MEMFENCE();
  STAMP(1);
  readX(0); readA(0, 0); readA(0, 1); readA(0, 2);
  SFOR(n, 28)
    SB();
    if constexpr (n == 27) {
#pragma unroll
      for (int j = 0; j < 3; j++) ldsw<unsigned>(aCSw, j * 512, hilo(csA[j][0]));
    }
    if constexpr ((n & 3) == 3) {
      if constexpr (n == 27) WAITV(0);
      else if constexpr (n == 23) asm volatile("s_waitcnt vmcnt(10)" : "+v"(grn) :: "memory");
      else if constexpr (n == 3 || n == 15 || n == 19) WAITV(11); else WAITV(10);
      { WAITL0(); BAR(); MEMFENCE(); }
      if constexpr (n == 27) STAMP(2);
    }
    constexpr int bar = (n + 1) >> 2, ph = (n + 1) & 3;
#pragma unroll
    for (int j = 0; j < 3; j++) {
      if constexpr (n < 27) { if (j == 0) readX(n + 1); readA(n + 1, j); }
      else { wa[0][j] = ldsr<bf16x8>(aCSr, j * 512); if (GG == 1 && j < 2) { readB(2 * j); readB(2 * j + 1); } }
      SB();
      accA[j] = MFMA(wa[n & 1][j], xf[n], accA[j]);
      const int slot = 3 * ph + j;
      if (bar == 0) issueG(0, slot - 2);
      else if (bar == 6) { if (slot < 5) issueG(bar, slot); }
      else if (bar <= 6) { if (slot == 0) issueG(bar, 0); if (slot == 2) issueG(bar, 1); if (slot == 5) issueG(bar, 2); if (slot == 7) issueG(bar, 3); if (slot == 10) issueG(bar, 4); }
      else if (j < 2) {
        if constexpr (AT) dma(lw2, L_FREE + j * 1024, lane * 16, mkrs(pphi, 16 * 1568), half * 8 * 1568 + w * 2048, j * 1024);
        else issueW2(0, j);
      }
      if (!NOCS && (n & 3) == 0) csA[j] = MFMA16(sel, wa[n & 1][j], csA[j]);
      if (n == 25 && j == 0) rowconst();
      if (n == 14 && j == 2) asm volatile("buffer_load_dwordx2 %0, %1, %2, %3 offen sc1" : "=v"(grn) : "v"(xrow * 8), "s"(rsWS), "s"(WS_GR + rb * 1024) : "memory");
      SB();
    }
  SEND
  { const bf16x8 xe = mkxe();
#pragma unroll
  for (int j = 0; j < 3; j++) accA[j] = MFMA(wa[0][j], xe, accA[j]); }
  asm volatile("" : "+v"(accA[0]), "+v"(accA[1]), "+v"(accA[2]));
  SB();

  const unsigned sFown = WS_FH + (rt * 32 + (AT ? 28 + 2 * half : 2 * c) + GG) * FS;
  const unsigned aWP = LB + (4 * hu + q4) * 56 + 8 * pc;
  const unsigned T12 = (E + 1) & 0xfffu, T16 = (E + 1) & 0xffffu;
  const unsigned rt2 = 2 * (8 * grp + 4 * half + rbl);
  const unsigned sF = WS_FH + rt2 * 32 * FS;
  const unsigned fc = l31 >> 1;
  const bool nd1 = GG ? (fc >> 1) == wr || (fc >= 8 && fc < 8 + XS / 2) : (fc >= 8 + XS / 2 && fc < 14) || fc == 14 + (wr >> 1);
  const unsigned aIW = LB + L_PHI + xrow * 64 + ((hu ^ ((xrow >> 1) & 7)) << 3);
  SmS sm;
  unsigned aWK = aWP + L_WPK, aWQ = aWP + L_FREE;
  asm volatile("" : "+v"(aWK)); if constexpr (AT) asm volatile("" : "+v"(aWQ));
  auto kstep = [&](auto s_) AI {
    constexpr int s = decltype(s_)::value;
    if constexpr (s == 0) { sm.w0 = tr2(aWK, 0, 8 * 56); sm.w1 = tr2(aWK, 16 * 56, 24 * 56); }
    if constexpr (s == 1) sm.p = featproj2(accA[0], sm.w0, sm.w1, hh);
    if constexpr (s == 2 || s == 3) {
#pragma unroll
      for (int k = 2 * s - 4; k < 2 * s - 2; k++) {
        u32x2 vv = {pk2(accA[1][4 * k], accA[1][4 * k + 1]), pk2(accA[1][4 * k + 2], accA[1][4 * k + 3])};
        if (k == 3) vv[0] = hh ? denb : vv[0];
        ldsw<u32x2>(aIW ^ (unsigned)(k << 4), L_V - L_PHI, vv);
      }
    }
    if constexpr (s == 4) sm.bx = -max16(sm.p) * rc.Lr;
    if constexpr (s == 5) sm.bn = min16(sm.p) * rc.Lr;
    if constexpr (s >= 6 && s < 22) {
      constexpr int i = s - 6;
      sm.e[i] = __builtin_amdgcn_exp2f(__builtin_fmaf(sm.p[i], rc.Lr, sm.bx)); sm.en[i] = __builtin_amdgcn_exp2f(__builtin_fmaf(sm.p[i], -rc.Lr, sm.bn));
      if constexpr (i == 0) { sm.sp = sm.e[0]; sm.sn = sm.en[0]; } else if constexpr (i < 12) { sm.sp += sm.e[i]; sm.sn += sm.en[i]; }
      else if constexpr (i == 12) { sm.tp = sm.e[12]; sm.tn = sm.en[12]; } else { sm.tp += sm.e[i]; sm.tn += sm.en[i]; }
    }
    if constexpr (s == 22) { sm.sp = __builtin_amdgcn_rcpf(xsum(sm.sp + (hh ? 0.f : sm.tp))) * rc.r; sm.sn = __builtin_amdgcn_rcpf(xsum(sm.sn + (hh ? 0.f : sm.tn))) * rc.r; }
    if constexpr (s >= 23) {
#pragma unroll
      for (int u = (s == 29 ? 6 : s - 23); u <= (s == 29 ? 7 : s - 23); u++) {
        const int k = u >> 1;
        if (u & 1) { u32x2 t = {pk2s(sm.en[4 * k] * sm.sn, sm.en[4 * k + 1] * sm.sn), pk2s(sm.en[4 * k + 2] * sm.sn, sm.en[4 * k + 3] * sm.sn)}; ldsw<u32x2>(aIW ^ (unsigned)(k << 4), 8192, t); }
        else { u32x2 t = {pk2s(sm.e[4 * k] * sm.sp, sm.e[4 * k + 1] * sm.sp), pk2s(sm.e[4 * k + 2] * sm.sp, sm.e[4 * k + 3] * sm.sp)}; ldsw<u32x2>(aIW ^ (unsigned)(k << 4), 0, t); }
      }
    }
  };
  auto qstep = [&](auto s_, f32x16& a, unsigned lh) AI {
    constexpr int s = decltype(s_)::value;
    if constexpr (s == 0) { sm.w0 = tr2(aWQ, lh * 1568, lh * 1568 + 8 * 56); sm.w1 = tr2(aWQ, lh * 1568 + 16 * 56, lh * 1568 + 24 * 56); }
    if constexpr (s == 1) sm.p = featproj2(a, sm.w0, sm.w1, hh);
    if constexpr (s == 3) sm.bx = -max16(sm.p) * rc.Lr;
    if constexpr (s == 4) sm.bn = min16(sm.p) * rc.Lr;
    if constexpr (s >= 5 && s < 21) {
      constexpr int i = (s - 5) >> 1;
      float t0, t1;
      if constexpr ((s - 5) & 1) { t0 = __builtin_fmaf(sm.p[2 * i], -rc.Lr, sm.bn); t1 = __builtin_fmaf(sm.p[2 * i + 1], -rc.Lr, sm.bn); }
      else { t0 = __builtin_fmaf(sm.p[2 * i], rc.Lr, sm.bx); t1 = __builtin_fmaf(sm.p[2 * i + 1], rc.Lr, sm.bx); }
      a[8 * ((s - 5) & 1) + i] = U2F(pk2(__builtin_amdgcn_exp2f(t0), __builtin_amdgcn_exp2f(t1)));
      if constexpr (s == 20) asm volatile("" : "+v"(a));
    }
  };
  bf16x8 ka_[4], kb_[2][4]; f32x4 kd[2];
  auto kvstep = [&](auto s_) AI {
    constexpr int s = decltype(s_)::value;
    if constexpr (s == 0) {
      const unsigned g4 = lane >> 4, r0 = 8 * g4 + q4, sw = 4 * blk + (q4 >> 1);
      unsigned b0 = LB + L_PHI + r0 * 64 + ((p4 ^ sw) << 3);
      unsigned a0 = (b0 ^ ((wr >> 1) << 5)) + (wr & 1) * 8192;
#pragma unroll
      for (int k = 0; k < 4; k++) {
        ka_[k] = __builtin_shufflevector(trd2(a0, k * 2048), trd2(a0 ^ 16u, k * 2048 + 256), 0, 1, 2, 3, 4, 5, 6, 7);
        kb_[0][k] = __builtin_shufflevector(trd2(b0, L_V - L_PHI + k * 2048), trd2(b0 ^ 16u, L_V - L_PHI + k * 2048 + 256), 0, 1, 2, 3, 4, 5, 6, 7);
        kb_[1][k] = __builtin_shufflevector(trd2(b0 ^ 32u, L_V - L_PHI + k * 2048), trd2(b0 ^ 48u, L_V - L_PHI + k * 2048 + 256), 0, 1, 2, 3, 4, 5, 6, 7);
      }
    }
    if constexpr (s == 1) {
      f32x4 z = {0.f, 0.f, 0.f, 0.f}; kd[0] = z; kd[1] = z;
#pragma unroll
      for (int k = 0; k < 4; k++) { kd[0] = MFMA16(ka_[k], kb_[0][k], kd[0]); kd[1] = MFMA16(ka_[k], kb_[1][k], kd[1]); }
    }
    if constexpr (s == 3) {
#pragma unroll
      for (int d = 0; d < 2; d++) {
        u32x4 t;
#pragma unroll
        for (int i = 0; i < 4; i++) t[i] = (F2U(kd[d][i]) & ~0xfffu) | T12;
        st16(t, rsWS, (lane >> 4) * 512 + (lane & 15) * 16, ((head * 16 + rb) * 2 + (wr & 1)) * 4096 + (wr >> 1) * 2048, WS_KVP + d * 256);
      }
    }
  };
  auto qexp2 = [&](f32x16& a0, unsigned l0, f32x16& a1, unsigned l1) AI {
    const f32x16 p0 = featproj(a0, aWQ, l0 * 1568, hh), p1 = featproj(a1, aWQ, l1 * 1568, hh);
    a0 = dualexpP(p0, rc.Lr); a1 = dualexpP(p1, rc.Lr); asm volatile("" : "+v"(a0), "+v"(a1));
  };

  unsigned fl1 = 0;
  EpiS es;
  u32x4 pv[8];
  const unsigned vKP = l31 * 16 + hu * 8192, sKP = (rb * 32 + half) * 4096 + wr * 1024 + GG * 512, sKR = (8 * half + w) * 8192;
  auto hop1 = [&]() AI {
    waittag<16384>(pv, rsWS, vKP, sKP + WS_KVP, T12, 0xfffu);
    u32x4 o;
#pragma unroll
    for (int k = 0; k < 4; k++) {
      float sum = U2F(pv[0][k]);
#pragma unroll
      for (int i = 1; i < 8; i++) sum += U2F(pv[i][k]);
      sum = xsum(sum);
      sum = l31 == 29 ? 1.f : sum;
      if (GG == 1) sum = wr == 3 ? 0.f : sum;
      o[k] = (pk2s(sum, sum) & 0xffff0000u) | T16;
    }
    st16(o, rsWS, hh ? 0x80000000u : l31 * 16, (rb * 2 + half) * 4096 + wr * 1024 + GG * 512, WS_KVR);
    if (b == 0 && w == 4) stflag(E + 4, rsWS, WS_EPOCH);
    SB();
  };
  bf16x8 cb[NT];
  auto ldflag = [&]() AI { asm volatile("buffer_load_dword %0, %1, %2, %3 offen sc1" : "=v"(fl1) : "v"(nd1 ? lane * FS : 0x80000000u), "s"(rsWS), "s"(sF) : "memory"); };
  auto touch = [&](unsigned wo4) AI {
    const unsigned rw = (rbl * 8 + w + wo4) * 44 + lane;
    const bool dn = rw < 896;
    const unsigned vo = lane < 44 ? (dn ? ((rw >> 6) * 128 + 64 + (rw & 63)) : rw - 896) * XROW + ci * 128 : 0x80000000u;
    asm volatile("s_add_u32 m0, %0, %c5\n\ts_nop 0\n\tbuffer_load_dword %1, %3, 0 offen lds\n\tbuffer_load_dword %2, %4, 0 offen lds"
                 :: "s"(LB + w * 256), "v"(dn ? vo : 0x80000000u), "v"(dn ? 0x80000000u : vo), "s"(rsWD), "s"(rsWO), "i"(L_ST) : "memory", "scc");
  };
  auto stream = [&]() AI {
    SFOR(q, 28 * NT)
      constexpr int n = q / NT, j = q % NT;
      if constexpr (j == 0) {
        if constexpr (n == 27) {
#pragma unroll
          for (int t = 0; t < NT; t++) ldsw<unsigned>(aCSw, ((3 + t) & 3) * 512, hilo(csB[t][0]));
          if constexpr (AT && GG) WAITV(8);
          WAITL0(); BAR(); MEMFENCE();
        }
        else if constexpr (!AT && GG && (n == S2N || n == B1N)) { BAR(); MEMFENCE(); }
        else if constexpr (!AT && !GG && (n & 3) == 3) { asm volatile("s_waitcnt lgkmcnt(4)" ::: "memory"); BAR(); MEMFENCE(); }
      }
      if constexpr (q + 4 < 28 * NT) readB(q + 4);
      if constexpr (n == 27) cb[j] = ldsr<bf16x8>(aCSr, ((3 + j) & 3) * 512);
      SB();
      accB[j] = MFMA(wq[q & 7], xf[n], accB[j]);
      if constexpr (!NOCS && (n & 3) == 1) csB[j] = MFMA16(sel, wq[q & 7], csB[j]);
      if constexpr (!AT) {
        if constexpr (GG == 1 && j == 0 && n >= B1N && n < B1N + 4) issueW2(1 + ((n - B1N) >> 1), (n - B1N) & 1);
        if constexpr (GG == 0) {
          if constexpr (j == 1 && n < 16) { epiSlice<(n >> 2) & 3, n & 3>(accA[2], rc, es); if constexpr (n == 15) st16(es.r, rsWS, vL16, sA2, 0); }
          if constexpr (j == 0 && n >= 3 && n < 7) issueW2(1 + ((n - 3) >> 1), (n - 3) & 1);
          if constexpr (j == 0 && n >= 7 && n < 27 && (n & 1)) issueW2(1 + ((n + 1) >> 2), ((n + 1) >> 1) & 1);
          if constexpr (n == 27) issueW2(8, j);
        }
      } else {
        if constexpr (GG == 1 && n < 16 && (n & 1) == 0) clri(n >> 1);
        if constexpr (GG == 0 && j < 2 && n >= 1 && 2 * (n - 1) + j < 21) qstep(IntC<2 * (n - 1) + j>{}, accA[2], 0);
        if constexpr (GG == 0 && q >= 3 * H0L && q < 3 * H0L + 8) pv[q - 3 * H0L] = ld16(rsWS, vKP, sKP, WS_KVP + (q - 3 * H0L) * 16384);
        if constexpr (GG == 0 && j == 2 && n == H0C) { asm volatile("" : "+v"(accB[0]), "+v"(accB[1]), "+v"(accB[NT - 1]), "+v"(csB[0]), "+v"(csB[1]), "+v"(csB[NT - 1])); STAMP(11); hop1(); STAMP(6); }
      }
      SB();
    SEND
    const bf16x8 xe = mkxe();
#pragma unroll
    for (int t = 0; t < NT; t++) { accB[t] = MFMA(cb[t], xe, accB[t]); asm volatile("" : "+v"(accB[t])); }
    SB();
  };
  unsigned ln = vL16, aW = 0;
  auto mkaw = [&]() AI {
    asm volatile("" : "+v"(ln));
    aW = lw2 + (ln & 0x200) + ((ln & 0xc0) << 1) + ((((ln >> 3) & 0x20) | ((ln >> 1) & 0x10)) ^ ((ln >> 1) & 0x40)) + ((ln >> 1) & 8);
  };
  bf16x8 wf[NS][2];
  auto readW = [&](int i) AI {
    const unsigned ra = ring_area(i % RING), la = aW + (ra & 0x30000u);
    wf[i][0] = tr2(la, ra & 0xffffu, (ra & 0xffffu) + 1024); wf[i][1] = tr2(la ^ 64u, ra & 0xffffu, (ra & 0xffffu) + 1024);
  };
  auto pairF = [&](auto p_, const f32x16& a) AI { constexpr int p = decltype(p_)::value; epiSlice<p, 0>(a, rc, es); epiSlice<p, 1>(a, rc, es); epiSlice<p, 2>(a, rc, es); epiSlice<p, 3>(a, rc, es); };
  if constexpr (GG == 0) {
    if constexpr (AT) { SFOR(q, 8) clri(q); SEND }
    SFOR(s, 30)
      kstep(s_);
      if constexpr (!AT && s == 1 && TEARLY) {
        const unsigned rw = (rbl * 4 + wr) * 56 + lane;
        dmat(LB + L_ST + w * 256, lane < 56 ? ((rw >> 6) * 128 + (rw & 63)) * XROW + ci * 128 : 0x80000000u, rsWD);
        if (TEARLY == 2) { touch(0); touch(4); }
      }
    SEND
    if constexpr (!AT && TPOS == 0) { touch(0); touch(4); }
    if constexpr (AT) WAITV(8);
    WAITL0(); SB(); BAR(); SB();
    STAMP(3);
    kvstep(IntC<0>{});
    readB(0); readB(1); readB(2); readB(3);
    kvstep(IntC<1>{}); kvstep(IntC<3>{});
    if constexpr (AT) { WAITV(2); stflag(E + 1, rsWS, sFown); }
    if constexpr (!AT && TPOS == 1) { touch(0); touch(4); }
    STAMP(4);
    SB();
    stream();
    if constexpr (!AT) { st16(epiP(accB[0], rc), rsWS, vL16, sA2, 4 * 1024); st16(epiP(accB[1 % NT], rc), rsWS, vL16, sA2, 5 * 1024); }
  } else {
    stream();
    STAMP(3);
    if constexpr (AT) {
      SFOR(s, 21) qstep(s_, accA[0], 1); SEND
      WAITV(0); stflag(E + 1, rsWS, sFown);
      SFOR(s, 21) qstep(s_, accA[1], 2); SEND
      SFOR(s, 21)
        if constexpr (s < 8) pv[s] = ld16(rsWS, vKP, sKP, WS_KVP + s * 16384);
        qstep(s_, accA[2], 3);
      SEND
      STAMP(4); hop1(); STAMP(6);
      SB(); BAR(); SB();
    } else {
      mkaw();
      if constexpr (TPOS == 2) { touch(0); touch(0xfffffffcu); }
      SFOR(e, 20)
        if constexpr (LREC && e == 16) { WAITV(10); SFOR(i, LREC) readW(i); SEND }
        if constexpr (e < 12) pairF(IntC<e & 3>{}, accA[e >> 2]); else pairF(IntC<e & 3>{}, accB[((e >> 2) - 3) % NT]);
        if constexpr ((e & 3) == 3) st16(es.r, rsWS, vL16, sA2, (e < 12 ? 1 + (e >> 2) : 3 + (e >> 2)) * 1024);
        if constexpr (e == 0) { issueW2(3, 0); issueW2(3, 1); }
        if constexpr (LREC && e == 16) { WAITL0(); SB(); SFOR(i, LREC) issueW2(9 + i, 0); issueW2(9 + i, 1); SEND }
        if constexpr ((e & 3) == 2) { SB(); BAR(); SB(); issueW2(4 + (e >> 2), 0); issueW2(4 + (e >> 2), 1); }
      SEND
    }
  }

  if constexpr (AT) {
    STAMP(5);
    auto qexp = [&](f32x16& a, unsigned lh) AI { a = dualexpP(featproj(a, aWQ, lh * 1568, hh), rc.Lr); asm volatile("" : "+v"(a)); SB(); };
    if constexpr (GG == 0) qexp(accB[0], 4);
#pragma unroll
    for (int i = 0; i < 8; i++) pv[i] = ld16(rsWS, vL16, sKR, WS_KVR + i * 1024);
    if constexpr (GG == 0) { qexp(accB[1 % NT], 5); qexp(accB[NT - 1], 6); } else qexp(accB[0], 7);
    STAMP(7);
    waittag<1024>(pv, rsWS, vL16, sKR + WS_KVR, T16, 0xffffu);
    STAMP(8);
#pragma unroll
    for (int i = 0; i < 4; i++) {
      u32x4 f = {__builtin_amdgcn_perm(pv[2 * i][1], pv[2 * i][0], 0x07060302u), __builtin_amdgcn_perm(pv[2 * i][3], pv[2 * i][2], 0x07060302u),
                 __builtin_amdgcn_perm(pv[2 * i + 1][1], pv[2 * i + 1][0], 0x07060302u), __builtin_amdgcn_perm(pv[2 * i + 1][3], pv[2 * i + 1][2], 0x07060302u)};
      ldsw<u32x4>(LB + w * 4096 + vL16, i * 1024, f);
    }
    WAITL0(); BAR(); MEMFENCE();
    STAMP(10);
    f32x16 op[4], on[4];
    auto omm = [&](const f32x16& phi, unsigned lh, int z) AI {
#pragma unroll
      for (int i = 0; i < 16; i++) { op[z][i] = 0.f; on[z][i] = 0.f; }
      bf16x8 kf[4];
#pragma unroll
      for (int i = 0; i < 4; i++) kf[i] = ldsr<bf16x8>(LB + vL16, lh * 4096 + i * 1024);
#pragma unroll
      for (int i = 0; i < 2; i++) { op[z] = MFMA(kf[i], sub4(phi, i), op[z]); on[z] = MFMA(kf[2 + i], sub4(phi, 2 + i), on[z]); }
    };
    auto ofin = [&](unsigned lh, int z) AI {
      const float ia = __builtin_amdgcn_rcpf(op[z][13]), ib = __builtin_amdgcn_rcpf(on[z][13]);
      const float id = __builtin_amdgcn_rcpf(__builtin_fmaf(op[z][12], ia, __builtin_fmaf(on[z][12], ib, 1e-6f)));
      const float ca = xhi(ia * id), cb2 = xhi(ib * id);
      const f32x2 c2 = dup2(ca), d2 = dup2(cb2);
      u32x4 f0, f1;
#pragma unroll
      for (int i = 0; i < 4; i++) {
        const f32x2 u = fma2(sub2(op[z], i), c2, sub2(on[z], i) * d2), v = fma2(sub2(op[z], 4 + i), c2, sub2(on[z], 4 + i) * d2);
        f0[i] = pk2(u[0], u[1]); f1[i] = pk2(v[0], v[1]);
      }
      f1[2] = hh ? 0u : f1[2]; f1[3] = hh ? 0u : f1[3];
      st16(f0, rsWS, OV, sA2, (112 + 2 * lh) * 1024); st16(f1, rsWS, OV, sA2, (113 + 2 * lh) * 1024);
      SB();
    };
    if constexpr (GG) { omm(accA[0], 1, 0); omm(accA[1], 2, 1); SB(); ofin(1, 0); omm(accA[2], 3, 2); omm(accB[0], 7, 3); SB(); ofin(2, 1); ofin(3, 2); ofin(7, 3); }
    else { omm(accA[2], 0, 0); omm(accB[0], 4, 1); SB(); ofin(0, 0); omm(accB[1 % NT], 5, 2); omm(accB[NT - 1], 6, 3); SB(); ofin(4, 1); ofin(5, 2); ofin(6, 3); }
    STAMP(9);
  } else {
    __builtin_amdgcn_s_setprio(0);
    STAMP(5);
    const unsigned sA = WS_A2 + rt2 * 144 * 1024;
    if constexpr (GG == 0) mkaw();
    f32x16 acc[2][2];
#pragma unroll
    for (int i = 0; i < 4; i++)
#pragma unroll
      for (int k = 0; k < 16; k++) acc[i >> 1][i & 1][k] = 0.f;
    u32x4 af[NS][2];
    u32x4 xr;
    auto loadA = [&](int i) AI { af[i][0] = ld16(rsWS, vL16, sA + ksb(i) * 1024, ksi(i) * 1024); af[i][1] = ld16(rsWS, vL16, sA + ksb(i) * 1024, (144 + ksi(i)) * 1024); };
    const unsigned vO = (ln >> 7) * XROW + (ln & 0x70), sO = (rt2 * 32 + 8 * w) * XROW + ci * 128;
    STAMP(6);
    constexpr int MV = NS - 9 < 9 ? NS - 9 : 9;
    constexpr int R0 = GG ? LREC : 0;
    waitv<GG ? 7 + (LREC ? 2 * LREC - 7 + 3 : 0) : 4>();
    SFOR(i, 7 - R0) readW(R0 + i); SEND
    WAITL0(); SB();
    SFOR(i, PRE) issueW2(9 + R0 + i, 0); issueW2(9 + R0 + i, 1); SEND
    waitv<2 * PRE>(); stflag(E + 1, rsWS, sFown);
    SFOR(i, MV - 7) readW(7 + i); SEND
    WAITL0(); SB();
    BAR();
    SB(); STAMP(11);
    if constexpr (GG && PON == -2) __builtin_amdgcn_s_setprio(1);
    constexpr int NB = MV - R0 - PRE, FP = NB >= 2 ? NB - 2 : 0;
    SFOR(i, NB + 1)
      if constexpr (i == FP) ldflag();
      if constexpr (i < NB) { issueW2(9 + R0 + PRE + i, 0); issueW2(9 + R0 + PRE + i, 1); }
    SEND
    asm volatile("s_waitcnt vmcnt(%c1)" : "+v"(fl1) : "i"(2 * (NB - FP)) : "memory");
    if (!ALL(!nd1 || fl1 == E + 1u)) poll(rsWS, lane * FS, sF, E, nd1 ? 1u : 0u);
    STAMP(7);
    if constexpr (GG && PON == -1) __builtin_amdgcn_s_setprio(1);
#pragma unroll
    for (int j = 0; j < PD; j++) loadA(j);
    asm volatile("buffer_load_dwordx4 %0, %1, %2, %3 offen" : "=v"(xr) : "v"(vO), "s"(rsX), "s"(sO) : "memory");
    SFOR(i, NS)
      asm volatile("s_waitcnt vmcnt(%c2)" : "+v"(af[i][0]), "+v"(af[i][1]) : "i"(later2(i, NS, GG)) : "memory");
      if constexpr (i + 1 >= MV && i + 1 < NS) readW(i + 1);
      if constexpr (GG == 0 && i >= NO) {
        auto stale = [&]() AI {
          unsigned m = UMAX(UMAX(UMAX(af[i][0][0], af[i][0][1]), UMAX(af[i][0][2], af[i][0][3])), UMAX(UMAX(af[i][1][0], af[i][1][1]), UMAX(af[i][1][2], af[i][1][3])));
          return ANY(m == ~0u);
        };
        if (__builtin_expect(stale(), 0)) {
          int it = 0;
#pragma nounroll
          do { __builtin_amdgcn_s_sleep(SLEEPR); rel1<true>(af[i][0], af[i][1], rsWS, vL16, sA + (ksb(i) + ksi(i)) * 1024); } while (++it < (1 << 14) && stale());
          SFOR(u, PD - 1) if constexpr (i + 1 + u < NS) rel1<false>(af[i + 1 + u][0], af[i + 1 + u][1], rsWS, vL16, sA + (ksb(i + 1 + u) + ksi(i + 1 + u)) * 1024); SEND
        }
      }
      if constexpr (GG && i == POFF) __builtin_amdgcn_s_setprio(0);
      if constexpr (GG && i == PON) __builtin_amdgcn_s_setprio(1);
      if constexpr (GG && PON == -3) __builtin_amdgcn_s_setprio(i & 1);
      if constexpr (i == 6) STAMP(8);
      if constexpr (i == 12) STAMP(9);
      SB();
      if constexpr (i + PD < NS) loadA(i + PD);
#pragma unroll
      for (int mm = 0; mm < 2; mm++) acc[0][mm] = MFMA(wf[i][0], __builtin_bit_cast(bf16x8, af[i][mm]), acc[0][mm]);
#pragma unroll
      for (int mm = 0; mm < 2; mm++) acc[1][mm] = MFMA(wf[i][1], __builtin_bit_cast(bf16x8, af[i][mm]), acc[1][mm]);
      if constexpr (NS == 20 && (i == 9 || i == 10)) { SB(); issueW2(i + 9, 0); issueW2(i + 9, 1); }
      SB();
    SEND
    STAMP(10);
    __builtin_amdgcn_s_setprio(0);
#pragma unroll
    for (int qd = 0; qd < 4; qd++)
#pragma unroll
      for (int k = 0; k < 4; k++) {
        const unsigned ra = ring_area(2 * qd + (k >> 1)) + (k & 1) * 1024;
        ldsw<bf16x8>(((lw2 + vL16) ^ (unsigned)(32 * (4 * (qd >> 1) + k))) + (ra & 0x30000u), ra & 0xffffu, sub4(acc[qd >> 1][qd & 1], k));
      }
    WAITL0(); BAR(); MEMFENCE();
    STAMP(12);
    const unsigned c8 = (ln >> 4) & 7, j2 = (c8 & 4) + 2 * GG + ((c8 >> 1) & 1);
    const unsigned rd = LB + ((0x51407362u >> (4 * j2)) & 7) * 16384 + (c8 & 1) * 1024 + (((8 * wr + (ln >> 7)) << 4) ^ (c8 << 5));
    asm volatile("" : "+v"(xr));
    f32x4 sum[2];
#pragma unroll
    for (int k = 0; k < 2; k++) {
      f32x4 t = {U2F(xr[2 * k] << 16), U2F(xr[2 * k] & 0xffff0000u), U2F(xr[2 * k + 1] << 16), U2F(xr[2 * k + 1] & 0xffff0000u)};
#pragma unroll
      for (int v = 0; v < 8; v++) t += ldsr<f32x4>(rd, v * 2048 + k * 512);
      sum[k] = t;
    }
    u32x4 o = {pk2(sum[0][0], sum[0][1]), pk2(sum[0][2], sum[0][3]), pk2(sum[1][0], sum[1][1]), pk2(sum[1][2], sum[1][3])};
    __builtin_amdgcn_raw_buffer_store_b128(o, mkrs(pout, 2048 * XROW), vO + sO, 0, OAUX);
  }
  STAMPS_OUT();
  };
  if (!attn && g) prog(BoolC<false>{}, IntC<1>{});
  else if (!attn) prog(BoolC<false>{}, IntC<0>{});
  else if (g) prog(BoolC<true>{}, IntC<1>{});
  else prog(BoolC<true>{}, IntC<0>{});
}

extern "C" void kernel_launch(void* const* d_in, const int* in_sizes, int n_in, void* d_out, int out_size, void* d_ws, size_t ws_size,
                              hipStream_t stream) {
  fused_kernel<<<256, 512, 0, stream>>>((const char*)d_in[0], (const char*)d_in[1], (const char*)d_in[5], (const char*)d_in[6], (char*)d_ws,
                                        (const char*)d_in[2], (const char*)d_in[7], (const char*)d_in[3], (char*)d_out);
}
```

```cpp
#include <hip/hip_runtime.h>
#include <hip/hip_bf16.h>
#include <utility>

#define DI __device__ __forceinline__
#define LAS __attribute__((address_space(3)))
typedef __attribute__((ext_vector_type(8))) short bf16x8;
typedef __attribute__((ext_vector_type(4))) short s16x4;
typedef __attribute__((ext_vector_type(2))) __bf16 bf2;
typedef __attribute__((ext_vector_type(2))) float f32x2;
typedef __attribute__((ext_vector_type(4))) float f32x4;
typedef __attribute__((ext_vector_type(16))) float f32x16;
typedef __attribute__((ext_vector_type(2))) unsigned u32x2;
typedef __attribute__((ext_vector_type(4))) unsigned u32x4;
typedef __amdgpu_buffer_rsrc_t rsrc_t;
#define F2U(x) __builtin_bit_cast(unsigned, (float)(x))
#define U2F(x) __builtin_bit_cast(float, (unsigned)(x))
#define ALL(x) (__builtin_amdgcn_ballot_w64(!(x)) == 0ull)
#define ANY(x) (__builtin_amdgcn_ballot_w64(x) != 0ull)
#define UMAX(a, b) __builtin_elementwise_max((unsigned)(a), (unsigned)(b))

#define NTX 0
#define NTW 0
#define NT2 0
#define XS 2
constexpr unsigned XROW = 896, QROW = 2688, FROW = 3584;
constexpr unsigned FS = 128;
constexpr unsigned WS_EPOCH = 0;
constexpr unsigned WS_GR = 4096;
constexpr unsigned WS_KVP = 131072;
constexpr unsigned WS_A2 = WS_KVP + 16 * 16 * 8192;
constexpr unsigned WS_ST = WS_A2 + 64 * 144 * 1024;
constexpr unsigned WS_FH = WS_ST + 262144;
constexpr unsigned WS_KVR = WS_FH + 2048 * FS;
constexpr unsigned WS_END = WS_KVR + 16 * 8192;
constexpr unsigned L_GU = 65536, L_KV = 131072;
constexpr unsigned L_TAIL = L_KV + 3 * 8192;
constexpr unsigned L_WPK = L_TAIL;
constexpr unsigned L_ST = L_TAIL + 2048;
constexpr unsigned L_CS = L_TAIL + 4096;
constexpr unsigned L_PHI = L_GU + 32768;
constexpr unsigned L_V = L_KV + 16384;
constexpr unsigned L_FREE = 32768;

#define MFMA(a, b, c) __builtin_amdgcn_mfma_f32_32x32x16_bf16((a), (b), (c), 0, 0, 0)
#define MFMA16(a, b, c) __builtin_amdgcn_mfma_f32_16x16x32_bf16((a), (b), (c), 0, 0, 0)
#define AI __attribute__((always_inline))
#define BAR() __builtin_amdgcn_s_barrier()
#define WAITV(n) asm volatile("s_waitcnt vmcnt(" #n ")" ::: "memory")
#define WAITL0() asm volatile("s_waitcnt lgkmcnt(0)" ::: "memory")
#define SB() __builtin_amdgcn_sched_barrier(0)
#define MEMFENCE() asm volatile("" ::: "memory")

template <int N> DI void waitv() { asm volatile("s_waitcnt vmcnt(%c0)" :: "i"(N) : "memory"); }

#define INSZ(x) (x)
DI rsrc_t mkrs(const void* p, unsigned bytes) { return __builtin_amdgcn_make_buffer_rsrc((void*)p, (short)0, (int)bytes, 0x00020000); }
template <bool NT = false> DI void dma(unsigned lds, unsigned limm, unsigned voff, rsrc_t rs, unsigned soff, unsigned simm) {
  unsigned t;
  if constexpr (NT) asm volatile("s_add_u32 m0, %1, %c5\n\ts_add_u32 %0, %4, %c6\n\tbuffer_load_dwordx4 %2, %3, %0 offen nt lds"
               : "=&s"(t) : "s"(lds), "v"(voff), "s"(rs), "s"(soff), "i"(limm), "i"(simm) : "memory", "scc");
  else asm volatile("s_add_u32 m0, %1, %c5\n\ts_add_u32 %0, %4, %c6\n\tbuffer_load_dwordx4 %2, %3, %0 offen lds"
               : "=&s"(t) : "s"(lds), "v"(voff), "s"(rs), "s"(soff), "i"(limm), "i"(simm) : "memory", "scc");
}
DI void dmat(unsigned lds, unsigned voff, rsrc_t rs) {
  asm volatile("s_mov_b32 m0, %0\n\ts_nop 0\n\tbuffer_load_dword %1, %2, 0 offen lds" :: "s"(lds), "v"(voff), "s"(rs) : "memory");
}
DI s16x4 trd(unsigned a) { return __builtin_amdgcn_ds_read_tr16_b64_v4i16((LAS s16x4*)(size_t)a); }
DI s16x4 trd2(unsigned a, unsigned off) { return __builtin_amdgcn_ds_read_tr16_b64_v4i16((LAS s16x4*)((LAS char*)(size_t)a + off)); }
DI bf16x8 tr2(unsigned a, unsigned off0, unsigned off1) {
  LAS char* p = (LAS char*)(size_t)a;
  s16x4 lo = __builtin_amdgcn_ds_read_tr16_b64_v4i16((LAS s16x4*)(p + off0));
  s16x4 hi = __builtin_amdgcn_ds_read_tr16_b64_v4i16((LAS s16x4*)(p + off1));
  return __builtin_shufflevector(lo, hi, 0, 1, 2, 3, 4, 5, 6, 7);
}
template <class T> DI T ldsr(unsigned a, unsigned off) { return *(LAS T*)((LAS char*)(size_t)a + off); }
template <class T> DI void ldsw(unsigned a, unsigned off, T v) { *(LAS T*)((LAS char*)(size_t)a + off) = v; }
DI unsigned pk2(float a, float b) { f32x2 f = {a, b}; return __builtin_bit_cast(unsigned, __builtin_convertvector(f, bf2)); }
DI float rbf(float a) { return U2F(pk2(a, a) << 16); }
DI float dot2(unsigned a, unsigned b, float c) { return __builtin_amdgcn_fdot2_f32_bf16(__builtin_bit_cast(bf2, a), __builtin_bit_cast(bf2, b), c, false); }
DI float xsum(float v) { auto r = __builtin_amdgcn_permlane32_swap(F2U(v), F2U(v), false, false); float a = U2F(r[0]); asm("" : "+v"(a)); return a + U2F(r[1]); }
DI float xmax(float v) { auto r = __builtin_amdgcn_permlane32_swap(F2U(v), F2U(v), false, false); return __builtin_fmaxf(U2F(r[0]), U2F(r[1])); }
DI float xmin(float v) { auto r = __builtin_amdgcn_permlane32_swap(F2U(v), F2U(v), false, false); return __builtin_fminf(U2F(r[0]), U2F(r[1])); }
DI float xhi(float v) { auto r = __builtin_amdgcn_permlane32_swap(F2U(v), F2U(v), false, false); return U2F(r[1]); }
#define STB "sc1"
DI void st16(u32x4 v, rsrc_t rs, unsigned voff, unsigned soff, unsigned simm) {
  unsigned t;
  asm volatile("s_add_u32 %0, %4, %c5\n\tbuffer_store_dwordx4 %1, %2, %3, %0 offen " STB "\n\ts_nop 1" : "=&s"(t) : "v"(v), "v"(voff), "s"(rs), "s"(soff), "i"(simm) : "memory", "scc");
}
DI void stflag(unsigned v, rsrc_t rs, unsigned soff) {
  unsigned voff = (__builtin_amdgcn_mbcnt_hi(~0u, __builtin_amdgcn_mbcnt_lo(~0u, 0u)) == 0u) ? 0u : 0x80000000u;
  __builtin_amdgcn_raw_buffer_store_b32(v, rs, voff, soff, 16);
}
#define XORCOND (n == 24)
#define PS 0
#define PV 0
#define KEEP2 0
#define S2N 14
#define B1N 23
#define DIST 2
#define P2G1 1
#define OAUX 0
#define RECY 0
#define POFF 99
#define H1S 0
#define H0L 14
#define H0C 22
#define Q3A 1
#define COOP 0
#define PON -2
#define XN 20
#define BAN 18
#define BBN 21
#define SL0 8
#define SLN 2
#define SLD 3
#define NSL 48
#define H1C 16
#define OV vL16
#define CV vL16
#define Q6E 0
#define TEARLY 0
#define NOCS 0
#define TPOS 0
#define PRE 0
#define LREC 0
#define CK1 20
#define EB 3
#define T2B 1
#define F1N 9
#define RC 3
#define G0B 9
#define TN 10
#define SLEEPR 4
#define SLEEPN 2
DI u32x4 ld16(rsrc_t rs, unsigned voff, unsigned soff, unsigned simm) {
  u32x4 r; unsigned t;
  asm volatile("s_add_u32 %1, %4, %c5\n\tbuffer_load_dwordx4 %0, %2, %3, %1 offen sc1" : "=v"(r), "=&s"(t) : "v"(voff), "s"(rs), "s"(soff), "i"(simm) : "memory", "scc");
  return r;
}
template <unsigned STEP> DI void ld8w(u32x4 (&p)[8], rsrc_t rs, unsigned voff, unsigned soff) {
  unsigned t;
#define L1(k) "buffer_load_dwordx4 %" #k ", %9, %10, %8 offen sc1\n\ts_add_u32 %8, %8, %c12\n\t"
  asm volatile("s_mov_b32 %8, %11\n\t" L1(0) L1(1) L1(2) L1(3) L1(4) L1(5) L1(6) L1(7) "s_waitcnt vmcnt(0)"
               : "=&v"(p[0]), "=&v"(p[1]), "=&v"(p[2]), "=&v"(p[3]), "=&v"(p[4]), "=&v"(p[5]), "=&v"(p[6]), "=&v"(p[7]), "=&s"(t)
               : "v"(voff), "s"(rs), "s"(soff), "i"(STEP) : "memory", "scc");
#undef L1
}
DI bool untag(u32x4 (&p)[8], unsigned tag, unsigned mask) {
  unsigned chk = 0;
#pragma unroll
  for (int i = 0; i < 8; i++)
#pragma unroll
    for (int k = 0; k < 4; k++) { p[i][k] ^= tag; chk |= p[i][k]; }
  return ALL((chk & mask) == 0u);
}
template <unsigned STEP> DI void waittag(u32x4 (&p)[8], rsrc_t rs, unsigned voff, unsigned soff, unsigned tag, unsigned mask) {
  asm volatile("s_waitcnt vmcnt(0)" : "+v"(p[0]), "+v"(p[1]), "+v"(p[2]), "+v"(p[3]), "+v"(p[4]), "+v"(p[5]), "+v"(p[6]), "+v"(p[7]) :: "memory");
#pragma nounroll
  for (int it = 0; !untag(p, tag, mask) && it < (1 << 16); ++it) { __builtin_amdgcn_s_sleep(SLEEPN); ld8w<STEP>(p, rs, voff, soff); }
}
template <bool WAIT> DI void rel1(u32x4& a, u32x4& b, rsrc_t rs, unsigned voff, unsigned so) {
  unsigned t;
  if constexpr (WAIT) asm volatile("buffer_load_dwordx4 %0, %3, %4, %5 offen sc1\n\ts_add_u32 %2, %5, 147456\n\tbuffer_load_dwordx4 %1, %3, %4, %2 offen sc1\n\ts_waitcnt vmcnt(0)"
                                   : "+v"(a), "+v"(b), "=&s"(t) : "v"(voff), "s"(rs), "s"(so) : "memory", "scc");
  else asm volatile("buffer_load_dwordx4 %0, %3, %4, %5 offen sc1\n\ts_add_u32 %2, %5, 147456\n\tbuffer_load_dwordx4 %1, %3, %4, %2 offen sc1"
                    : "+v"(a), "+v"(b), "=&s"(t) : "v"(voff), "s"(rs), "s"(so) : "memory", "scc");
}
DI void poll(rsrc_t rs, unsigned voff, unsigned soff, unsigned E, unsigned need) {
  voff = need ? voff : 0x80000000u;
#pragma nounroll
  for (int it = 0; it < (1 << 16); ++it) {
    unsigned v;
    asm volatile("buffer_load_dword %0, %1, %2, %3 offen sc1\n\ts_waitcnt vmcnt(0)" : "=v"(v) : "v"(voff), "s"(rs), "s"(soff) : "memory");
    bool ok = (need == 0u) || ((v - E - need) <= (3u - need));
    if (ALL(ok)) break;
    __builtin_amdgcn_s_sleep(SLEEPN);
  }
}

template <bool B> struct BoolC { static constexpr bool value = B; };
template <int I> struct IntC { static constexpr int value = I; };
template <int... I, class F> DI void sfor_impl(std::integer_sequence<int, I...>, F&& f) { (f(IntC<I>{}), ...); }
template <int N, class F> DI void sfor(F&& f) { sfor_impl(std::make_integer_sequence<int, N>{}, f); }
#define SFOR(n, N) sfor<N>([&](auto n##_) AI { constexpr int n = decltype(n##_)::value;
#define SEND });
struct RowC { float r, K1, K2, Ri, Lr; };

DI unsigned hilo(float v) { float hi = rbf(v); return pk2(hi, v - hi); }
DI float max3(float a, float b, float c) { return __builtin_fmaxf(__builtin_fmaxf(a, b), c); }
DI float min3(float a, float b, float c) { return __builtin_fminf(__builtin_fminf(a, b), c); }
DI unsigned pk2s(float a, float b) { unsigned r; asm("v_cvt_pk_bf16_f32 %0, %1, %2" : "=v"(r) : "v"(a), "v"(b)); return r; }
DI f32x2 sub2(const f32x16& v, int k) { f32x2 t = {v[2 * k], v[2 * k + 1]}; return t; }
DI f32x2 exp2v(f32x2 v) { f32x2 r = {__builtin_amdgcn_exp2f(v[0]), __builtin_amdgcn_exp2f(v[1])}; return r; }
DI f32x2 dup2(float a) { f32x2 r = {a, a}; asm("" : "+v"(r)); return r; }
DI f32x2 fma2(f32x2 a, f32x2 b, f32x2 c) { return __builtin_elementwise_fma(a, b, c); }
DI u32x4 epiP(const f32x16& a, const RowC& c) {
  u32x4 r;
  const f32x2 K1 = dup2(c.K1), K2 = dup2(c.K2), Ri = dup2(c.Ri);
#pragma unroll
  for (int i = 0; i < 4; i++) {
    const f32x2 g = sub2(a, i), d = fma2(exp2v(g * fma2(g * g, K2, K1)), Ri, Ri);
    const f32x2 s = {__builtin_amdgcn_rcpf(d[0]), __builtin_amdgcn_rcpf(d[1])}, h = (g * sub2(a, 4 + i)) * s;
    r[i] = pk2(h[0], h[1]);
  }
  return r;
}
struct EpiS { float x0, x1, m0, m1; u32x4 r; };
template <int P, int PH> DI void epiSlice(const f32x16& a, const RowC& c, EpiS& s) {
  const float g0 = a[2 * P], g1 = a[2 * P + 1];
  if constexpr (PH == 0) { s.x0 = g0 * __builtin_fmaf(g0 * g0, c.K2, c.K1); s.x1 = g1 * __builtin_fmaf(g1 * g1, c.K2, c.K1); }
  if constexpr (PH == 1) { s.x0 = __builtin_amdgcn_exp2f(s.x0); s.x1 = __builtin_amdgcn_exp2f(s.x1); s.m0 = g0 * a[8 + 2 * P]; s.m1 = g1 * a[9 + 2 * P]; }
  if constexpr (PH == 2) { s.x0 = __builtin_amdgcn_rcpf(__builtin_fmaf(s.x0, c.Ri, c.Ri)); s.x1 = __builtin_amdgcn_rcpf(__builtin_fmaf(s.x1, c.Ri, c.Ri)); }
  if constexpr (PH == 3) s.r[P] = pk2s(s.m0 * s.x0, s.m1 * s.x1);
}
DI f32x16 featproj2(const f32x16& a, bf16x8 w0, bf16x8 w1, bool hh) {
  u32x4 q0, q1;
#pragma unroll
  for (int i = 0; i < 4; i++) { q0[i] = pk2(a[2 * i], a[2 * i + 1]); q1[i] = pk2(a[8 + 2 * i], a[9 + 2 * i]); }
  q1[2] = hh ? 0u : q1[2]; q1[3] = hh ? 0u : q1[3];
  f32x16 z;
#pragma unroll
  for (int i = 0; i < 16; i++) z[i] = 0.f;
  z = MFMA(w0, __builtin_bit_cast(bf16x8, q0), z);
  z = MFMA(w1, __builtin_bit_cast(bf16x8, q1), z);
  return z;
}
DI f32x16 featproj(const f32x16& a, unsigned wpaddr, unsigned off, bool hh) {
  return featproj2(a, tr2(wpaddr, off, off + 8 * 56), tr2(wpaddr, off + 16 * 56, off + 24 * 56), hh);
}
DI float max16(const f32x16& p) { float m = max3(p[0], p[1], p[2]); _Pragma("unroll") for (int i = 3; i < 15; i += 2) m = max3(m, p[i], p[i + 1]); return xmax(__builtin_fmaxf(m, p[15])); }
DI float min16(const f32x16& p) { float m = min3(p[0], p[1], p[2]); _Pragma("unroll") for (int i = 3; i < 15; i += 2) m = min3(m, p[i], p[i + 1]); return xmin(__builtin_fminf(m, p[15])); }
DI f32x16 dualexpP(const f32x16& p, float Lr) {
  const f32x2 bx = dup2(-max16(p) * Lr), bn = dup2(min16(p) * Lr), L2 = dup2(Lr);
  f32x16 o;
#pragma unroll
  for (int i = 0; i < 8; i++) {
    const f32x2 ep = exp2v(fma2(sub2(p, i), L2, bx)), en = exp2v(fma2(sub2(p, i), -L2, bn));
    o[i] = U2F(pk2(ep[0], ep[1])); o[8 + i] = U2F(pk2(en[0], en[1]));
  }
  return o;
}
struct SmS { f32x16 p, e; float en[16], bx, bn, sp, sn, tp, tn; bf16x8 w0, w1; };
DI bf16x8 sub4(const f32x16& v, int k) { f32x4 t = {v[4 * k], v[4 * k + 1], v[4 * k + 2], v[4 * k + 3]}; return __builtin_bit_cast(bf16x8, t); }

DI constexpr unsigned lc1_base(int ch) { return ch == 6 ? L_KV : ((ch & 1) ? L_GU : 0u) + (ch < 2 ? 49152u : ch < 4 ? 0u : 16384u); }
template <int CTRL, int RM> DI float dppadd(float v) { return v + U2F(__builtin_amdgcn_update_dpp(0u, F2U(v), CTRL, RM, 0xf, false)); }
DI float wavesum63(float v) {
  v = dppadd<0x111, 0xf>(v); v = dppadd<0x112, 0xf>(v); v = dppadd<0x114, 0xf>(v); v = dppadd<0x118, 0xf>(v);
  v = dppadd<0x142, 0xa>(v); return dppadd<0x143, 0xc>(v);
}

#define STAMP(i)
#define STAMPS_OUT()
#define CSTAMP(i)

#define PDV 6
constexpr int PD = PDV, RING = 9;
constexpr int later2(int i, int NS, int GG) {
  const int younger = 2 * ((i + PD <= NS ? i + PD : NS) - 1 - i);
  int extra = i < PD ? 1 : 0;
  if (NS == 20 && (i == 10 || i == 11)) extra += 2 * (i - 9);
  return younger + extra;
}
DI constexpr unsigned ring_area(int j) { return j == 0 ? L_FREE : j == 1 ? L_PHI : lc1_base(j - 2); }
constexpr int cumG1(int s) { return s < 4 ? 0 : s < 22 ? (s - 4) / 2 : (9 + (s - 22) * 5 / 4 < 48 ? 9 + (s - 22) * 5 / 4 : 48); }

#define CODE_BYTES 65536
__global__ __launch_bounds__(512, 2) void fused_kernel(const char* px_, const char* pqkv_, const char* pwg_, const char* pwu_, char* ws_, const char* pphi_,
                                                       const char* pwd_, const char* pwo_, char* pout_) {
  typedef __attribute__((ext_vector_type(8))) unsigned long long u64x8;
  u64x8 ka; unsigned long long kb;
  asm volatile("s_load_dwordx16 %0, %2, 0\n\ts_load_dwordx2 %1, %2, 64" : "=&s"(ka), "=&s"(kb) : "s"(__builtin_amdgcn_kernarg_segment_ptr()));
  __shared__ __attribute__((aligned(16))) char smem[163840];
  const unsigned LB = (unsigned)(size_t)smem;
  const unsigned tid = __builtin_amdgcn_workitem_id_x(), lane = tid & 63;
  const unsigned w = __builtin_amdgcn_readfirstlane(tid >> 6), g = w >> 2, wr = w & 3;
  const unsigned b = __builtin_amdgcn_workgroup_id_x(), xcd = b & 7, bi = b >> 3, grp = xcd >> 1, half = xcd & 1, rbl = bi & 3, cp = bi >> 2, ci = cp - 1;
  const unsigned rb = 4 * grp + rbl, c = 7 * half + ci;
  const unsigned head = 8 * half + (((half ? 0x76425310u : 0x75316420u) >> (4 * cp)) & 7);
  const bool attn = cp == 0;
  STAMP(0); CSTAMP(14);
  const unsigned wstr = attn ? QROW : FROW;
  const unsigned sG0 = half * (attn ? 448 : 1792) + (attn ? 0 : ci * 256), sX0 = rb * 128 * XROW;
  unsigned vx0, vx1, vG, vU, vKV;
  auto voffs = [&](unsigned ln) AI {
    const unsigned r8 = ln >> 3, c8 = ln & 7, sa = (r8 >> 1) & 1, lc = c8 ^ (4 * sa);
    vx0 = (16 * w + r8) * XROW + ((c8 ^ (r8 >> 1)) << 4); vx1 = (vx0 ^ 64u) + 8 * XROW;
    const unsigned cg = attn ? lc : (c8 ^ (2 * sa)), cu = attn ? (lc >= 6 ? lc - 2 : lc) : (cg ^ 4);
    vG = (8 * w + r8) * wstr + (cg << 4); vU = (8 * w + r8) * wstr + (cu << 4);
    vKV = (8 * w + r8) * QROW + 896 * (1 + (lc >> 2)) + ((lc & 3) << 4) + ((56 * head) & ~15u);
  };
  voffs(lane);
  const unsigned lw1 = LB + w * 1024, lw2 = LB + w * 2048;
#define ROT (((0x24924u >> (2 * (cp + rbl))) & 3) * 2)
  const unsigned rot = ROT;
  const unsigned srow = 128 * rb + 64 * half + 8 * cp + w;
  unsigned vrow = lane < 56 ? lane * 16 : 0x80000000u;
  asm volatile("s_waitcnt lgkmcnt(0)" : "+s"(ka), "+s"(kb), "+v"(vx0), "+v"(vx1), "+v"(vG), "+v"(vU), "+v"(vKV), "+v"(vrow));
  const char *px = (const char*)ka[0], *pqkv = (const char*)ka[1], *pwg = (const char*)ka[2], *pwu = (const char*)ka[3], *pphi = (const char*)ka[5],
             *pwd = (const char*)ka[6], *pwo = (const char*)ka[7];
  char *ws = (char*)ka[4], *pout = (char*)kb;
  const rsrc_t rsX = mkrs(px, INSZ(2048 * XROW)), rsQ = mkrs(pqkv, INSZ(448 * QROW));
  const rsrc_t rsG = mkrs(attn ? pqkv : pwg, INSZ(448 * wstr)), rsU = mkrs(attn ? pqkv + 128 : pwu, INSZ(448 * wstr - (attn ? 128 : 0)));
  auto cidx = [&](int st) AI -> unsigned { unsigned t = rot + st; return t >= 7 ? t - 7 : t; };
  auto issueA = [&](int st, int k) AI {
    const unsigned sl = st & 3, ch = cidx(st);
    if (k == 0) dma<NTX>(lw2, sl * 16384, vx0, rsX, sX0 + ch * 128, 0);
    if (k == 1) dma<NTX>(lw2, sl * 16384 + 1024, vx1, rsX, sX0 + ch * 128, 0);
    if (k == 2) dma<NTW>(lw1, L_GU + sl * 16384, vG, rsG, sG0 + ch * 64 * wstr, 0);
    if (k == 3) dma<NTW>(lw1, L_GU + sl * 16384 + 8192, vU, rsU, sG0 + ch * 64 * wstr, 0);
    if (k == 4) dma<NTW>(lw1, L_KV + sl * 8192, vKV, rsQ, ch * 64 * QROW, 0);
  };
  const rsrc_t rsWS = mkrs(ws, WS_END);
  unsigned Ev, cpf; u32x4 xrw;
  asm volatile("buffer_load_dwordx4 %0, %1, %2, %3 offen" : "=v"(xrw) : "v"(vrow), "s"(rsX), "s"(srow * XROW) : "memory");
  asm volatile("buffer_load_dword %0, off, %1, 0 sc1" : "=v"(Ev) : "s"(rsWS) : "memory");
  {
    unsigned long long pc; asm volatile("s_getpc_b64 %0" : "=s"(pc));
    const unsigned vo = w == 0 && lane < 24 ? (bi * 24 + lane) * 128 : 0x80000000u;
    asm volatile("buffer_load_dword %0, %1, %2, 0 offen" : "=v"(cpf) : "v"(vo), "s"(mkrs((const void*)pc, CODE_BYTES)) : "memory");
  }
#pragma unroll
  for (int k = 0; k < 5; k++) issueA(0, k);
  asm volatile("s_waitcnt vmcnt(%c3)" : "+v"(xrw), "+v"(Ev), "+v"(cpf) : "i"(5 + COOP) : "memory");
#pragma unroll
  for (int k = 0; k < 5; k++) issueA(1, k);
  issueA(2, 0);
  const unsigned E = __builtin_amdgcn_readfirstlane(Ev);
  {
    float s1 = 0.f, s2 = 0.f;
#pragma unroll
    for (int i = 0; i < 4; i++) { s1 = dot2(xrw[i], 0x3f803f80u, s1); s2 = dot2(xrw[i], xrw[i], s2); }
    s1 = wavesum63(s1); s2 = wavesum63(s2);
    const float mean = s1 * (1.f / 448.f), var = __builtin_fmaxf(s2 * (1.f / 448.f) - mean * mean, 0.f);
    u32x2 gr = {pk2(-mean, __builtin_amdgcn_sqrtf(rbf(rbf(var) + 1e-5f))), E + 1};
    __builtin_amdgcn_raw_buffer_store_b64(gr, rsWS, lane == 63 ? 0u : 0x80000000u, WS_GR + srow * 8, 16);
  }

  auto prog = [&](auto at_, auto gg_) AI {
  constexpr bool AT = decltype(at_)::value; constexpr int GG = decltype(gg_)::value;
  constexpr unsigned lcstep = AT ? 224 : 128;
  unsigned lane;
  asm volatile("v_mbcnt_lo_u32_b32 %0, -1, 0\n\tv_mbcnt_hi_u32_b32 %0, -1, %0" : "=v"(lane));
  __builtin_assume(lane < 64);
  voffs(lane);
  auto issueL = [&](int hc) AI {
    const int st = hc >> 1; const unsigned ch = cidx(st);
    if (hc & 1) dma<NTW>(lw1, lc1_base(st) + 8192, vU, rsU, sG0 + ch * 64 * wstr, lcstep);
    else dma<NTW>(lw1, lc1_base(st), vG, rsG, sG0 + ch * 64 * wstr, lcstep);
  };
  auto issueG = [&](int bar, int k) AI {
    if (bar == 0) issueA(2 + k / 5, k % 5);
    else if (bar <= 3) issueA(bar + 3, k);
    else if (k < 4) issueL(4 * (bar - 4) + k);
    else if (bar == 4) dma(LB + (w & 1) * 1024, L_WPK, lane * 16, mkrs(pphi, 16 * 1568), head * 1568 + (w & 1) * 1024, 0);
    else issueL(12 + (bar - 5));
  };

  const unsigned rt = 4 * rb + wr;
  const unsigned sA2 = WS_A2 + rt * 144 * 1024 + (AT ? half * 16384 : c * 8192), vL16 = lane * 16;
  const u32x4 sent = {~0u, ~0u, ~0u, ~0u};
  auto clr = [&](int ks) AI { st16(sent, rsWS, CV, sA2, ks * 1024); };

  auto clri = [&](int q) AI { clr(GG ? (q < 6 ? 114 + q : 120 + q) : (q < 2 ? 112 + q : 118 + q)); };

  const bool hh = lane >= 32;
  const unsigned hu = lane >> 5, blk = (lane >> 4) & 1, q4 = (lane >> 2) & 3, p4 = lane & 3, ta = (q4 >> 1) & 1, l31 = lane & 31;
  const unsigned perm = wr ^ (2 * GG);
  const unsigned rowoff = (8 * hu + q4) * 128 + (perm << 11);
  const unsigned pc = (4 * blk + p4) < 6 ? (4 * blk + p4) : 6;
  auto tileaddr = [&](unsigned t) AI -> unsigned {
    if constexpr (AT) { unsigned o = 56 * t + 8 * pc; return (o >> 7) * 8192 + ((((o >> 4) & 7) ^ (4 * ta)) << 4) + (o & 8) + rowoff; }
    else return blk * 8192 + (((2 * t + (p4 >> 1)) ^ (4 * blk + 2 * ta)) << 4) + 8 * (p4 & 1) + rowoff;
  };
  constexpr int NT = !AT ? 2 : GG ? 1 : 3, TB0 = GG ? 4 - NT : 0;
  unsigned aA[3], aB[NT];
  {
    unsigned o = (head & 1) * 8 + 8 * pc;
    unsigned ak = L_KV + (((o >> 4) ^ (4 * ta)) << 4) + (o & 8) + rowoff;
    aA[0] = LB + (GG ? L_GU + tileaddr(1) : ak);
    aA[1] = LB + (GG ? L_GU + tileaddr(2) : (ak ^ 64u));
    aA[2] = LB + L_GU + tileaddr(GG ? 3 : 0);
#pragma unroll
    for (int j = 0; j < NT; j++) aB[j] = LB + tileaddr(TB0 + j);
  }
  const unsigned xrow = 32 * wr + l31;
  const unsigned aX = LB + xrow * 128 + (((2 * perm + hu) ^ ((xrow >> 1) & 7)) << 4);
  const unsigned selv = (((lane >> 4) ^ (lane >> 2)) & 1) ? 0u : 0x3f803f80u;
  const u32x4 sel4 = {selv, selv, selv, selv};
  const bf16x8 sel = __builtin_bit_cast(bf16x8, sel4);

  bf16x8 xf[28], wa[2][3], wq[8];
  f32x16 accA[3], accB[NT];
  f32x4 csA[3], csB[NT];
#pragma unroll
  for (int j = 0; j < 3; j++) { for (int i = 0; i < 16; i++) accA[j][i] = 0.f; for (int i = 0; i < 4; i++) csA[j][i] = 0.f; }
#pragma unroll
  for (int j = 0; j < NT; j++) { for (int i = 0; i < 16; i++) accB[j][i] = 0.f; for (int i = 0; i < 4; i++) csB[j][i] = 0.f; }
  u32x2 grn;

  const rsrc_t rsWD = mkrs(pwd, INSZ(1792 * XROW)), rsWO = mkrs(pwo, INSZ(448 * XROW));
  const unsigned vW2 = (lane >> 3) * XROW + (((lane & 7) ^ ((lane >> 2) & 4)) << 4) + ci * 128;
  constexpr int NS = GG ? 16 + XS : 20 - XS, NC = 6 - XS / 2, NO = 2 * NC;
#define RHO ((rbl + cp) & 3)
  const unsigned rho = RHO;
  auto t_of = [](int i) constexpr { return (0x54763210u >> (4 * ((i >> 1) & 7))) & 7; };
  constexpr int NE = GG ? 9 : 5;
  auto g1i = [](int i) constexpr { return i < 8 ? i : i == 8 ? 16 : i < 17 ? i - 1 : 17; };
  auto ksb = [&](int i) AI -> unsigned {
    if (GG) i = g1i(i);
    if (GG) return i < 16 ? 16 * wr + ((t_of(i) & 3) ^ rho) : wr;
    if (i < NO) { const unsigned cc = i % NC + rho + (XS ? 0 : rho >> 1); return wr + 8 * (cc >= NC ? cc - NC : cc); }
    return 8 * wr + 2 * (((i - NO) >> 1) ^ rho);
  };
  auto ksi = [&](int i) AI -> unsigned { if (GG) i = g1i(i); return GG ? (i < 16 ? 8 * (i & 1) + (t_of(i) & 4) : 4 * i) : i < NO ? 64 + 4 * XS + 4 * (i / NC) : 112 + (i & 1); };
  auto issueW2 = [&](int i, int hf) AI {
    if (GG == 0 && i >= NO) dma<NT2>(lw2, ring_area(i % RING) + hf * 1024, vW2, rsWO, ksb(i) * 14 * XROW, (16 * (i & 1) + 8 * hf) * XROW);
    else dma<NT2>(lw2, ring_area(i % RING) + hf * 1024, vW2, rsWD, ksb(i) * 16 * XROW, (ksi(i) * 16 + 8 * hf) * XROW);
  };

  const unsigned aCSw = LB + L_CS + l31 * 16 + wr * 4 + GG * 2048, aCSr = LB + L_CS + l31 * 16 + GG * 2048;
  auto readX = [&](int n) AI { xf[n] = ldsr<bf16x8>(aX ^ (unsigned)((n & 3) << 5), ((n >> 2) & 3) * 16384); };
  auto readA = [&](int n, int j) AI {
    const unsigned o = ((n >> 2) & 3) * ((GG == 0 && j < 2) ? 8192 : 16384);
    wa[n & 1][j] = tr2(aA[j] ^ (unsigned)((n & 3) << 11), o, o + 512);
  };
  auto readB = [&](int q) AI {
    const int n = q / NT, j = q % NT;
    const unsigned base = lc1_base(n >> 2);
    if ((NT == 3 && n < XN) ? (n & 3) == 0 : n == 24) asm volatile("" : "+v"(aB[j]));
    wq[q & 7] = tr2(aB[j] ^ (((n & 3) << 11) | (base & 0x30000u)), base & 0xffffu, (base & 0xffffu) + 512);
  };
  RowC rc; unsigned nm, denb;
  auto mkxe = [&]() AI { asm volatile("" : "+v"(nm)); u32x4 t = {nm, nm, nm, nm}; return __builtin_bit_cast(bf16x8, t); };
  auto rowconst = [&]() AI {
#pragma nounroll
    for (int it = 0; it < (1 << 16) && !ALL(grn[1] == E + 1); ++it) {
      __builtin_amdgcn_s_sleep(SLEEPN);
      asm volatile("buffer_load_dwordx2 %0, %1, %2, %3 offen sc1\n\ts_waitcnt vmcnt(0)" : "=v"(grn) : "v"(xrow * 8), "s"(rsWS), "s"(WS_GR + rb * 1024) : "memory");
    }
    const float den = U2F(grn[0] & 0xffff0000u);
    nm = hh ? 0u : __builtin_amdgcn_perm(grn[0], grn[0], 0x01000100u);
    denb = grn[0] >> 16;
    rc.r = __builtin_amdgcn_rcpf(den);
    const float k1 = -2.f * 1.4426950408889634f * 0.7978845608028654f;
    rc.K1 = k1 * rc.r; rc.K2 = (k1 * 0.044715f) * rc.r * (rc.r * rc.r); rc.Ri = den * den; rc.Lr = 1.4426950408889634f * rc.r;
  };
  waitv<7 + COOP>();
  BAR(); MEMFENCE();
  STAMP(1);
  readX(0); readA(0, 0); readA(0, 1); readA(0, 2);
  SFOR(n, 28)
    SB();
    if constexpr (n == 27) {
#pragma unroll
      for (int j = 0; j < 3; j++) ldsw<unsigned>(aCSw, j * 512, hilo(csA[j][0]));
    }
    if constexpr ((n & 3) == 3) {
      if constexpr (n == 27) WAITV(0);
      else if constexpr (n == 23) asm volatile("s_waitcnt vmcnt(10)" : "+v"(grn) :: "memory");
      else if constexpr (n == 3 || n == 15 || n == 19) WAITV(11); else WAITV(10);
      { WAITL0(); BAR(); MEMFENCE(); }
      if constexpr (n == 27) STAMP(2);
    }
    constexpr int bar = (n + 1) >> 2, ph = (n + 1) & 3;
#pragma unroll
    for (int j = 0; j < 3; j++) {
      if constexpr (n < 27) { if (j == 0) readX(n + 1); readA(n + 1, j); }
      else { wa[0][j] = ldsr<bf16x8>(aCSr, j * 512); if (AT && GG == 1 && j < 2) { readB(2 * j); readB(2 * j + 1); } }
      SB();
      accA[j] = MFMA(wa[n & 1][j], xf[n], accA[j]);
      const int slot = 3 * ph + j;
      if (bar == 0) issueG(0, slot - 2);
      else if (bar == 6) { if (slot < 5) issueG(bar, slot); }
      else if (bar <= 6) { if (slot == 0) issueG(bar, 0); if (slot == 2) issueG(bar, 1); if (slot == 5) issueG(bar, 2); if (slot == 7) issueG(bar, 3); if (slot == 10) issueG(bar, 4); }
      else if (j < 2) {
        if constexpr (AT) dma(lw2, L_FREE + j * 1024, lane * 16, mkrs(pphi, 16 * 1568), half * 8 * 1568 + w * 2048, j * 1024);
        else issueW2(0, j);
      }
      if (!NOCS && (n & 3) == 0) csA[j] = MFMA16(sel, wa[n & 1][j], csA[j]);
      if (n == 25 && j == 0) rowconst();
      if (n == 14 && j == 2) asm volatile("buffer_load_dwordx2 %0, %1, %2, %3 offen sc1" : "=v"(grn) : "v"(xrow * 8), "s"(rsWS), "s"(WS_GR + rb * 1024) : "memory");
      SB();
    }
  SEND
  { const bf16x8 xe = mkxe();
#pragma unroll
  for (int j = 0; j < 3; j++) accA[j] = MFMA(wa[0][j], xe, accA[j]); }
  asm volatile("" : "+v"(accA[0]), "+v"(accA[1]), "+v"(accA[2]));
  SB();

  const unsigned sFown = WS_FH + (rt * 32 + (AT ? 28 + 2 * half : 2 * c) + GG) * FS;
  const unsigned aWP = LB + (4 * hu + q4) * 56 + 8 * pc;
  const unsigned T12 = (E + 1) & 0xfffu, T16 = (E + 1) & 0xffffu;
  const unsigned rt2 = 2 * (8 * grp + 4 * half + rbl);
  const unsigned sF = WS_FH + rt2 * 32 * FS;
  const unsigned fc = l31 >> 1;
  const bool nd1 = GG ? (fc >> 1) == wr || (fc >= 8 && fc < 8 + XS / 2) : (fc >= 8 + XS / 2 && fc < 14) || fc == 14 + (wr >> 1);
  const unsigned aIW = LB + L_PHI + xrow * 64 + ((hu ^ ((xrow >> 1) & 7)) << 3);
  SmS sm;
  unsigned aWK = aWP + L_WPK, aWQ = aWP + L_FREE;
  asm volatile("" : "+v"(aWK)); if constexpr (AT) asm volatile("" : "+v"(aWQ));
  auto kstep = [&](auto s_) AI {
    constexpr int s = decltype(s_)::value;
    if constexpr (s == 0) { sm.w0 = tr2(aWK, 0, 8 * 56); sm.w1 = tr2(aWK, 16 * 56, 24 * 56); }
    if constexpr (s == 1) sm.p = featproj2(accA[0], sm.w0, sm.w1, hh);
    if constexpr (s == 2 || s == 3) {
#pragma unroll
      for (int k = 2 * s - 4; k < 2 * s - 2; k++) {
        u32x2 vv = {pk2(accA[1][4 * k], accA[1][4 * k + 1]), pk2(accA[1][4 * k + 2], accA[1][4 * k + 3])};
        if (k == 3) vv[0] = hh ? denb : vv[0];
        ldsw<u32x2>(aIW ^ (unsigned)(k << 4), L_V - L_PHI, vv);
      }
    }
    if constexpr (s == 4) sm.bx = -max16(sm.p) * rc.Lr;
    if constexpr (s == 5) sm.bn = min16(sm.p) * rc.Lr;
    if constexpr (s >= 6 && s < 22) {
      constexpr int i = s - 6;
      sm.e[i] = __builtin_amdgcn_exp2f(__builtin_fmaf(sm.p[i], rc.Lr, sm.bx)); sm.en[i] = __builtin_amdgcn_exp2f(__builtin_fmaf(sm.p[i], -rc.Lr, sm.bn));
      if constexpr (i == 0) { sm.sp = sm.e[0]; sm.sn = sm.en[0]; } else if constexpr (i < 12) { sm.sp += sm.e[i]; sm.sn += sm.en[i]; }
      else if constexpr (i == 12) { sm.tp = sm.e[12]; sm.tn = sm.en[12]; } else { sm.tp += sm.e[i]; sm.tn += sm.en[i]; }
    }
    if constexpr (s == 22) { sm.sp = __builtin_amdgcn_rcpf(xsum(sm.sp + (hh ? 0.f : sm.tp))) * rc.r; sm.sn = __builtin_amdgcn_rcpf(xsum(sm.sn + (hh ? 0.f : sm.tn))) * rc.r; }
    if constexpr (s >= 23) {
#pragma unroll
      for (int u = (s == 29 ? 6 : s - 23); u <= (s == 29 ? 7 : s - 23); u++) {
        const int k = u >> 1;
        if (u & 1) { u32x2 t = {pk2s(sm.en[4 * k] * sm.sn, sm.en[4 * k + 1] * sm.sn), pk2s(sm.en[4 * k + 2] * sm.sn, sm.en[4 * k + 3] * sm.sn)}; ldsw<u32x2>(aIW ^ (unsigned)(k << 4), 8192, t); }
        else { u32x2 t = {pk2s(sm.e[4 * k] * sm.sp, sm.e[4 * k + 1] * sm.sp), pk2s(sm.e[4 * k + 2] * sm.sp, sm.e[4 * k + 3] * sm.sp)}; ldsw<u32x2>(aIW ^ (unsigned)(k << 4), 0, t); }
      }
    }
  };
  auto qstep = [&](auto s_, f32x16& a, unsigned lh) AI {
    constexpr int s = decltype(s_)::value;
    if constexpr (s == 0) { sm.w0 = tr2(aWQ, lh * 1568, lh * 1568 + 8 * 56); sm.w1 = tr2(aWQ, lh * 1568 + 16 * 56, lh * 1568 + 24 * 56); }
    if constexpr (s == 1) sm.p = featproj2(a, sm.w0, sm.w1, hh);
    if constexpr (s == 3) sm.bx = -max16(sm.p) * rc.Lr;
    if constexpr (s == 4) sm.bn = min16(sm.p) * rc.Lr;
    if constexpr (s >= 5 && s < 21) {
      constexpr int i = (s - 5) >> 1;
      float t0, t1;
      if constexpr ((s - 5) & 1) { t0 = __builtin_fmaf(sm.p[2 * i], -rc.Lr, sm.bn); t1 = __builtin_fmaf(sm.p[2 * i + 1], -rc.Lr, sm.bn); }
      else { t0 = __builtin_fmaf(sm.p[2 * i], rc.Lr, sm.bx); t1 = __builtin_fmaf(sm.p[2 * i + 1], rc.Lr, sm.bx); }
      a[8 * ((s - 5) & 1) + i] = U2F(pk2(__builtin_amdgcn_exp2f(t0), __builtin_amdgcn_exp2f(t1)));
      if constexpr (s == 20) asm volatile("" : "+v"(a));
    }
  };
  bf16x8 ka_[4], kb_[2][4]; f32x4 kd[2];
  auto kvstep = [&](auto s_) AI {
    constexpr int s = decltype(s_)::value;
    if constexpr (s == 0) {
      const unsigned g4 = lane >> 4, r0 = 8 * g4 + q4, sw = 4 * blk + (q4 >> 1);
      unsigned b0 = LB + L_PHI + r0 * 64 + ((p4 ^ sw) << 3);
      unsigned a0 = (b0 ^ ((wr >> 1) << 5)) + (wr & 1) * 8192;
#pragma unroll
      for (int k = 0; k < 4; k++) {
        ka_[k] = __builtin_shufflevector(trd2(a0, k * 2048), trd2(a0 ^ 16u, k * 2048 + 256), 0, 1, 2, 3, 4, 5, 6, 7);
        kb_[0][k] = __builtin_shufflevector(trd2(b0, L_V - L_PHI + k * 2048), trd2(b0 ^ 16u, L_V - L_PHI + k * 2048 + 256), 0, 1, 2, 3, 4, 5, 6, 7);
        kb_[1][k] = __builtin_shufflevector(trd2(b0 ^ 32u, L_V - L_PHI + k * 2048), trd2(b0 ^ 48u, L_V - L_PHI + k * 2048 + 256), 0, 1, 2, 3, 4, 5, 6, 7);
      }
    }
    if constexpr (s == 1) {
      f32x4 z = {0.f, 0.f, 0.f, 0.f}; kd[0] = z; kd[1] = z;
#pragma unroll
      for (int k = 0; k < 4; k++) { kd[0] = MFMA16(ka_[k], kb_[0][k], kd[0]); kd[1] = MFMA16(ka_[k], kb_[1][k], kd[1]); }
    }
    if constexpr (s == 3) {
#pragma unroll
      for (int d = 0; d < 2; d++) {
        u32x4 t;
#pragma unroll
        for (int i = 0; i < 4; i++) t[i] = (F2U(kd[d][i]) & ~0xfffu) | T12;
        st16(t, rsWS, (lane >> 4) * 512 + (lane & 15) * 16, ((head * 16 + rb) * 2 + (wr & 1)) * 4096 + (wr >> 1) * 2048, WS_KVP + d * 256);
      }
    }
  };
  auto qexp2 = [&](f32x16& a0, unsigned l0, f32x16& a1, unsigned l1) AI {
    const f32x16 p0 = featproj(a0, aWQ, l0 * 1568, hh), p1 = featproj(a1, aWQ, l1 * 1568, hh);
    a0 = dualexpP(p0, rc.Lr); a1 = dualexpP(p1, rc.Lr); asm volatile("" : "+v"(a0), "+v"(a1));
  };

  unsigned fl1 = 0;
  EpiS es;
  u32x4 pv[8];
  const unsigned vKP = l31 * 16 + hu * 8192, sKP = (rb * 32 + half) * 4096 + wr * 1024 + GG * 512, sKR = (8 * half + w) * 8192;
  auto hop1 = [&]() AI {
    waittag<16384>(pv, rsWS, vKP, sKP + WS_KVP, T12, 0xfffu);
    u32x4 o;
#pragma unroll
    for (int k = 0; k < 4; k++) {
      float sum = U2F(pv[0][k]);
#pragma unroll
      for (int i = 1; i < 8; i++) sum += U2F(pv[i][k]);
      sum = xsum(sum);
      sum = l31 == 29 ? 1.f : sum;
      if (GG == 1) sum = wr == 3 ? 0.f : sum;
      o[k] = (pk2s(sum, sum) & 0xffff0000u) | T16;
    }
    st16(o, rsWS, hh ? 0x80000000u : l31 * 16, (rb * 2 + half) * 4096 + wr * 1024 + GG * 512, WS_KVR);
    if (b == 0 && w == 4) stflag(E + 4, rsWS, WS_EPOCH);
    SB();
  };
  bf16x8 cb[NT];
  auto ldflag = [&]() AI { asm volatile("buffer_load_dword %0, %1, %2, %3 offen sc1" : "=v"(fl1) : "v"(nd1 ? lane * FS : 0x80000000u), "s"(rsWS), "s"(sF) : "memory"); };
  auto touch = [&](unsigned wo4) AI {
    const unsigned rw = (rbl * 8 + w + wo4) * 44 + lane;
    const bool dn = rw < 896;
    const unsigned vo = lane < 44 ? (dn ? ((rw >> 6) * 128 + 64 + (rw & 63)) : rw - 896) * XROW + ci * 128 : 0x80000000u;
    asm volatile("s_add_u32 m0, %0, %c5\n\ts_nop 0\n\tbuffer_load_dword %1, %3, 0 offen lds\n\tbuffer_load_dword %2, %4, 0 offen lds"
                 :: "s"(LB + w * 256), "v"(dn ? vo : 0x80000000u), "v"(dn ? 0x80000000u : vo), "s"(rsWD), "s"(rsWO), "i"(L_ST) : "memory", "scc");
  };
  auto kpath = [&]() AI {
    const f32x16 p = featproj(accA[0], aWK, 0, hh);
#pragma unroll
    for (int k = 0; k < 4; k++) {
      u32x2 vv = {pk2(accA[1][4 * k], accA[1][4 * k + 1]), pk2(accA[1][4 * k + 2], accA[1][4 * k + 3])};
      if (k == 3) vv[0] = hh ? denb : vv[0];
      ldsw<u32x2>(aIW ^ (unsigned)(k << 4), L_V - L_PHI, vv);
    }
    const f32x2 bx = dup2(-max16(p) * rc.Lr), bn = dup2(min16(p) * rc.Lr), L2 = dup2(rc.Lr);
    f32x2 ep[8], en[8];
#pragma unroll
    for (int i = 0; i < 8; i++) { ep[i] = exp2v(fma2(sub2(p, i), L2, bx)); en[i] = exp2v(fma2(sub2(p, i), -L2, bn)); }
    f32x2 sp = ((ep[0] + ep[1]) + (ep[2] + ep[3])) + (ep[4] + ep[5]), sn = ((en[0] + en[1]) + (en[2] + en[3])) + (en[4] + en[5]);
    f32x2 tp = ep[6] + ep[7], tn = en[6] + en[7];
    float a0 = sp[0], a1 = sn[0], a2 = tp[0], a3 = tn[0];
    asm("" : "+v"(a0), "+v"(a1), "+v"(a2), "+v"(a3));
    a0 += sp[1]; a2 += tp[1]; asm("" : "+v"(a0), "+v"(a2)); a1 += sn[1]; a3 += tn[1];
    const f32x2 ip = dup2(__builtin_amdgcn_rcpf(xsum(a0 + (hh ? 0.f : a2))) * rc.r), in = dup2(__builtin_amdgcn_rcpf(xsum(a1 + (hh ? 0.f : a3))) * rc.r);
#pragma unroll
    for (int k = 0; k < 4; k++) {
      const f32x2 u0 = ep[2 * k] * ip, u1 = ep[2 * k + 1] * ip, v0 = en[2 * k] * in, v1 = en[2 * k + 1] * in;
      u32x2 t0 = {pk2(u0[0], u0[1]), pk2(u1[0], u1[1])}, t1 = {pk2(v0[0], v0[1]), pk2(v1[0], v1[1])};
      ldsw<u32x2>(aIW ^ (unsigned)(k << 4), 0, t0); ldsw<u32x2>(aIW ^ (unsigned)(k << 4), 8192, t1);
    }
  };
  const unsigned sA = WS_A2 + rt2 * 144 * 1024;
  u32x4 af[NS][2];
  auto loadA1 = [&](int i, int m) AI { af[i][m] = ld16(rsWS, vL16, sA + ksb(i) * 1024, (144 * m + ksi(i)) * 1024); };
  auto stream = [&]() AI {
    SFOR(q, 28 * NT)
      constexpr int n = q / NT, j = q % NT;
      if constexpr (j == 0) {
        if constexpr (n == 27) {
#pragma unroll
          for (int t = 0; t < NT; t++) ldsw<unsigned>(aCSw, ((3 + t) & 3) * 512, hilo(csB[t][0]));
          if constexpr (AT && GG) WAITV(8);
          WAITL0(); BAR(); MEMFENCE();
        }
        else if constexpr (!AT && (n & 3) == 3) { asm volatile("s_waitcnt lgkmcnt(4)" ::: "memory"); BAR(); MEMFENCE(); }
      }
      if constexpr (q + 4 < 28 * NT) readB(q + 4);
      if constexpr (n == 27) cb[j] = ldsr<bf16x8>(aCSr, ((3 + j) & 3) * 512);
      SB();
      accB[j] = MFMA(wq[q & 7], xf[n], accB[j]);
      if constexpr (!NOCS && (n & 3) == 1) csB[j] = MFMA16(sel, wq[q & 7], csB[j]);
      if constexpr (!AT) {
        if constexpr (j == 0 && n >= 3 && n < 7) issueW2(1 + ((n - 3) >> 1), (n - 3) & 1);
        if constexpr (j == 0 && n >= 7 && n < 27 && (n & 1)) issueW2(1 + ((n + 1) >> 2), ((n + 1) >> 1) & 1);
        if constexpr (n == 27) issueW2(8, j);
        if constexpr (j == 1 && n == F1N) { waitv<(F1N < 7 ? F1N - 2 : 4 + (F1N - 5) / 2) + (TN < F1N ? 2 : 0)>(); stflag(E + 1, rsWS, sFown); }
        if constexpr (j == 1 && n == TN) touch(0);
        if constexpr (q == 2 * CK1 - 7) ldflag();
        if constexpr (q == 2 * CK1 - 1) {
          asm volatile("s_waitcnt vmcnt(%c5)" : "+v"(fl1), "+v"(accB[0]), "+v"(accB[NT - 1]), "+v"(csB[0]), "+v"(csB[NT - 1]) : "i"((CK1 & 1) ? 1 : 2) : "memory");
          const bool nde = nd1 && fc < 14;
          if (!ALL(!nde || (fl1 - E - 1u) <= 1u)) poll(rsWS, lane * FS, sF, E, nde ? 1u : 0u);
        }
        if constexpr (q >= 2 * CK1 && q < 2 * CK1 + 2 * NE) loadA1((q - 2 * CK1) >> 1, q & 1);
      } else {
        if constexpr (GG == 1 && n < 16 && (n & 1) == 0) clri(n >> 1);
        if constexpr (GG == 0 && j < 2 && n >= 1 && 2 * (n - 1) + j < 21) qstep(IntC<2 * (n - 1) + j>{}, accA[2], 0);
        if constexpr (GG == 0 && q >= 3 * H0L && q < 3 * H0L + 8) pv[q - 3 * H0L] = ld16(rsWS, vKP, sKP, WS_KVP + (q - 3 * H0L) * 16384);
        if constexpr (GG == 0 && j == 2 && n == H0C) { asm volatile("" : "+v"(accB[0]), "+v"(accB[1]), "+v"(accB[NT - 1]), "+v"(csB[0]), "+v"(csB[1]), "+v"(csB[NT - 1])); STAMP(11); hop1(); STAMP(6); }
      }
      SB();
    SEND
    const bf16x8 xe = mkxe();
#pragma unroll
    for (int t = 0; t < NT; t++) { accB[t] = MFMA(cb[t], xe, accB[t]); asm volatile("" : "+v"(accB[t])); }
    SB();
  };
  unsigned ln = vL16, aW = 0;
  auto mkaw = [&]() AI {
    asm volatile("" : "+v"(ln));
    aW = lw2 + (ln & 0x200) + ((ln & 0xc0) << 1) + ((((ln >> 3) & 0x20) | ((ln >> 1) & 0x10)) ^ ((ln >> 1) & 0x40)) + ((ln >> 1) & 8);
  };
  bf16x8 wf[NS][2];
  auto readW = [&](int i) AI {
    const unsigned ra = ring_area(i % RING), la = aW + (ra & 0x30000u);
    wf[i][0] = tr2(la, ra & 0xffffu, (ra & 0xffffu) + 1024); wf[i][1] = tr2(la ^ 64u, ra & 0xffffu, (ra & 0xffffu) + 1024);
  };
  auto tile = [&](f32x16& a, int t) AI { asm volatile("" : "+v"(a)); st16(epiP(a, rc), rsWS, vL16, sA2, t * 1024); SB(); };
  if constexpr (!AT) {
    if constexpr (GG == 0) {
      kpath(); WAITL0(); SB(); BAR(); SB(); STAMP(3);
      kvstep(IntC<0>{}); kvstep(IntC<1>{}); kvstep(IntC<3>{}); SB();
      tile(accA[2], 0);
    } else { tile(accA[0], 1); if (T2B) tile(accA[1], 2); BAR(); SB(); STAMP(3); if (!T2B) tile(accA[1], 2); tile(accA[2], 3); }
    readB(0); readB(1); readB(2); readB(3);
    STAMP(4);
    stream();
    SFOR(u, (2 * CK1 + 2 * NE > 56 ? 2 * CK1 + 2 * NE - 56 : 0)) loadA1((56 + u - 2 * CK1) >> 1, u & 1); SEND
    STAMP(5);
    mkaw();
    SFOR(i, RC) readW(i); SEND
    if constexpr (RC > 0) { WAITL0(); SB(); SFOR(i, RC) issueW2(9 + i, 0); issueW2(9 + i, 1); SEND }
    tile(accB[0], 4 + 2 * GG); tile(accB[NT - 1], 5 + 2 * GG);
  } else if constexpr (GG == 0) {
    SFOR(q, 8) clri(q); SEND
    SFOR(s, 30) kstep(s_); SEND
    WAITV(8);
    WAITL0(); SB(); BAR(); SB();
    STAMP(3);
    kvstep(IntC<0>{});
    readB(0); readB(1); readB(2); readB(3);
    kvstep(IntC<1>{}); kvstep(IntC<3>{});
    WAITV(2); stflag(E + 1, rsWS, sFown);
    STAMP(4);
    SB();
    stream();
  } else {
    stream();
    STAMP(3);
    SFOR(s, 21) qstep(s_, accA[0], 1); SEND
    WAITV(0); stflag(E + 1, rsWS, sFown);
    SFOR(s, 21) qstep(s_, accA[1], 2); SEND
    SFOR(s, 21)
      if constexpr (s < 8) pv[s] = ld16(rsWS, vKP, sKP, WS_KVP + s * 16384);
      qstep(s_, accA[2], 3);
    SEND
    STAMP(4); hop1(); STAMP(6);
    SB(); BAR(); SB();
  }

  if constexpr (AT) {
    STAMP(5);
    auto qexp = [&](f32x16& a, unsigned lh) AI { a = dualexpP(featproj(a, aWQ, lh * 1568, hh), rc.Lr); asm volatile("" : "+v"(a)); SB(); };
    if constexpr (GG == 0) qexp(accB[0], 4);
#pragma unroll
    for (int i = 0; i < 8; i++) pv[i] = ld16(rsWS, vL16, sKR, WS_KVR + i * 1024);
    if constexpr (GG == 0) { qexp(accB[1 % NT], 5); qexp(accB[NT - 1], 6); } else qexp(accB[0], 7);
    STAMP(7);
    waittag<1024>(pv, rsWS, vL16, sKR + WS_KVR, T16, 0xffffu);
    STAMP(8);
#pragma unroll
    for (int i = 0; i < 4; i++) {
      u32x4 f = {__builtin_amdgcn_perm(pv[2 * i][1], pv[2 * i][0], 0x07060302u), __builtin_amdgcn_perm(pv[2 * i][3], pv[2 * i][2], 0x07060302u),
                 __builtin_amdgcn_perm(pv[2 * i + 1][1], pv[2 * i + 1][0], 0x07060302u), __builtin_amdgcn_perm(pv[2 * i + 1][3], pv[2 * i + 1][2], 0x07060302u)};
      ldsw<u32x4>(LB + w * 4096 + vL16, i * 1024, f);
    }
    WAITL0(); BAR(); MEMFENCE();
    STAMP(10);
    f32x16 op[4], on[4];
    auto omm = [&](const f32x16& phi, unsigned lh, int z) AI {
#pragma unroll
      for (int i = 0; i < 16; i++) { op[z][i] = 0.f; on[z][i] = 0.f; }
      bf16x8 kf[4];
#pragma unroll
      for (int i = 0; i < 4; i++) kf[i] = ldsr<bf16x8>(LB + vL16, lh * 4096 + i * 1024);
#pragma unroll
      for (int i = 0; i < 2; i++) { op[z] = MFMA(kf[i], sub4(phi, i), op[z]); on[z] = MFMA(kf[2 + i], sub4(phi, 2 + i), on[z]); }
    };
    auto ofin = [&](unsigned lh, int z) AI {
      const float ia = __builtin_amdgcn_rcpf(op[z][13]), ib = __builtin_amdgcn_rcpf(on[z][13]);
      const float id = __builtin_amdgcn_rcpf(__builtin_fmaf(op[z][12], ia, __builtin_fmaf(on[z][12], ib, 1e-6f)));
      const float ca = xhi(ia * id), cb2 = xhi(ib * id);
      const f32x2 c2 = dup2(ca), d2 = dup2(cb2);
      u32x4 f0, f1;
#pragma unroll
      for (int i = 0; i < 4; i++) {
        const f32x2 u = fma2(sub2(op[z], i), c2, sub2(on[z], i) * d2), v = fma2(sub2(op[z], 4 + i), c2, sub2(on[z], 4 + i) * d2);
        f0[i] = pk2(u[0], u[1]); f1[i] = pk2(v[0], v[1]);
      }
      f1[2] = hh ? 0u : f1[2]; f1[3] = hh ? 0u : f1[3];
      st16(f0, rsWS, OV, sA2, (112 + 2 * lh) * 1024); st16(f1, rsWS, OV, sA2, (113 + 2 * lh) * 1024);
      SB();
    };
    if constexpr (GG) { omm(accA[0], 1, 0); omm(accA[1], 2, 1); SB(); ofin(1, 0); omm(accA[2], 3, 2); omm(accB[0], 7, 3); SB(); ofin(2, 1); ofin(3, 2); ofin(7, 3); }
    else { omm(accA[2], 0, 0); omm(accB[0], 4, 1); SB(); ofin(0, 0); omm(accB[1 % NT], 5, 2); omm(accB[NT - 1], 6, 3); SB(); ofin(4, 1); ofin(5, 2); ofin(6, 3); }
    STAMP(9);
  } else {
    f32x16 acc[2][2];
#pragma unroll
    for (int i = 0; i < 4; i++)
#pragma unroll
      for (int k = 0; k < 16; k++) acc[i >> 1][i & 1][k] = 0.f;
    u32x4 xr;
    const unsigned vO = (ln >> 7) * XROW + (ln & 0x70), sO = (rt2 * 32 + 8 * w) * XROW + ci * 128;
    auto mm4 = [&](int i) AI {
#pragma unroll
      for (int nn = 0; nn < 2; nn++)
#pragma unroll
        for (int mm = 0; mm < 2; mm++) acc[nn][mm] = MFMA(wf[i][nn], __builtin_bit_cast(bf16x8, af[i][mm]), acc[nn][mm]);
      SB();
    };
    STAMP(6);
    waitv<2 + 2 * RC>();
#pragma unroll
    for (int i = 0; i < NE; i++) asm volatile("" : "+v"(af[i][0]), "+v"(af[i][1]));
    SFOR(i, 9 - RC) readW(RC + i); SEND
    SB();
    SFOR(i, EB) mm4(i); SEND
    WAITV(0); stflag(E + 2, rsWS, sFown);
    WAITL0(); SB();
    BAR();
    SB(); STAMP(11);
    if constexpr (GG) __builtin_amdgcn_s_setprio(P2G1);
    constexpr int NB = (GG ? 9 : G0B) - RC;
    SFOR(i, 9)
      if constexpr (i == NB - 2) ldflag();
      if constexpr (i < NB) { issueW2(9 + RC + i, 0); issueW2(9 + RC + i, 1); SB(); }
      if constexpr (EB + i < NE) mm4(EB + i);
    SEND
    asm volatile("s_waitcnt vmcnt(4)" : "+v"(fl1) :: "memory");
    { const unsigned need = !nd1 ? 0u : fc < 14 ? 2u : 1u;
      if (!ALL(!nd1 || fl1 == E + need)) poll(rsWS, lane * FS, sF, E, need); }
    STAMP(7);
#pragma unroll
    for (int j = NE; j < NE + PD; j++) { loadA1(j, 0); loadA1(j, 1); }
    asm volatile("buffer_load_dwordx4 %0, %1, %2, %3 offen" : "=v"(xr) : "v"(vO), "s"(rsX), "s"(sO) : "memory");
    SFOR(i, 9 - RC - NB) issueW2(9 + RC + NB + i, 0); issueW2(9 + RC + NB + i, 1); SEND
    SFOR(i1, NS - NE)
      constexpr int i = NE + i1;
      asm volatile("s_waitcnt vmcnt(%c2)" : "+v"(af[i][0]), "+v"(af[i][1]) : "i"(2 * ((i + PD <= NS ? i + PD : NS) - 1 - i) + (i1 < PD && i < NO + 8 * GG ? 1 + 2 * (9 - RC - NB) : 0)) : "memory");
      if constexpr (i == 9 && i1 == 0) readW(9);
      if constexpr (i + 1 >= 9 && i + 1 < NS) readW(i + 1);
      if constexpr (GG == 0 && i >= NO) {
        auto stale = [&]() AI {
          unsigned m = UMAX(UMAX(UMAX(af[i][0][0], af[i][0][1]), UMAX(af[i][0][2], af[i][0][3])), UMAX(UMAX(af[i][1][0], af[i][1][1]), UMAX(af[i][1][2], af[i][1][3])));
          return ANY(m == ~0u);
        };
        if (__builtin_expect(stale(), 0)) {
          int it = 0;
#pragma nounroll
          do { __builtin_amdgcn_s_sleep(SLEEPR); rel1<true>(af[i][0], af[i][1], rsWS, vL16, sA + (ksb(i) + ksi(i)) * 1024); } while (++it < (1 << 14) && stale());
          SFOR(u, PD - 1) if constexpr (i + 1 + u < NS) rel1<false>(af[i + 1 + u][0], af[i + 1 + u][1], rsWS, vL16, sA + (ksb(i + 1 + u) + ksi(i + 1 + u)) * 1024); SEND
        }
      }
      if constexpr (i == 10) STAMP(8);
      if constexpr (i == 14) STAMP(9);
      SB();
      if constexpr (i + PD < NS) { loadA1(i + PD, 0); loadA1(i + PD, 1); }
      mm4(i);
    SEND
    STAMP(10);
    __builtin_amdgcn_s_setprio(0);
#pragma unroll
    for (int qd = 0; qd < 4; qd++)
#pragma unroll
      for (int k = 0; k < 4; k++) {
        const unsigned ra = ring_area(2 * qd + (k >> 1)) + (k & 1) * 1024;
        ldsw<bf16x8>(((lw2 + vL16) ^ (unsigned)(32 * (4 * (qd >> 1) + k))) + (ra & 0x30000u), ra & 0xffffu, sub4(acc[qd >> 1][qd & 1], k));
      }
    WAITL0(); BAR(); MEMFENCE();
    STAMP(12);
    const unsigned c8 = (ln >> 4) & 7, j2 = (c8 & 4) + 2 * GG + ((c8 >> 1) & 1);
    const unsigned rd = LB + ((0x51407362u >> (4 * j2)) & 7) * 16384 + (c8 & 1) * 1024 + (((8 * wr + (ln >> 7)) << 4) ^ (c8 << 5));
    asm volatile("" : "+v"(xr));
    f32x4 sum[2];
#pragma unroll
    for (int k = 0; k < 2; k++) {
      f32x4 t = {U2F(xr[2 * k] << 16), U2F(xr[2 * k] & 0xffff0000u), U2F(xr[2 * k + 1] << 16), U2F(xr[2 * k + 1] & 0xffff0000u)};
#pragma unroll
      for (int v = 0; v < 8; v++) t += ldsr<f32x4>(rd, v * 2048 + k * 512);
      sum[k] = t;
    }
    u32x4 o = {pk2(sum[0][0], sum[0][1]), pk2(sum[0][2], sum[0][3]), pk2(sum[1][0], sum[1][1]), pk2(sum[1][2], sum[1][3])};
    __builtin_amdgcn_raw_buffer_store_b128(o, mkrs(pout, 2048 * XROW), vO + sO, 0, OAUX);
  }
  STAMPS_OUT();
  };
  if (!attn && g) prog(BoolC<false>{}, IntC<1>{});
  else if (!attn) prog(BoolC<false>{}, IntC<0>{});
  else if (g) prog(BoolC<true>{}, IntC<1>{});
  else prog(BoolC<true>{}, IntC<0>{});
}

extern "C" void kernel_launch(void* const* d_in, const int* in_sizes, int n_in, void* d_out, int out_size, void* d_ws, size_t ws_size,
                              hipStream_t stream) {
  fused_kernel<<<256, 512, 0, stream>>>((const char*)d_in[0], (const char*)d_in[1], (const char*)d_in[5], (const char*)d_in[6], (char*)d_ws,
                                        (const char*)d_in[2], (const char*)d_in[7], (const char*)d_in[3], (char*)d_out);
}
```

```cpp
#define ENC 2
#define ENCAT 1
#include <hip/hip_runtime.h>
#include <hip/hip_bf16.h>
#include <utility>

#define DI __device__ __forceinline__
#define LAS __attribute__((address_space(3)))
typedef __attribute__((ext_vector_type(8))) short bf16x8;
typedef __attribute__((ext_vector_type(4))) short s16x4;
typedef __attribute__((ext_vector_type(2))) __bf16 bf2;
typedef __attribute__((ext_vector_type(2))) float f32x2;
typedef __attribute__((ext_vector_type(4))) float f32x4;
typedef __attribute__((ext_vector_type(16))) float f32x16;
typedef __attribute__((ext_vector_type(2))) unsigned u32x2;
typedef __attribute__((ext_vector_type(4))) unsigned u32x4;
typedef __amdgpu_buffer_rsrc_t rsrc_t;
#define F2U(x) __builtin_bit_cast(unsigned, (float)(x))
#define U2F(x) __builtin_bit_cast(float, (unsigned)(x))
#define ALL(x) (__builtin_amdgcn_ballot_w64(!(x)) == 0ull)
#define ANY(x) (__builtin_amdgcn_ballot_w64(x) != 0ull)
#define UMAX(a, b) __builtin_elementwise_max((unsigned)(a), (unsigned)(b))

#define NTX 0
#define NTW 0
#define NT2 0
#define XS 2
constexpr unsigned XROW = 896, QROW = 2688, FROW = 3584;
constexpr unsigned FS = 128;
constexpr unsigned WS_EPOCH = 0;
constexpr unsigned WS_GR = 4096;
constexpr unsigned WS_KVP = 131072;
constexpr unsigned WS_A2 = WS_KVP + 16 * 16 * 8192;
constexpr unsigned WS_ST = WS_A2 + 64 * 144 * 1024;
constexpr unsigned WS_FH = WS_ST + 262144;
constexpr unsigned WS_KVR = WS_FH + 2048 * FS;
constexpr unsigned WS_END = WS_KVR + 16 * 8192;
constexpr unsigned L_GU = 65536, L_KV = 131072;
constexpr unsigned L_TAIL = L_KV + 3 * 8192;
constexpr unsigned L_WPK = L_TAIL;
constexpr unsigned L_ST = L_TAIL + 2048;
constexpr unsigned L_CS = L_TAIL + 4096;
constexpr unsigned L_PHI = L_GU + 32768;
constexpr unsigned L_V = L_KV + 16384;
constexpr unsigned L_FREE = 32768;

#define MFMA(a, b, c) __builtin_amdgcn_mfma_f32_32x32x16_bf16((a), (b), (c), 0, 0, 0)
#define MFMA16(a, b, c) __builtin_amdgcn_mfma_f32_16x16x32_bf16((a), (b), (c), 0, 0, 0)
#define AI __attribute__((always_inline))
#define BAR() __builtin_amdgcn_s_barrier()
#define WAITV(n) asm volatile("s_waitcnt vmcnt(" #n ")" ::: "memory")
#define WAITL0() asm volatile("s_waitcnt lgkmcnt(0)" ::: "memory")
#define SB() __builtin_amdgcn_sched_barrier(0)
#define MEMFENCE() asm volatile("" ::: "memory")

template <int N> DI void waitv() { asm volatile("s_waitcnt vmcnt(%c0)" :: "i"(N) : "memory"); }

#define INSZ(x) (x)
DI rsrc_t mkrs(const void* p, unsigned bytes) { return __builtin_amdgcn_make_buffer_rsrc((void*)p, (short)0, (int)bytes, 0x00020000); }
template <bool NT = false> DI void dma(unsigned lds, unsigned limm, unsigned voff, rsrc_t rs, unsigned soff, unsigned simm) {
  unsigned t;
  if constexpr (NT) asm volatile("s_add_u32 m0, %1, %c5\n\ts_add_u32 %0, %4, %c6\n\tbuffer_load_dwordx4 %2, %3, %0 offen nt lds"
               : "=&s"(t) : "s"(lds), "v"(voff), "s"(rs), "s"(soff), "i"(limm), "i"(simm) : "memory", "scc");
  else asm volatile("s_add_u32 m0, %1, %c5\n\ts_add_u32 %0, %4, %c6\n\tbuffer_load_dwordx4 %2, %3, %0 offen lds"
               : "=&s"(t) : "s"(lds), "v"(voff), "s"(rs), "s"(soff), "i"(limm), "i"(simm) : "memory", "scc");
}
DI void dmat(unsigned lds, unsigned voff, rsrc_t rs) {
  asm volatile("s_mov_b32 m0, %0\n\ts_nop 0\n\tbuffer_load_dword %1, %2, 0 offen lds" :: "s"(lds), "v"(voff), "s"(rs) : "memory");
}
DI s16x4 trd(unsigned a) { return __builtin_amdgcn_ds_read_tr16_b64_v4i16((LAS s16x4*)(size_t)a); }
DI s16x4 trd2(unsigned a, unsigned off) { return __builtin_amdgcn_ds_read_tr16_b64_v4i16((LAS s16x4*)((LAS char*)(size_t)a + off)); }
DI bf16x8 tr2(unsigned a, unsigned off0, unsigned off1) {
  LAS char* p = (LAS char*)(size_t)a;
  s16x4 lo = __builtin_amdgcn_ds_read_tr16_b64_v4i16((LAS s16x4*)(p + off0));
  s16x4 hi = __builtin_amdgcn_ds_read_tr16_b64_v4i16((LAS s16x4*)(p + off1));
  return __builtin_shufflevector(lo, hi, 0, 1, 2, 3, 4, 5, 6, 7);
}
template <class T> DI T ldsr(unsigned a, unsigned off) { return *(LAS T*)((LAS char*)(size_t)a + off); }
template <class T> DI void ldsw(unsigned a, unsigned off, T v) { *(LAS T*)((LAS char*)(size_t)a + off) = v; }
DI unsigned pk2(float a, float b) { f32x2 f = {a, b}; return __builtin_bit_cast(unsigned, __builtin_convertvector(f, bf2)); }
DI float rbf(float a) { return U2F(pk2(a, a) << 16); }
DI float dot2(unsigned a, unsigned b, float c) { return __builtin_amdgcn_fdot2_f32_bf16(__builtin_bit_cast(bf2, a), __builtin_bit_cast(bf2, b), c, false); }
DI float xsum(float v) { auto r = __builtin_amdgcn_permlane32_swap(F2U(v), F2U(v), false, false); float a = U2F(r[0]); asm("" : "+v"(a)); return a + U2F(r[1]); }
DI float xmax(float v) { auto r = __builtin_amdgcn_permlane32_swap(F2U(v), F2U(v), false, false); return __builtin_fmaxf(U2F(r[0]), U2F(r[1])); }
DI float xmin(float v) { auto r = __builtin_amdgcn_permlane32_swap(F2U(v), F2U(v), false, false); return __builtin_fminf(U2F(r[0]), U2F(r[1])); }
DI float xhi(float v) { auto r = __builtin_amdgcn_permlane32_swap(F2U(v), F2U(v), false, false); return U2F(r[1]); }
#define STB "sc1"
DI void st16(u32x4 v, rsrc_t rs, unsigned voff, unsigned soff, unsigned simm) {
  unsigned t;
  asm volatile("s_add_u32 %0, %4, %c5\n\tbuffer_store_dwordx4 %1, %2, %3, %0 offen " STB "\n\ts_nop 1" : "=&s"(t) : "v"(v), "v"(voff), "s"(rs), "s"(soff), "i"(simm) : "memory", "scc");
}
DI void stflag(unsigned v, rsrc_t rs, unsigned soff) {
  unsigned voff = (__builtin_amdgcn_mbcnt_hi(~0u, __builtin_amdgcn_mbcnt_lo(~0u, 0u)) == 0u) ? 0u : 0x80000000u;
  __builtin_amdgcn_raw_buffer_store_b32(v, rs, voff, soff, 16);
}
#define XORCOND (n == 24)
#define PS 0
#define PV 0
#define KEEP2 0
#define S2N 14
#define B1N 23
#define DIST 2
#define P2G1 1
#define OAUX 0
#define RECY 0
#define POFF 99
#define H1S 0
#define H0L 14
#define H0C 22
#define Q3A 1
#define COOP 0
#define PON -2
#define XN 20
#define BAN 18
#define BBN 21
#define SL0 8
#define SLN 2
#define SLD 3
#define NSL 48
#define H1C 16
#define OV vL16
#define CV vL16
#define Q6E 0
#define TEARLY 0
#define NOCS 0
#define TPOS 0
#define PRE 0
#define LREC 0
#define CK1 20
#define EB 3
#define T2B 1
#define F1N 9
#define RC 3
#define G0B 9
#define TN 10
#define SLEEPR 4
#define SLEEPN 2
DI u32x4 ld16(rsrc_t rs, unsigned voff, unsigned soff, unsigned simm) {
  u32x4 r; unsigned t;
  asm volatile("s_add_u32 %1, %4, %c5\n\tbuffer_load_dwordx4 %0, %2, %3, %1 offen sc1" : "=v"(r), "=&s"(t) : "v"(voff), "s"(rs), "s"(soff), "i"(simm) : "memory", "scc");
  return r;
}
template <unsigned STEP> DI void ld8w(u32x4 (&p)[8], rsrc_t rs, unsigned voff, unsigned soff) {
  unsigned t;
#define L1(k) "buffer_load_dwordx4 %" #k ", %9, %10, %8 offen sc1\n\ts_add_u32 %8, %8, %c12\n\t"
  asm volatile("s_mov_b32 %8, %11\n\t" L1(0) L1(1) L1(2) L1(3) L1(4) L1(5) L1(6) L1(7) "s_waitcnt vmcnt(0)"
               : "=&v"(p[0]), "=&v"(p[1]), "=&v"(p[2]), "=&v"(p[3]), "=&v"(p[4]), "=&v"(p[5]), "=&v"(p[6]), "=&v"(p[7]), "=&s"(t)
               : "v"(voff), "s"(rs), "s"(soff), "i"(STEP) : "memory", "scc");
#undef L1
}
DI bool untag(u32x4 (&p)[8], unsigned tag, unsigned mask) {
  unsigned chk = 0;
#pragma unroll
  for (int i = 0; i < 8; i++)
#pragma unroll
    for (int k = 0; k < 4; k++) { p[i][k] ^= tag; chk |= p[i][k]; }
  return ALL((chk & mask) == 0u);
}
template <unsigned STEP> DI void waittag(u32x4 (&p)[8], rsrc_t rs, unsigned voff, unsigned soff, unsigned tag, unsigned mask) {
  asm volatile("s_waitcnt vmcnt(0)" : "+v"(p[0]), "+v"(p[1]), "+v"(p[2]), "+v"(p[3]), "+v"(p[4]), "+v"(p[5]), "+v"(p[6]), "+v"(p[7]) :: "memory");
#pragma nounroll
  for (int it = 0; !untag(p, tag, mask) && it < (1 << 16); ++it) { __builtin_amdgcn_s_sleep(SLEEPN); ld8w<STEP>(p, rs, voff, soff); }
}
template <bool WAIT> DI void rel1(u32x4& a, u32x4& b, rsrc_t rs, unsigned voff, unsigned so) {
  unsigned t;
  if constexpr (WAIT) asm volatile("buffer_load_dwordx4 %0, %3, %4, %5 offen sc1\n\ts_add_u32 %2, %5, 147456\n\tbuffer_load_dwordx4 %1, %3, %4, %2 offen sc1\n\ts_waitcnt vmcnt(0)"
                                   : "+v"(a), "+v"(b), "=&s"(t) : "v"(voff), "s"(rs), "s"(so) : "memory", "scc");
  else asm volatile("buffer_load_dwordx4 %0, %3, %4, %5 offen sc1\n\ts_add_u32 %2, %5, 147456\n\tbuffer_load_dwordx4 %1, %3, %4, %2 offen sc1"
                    : "+v"(a), "+v"(b), "=&s"(t) : "v"(voff), "s"(rs), "s"(so) : "memory", "scc");
}
DI void poll(rsrc_t rs, unsigned voff, unsigned soff, unsigned E, unsigned need) {
  voff = need ? voff : 0x80000000u;
#pragma nounroll
  for (int it = 0; it < (1 << 16); ++it) {
    unsigned v;
    asm volatile("buffer_load_dword %0, %1, %2, %3 offen sc1\n\ts_waitcnt vmcnt(0)" : "=v"(v) : "v"(voff), "s"(rs), "s"(soff) : "memory");
    bool ok = (need == 0u) || ((v - E - need) <= (3u - need));
    if (ALL(ok)) break;
    __builtin_amdgcn_s_sleep(SLEEPN);
  }
}

template <bool B> struct BoolC { static constexpr bool value = B; };
template <int I> struct IntC { static constexpr int value = I; };
template <int... I, class F> DI void sfor_impl(std::integer_sequence<int, I...>, F&& f) { (f(IntC<I>{}), ...); }
template <int N, class F> DI void sfor(F&& f) { sfor_impl(std::make_integer_sequence<int, N>{}, f); }
#define SFOR(n, N) sfor<N>([&](auto n##_) AI { constexpr int n = decltype(n##_)::value;
#define SEND });
struct RowC { float r, K1, K2, Ri, Lr; };

DI unsigned hilo(float v) { float hi = rbf(v); return pk2(hi, v - hi); }
DI float max3(float a, float b, float c) { return __builtin_fmaxf(__builtin_fmaxf(a, b), c); }
DI float min3(float a, float b, float c) { return __builtin_fminf(__builtin_fminf(a, b), c); }
DI unsigned pk2s(float a, float b) { unsigned r; asm("v_cvt_pk_bf16_f32 %0, %1, %2" : "=v"(r) : "v"(a), "v"(b)); return r; }
DI f32x2 sub2(const f32x16& v, int k) { f32x2 t = {v[2 * k], v[2 * k + 1]}; return t; }
DI f32x2 exp2v(f32x2 v) { f32x2 r = {__builtin_amdgcn_exp2f(v[0]), __builtin_amdgcn_exp2f(v[1])}; return r; }
DI f32x2 dup2(float a) { f32x2 r = {a, a}; asm("" : "+v"(r)); return r; }
DI f32x2 fma2(f32x2 a, f32x2 b, f32x2 c) { return __builtin_elementwise_fma(a, b, c); }
DI u32x4 epiP(const f32x16& a, const RowC& c) {
  u32x4 r;
  const f32x2 K1 = dup2(c.K1), K2 = dup2(c.K2), Ri = dup2(c.Ri);
#pragma unroll
  for (int i = 0; i < 4; i++) {
    const f32x2 g = sub2(a, i), d = fma2(exp2v(g * fma2(g * g, K2, K1)), Ri, Ri);
    const f32x2 s = {__builtin_amdgcn_rcpf(d[0]), __builtin_amdgcn_rcpf(d[1])}, h = (g * sub2(a, 4 + i)) * s;
    r[i] = pk2(h[0], h[1]);
  }
  return r;
}
struct EpiS { float x0, x1, m0, m1; u32x4 r; };
template <int P, int PH> DI void epiSlice(const f32x16& a, const RowC& c, EpiS& s) {
  const float g0 = a[2 * P], g1 = a[2 * P + 1];
  if constexpr (PH == 0) { s.x0 = g0 * __builtin_fmaf(g0 * g0, c.K2, c.K1); s.x1 = g1 * __builtin_fmaf(g1 * g1, c.K2, c.K1); }
  if constexpr (PH == 1) { s.x0 = __builtin_amdgcn_exp2f(s.x0); s.x1 = __builtin_amdgcn_exp2f(s.x1); s.m0 = g0 * a[8 + 2 * P]; s.m1 = g1 * a[9 + 2 * P]; }
  if constexpr (PH == 2) { s.x0 = __builtin_amdgcn_rcpf(__builtin_fmaf(s.x0, c.Ri, c.Ri)); s.x1 = __builtin_amdgcn_rcpf(__builtin_fmaf(s.x1, c.Ri, c.Ri)); }
  if constexpr (PH == 3) s.r[P] = pk2s(s.m0 * s.x0, s.m1 * s.x1);
}
DI f32x16 featproj2(const f32x16& a, bf16x8 w0, bf16x8 w1, bool hh) {
  u32x4 q0, q1;
#pragma unroll
  for (int i = 0; i < 4; i++) { q0[i] = pk2(a[2 * i], a[2 * i + 1]); q1[i] = pk2(a[8 + 2 * i], a[9 + 2 * i]); }
  q1[2] = hh ? 0u : q1[2]; q1[3] = hh ? 0u : q1[3];
  f32x16 z;
#pragma unroll
  for (int i = 0; i < 16; i++) z[i] = 0.f;
  z = MFMA(w0, __builtin_bit_cast(bf16x8, q0), z);
  z = MFMA(w1, __builtin_bit_cast(bf16x8, q1), z);
  return z;
}
DI f32x16 featproj(const f32x16& a, unsigned wpaddr, unsigned off, bool hh) {
  return featproj2(a, tr2(wpaddr, off, off + 8 * 56), tr2(wpaddr, off + 16 * 56, off + 24 * 56), hh);
}
DI float max16(const f32x16& p) { float m = max3(p[0], p[1], p[2]); _Pragma("unroll") for (int i = 3; i < 15; i += 2) m = max3(m, p[i], p[i + 1]); return xmax(__builtin_fmaxf(m, p[15])); }
DI float min16(const f32x16& p) { float m = min3(p[0], p[1], p[2]); _Pragma("unroll") for (int i = 3; i < 15; i += 2) m = min3(m, p[i], p[i + 1]); return xmin(__builtin_fminf(m, p[15])); }
DI f32x16 dualexpP(const f32x16& p, float Lr) {
  const f32x2 bx = dup2(-max16(p) * Lr), bn = dup2(min16(p) * Lr), L2 = dup2(Lr);
  f32x16 o;
#pragma unroll
  for (int i = 0; i < 8; i++) {
    const f32x2 ep = exp2v(fma2(sub2(p, i), L2, bx)), en = exp2v(fma2(sub2(p, i), -L2, bn));
    o[i] = U2F(pk2(ep[0], ep[1])); o[8 + i] = U2F(pk2(en[0], en[1]));
  }
  return o;
}
struct SmS { f32x16 p, e; float en[16], bx, bn, sp, sn, tp, tn; bf16x8 w0, w1; };
DI bf16x8 sub4(const f32x16& v, int k) { f32x4 t = {v[4 * k], v[4 * k + 1], v[4 * k + 2], v[4 * k + 3]}; return __builtin_bit_cast(bf16x8, t); }

DI constexpr unsigned lc1_base(int ch) { return ch == 6 ? L_KV : ((ch & 1) ? L_GU : 0u) + (ch < 2 ? 49152u : ch < 4 ? 0u : 16384u); }
template <int CTRL, int RM> DI float dppadd(float v) { return v + U2F(__builtin_amdgcn_update_dpp(0u, F2U(v), CTRL, RM, 0xf, false)); }
DI float wavesum63(float v) {
  v = dppadd<0x111, 0xf>(v); v = dppadd<0x112, 0xf>(v); v = dppadd<0x114, 0xf>(v); v = dppadd<0x118, 0xf>(v);
  v = dppadd<0x142, 0xa>(v); return dppadd<0x143, 0xc>(v);
}

#define STAMP(i) do { if ((i) == 0 || (i) == ENC) { unsigned long long t_; asm volatile("s_memrealtime %0\n\ts_waitcnt lgkmcnt(0)" : "=s"(t_) :: "memory"); stv[(i) != 0] = (unsigned)t_; } } while (0)
#define CSTAMP(i)
#define STAMPS_OUT() do { STAMP(13); const unsigned lim_ = 3000u + ((stv[1] && (ENCAT ? AT : !AT)) ? stv[1] - stv[0] : 0u); while ((unsigned)__builtin_amdgcn_s_memrealtime() - stv[0] < lim_) __builtin_amdgcn_s_sleep(1); } while (0)

#define PDV 6
constexpr int PD = PDV, RING = 9;
constexpr int later2(int i, int NS, int GG) {
  const int younger = 2 * ((i + PD <= NS ? i + PD : NS) - 1 - i);
  int extra = i < PD ? 1 : 0;
  if (NS == 20 && (i == 10 || i == 11)) extra += 2 * (i - 9);
  return younger + extra;
}
DI constexpr unsigned ring_area(int j) { return j == 0 ? L_FREE : j == 1 ? L_PHI : lc1_base(j - 2); }
constexpr int cumG1(int s) { return s < 4 ? 0 : s < 22 ? (s - 4) / 2 : (9 + (s - 22) * 5 / 4 < 48 ? 9 + (s - 22) * 5 / 4 : 48); }

#define CODE_BYTES 65536
__global__ __launch_bounds__(512, 2) void fused_kernel(const char* px_, const char* pqkv_, const char* pwg_, const char* pwu_, char* ws_, const char* pphi_,
                                                       const char* pwd_, const char* pwo_, char* pout_) {
  typedef __attribute__((ext_vector_type(8))) unsigned long long u64x8;
  u64x8 ka; unsigned long long kb;
  asm volatile("s_load_dwordx16 %0, %2, 0\n\ts_load_dwordx2 %1, %2, 64" : "=&s"(ka), "=&s"(kb) : "s"(__builtin_amdgcn_kernarg_segment_ptr()));
  __shared__ __attribute__((aligned(16))) char smem[163840];
  const unsigned LB = (unsigned)(size_t)smem;
  const unsigned tid = __builtin_amdgcn_workitem_id_x(), lane = tid & 63;
  const unsigned w = __builtin_amdgcn_readfirstlane(tid >> 6), g = w >> 2, wr = w & 3;
  const unsigned b = __builtin_amdgcn_workgroup_id_x(), xcd = b & 7, bi = b >> 3, grp = xcd >> 1, half = xcd & 1, rbl = bi & 3, cp = bi >> 2, ci = cp - 1;
  const unsigned rb = 4 * grp + rbl, c = 7 * half + ci;
  const unsigned head = 8 * half + (((half ? 0x76425310u : 0x75316420u) >> (4 * cp)) & 7);
  const bool attn = cp == 0;
  unsigned stv[2] = {0, 0};
  STAMP(0); CSTAMP(14);
  const unsigned wstr = attn ? QROW : FROW;
  const unsigned sG0 = half * (attn ? 448 : 1792) + (attn ? 0 : ci * 256), sX0 = rb * 128 * XROW;
  unsigned vx0, vx1, vG, vU, vKV;
  auto voffs = [&](unsigned ln) AI {
    const unsigned r8 = ln >> 3, c8 = ln & 7, sa = (r8 >> 1) & 1, lc = c8 ^ (4 * sa);
    vx0 = (16 * w + r8) * XROW + ((c8 ^ (r8 >> 1)) << 4); vx1 = (vx0 ^ 64u) + 8 * XROW;
    const unsigned cg = attn ? lc : (c8 ^ (2 * sa)), cu = attn ? (lc >= 6 ? lc - 2 : lc) : (cg ^ 4);
    vG = (8 * w + r8) * wstr + (cg << 4); vU = (8 * w + r8) * wstr + (cu << 4);
    vKV = (8 * w + r8) * QROW + 896 * (1 + (lc >> 2)) + ((lc & 3) << 4) + ((56 * head) & ~15u);
  };
  voffs(lane);
  const unsigned lw1 = LB + w * 1024, lw2 = LB + w * 2048;
#define ROT (((0x24924u >> (2 * (cp + rbl))) & 3) * 2)
  const unsigned rot = ROT;
  const unsigned srow = 128 * rb + 64 * half + 8 * cp + w;
  unsigned vrow = lane < 56 ? lane * 16 : 0x80000000u;
  asm volatile("s_waitcnt lgkmcnt(0)" : "+s"(ka), "+s"(kb), "+v"(vx0), "+v"(vx1), "+v"(vG), "+v"(vU), "+v"(vKV), "+v"(vrow));
  const char *px = (const char*)ka[0], *pqkv = (const char*)ka[1], *pwg = (const char*)ka[2], *pwu = (const char*)ka[3], *pphi = (const char*)ka[5],
             *pwd = (const char*)ka[6], *pwo = (const char*)ka[7];
  char *ws = (char*)ka[4], *pout = (char*)kb;
  const rsrc_t rsX = mkrs(px, INSZ(2048 * XROW)), rsQ = mkrs(pqkv, INSZ(448 * QROW));
  const rsrc_t rsG = mkrs(attn ? pqkv : pwg, INSZ(448 * wstr)), rsU = mkrs(attn ? pqkv + 128 : pwu, INSZ(448 * wstr - (attn ? 128 : 0)));
  auto cidx = [&](int st) AI -> unsigned { unsigned t = rot + st; return t >= 7 ? t - 7 : t; };
  auto issueA = [&](int st, int k) AI {
    const unsigned sl = st & 3, ch = cidx(st);
    if (k == 0) dma<NTX>(lw2, sl * 16384, vx0, rsX, sX0 + ch * 128, 0);
    if (k == 1) dma<NTX>(lw2, sl * 16384 + 1024, vx1, rsX, sX0 + ch * 128, 0);
    if (k == 2) dma<NTW>(lw1, L_GU + sl * 16384, vG, rsG, sG0 + ch * 64 * wstr, 0);
    if (k == 3) dma<NTW>(lw1, L_GU + sl * 16384 + 8192, vU, rsU, sG0 + ch * 64 * wstr, 0);
    if (k == 4) dma<NTW>(lw1, L_KV + sl * 8192, vKV, rsQ, ch * 64 * QROW, 0);
  };
  const rsrc_t rsWS = mkrs(ws, WS_END);
  unsigned Ev, cpf; u32x4 xrw;
  asm volatile("buffer_load_dwordx4 %0, %1, %2, %3 offen" : "=v"(xrw) : "v"(vrow), "s"(rsX), "s"(srow * XROW) : "memory");
  asm volatile("buffer_load_dword %0, off, %1, 0 sc1" : "=v"(Ev) : "s"(rsWS) : "memory");
  {
    unsigned long long pc; asm volatile("s_getpc_b64 %0" : "=s"(pc));
    const unsigned vo = w == 0 && lane < 24 ? (bi * 24 + lane) * 128 : 0x80000000u;
    asm volatile("buffer_load_dword %0, %1, %2, 0 offen" : "=v"(cpf) : "v"(vo), "s"(mkrs((const void*)pc, CODE_BYTES)) : "memory");
  }
#pragma unroll
  for (int k = 0; k < 5; k++) issueA(0, k);
  asm volatile("s_waitcnt vmcnt(%c3)" : "+v"(xrw), "+v"(Ev), "+v"(cpf) : "i"(5 + COOP) : "memory");
#pragma unroll
  for (int k = 0; k < 5; k++) issueA(1, k);
  issueA(2, 0);
  const unsigned E = __builtin_amdgcn_readfirstlane(Ev);
  {
    float s1 = 0.f, s2 = 0.f;
#pragma unroll
    for (int i = 0; i < 4; i++) { s1 = dot2(xrw[i], 0x3f803f80u, s1); s2 = dot2(xrw[i], xrw[i], s2); }
    s1 = wavesum63(s1); s2 = wavesum63(s2);
    const float mean = s1 * (1.f / 448.f), var = __builtin_fmaxf(s2 * (1.f / 448.f) - mean * mean, 0.f);
    u32x2 gr = {pk2(-mean, __builtin_amdgcn_sqrtf(rbf(rbf(var) + 1e-5f))), E + 1};
    __builtin_amdgcn_raw_buffer_store_b64(gr, rsWS, lane == 63 ? 0u : 0x80000000u, WS_GR + srow * 8, 16);
  }

  auto prog = [&](auto at_, auto gg_) AI {
  constexpr bool AT = decltype(at_)::value; constexpr int GG = decltype(gg_)::value;
  constexpr unsigned lcstep = AT ? 224 : 128;
  unsigned lane;
  asm volatile("v_mbcnt_lo_u32_b32 %0, -1, 0\n\tv_mbcnt_hi_u32_b32 %0, -1, %0" : "=v"(lane));
  __builtin_assume(lane < 64);
  voffs(lane);
  auto issueL = [&](int hc) AI {
    const int st = hc >> 1; const unsigned ch = cidx(st);
    if (hc & 1) dma<NTW>(lw1, lc1_base(st) + 8192, vU, rsU, sG0 + ch * 64 * wstr, lcstep);
    else dma<NTW>(lw1, lc1_base(st), vG, rsG, sG0 + ch * 64 * wstr, lcstep);
  };
  auto issueG = [&](int bar, int k) AI {
    if (bar == 0) issueA(2 + k / 5, k % 5);
    else if (bar <= 3) issueA(bar + 3, k);
    else if (k < 4) issueL(4 * (bar - 4) + k);
    else if (bar == 4) dma(LB + (w & 1) * 1024, L_WPK, lane * 16, mkrs(pphi, 16 * 1568), head * 1568 + (w & 1) * 1024, 0);
    else issueL(12 + (bar - 5));
  };

  const unsigned rt = 4 * rb + wr;
  const unsigned sA2 = WS_A2 + rt * 144 * 1024 + (AT ? half * 16384 : c * 8192), vL16 = lane * 16;
  const u32x4 sent = {~0u, ~0u, ~0u, ~0u};
  auto clr = [&](int ks) AI { st16(sent, rsWS, CV, sA2, ks * 1024); };

  auto clri = [&](int q) AI { clr(GG ? (q < 6 ? 114 + q : 120 + q) : (q < 2 ? 112 + q : 118 + q)); };

  const bool hh = lane >= 32;
  const unsigned hu = lane >> 5, blk = (lane >> 4) & 1, q4 = (lane >> 2) & 3, p4 = lane & 3, ta = (q4 >> 1) & 1, l31 = lane & 31;
  const unsigned perm = wr ^ (2 * GG);
  const unsigned rowoff = (8 * hu + q4) * 128 + (perm << 11);
  const unsigned pc = (4 * blk + p4) < 6 ? (4 * blk + p4) : 6;
  auto tileaddr = [&](unsigned t) AI -> unsigned {
    if constexpr (AT) { unsigned o = 56 * t + 8 * pc; return (o >> 7) * 8192 + ((((o >> 4) & 7) ^ (4 * ta)) << 4) + (o & 8) + rowoff; }
    else return blk * 8192 + (((2 * t + (p4 >> 1)) ^ (4 * blk + 2 * ta)) << 4) + 8 * (p4 & 1) + rowoff;
  };
  constexpr int NT = !AT ? 2 : GG ? 1 : 3, TB0 = GG ? 4 - NT : 0;
  unsigned aA[3], aB[NT];
  {
    unsigned o = (head & 1) * 8 + 8 * pc;
    unsigned ak = L_KV + (((o >> 4) ^ (4 * ta)) << 4) + (o & 8) + rowoff;
    aA[0] = LB + (GG ? L_GU + tileaddr(1) : ak);
    aA[1] = LB + (GG ? L_GU + tileaddr(2) : (ak ^ 64u));
    aA[2] = LB + L_GU + tileaddr(GG ? 3 : 0);
#pragma unroll
    for (int j = 0; j < NT; j++) aB[j] = LB + tileaddr(TB0 + j);
  }
  const unsigned xrow = 32 * wr + l31;
  const unsigned aX = LB + xrow * 128 + (((2 * perm + hu) ^ ((xrow >> 1) & 7)) << 4);
  const unsigned selv = (((lane >> 4) ^ (lane >> 2)) & 1) ? 0u : 0x3f803f80u;
  const u32x4 sel4 = {selv, selv, selv, selv};
  const bf16x8 sel = __builtin_bit_cast(bf16x8, sel4);

  bf16x8 xf[28], wa[2][3], wq[8];
  f32x16 accA[3], accB[NT];
  f32x4 csA[3], csB[NT];
#pragma unroll
  for (int j = 0; j < 3; j++) { for (int i = 0; i < 16; i++) accA[j][i] = 0.f; for (int i = 0; i < 4; i++) csA[j][i] = 0.f; }
#pragma unroll
  for (int j = 0; j < NT; j++) { for (int i = 0; i < 16; i++) accB[j][i] = 0.f; for (int i = 0; i < 4; i++) csB[j][i] = 0.f; }
  u32x2 grn;

  const rsrc_t rsWD = mkrs(pwd, INSZ(1792 * XROW)), rsWO = mkrs(pwo, INSZ(448 * XROW));
  const unsigned vW2 = (lane >> 3) * XROW + (((lane & 7) ^ ((lane >> 2) & 4)) << 4) + ci * 128;
  constexpr int NS = GG ? 16 + XS : 20 - XS, NC = 6 - XS / 2, NO = 2 * NC;
#define RHO ((rbl + cp) & 3)
  const unsigned rho = RHO;
  auto t_of = [](int i) constexpr { return (0x54763210u >> (4 * ((i >> 1) & 7))) & 7; };
  constexpr int NE = GG ? 9 : 5;
  auto g1i = [](int i) constexpr { return i < 8 ? i : i == 8 ? 16 : i < 17 ? i - 1 : 17; };
  auto ksb = [&](int i) AI -> unsigned {
    if (GG) i = g1i(i);
    if (GG) return i < 16 ? 16 * wr + ((t_of(i) & 3) ^ rho) : wr;
    if (i < NO) { const unsigned cc = i % NC + rho + (XS ? 0 : rho >> 1); return wr + 8 * (cc >= NC ? cc - NC : cc); }
    return 8 * wr + 2 * (((i - NO) >> 1) ^ rho);
  };
  auto ksi = [&](int i) AI -> unsigned { if (GG) i = g1i(i); return GG ? (i < 16 ? 8 * (i & 1) + (t_of(i) & 4) : 4 * i) : i < NO ? 64 + 4 * XS + 4 * (i / NC) : 112 + (i & 1); };
  auto issueW2 = [&](int i, int hf) AI {
    if (GG == 0 && i >= NO) dma<NT2>(lw2, ring_area(i % RING) + hf * 1024, vW2, rsWO, ksb(i) * 14 * XROW, (16 * (i & 1) + 8 * hf) * XROW);
    else dma<NT2>(lw2, ring_area(i % RING) + hf * 1024, vW2, rsWD, ksb(i) * 16 * XROW, (ksi(i) * 16 + 8 * hf) * XROW);
  };

  const unsigned aCSw = LB + L_CS + l31 * 16 + wr * 4 + GG * 2048, aCSr = LB + L_CS + l31 * 16 + GG * 2048;
  auto readX = [&](int n) AI { xf[n] = ldsr<bf16x8>(aX ^ (unsigned)((n & 3) << 5), ((n >> 2) & 3) * 16384); };
  auto readA = [&](int n, int j) AI {
    const unsigned o = ((n >> 2) & 3) * ((GG == 0 && j < 2) ? 8192 : 16384);
    wa[n & 1][j] = tr2(aA[j] ^ (unsigned)((n & 3) << 11), o, o + 512);
  };
  auto readB = [&](int q) AI {
    const int n = q / NT, j = q % NT;
    const unsigned base = lc1_base(n >> 2);
    if ((NT == 3 && n < XN) ? (n & 3) == 0 : n == 24) asm volatile("" : "+v"(aB[j]));
    wq[q & 7] = tr2(aB[j] ^ (((n & 3) << 11) | (base & 0x30000u)), base & 0xffffu, (base & 0xffffu) + 512);
  };
  RowC rc; unsigned nm, denb;
  auto mkxe = [&]() AI { asm volatile("" : "+v"(nm)); u32x4 t = {nm, nm, nm, nm}; return __builtin_bit_cast(bf16x8, t); };
  auto rowconst = [&]() AI {
#pragma nounroll
    for (int it = 0; it < (1 << 16) && !ALL(grn[1] == E + 1); ++it) {
      __builtin_amdgcn_s_sleep(SLEEPN);
      asm volatile("buffer_load_dwordx2 %0, %1, %2, %3 offen sc1\n\ts_waitcnt vmcnt(0)" : "=v"(grn) : "v"(xrow * 8), "s"(rsWS), "s"(WS_GR + rb * 1024) : "memory");
    }
    const float den = U2F(grn[0] & 0xffff0000u);
    nm = hh ? 0u : __builtin_amdgcn_perm(grn[0], grn[0], 0x01000100u);
    denb = grn[0] >> 16;
    rc.r = __builtin_amdgcn_rcpf(den);
    const float k1 = -2.f * 1.4426950408889634f * 0.7978845608028654f;
    rc.K1 = k1 * rc.r; rc.K2 = (k1 * 0.044715f) * rc.r * (rc.r * rc.r); rc.Ri = den * den; rc.Lr = 1.4426950408889634f * rc.r;
  };
  waitv<7 + COOP>();
  BAR(); MEMFENCE();
  STAMP(1);
  readX(0); readA(0, 0); readA(0, 1); readA(0, 2);
  SFOR(n, 28)
    SB();
    if constexpr (n == 27) {
#pragma unroll
      for (int j = 0; j < 3; j++) ldsw<unsigned>(aCSw, j * 512, hilo(csA[j][0]));
    }
    if constexpr ((n & 3) == 3) {
      if constexpr (n == 27) WAITV(0);
      else if constexpr (n == 23) asm volatile("s_waitcnt vmcnt(10)" : "+v"(grn) :: "memory");
      else if constexpr (n == 3 || n == 15 || n == 19) WAITV(11); else WAITV(10);
      { WAITL0(); BAR(); MEMFENCE(); }
      if constexpr (n == 27) STAMP(2);
    }
    constexpr int bar = (n + 1) >> 2, ph = (n + 1) & 3;
#pragma unroll
    for (int j = 0; j < 3; j++) {
      if constexpr (n < 27) { if (j == 0) readX(n + 1); readA(n + 1, j); }
      else { wa[0][j] = ldsr<bf16x8>(aCSr, j * 512); if (AT && GG == 1 && j < 2) { readB(2 * j); readB(2 * j + 1); } }
      SB();
      accA[j] = MFMA(wa[n & 1][j], xf[n], accA[j]);
      const int slot = 3 * ph + j;
      if (bar == 0) issueG(0, slot - 2);
      else if (bar == 6) { if (slot < 5) issueG(bar, slot); }
      else if (bar <= 6) { if (slot == 0) issueG(bar, 0); if (slot == 2) issueG(bar, 1); if (slot == 5) issueG(bar, 2); if (slot == 7) issueG(bar, 3); if (slot == 10) issueG(bar, 4); }
      else if (j < 2) {
        if constexpr (AT) dma(lw2, L_FREE + j * 1024, lane * 16, mkrs(pphi, 16 * 1568), half * 8 * 1568 + w * 2048, j * 1024);
        else issueW2(0, j);
      }
      if (!NOCS && (n & 3) == 0) csA[j] = MFMA16(sel, wa[n & 1][j], csA[j]);
      if (n == 25 && j == 0) rowconst();
      if (n == 14 && j == 2) asm volatile("buffer_load_dwordx2 %0, %1, %2, %3 offen sc1" : "=v"(grn) : "v"(xrow * 8), "s"(rsWS), "s"(WS_GR + rb * 1024) : "memory");
      SB();
    }
  SEND
  { const bf16x8 xe = mkxe();
#pragma unroll
  for (int j = 0; j < 3; j++) accA[j] = MFMA(wa[0][j], xe, accA[j]); }
  asm volatile("" : "+v"(accA[0]), "+v"(accA[1]), "+v"(accA[2]));
  SB();

  const unsigned sFown = WS_FH + (rt * 32 + (AT ? 28 + 2 * half : 2 * c) + GG) * FS;
  const unsigned aWP = LB + (4 * hu + q4) * 56 + 8 * pc;
  const unsigned T12 = (E + 1) & 0xfffu, T16 = (E + 1) & 0xffffu;
  const unsigned rt2 = 2 * (8 * grp + 4 * half + rbl);
  const unsigned sF = WS_FH + rt2 * 32 * FS;
  const unsigned fc = l31 >> 1;
  const bool nd1 = GG ? (fc >> 1) == wr || (fc >= 8 && fc < 8 + XS / 2) : (fc >= 8 + XS / 2 && fc < 14) || fc == 14 + (wr >> 1);
  const unsigned aIW = LB + L_PHI + xrow * 64 + ((hu ^ ((xrow >> 1) & 7)) << 3);
  SmS sm;
  unsigned aWK = aWP + L_WPK, aWQ = aWP + L_FREE;
  asm volatile("" : "+v"(aWK)); if constexpr (AT) asm volatile("" : "+v"(aWQ));
  auto kstep = [&](auto s_) AI {
    constexpr int s = decltype(s_)::value;
    if constexpr (s == 0) { sm.w0 = tr2(aWK, 0, 8 * 56); sm.w1 = tr2(aWK, 16 * 56, 24 * 56); }
    if constexpr (s == 1) sm.p = featproj2(accA[0], sm.w0, sm.w1, hh);
    if constexpr (s == 2 || s == 3) {
#pragma unroll
      for (int k = 2 * s - 4; k < 2 * s - 2; k++) {
        u32x2 vv = {pk2(accA[1][4 * k], accA[1][4 * k + 1]), pk2(accA[1][4 * k + 2], accA[1][4 * k + 3])};
        if (k == 3) vv[0] = hh ? denb : vv[0];
        ldsw<u32x2>(aIW ^ (unsigned)(k << 4), L_V - L_PHI, vv);
      }
    }
    if constexpr (s == 4) sm.bx = -max16(sm.p) * rc.Lr;
    if constexpr (s == 5) sm.bn = min16(sm.p) * rc.Lr;
    if constexpr (s >= 6 && s < 22) {
      constexpr int i = s - 6;
      sm.e[i] = __builtin_amdgcn_exp2f(__builtin_fmaf(sm.p[i], rc.Lr, sm.bx)); sm.en[i] = __builtin_amdgcn_exp2f(__builtin_fmaf(sm.p[i], -rc.Lr, sm.bn));
      if constexpr (i == 0) { sm.sp = sm.e[0]; sm.sn = sm.en[0]; } else if constexpr (i < 12) { sm.sp += sm.e[i]; sm.sn += sm.en[i]; }
      else if constexpr (i == 12) { sm.tp = sm.e[12]; sm.tn = sm.en[12]; } else { sm.tp += sm.e[i]; sm.tn += sm.en[i]; }
    }
    if constexpr (s == 22) { sm.sp = __builtin_amdgcn_rcpf(xsum(sm.sp + (hh ? 0.f : sm.tp))) * rc.r; sm.sn = __builtin_amdgcn_rcpf(xsum(sm.sn + (hh ? 0.f : sm.tn))) * rc.r; }
    if constexpr (s >= 23) {
#pragma unroll
      for (int u = (s == 29 ? 6 : s - 23); u <= (s == 29 ? 7 : s - 23); u++) {
        const int k = u >> 1;
        if (u & 1) { u32x2 t = {pk2s(sm.en[4 * k] * sm.sn, sm.en[4 * k + 1] * sm.sn), pk2s(sm.en[4 * k + 2] * sm.sn, sm.en[4 * k + 3] * sm.sn)}; ldsw<u32x2>(aIW ^ (unsigned)(k << 4), 8192, t); }
        else { u32x2 t = {pk2s(sm.e[4 * k] * sm.sp, sm.e[4 * k + 1] * sm.sp), pk2s(sm.e[4 * k + 2] * sm.sp, sm.e[4 * k + 3] * sm.sp)}; ldsw<u32x2>(aIW ^ (unsigned)(k << 4), 0, t); }
      }
    }
  };
  auto qstep = [&](auto s_, f32x16& a, unsigned lh) AI {
    constexpr int s = decltype(s_)::value;
    if constexpr (s == 0) { sm.w0 = tr2(aWQ, lh * 1568, lh * 1568 + 8 * 56); sm.w1 = tr2(aWQ, lh * 1568 + 16 * 56, lh * 1568 + 24 * 56); }
    if constexpr (s == 1) sm.p = featproj2(a, sm.w0, sm.w1, hh);
    if constexpr (s == 3) sm.bx = -max16(sm.p) * rc.Lr;
    if constexpr (s == 4) sm.bn = min16(sm.p) * rc.Lr;
    if constexpr (s >= 5 && s < 21) {
      constexpr int i = (s - 5) >> 1;
      float t0, t1;
      if constexpr ((s - 5) & 1) { t0 = __builtin_fmaf(sm.p[2 * i], -rc.Lr, sm.bn); t1 = __builtin_fmaf(sm.p[2 * i + 1], -rc.Lr, sm.bn); }
      else { t0 = __builtin_fmaf(sm.p[2 * i], rc.Lr, sm.bx); t1 = __builtin_fmaf(sm.p[2 * i + 1], rc.Lr, sm.bx); }
      a[8 * ((s - 5) & 1) + i] = U2F(pk2(__builtin_amdgcn_exp2f(t0), __builtin_amdgcn_exp2f(t1)));
      if constexpr (s == 20) asm volatile("" : "+v"(a));
    }
  };
  bf16x8 ka_[4], kb_[2][4]; f32x4 kd[2];
  auto kvstep = [&](auto s_) AI {
    constexpr int s = decltype(s_)::value;
    if constexpr (s == 0) {
      const unsigned g4 = lane >> 4, r0 = 8 * g4 + q4, sw = 4 * blk + (q4 >> 1);
      unsigned b0 = LB + L_PHI + r0 * 64 + ((p4 ^ sw) << 3);
      unsigned a0 = (b0 ^ ((wr >> 1) << 5)) + (wr & 1) * 8192;
#pragma unroll
      for (int k = 0; k < 4; k++) {
        ka_[k] = __builtin_shufflevector(trd2(a0, k * 2048), trd2(a0 ^ 16u, k * 2048 + 256), 0, 1, 2, 3, 4, 5, 6, 7);
        kb_[0][k] = __builtin_shufflevector(trd2(b0, L_V - L_PHI + k * 2048), trd2(b0 ^ 16u, L_V - L_PHI + k * 2048 + 256), 0, 1, 2, 3, 4, 5, 6, 7);
        kb_[1][k] = __builtin_shufflevector(trd2(b0 ^ 32u, L_V - L_PHI + k * 2048), trd2(b0 ^ 48u, L_V - L_PHI + k * 2048 + 256), 0, 1, 2, 3, 4, 5, 6, 7);
      }
    }
    if constexpr (s == 1) {
      f32x4 z = {0.f, 0.f, 0.f, 0.f}; kd[0] = z; kd[1] = z;
#pragma unroll
      for (int k = 0; k < 4; k++) { kd[0] = MFMA16(ka_[k], kb_[0][k], kd[0]); kd[1] = MFMA16(ka_[k], kb_[1][k], kd[1]); }
    }
    if constexpr (s == 3) {
#pragma unroll
      for (int d = 0; d < 2; d++) {
        u32x4 t;
#pragma unroll
        for (int i = 0; i < 4; i++) t[i] = (F2U(kd[d][i]) & ~0xfffu) | T12;
        st16(t, rsWS, (lane >> 4) * 512 + (lane & 15) * 16, ((head * 16 + rb) * 2 + (wr & 1)) * 4096 + (wr >> 1) * 2048, WS_KVP + d * 256);
      }
    }
  };
  auto qexp2 = [&](f32x16& a0, unsigned l0, f32x16& a1, unsigned l1) AI {
    const f32x16 p0 = featproj(a0, aWQ, l0 * 1568, hh), p1 = featproj(a1, aWQ, l1 * 1568, hh);
    a0 = dualexpP(p0, rc.Lr); a1 = dualexpP(p1, rc.Lr); asm volatile("" : "+v"(a0), "+v"(a1));
  };

  unsigned fl1 = 0;
  EpiS es;
  u32x4 pv[8];
  const unsigned vKP = l31 * 16 + hu * 8192, sKP = (rb * 32 + half) * 4096 + wr * 1024 + GG * 512, sKR = (8 * half + w) * 8192;
  auto hop1 = [&]() AI {
    waittag<16384>(pv, rsWS, vKP, sKP + WS_KVP, T12, 0xfffu);
    u32x4 o;
#pragma unroll
    for (int k = 0; k < 4; k++) {
      float sum = U2F(pv[0][k]);
#pragma unroll
      for (int i = 1; i < 8; i++) sum += U2F(pv[i][k]);
      sum = xsum(sum);
      sum = l31 == 29 ? 1.f : sum;
      if (GG == 1) sum = wr == 3 ? 0.f : sum;
      o[k] = (pk2s(sum, sum) & 0xffff0000u) | T16;
    }
    st16(o, rsWS, hh ? 0x80000000u : l31 * 16, (rb * 2 + half) * 4096 + wr * 1024 + GG * 512, WS_KVR);
    if (b == 0 && w == 4) stflag(E + 4, rsWS, WS_EPOCH);
    SB();
  };
  bf16x8 cb[NT];
  auto ldflag = [&]() AI { asm volatile("buffer_load_dword %0, %1, %2, %3 offen sc1" : "=v"(fl1) : "v"(nd1 ? lane * FS : 0x80000000u), "s"(rsWS), "s"(sF) : "memory"); };
  auto touch = [&](unsigned wo4) AI {
    const unsigned rw = (rbl * 8 + w + wo4) * 44 + lane;
    const bool dn = rw < 896;
    const unsigned vo = lane < 44 ? (dn ? ((rw >> 6) * 128 + 64 + (rw & 63)) : rw - 896) * XROW + ci * 128 : 0x80000000u;
    asm volatile("s_add_u32 m0, %0, %c5\n\ts_nop 0\n\tbuffer_load_dword %1, %3, 0 offen lds\n\tbuffer_load_dword %2, %4, 0 offen lds"
                 :: "s"(LB + w * 256), "v"(dn ? vo : 0x80000000u), "v"(dn ? 0x80000000u : vo), "s"(rsWD), "s"(rsWO), "i"(L_ST) : "memory", "scc");
  };
  auto kpath = [&]() AI {
    const f32x16 p = featproj(accA[0], aWK, 0, hh);
#pragma unroll
    for (int k = 0; k < 4; k++) {
      u32x2 vv = {pk2(accA[1][4 * k], accA[1][4 * k + 1]), pk2(accA[1][4 * k + 2], accA[1][4 * k + 3])};
      if (k == 3) vv[0] = hh ? denb : vv[0];
      ldsw<u32x2>(aIW ^ (unsigned)(k << 4), L_V - L_PHI, vv);
    }
    const f32x2 bx = dup2(-max16(p) * rc.Lr), bn = dup2(min16(p) * rc.Lr), L2 = dup2(rc.Lr);
    f32x2 ep[8], en[8];
#pragma unroll
    for (int i = 0; i < 8; i++) { ep[i] = exp2v(fma2(sub2(p, i), L2, bx)); en[i] = exp2v(fma2(sub2(p, i), -L2, bn)); }
    f32x2 sp = ((ep[0] + ep[1]) + (ep[2] + ep[3])) + (ep[4] + ep[5]), sn = ((en[0] + en[1]) + (en[2] + en[3])) + (en[4] + en[5]);
    f32x2 tp = ep[6] + ep[7], tn = en[6] + en[7];
    float a0 = sp[0], a1 = sn[0], a2 = tp[0], a3 = tn[0];
    asm("" : "+v"(a0), "+v"(a1), "+v"(a2), "+v"(a3));
    a0 += sp[1]; a2 += tp[1]; asm("" : "+v"(a0), "+v"(a2)); a1 += sn[1]; a3 += tn[1];
    const f32x2 ip = dup2(__builtin_amdgcn_rcpf(xsum(a0 + (hh ? 0.f : a2))) * rc.r), in = dup2(__builtin_amdgcn_rcpf(xsum(a1 + (hh ? 0.f : a3))) * rc.r);
#pragma unroll
    for (int k = 0; k < 4; k++) {
      const f32x2 u0 = ep[2 * k] * ip, u1 = ep[2 * k + 1] * ip, v0 = en[2 * k] * in, v1 = en[2 * k + 1] * in;
      u32x2 t0 = {pk2(u0[0], u0[1]), pk2(u1[0], u1[1])}, t1 = {pk2(v0[0], v0[1]), pk2(v1[0], v1[1])};
      ldsw<u32x2>(aIW ^ (unsigned)(k << 4), 0, t0); ldsw<u32x2>(aIW ^ (unsigned)(k << 4), 8192, t1);
    }
  };
  const unsigned sA = WS_A2 + rt2 * 144 * 1024;
  u32x4 af[NS][2];
  auto loadA1 = [&](int i, int m) AI { af[i][m] = ld16(rsWS, vL16, sA + ksb(i) * 1024, (144 * m + ksi(i)) * 1024); };
  auto stream = [&]() AI {
    SFOR(q, 28 * NT)
      constexpr int n = q / NT, j = q % NT;
      if constexpr (j == 0) {
        if constexpr (n == 27) {
#pragma unroll
          for (int t = 0; t < NT; t++) ldsw<unsigned>(aCSw, ((3 + t) & 3) * 512, hilo(csB[t][0]));
          if constexpr (AT && GG) WAITV(8);
          WAITL0(); BAR(); MEMFENCE();
        }
        else if constexpr (!AT && (n & 3) == 3) { asm volatile("s_waitcnt lgkmcnt(4)" ::: "memory"); BAR(); MEMFENCE(); }
      }
      if constexpr (q + 4 < 28 * NT) readB(q + 4);
      if constexpr (n == 27) cb[j] = ldsr<bf16x8>(aCSr, ((3 + j) & 3) * 512);
      SB();
      accB[j] = MFMA(wq[q & 7], xf[n], accB[j]);
      if constexpr (!NOCS && (n & 3) == 1) csB[j] = MFMA16(sel, wq[q & 7], csB[j]);
      if constexpr (!AT) {
        if constexpr (j == 0 && n >= 3 && n < 7) issueW2(1 + ((n - 3) >> 1), (n - 3) & 1);
        if constexpr (j == 0 && n >= 7 && n < 27 && (n & 1)) issueW2(1 + ((n + 1) >> 2), ((n + 1) >> 1) & 1);
        if constexpr (n == 27) issueW2(8, j);
        if constexpr (j == 1 && n == F1N) { waitv<(F1N < 7 ? F1N - 2 : 4 + (F1N - 5) / 2) + (TN < F1N ? 2 : 0)>(); stflag(E + 1, rsWS, sFown); }
        if constexpr (j == 1 && n == TN) touch(0);
        if constexpr (q == 2 * CK1 - 7) ldflag();
        if constexpr (q == 2 * CK1 - 1) {
          asm volatile("s_waitcnt vmcnt(%c5)" : "+v"(fl1), "+v"(accB[0]), "+v"(accB[NT - 1]), "+v"(csB[0]), "+v"(csB[NT - 1]) : "i"((CK1 & 1) ? 1 : 2) : "memory");
          const bool nde = nd1 && fc < 14;
          if (!ALL(!nde || (fl1 - E - 1u) <= 1u)) poll(rsWS, lane * FS, sF, E, nde ? 1u : 0u);
        }
        if constexpr (q >= 2 * CK1 && q < 2 * CK1 + 2 * NE) loadA1((q - 2 * CK1) >> 1, q & 1);
      } else {
        if constexpr (GG == 1 && n < 16 && (n & 1) == 0) clri(n >> 1);
        if constexpr (GG == 0 && j < 2 && n >= 1 && 2 * (n - 1) + j < 21) qstep(IntC<2 * (n - 1) + j>{}, accA[2], 0);
        if constexpr (GG == 0 && q >= 3 * H0L && q < 3 * H0L + 8) pv[q - 3 * H0L] = ld16(rsWS, vKP, sKP, WS_KVP + (q - 3 * H0L) * 16384);
        if constexpr (GG == 0 && j == 2 && n == H0C) { asm volatile("" : "+v"(accB[0]), "+v"(accB[1]), "+v"(accB[NT - 1]), "+v"(csB[0]), "+v"(csB[1]), "+v"(csB[NT - 1])); STAMP(11); hop1(); STAMP(6); }
      }
      SB();
    SEND
    const bf16x8 xe = mkxe();
#pragma unroll
    for (int t = 0; t < NT; t++) { accB[t] = MFMA(cb[t], xe, accB[t]); asm volatile("" : "+v"(accB[t])); }
    SB();
  };
  unsigned ln = vL16, aW = 0;
  auto mkaw = [&]() AI {
    asm volatile("" : "+v"(ln));
    aW = lw2 + (ln & 0x200) + ((ln & 0xc0) << 1) + ((((ln >> 3) & 0x20) | ((ln >> 1) & 0x10)) ^ ((ln >> 1) & 0x40)) + ((ln >> 1) & 8);
  };
  bf16x8 wf[NS][2];
  auto readW = [&](int i) AI {
    const unsigned ra = ring_area(i % RING), la = aW + (ra & 0x30000u);
    wf[i][0] = tr2(la, ra & 0xffffu, (ra & 0xffffu) + 1024); wf[i][1] = tr2(la ^ 64u, ra & 0xffffu, (ra & 0xffffu) + 1024);
  };
  auto tile = [&](f32x16& a, int t) AI { asm volatile("" : "+v"(a)); st16(epiP(a, rc), rsWS, vL16, sA2, t * 1024); SB(); };
  if constexpr (!AT) {
    if constexpr (GG == 0) {
      kpath(); WAITL0(); SB(); BAR(); SB(); STAMP(3);
      kvstep(IntC<0>{}); kvstep(IntC<1>{}); kvstep(IntC<3>{}); SB();
      tile(accA[2], 0);
    } else { tile(accA[0], 1); if (T2B) tile(accA[1], 2); BAR(); SB(); STAMP(3); if (!T2B) tile(accA[1], 2); tile(accA[2], 3); }
    readB(0); readB(1); readB(2); readB(3);
    STAMP(4);
    stream();
    SFOR(u, (2 * CK1 + 2 * NE > 56 ? 2 * CK1 + 2 * NE - 56 : 0)) loadA1((56 + u - 2 * CK1) >> 1, u & 1); SEND
    STAMP(5);
    mkaw();
    SFOR(i, RC) readW(i); SEND
    if constexpr (RC > 0) { WAITL0(); SB(); SFOR(i, RC) issueW2(9 + i, 0); issueW2(9 + i, 1); SEND }
    tile(accB[0], 4 + 2 * GG); tile(accB[NT - 1], 5 + 2 * GG);
  } else if constexpr (GG == 0) {
    SFOR(q, 8) clri(q); SEND
    SFOR(s, 30) kstep(s_); SEND
    WAITV(8);
    WAITL0(); SB(); BAR(); SB();
    STAMP(3);
    kvstep(IntC<0>{});
    readB(0); readB(1); readB(2); readB(3);
    kvstep(IntC<1>{}); kvstep(IntC<3>{});
    WAITV(2); stflag(E + 1, rsWS, sFown);
    STAMP(4);
    SB();
    stream();
  } else {
    stream();
    STAMP(3);
    SFOR(s, 21) qstep(s_, accA[0], 1); SEND
    WAITV(0); stflag(E + 1, rsWS, sFown);
    SFOR(s, 21) qstep(s_, accA[1], 2); SEND
    SFOR(s, 21)
      if constexpr (s < 8) pv[s] = ld16(rsWS, vKP, sKP, WS_KVP + s * 16384);
      qstep(s_, accA[2], 3);
    SEND
    STAMP(4); hop1(); STAMP(6);
    SB(); BAR(); SB();
  }

  if constexpr (AT) {
    STAMP(5);
    auto qexp = [&](f32x16& a, unsigned lh) AI { a = dualexpP(featproj(a, aWQ, lh * 1568, hh), rc.Lr); asm volatile("" : "+v"(a)); SB(); };
    if constexpr (GG == 0) qexp(accB[0], 4);
#pragma unroll
    for (int i = 0; i < 8; i++) pv[i] = ld16(rsWS, vL16, sKR, WS_KVR + i * 1024);
    if constexpr (GG == 0) { qexp(accB[1 % NT], 5); qexp(accB[NT - 1], 6); } else qexp(accB[0], 7);
    STAMP(7);
    waittag<1024>(pv, rsWS, vL16, sKR + WS_KVR, T16, 0xffffu);
    STAMP(8);
#pragma unroll
    for (int i = 0; i < 4; i++) {
      u32x4 f = {__builtin_amdgcn_perm(pv[2 * i][1], pv[2 * i][0], 0x07060302u), __builtin_amdgcn_perm(pv[2 * i][3], pv[2 * i][2], 0x07060302u),
                 __builtin_amdgcn_perm(pv[2 * i + 1][1], pv[2 * i + 1][0], 0x07060302u), __builtin_amdgcn_perm(pv[2 * i + 1][3], pv[2 * i + 1][2], 0x07060302u)};
      ldsw<u32x4>(LB + w * 4096 + vL16, i * 1024, f);
    }
    WAITL0(); BAR(); MEMFENCE();
    STAMP(10);
    f32x16 op[4], on[4];
    auto omm = [&](const f32x16& phi, unsigned lh, int z) AI {
#pragma unroll
      for (int i = 0; i < 16; i++) { op[z][i] = 0.f; on[z][i] = 0.f; }
      bf16x8 kf[4];
#pragma unroll
      for (int i = 0; i < 4; i++) kf[i] = ldsr<bf16x8>(LB + vL16, lh * 4096 + i * 1024);
#pragma unroll
      for (int i = 0; i < 2; i++) { op[z] = MFMA(kf[i], sub4(phi, i), op[z]); on[z] = MFMA(kf[2 + i], sub4(phi, 2 + i), on[z]); }
    };
    auto ofin = [&](unsigned lh, int z) AI {
      const float ia = __builtin_amdgcn_rcpf(op[z][13]), ib = __builtin_amdgcn_rcpf(on[z][13]);
      const float id = __builtin_amdgcn_rcpf(__builtin_fmaf(op[z][12], ia, __builtin_fmaf(on[z][12], ib, 1e-6f)));
      const float ca = xhi(ia * id), cb2 = xhi(ib * id);
      const f32x2 c2 = dup2(ca), d2 = dup2(cb2);
      u32x4 f0, f1;
#pragma unroll
      for (int i = 0; i < 4; i++) {
        const f32x2 u = fma2(sub2(op[z], i), c2, sub2(on[z], i) * d2), v = fma2(sub2(op[z], 4 + i), c2, sub2(on[z], 4 + i) * d2);
        f0[i] = pk2(u[0], u[1]); f1[i] = pk2(v[0], v[1]);
      }
      f1[2] = hh ? 0u : f1[2]; f1[3] = hh ? 0u : f1[3];
      st16(f0, rsWS, OV, sA2, (112 + 2 * lh) * 1024); st16(f1, rsWS, OV, sA2, (113 + 2 * lh) * 1024);
      SB();
    };
    if constexpr (GG) { omm(accA[0], 1, 0); omm(accA[1], 2, 1); SB(); ofin(1, 0); omm(accA[2], 3, 2); omm(accB[0], 7, 3); SB(); ofin(2, 1); ofin(3, 2); ofin(7, 3); }
    else { omm(accA[2], 0, 0); omm(accB[0], 4, 1); SB(); ofin(0, 0); omm(accB[1 % NT], 5, 2); omm(accB[NT - 1], 6, 3); SB(); ofin(4, 1); ofin(5, 2); ofin(6, 3); }
    STAMP(9);
  } else {
    f32x16 acc[2][2];
#pragma unroll
    for (int i = 0; i < 4; i++)
#pragma unroll
      for (int k = 0; k < 16; k++) acc[i >> 1][i & 1][k] = 0.f;
    u32x4 xr;
    const unsigned vO = (ln >> 7) * XROW + (ln & 0x70), sO = (rt2 * 32 + 8 * w) * XROW + ci * 128;
    auto mm4 = [&](int i) AI {
#pragma unroll
      for (int nn = 0; nn < 2; nn++)
#pragma unroll
        for (int mm = 0; mm < 2; mm++) acc[nn][mm] = MFMA(wf[i][nn], __builtin_bit_cast(bf16x8, af[i][mm]), acc[nn][mm]);
      SB();
    };
    STAMP(6);
    waitv<2 + 2 * RC>();
#pragma unroll
    for (int i = 0; i < NE; i++) asm volatile("" : "+v"(af[i][0]), "+v"(af[i][1]));
    SFOR(i, 9 - RC) readW(RC + i); SEND
    SB();
    SFOR(i, EB) mm4(i); SEND
    WAITV(0); stflag(E + 2, rsWS, sFown);
    WAITL0(); SB();
    BAR();
    SB(); STAMP(11);
    if constexpr (GG) __builtin_amdgcn_s_setprio(P2G1);
    constexpr int NB = (GG ? 9 : G0B) - RC;
    SFOR(i, 9)
      if constexpr (i == NB - 2) ldflag();
      if constexpr (i < NB) { issueW2(9 + RC + i, 0); issueW2(9 + RC + i, 1); SB(); }
      if constexpr (EB + i < NE) mm4(EB + i);
    SEND
    asm volatile("s_waitcnt vmcnt(4)" : "+v"(fl1) :: "memory");
    { const unsigned need = !nd1 ? 0u : fc < 14 ? 2u : 1u;
      if (!ALL(!nd1 || fl1 == E + need)) poll(rsWS, lane * FS, sF, E, need); }
    STAMP(7);
#pragma unroll
    for (int j = NE; j < NE + PD; j++) { loadA1(j, 0); loadA1(j, 1); }
    asm volatile("buffer_load_dwordx4 %0, %1, %2, %3 offen" : "=v"(xr) : "v"(vO), "s"(rsX), "s"(sO) : "memory");
    SFOR(i, 9 - RC - NB) issueW2(9 + RC + NB + i, 0); issueW2(9 + RC + NB + i, 1); SEND
    SFOR(i1, NS - NE)
      constexpr int i = NE + i1;
      asm volatile("s_waitcnt vmcnt(%c2)" : "+v"(af[i][0]), "+v"(af[i][1]) : "i"(2 * ((i + PD <= NS ? i + PD : NS) - 1 - i) + (i1 < PD && i < NO + 8 * GG ? 1 + 2 * (9 - RC - NB) : 0)) : "memory");
      if constexpr (i == 9 && i1 == 0) readW(9);
      if constexpr (i + 1 >= 9 && i + 1 < NS) readW(i + 1);
      if constexpr (GG == 0 && i >= NO) {
        auto stale = [&]() AI {
          unsigned m = UMAX(UMAX(UMAX(af[i][0][0], af[i][0][1]), UMAX(af[i][0][2], af[i][0][3])), UMAX(UMAX(af[i][1][0], af[i][1][1]), UMAX(af[i][1][2], af[i][1][3])));
          return ANY(m == ~0u);
        };
        if (__builtin_expect(stale(), 0)) {
          int it = 0;
#pragma nounroll
          do { __builtin_amdgcn_s_sleep(SLEEPR); rel1<true>(af[i][0], af[i][1], rsWS, vL16, sA + (ksb(i) + ksi(i)) * 1024); } while (++it < (1 << 14) && stale());
          SFOR(u, PD - 1) if constexpr (i + 1 + u < NS) rel1<false>(af[i + 1 + u][0], af[i + 1 + u][1], rsWS, vL16, sA + (ksb(i + 1 + u) + ksi(i + 1 + u)) * 1024); SEND
        }
      }
      if constexpr (i == 10) STAMP(8);
      if constexpr (i == 14) STAMP(9);
      SB();
      if constexpr (i + PD < NS) { loadA1(i + PD, 0); loadA1(i + PD, 1); }
      mm4(i);
    SEND
    STAMP(10);
    __builtin_amdgcn_s_setprio(0);
#pragma unroll
    for (int qd = 0; qd < 4; qd++)
#pragma unroll
      for (int k = 0; k < 4; k++) {
        const unsigned ra = ring_area(2 * qd + (k >> 1)) + (k & 1) * 1024;
        ldsw<bf16x8>(((lw2 + vL16) ^ (unsigned)(32 * (4 * (qd >> 1) + k))) + (ra & 0x30000u), ra & 0xffffu, sub4(acc[qd >> 1][qd & 1], k));
      }
    WAITL0(); BAR(); MEMFENCE();
    STAMP(12);
    const unsigned c8 = (ln >> 4) & 7, j2 = (c8 & 4) + 2 * GG + ((c8 >> 1) & 1);
    const unsigned rd = LB + ((0x51407362u >> (4 * j2)) & 7) * 16384 + (c8 & 1) * 1024 + (((8 * wr + (ln >> 7)) << 4) ^ (c8 << 5));
    asm volatile("" : "+v"(xr));
    f32x4 sum[2];
#pragma unroll
    for (int k = 0; k < 2; k++) {
      f32x4 t = {U2F(xr[2 * k] << 16), U2F(xr[2 * k] & 0xffff0000u), U2F(xr[2 * k + 1] << 16), U2F(xr[2 * k + 1] & 0xffff0000u)};
#pragma unroll
      for (int v = 0; v < 8; v++) t += ldsr<f32x4>(rd, v * 2048 + k * 512);
      sum[k] = t;
    }
    u32x4 o = {pk2(sum[0][0], sum[0][1]), pk2(sum[0][2], sum[0][3]), pk2(sum[1][0], sum[1][1]), pk2(sum[1][2], sum[1][3])};
    __builtin_amdgcn_raw_buffer_store_b128(o, mkrs(pout, 2048 * XROW), vO + sO, 0, OAUX);
  }
  STAMPS_OUT();
  };
  if (!attn && g) prog(BoolC<false>{}, IntC<1>{});
  else if (!attn) prog(BoolC<false>{}, IntC<0>{});
  else if (g) prog(BoolC<true>{}, IntC<1>{});
  else prog(BoolC<true>{}, IntC<0>{});
}

extern "C" void kernel_launch(void* const* d_in, const int* in_sizes, int n_in, void* d_out, int out_size, void* d_ws, size_t ws_size,
                              hipStream_t stream) {
  fused_kernel<<<256, 512, 0, stream>>>((const char*)d_in[0], (const char*)d_in[1], (const char*)d_in[5], (const char*)d_in[6], (char*)d_ws,
                                        (const char*)d_in[2], (const char*)d_in[7], (const char*)d_in[3], (char*)d_out);
}
```

```cpp
#include <hip/hip_runtime.h>
#include <hip/hip_bf16.h>
#include <utility>

#define DI __device__ __forceinline__
#define LAS __attribute__((address_space(3)))
typedef __attribute__((ext_vector_type(8))) short bf16x8;
typedef __attribute__((ext_vector_type(4))) short s16x4;
typedef __attribute__((ext_vector_type(2))) __bf16 bf2;
typedef __attribute__((ext_vector_type(2))) float f32x2;
typedef __attribute__((ext_vector_type(4))) float f32x4;
typedef __attribute__((ext_vector_type(16))) float f32x16;
typedef __attribute__((ext_vector_type(2))) unsigned u32x2;
typedef __attribute__((ext_vector_type(4))) unsigned u32x4;
typedef __amdgpu_buffer_rsrc_t rsrc_t;
#define F2U(x) __builtin_bit_cast(unsigned, (float)(x))
#define U2F(x) __builtin_bit_cast(float, (unsigned)(x))
#define ALL(x) (__builtin_amdgcn_ballot_w64(!(x)) == 0ull)
#define ANY(x) (__builtin_amdgcn_ballot_w64(x) != 0ull)
#define UMAX(a, b) __builtin_elementwise_max((unsigned)(a), (unsigned)(b))

#define NTX 0
#define NTW 0
#define NT2 0
#define XS 2
#define MAP2 1
constexpr unsigned XROW = 896, QROW = 2688, FROW = 3584;
constexpr unsigned FS = 128;
constexpr unsigned WS_EPOCH = 0;
constexpr unsigned WS_GR = 4096;
constexpr unsigned WS_KVP = 131072;
constexpr unsigned WS_A2 = WS_KVP + 16 * 16 * 8192;
constexpr unsigned WS_ST = WS_A2 + 64 * 144 * 1024;
constexpr unsigned WS_FH = WS_ST + 262144;
constexpr unsigned WS_KVR = WS_FH + 2048 * FS;
constexpr unsigned WS_END = WS_KVR + 16 * 8192;
constexpr unsigned L_GU = 65536, L_KV = 131072;
constexpr unsigned L_TAIL = L_KV + 3 * 8192;
constexpr unsigned L_WPK = L_TAIL;
constexpr unsigned L_ST = L_TAIL + 2048;
constexpr unsigned L_CS = L_TAIL + 4096;
constexpr unsigned L_PHI = L_GU + 32768;
constexpr unsigned L_V = L_KV + 16384;
constexpr unsigned L_FREE = 32768;

#define MFMA(a, b, c) __builtin_amdgcn_mfma_f32_32x32x16_bf16((a), (b), (c), 0, 0, 0)
#define MFMA16(a, b, c) __builtin_amdgcn_mfma_f32_16x16x32_bf16((a), (b), (c), 0, 0, 0)
#define AI __attribute__((always_inline))
#define BAR() __builtin_amdgcn_s_barrier()
#define WAITV(n) asm volatile("s_waitcnt vmcnt(" #n ")" ::: "memory")
#define WAITL0() asm volatile("s_waitcnt lgkmcnt(0)" ::: "memory")
#define SB() __builtin_amdgcn_sched_barrier(0)
#define MEMFENCE() asm volatile("" ::: "memory")

template <int N> DI void waitv() { asm volatile("s_waitcnt vmcnt(%c0)" :: "i"(N) : "memory"); }

#define INSZ(x) (x)
DI rsrc_t mkrs(const void* p, unsigned bytes) { return __builtin_amdgcn_make_buffer_rsrc((void*)p, (short)0, (int)bytes, 0x00020000); }
template <bool NT = false> DI void dma(unsigned lds, unsigned limm, unsigned voff, rsrc_t rs, unsigned soff, unsigned simm) {
  unsigned t;
  if constexpr (NT) asm volatile("s_add_u32 m0, %1, %c5\n\ts_add_u32 %0, %4, %c6\n\tbuffer_load_dwordx4 %2, %3, %0 offen nt lds"
               : "=&s"(t) : "s"(lds), "v"(voff), "s"(rs), "s"(soff), "i"(limm), "i"(simm) : "memory", "scc");
  else asm volatile("s_add_u32 m0, %1, %c5\n\ts_add_u32 %0, %4, %c6\n\tbuffer_load_dwordx4 %2, %3, %0 offen lds"
               : "=&s"(t) : "s"(lds), "v"(voff), "s"(rs), "s"(soff), "i"(limm), "i"(simm) : "memory", "scc");
}
DI void dmat(unsigned lds, unsigned voff, rsrc_t rs) {
  asm volatile("s_mov_b32 m0, %0\n\ts_nop 0\n\tbuffer_load_dword %1, %2, 0 offen lds" :: "s"(lds), "v"(voff), "s"(rs) : "memory");
}
DI s16x4 trd(unsigned a) { return __builtin_amdgcn_ds_read_tr16_b64_v4i16((LAS s16x4*)(size_t)a); }
DI s16x4 trd2(unsigned a, unsigned off) { return __builtin_amdgcn_ds_read_tr16_b64_v4i16((LAS s16x4*)((LAS char*)(size_t)a + off)); }
DI bf16x8 tr2(unsigned a, unsigned off0, unsigned off1) {
  LAS char* p = (LAS char*)(size_t)a;
  s16x4 lo = __builtin_amdgcn_ds_read_tr16_b64_v4i16((LAS s16x4*)(p + off0));
  s16x4 hi = __builtin_amdgcn_ds_read_tr16_b64_v4i16((LAS s16x4*)(p + off1));
  return __builtin_shufflevector(lo, hi, 0, 1, 2, 3, 4, 5, 6, 7);
}
template <class T> DI T ldsr(unsigned a, unsigned off) { return *(LAS T*)((LAS char*)(size_t)a + off); }
template <class T> DI void ldsw(unsigned a, unsigned off, T v) { *(LAS T*)((LAS char*)(size_t)a + off) = v; }
DI unsigned pk2(float a, float b) { f32x2 f = {a, b}; return __builtin_bit_cast(unsigned, __builtin_convertvector(f, bf2)); }
DI float rbf(float a) { return U2F(pk2(a, a) << 16); }
DI float dot2(unsigned a, unsigned b, float c) { return __builtin_amdgcn_fdot2_f32_bf16(__builtin_bit_cast(bf2, a), __builtin_bit_cast(bf2, b), c, false); }
DI float xsum(float v) { auto r = __builtin_amdgcn_permlane32_swap(F2U(v), F2U(v), false, false); float a = U2F(r[0]); asm("" : "+v"(a)); return a + U2F(r[1]); }
DI float xmax(float v) { auto r = __builtin_amdgcn_permlane32_swap(F2U(v), F2U(v), false, false); return __builtin_fmaxf(U2F(r[0]), U2F(r[1])); }
DI float xmin(float v) { auto r = __builtin_amdgcn_permlane32_swap(F2U(v), F2U(v), false, false); return __builtin_fminf(U2F(r[0]), U2F(r[1])); }
DI float xhi(float v) { auto r = __builtin_amdgcn_permlane32_swap(F2U(v), F2U(v), false, false); return U2F(r[1]); }
#define STB "sc1"
DI void st16(u32x4 v, rsrc_t rs, unsigned voff, unsigned soff, unsigned simm) {
  unsigned t;
  asm volatile("s_add_u32 %0, %4, %c5\n\tbuffer_store_dwordx4 %1, %2, %3, %0 offen " STB "\n\ts_nop 1" : "=&s"(t) : "v"(v), "v"(voff), "s"(rs), "s"(soff), "i"(simm) : "memory", "scc");
}
DI void stflag(unsigned v, rsrc_t rs, unsigned soff) {
  unsigned voff = (__builtin_amdgcn_mbcnt_hi(~0u, __builtin_amdgcn_mbcnt_lo(~0u, 0u)) == 0u) ? 0u : 0x80000000u;
  __builtin_amdgcn_raw_buffer_store_b32(v, rs, voff, soff, 16);
}
#define XORCOND (n == 24)
#define PS 0
#define PV 0
#define KEEP2 0
#define S2N 14
#define B1N 23
#define DIST 2
#define P2G1 1
#define OAUX 0
#define RECY 0
#define POFF 99
#define H1S 0
#define H0L 14
#define H0C 22
#define Q3A 1
#define COOP 0
#define PON -2
#define XN 20
#define BAN 18
#define BBN 21
#define SL0 8
#define SLN 2
#define SLD 3
#define NSL 48
#define H1C 16
#define OV vL16
#define CV vL16
#define Q6E 0
#define TEARLY 0
#define NOCS 0
#define TPOS 0
#define PRE 0
#define LREC 0
#define CK1 20
#define EB 3
#define T2B 1
#define F1N 9
#define RC 3
#define G0B 9
#define TN 10
#define SLEEPR 4
#define SLEEPN 2
DI u32x4 ld16(rsrc_t rs, unsigned voff, unsigned soff, unsigned simm) {
  u32x4 r; unsigned t;
  asm volatile("s_add_u32 %1, %4, %c5\n\tbuffer_load_dwordx4 %0, %2, %3, %1 offen sc1" : "=v"(r), "=&s"(t) : "v"(voff), "s"(rs), "s"(soff), "i"(simm) : "memory", "scc");
  return r;
}
template <unsigned STEP> DI void ld8w(u32x4 (&p)[8], rsrc_t rs, unsigned voff, unsigned soff) {
  unsigned t;
#define L1(k) "buffer_load_dwordx4 %" #k ", %9, %10, %8 offen sc1\n\ts_add_u32 %8, %8, %c12\n\t"
  asm volatile("s_mov_b32 %8, %11\n\t" L1(0) L1(1) L1(2) L1(3) L1(4) L1(5) L1(6) L1(7) "s_waitcnt vmcnt(0)"
               : "=&v"(p[0]), "=&v"(p[1]), "=&v"(p[2]), "=&v"(p[3]), "=&v"(p[4]), "=&v"(p[5]), "=&v"(p[6]), "=&v"(p[7]), "=&s"(t)
               : "v"(voff), "s"(rs), "s"(soff), "i"(STEP) : "memory", "scc");
#undef L1
}
DI bool untag(u32x4 (&p)[8], unsigned tag, unsigned mask) {
  unsigned chk = 0;
#pragma unroll
  for (int i = 0; i < 8; i++)
#pragma unroll
    for (int k = 0; k < 4; k++) { p[i][k] ^= tag; chk |= p[i][k]; }
  return ALL((chk & mask) == 0u);
}
template <unsigned STEP> DI void waittag(u32x4 (&p)[8], rsrc_t rs, unsigned voff, unsigned soff, unsigned tag, unsigned mask) {
  asm volatile("s_waitcnt vmcnt(0)" : "+v"(p[0]), "+v"(p[1]), "+v"(p[2]), "+v"(p[3]), "+v"(p[4]), "+v"(p[5]), "+v"(p[6]), "+v"(p[7]) :: "memory");
#pragma nounroll
  for (int it = 0; !untag(p, tag, mask) && it < (1 << 16); ++it) { __builtin_amdgcn_s_sleep(SLEEPN); ld8w<STEP>(p, rs, voff, soff); }
}
template <bool WAIT> DI void rel1(u32x4& a, u32x4& b, rsrc_t rs, unsigned voff, unsigned so) {
  unsigned t;
  if constexpr (WAIT) asm volatile("buffer_load_dwordx4 %0, %3, %4, %5 offen sc1\n\ts_add_u32 %2, %5, 147456\n\tbuffer_load_dwordx4 %1, %3, %4, %2 offen sc1\n\ts_waitcnt vmcnt(0)"
                                   : "+v"(a), "+v"(b), "=&s"(t) : "v"(voff), "s"(rs), "s"(so) : "memory", "scc");
  else asm volatile("buffer_load_dwordx4 %0, %3, %4, %5 offen sc1\n\ts_add_u32 %2, %5, 147456\n\tbuffer_load_dwordx4 %1, %3, %4, %2 offen sc1"
                    : "+v"(a), "+v"(b), "=&s"(t) : "v"(voff), "s"(rs), "s"(so) : "memory", "scc");
}
DI void poll(rsrc_t rs, unsigned voff, unsigned soff, unsigned E, unsigned need) {
  voff = need ? voff : 0x80000000u;
#pragma nounroll
  for (int it = 0; it < (1 << 16); ++it) {
    unsigned v;
    asm volatile("buffer_load_dword %0, %1, %2, %3 offen sc1\n\ts_waitcnt vmcnt(0)" : "=v"(v) : "v"(voff), "s"(rs), "s"(soff) : "memory");
    bool ok = (need == 0u) || ((v - E - need) <= (3u - need));
    if (ALL(ok)) break;
    __builtin_amdgcn_s_sleep(SLEEPN);
  }
}

template <bool B> struct BoolC { static constexpr bool value = B; };
template <int I> struct IntC { static constexpr int value = I; };
template <int... I, class F> DI void sfor_impl(std::integer_sequence<int, I...>, F&& f) { (f(IntC<I>{}), ...); }
template <int N, class F> DI void sfor(F&& f) { sfor_impl(std::make_integer_sequence<int, N>{}, f); }
#define SFOR(n, N) sfor<N>([&](auto n##_) AI { constexpr int n = decltype(n##_)::value;
#define SEND });
struct RowC { float r, K1, K2, Ri, Lr; };

DI unsigned hilo(float v) { float hi = rbf(v); return pk2(hi, v - hi); }
DI float max3(float a, float b, float c) { return __builtin_fmaxf(__builtin_fmaxf(a, b), c); }
DI float min3(float a, float b, float c) { return __builtin_fminf(__builtin_fminf(a, b), c); }
DI unsigned pk2s(float a, float b) { unsigned r; asm("v_cvt_pk_bf16_f32 %0, %1, %2" : "=v"(r) : "v"(a), "v"(b)); return r; }
DI f32x2 sub2(const f32x16& v, int k) { f32x2 t = {v[2 * k], v[2 * k + 1]}; return t; }
DI f32x2 exp2v(f32x2 v) { f32x2 r = {__builtin_amdgcn_exp2f(v[0]), __builtin_amdgcn_exp2f(v[1])}; return r; }
DI f32x2 dup2(float a) { f32x2 r = {a, a}; asm("" : "+v"(r)); return r; }
DI f32x2 fma2(f32x2 a, f32x2 b, f32x2 c) { return __builtin_elementwise_fma(a, b, c); }
DI u32x4 epiP(const f32x16& a, const RowC& c) {
  u32x4 r;
  const f32x2 K1 = dup2(c.K1), K2 = dup2(c.K2), Ri = dup2(c.Ri);
#pragma unroll
  for (int i = 0; i < 4; i++) {
    const f32x2 g = sub2(a, i), d = fma2(exp2v(g * fma2(g * g, K2, K1)), Ri, Ri);
    const f32x2 s = {__builtin_amdgcn_rcpf(d[0]), __builtin_amdgcn_rcpf(d[1])}, h = (g * sub2(a, 4 + i)) * s;
    r[i] = pk2(h[0], h[1]);
  }
  return r;
}
struct EpiS { float x0, x1, m0, m1; u32x4 r; };
template <int P, int PH> DI void epiSlice(const f32x16& a, const RowC& c, EpiS& s) {
  const float g0 = a[2 * P], g1 = a[2 * P + 1];
  if constexpr (PH == 0) { s.x0 = g0 * __builtin_fmaf(g0 * g0, c.K2, c.K1); s.x1 = g1 * __builtin_fmaf(g1 * g1, c.K2, c.K1); }
  if constexpr (PH == 1) { s.x0 = __builtin_amdgcn_exp2f(s.x0); s.x1 = __builtin_amdgcn_exp2f(s.x1); s.m0 = g0 * a[8 + 2 * P]; s.m1 = g1 * a[9 + 2 * P]; }
  if constexpr (PH == 2) { s.x0 = __builtin_amdgcn_rcpf(__builtin_fmaf(s.x0, c.Ri, c.Ri)); s.x1 = __builtin_amdgcn_rcpf(__builtin_fmaf(s.x1, c.Ri, c.Ri)); }
  if constexpr (PH == 3) s.r[P] = pk2s(s.m0 * s.x0, s.m1 * s.x1);
}
DI f32x16 featproj2(const f32x16& a, bf16x8 w0, bf16x8 w1, bool hh) {
  u32x4 q0, q1;
#pragma unroll
  for (int i = 0; i < 4; i++) { q0[i] = pk2(a[2 * i], a[2 * i + 1]); q1[i] = pk2(a[8 + 2 * i], a[9 + 2 * i]); }
  q1[2] = hh ? 0u : q1[2]; q1[3] = hh ? 0u : q1[3];
  f32x16 z;
#pragma unroll
  for (int i = 0; i < 16; i++) z[i] = 0.f;
  z = MFMA(w0, __builtin_bit_cast(bf16x8, q0), z);
  z = MFMA(w1, __builtin_bit_cast(bf16x8, q1), z);
  return z;
}
DI f32x16 featproj(const f32x16& a, unsigned wpaddr, unsigned off, bool hh) {
  return featproj2(a, tr2(wpaddr, off, off + 8 * 56), tr2(wpaddr, off + 16 * 56, off + 24 * 56), hh);
}
DI float max16(const f32x16& p) { float m = max3(p[0], p[1], p[2]); _Pragma("unroll") for (int i = 3; i < 15; i += 2) m = max3(m, p[i], p[i + 1]); return xmax(__builtin_fmaxf(m, p[15])); }
DI float min16(const f32x16& p) { float m = min3(p[0], p[1], p[2]); _Pragma("unroll") for (int i = 3; i < 15; i += 2) m = min3(m, p[i], p[i + 1]); return xmin(__builtin_fminf(m, p[15])); }
DI f32x16 dualexpP(const f32x16& p, float Lr) {
  const f32x2 bx = dup2(-max16(p) * Lr), bn = dup2(min16(p) * Lr), L2 = dup2(Lr);
  f32x16 o;
#pragma unroll
  for (int i = 0; i < 8; i++) {
    const f32x2 ep = exp2v(fma2(sub2(p, i), L2, bx)), en = exp2v(fma2(sub2(p, i), -L2, bn));
    o[i] = U2F(pk2(ep[0], ep[1])); o[8 + i] = U2F(pk2(en[0], en[1]));
  }
  return o;
}
struct SmS { f32x16 p, e; float en[16], bx, bn, sp, sn, tp, tn; bf16x8 w0, w1; };
DI bf16x8 sub4(const f32x16& v, int k) { f32x4 t = {v[4 * k], v[4 * k + 1], v[4 * k + 2], v[4 * k + 3]}; return __builtin_bit_cast(bf16x8, t); }

DI constexpr unsigned lc1_base(int ch) { return ch == 6 ? L_KV : ((ch & 1) ? L_GU : 0u) + (ch < 2 ? 49152u : ch < 4 ? 0u : 16384u); }
template <int CTRL, int RM> DI float dppadd(float v) { return v + U2F(__builtin_amdgcn_update_dpp(0u, F2U(v), CTRL, RM, 0xf, false)); }
DI float wavesum63(float v) {
  v = dppadd<0x111, 0xf>(v); v = dppadd<0x112, 0xf>(v); v = dppadd<0x114, 0xf>(v); v = dppadd<0x118, 0xf>(v);
  v = dppadd<0x142, 0xa>(v); return dppadd<0x143, 0xc>(v);
}

#define STAMP(i)
#define STAMPS_OUT()
#define CSTAMP(i)

#define PDV 6
constexpr int PD = PDV, RING = 9;
constexpr int later2(int i, int NS, int GG) {
  const int younger = 2 * ((i + PD <= NS ? i + PD : NS) - 1 - i);
  int extra = i < PD ? 1 : 0;
  if (NS == 20 && (i == 10 || i == 11)) extra += 2 * (i - 9);
  return younger + extra;
}
DI constexpr unsigned ring_area(int j) { return j == 0 ? L_FREE : j == 1 ? L_PHI : lc1_base(j - 2); }
constexpr int cumG1(int s) { return s < 4 ? 0 : s < 22 ? (s - 4) / 2 : (9 + (s - 22) * 5 / 4 < 48 ? 9 + (s - 22) * 5 / 4 : 48); }

#define CODE_BYTES 65536
__global__ __launch_bounds__(512, 2) void fused_kernel(const char* px_, const char* pqkv_, const char* pwg_, const char* pwu_, char* ws_, const char* pphi_,
                                                       const char* pwd_, const char* pwo_, char* pout_) {
  typedef __attribute__((ext_vector_type(8))) unsigned long long u64x8;
  u64x8 ka; unsigned long long kb;
  asm volatile("s_load_dwordx16 %0, %2, 0\n\ts_load_dwordx2 %1, %2, 64" : "=&s"(ka), "=&s"(kb) : "s"(__builtin_amdgcn_kernarg_segment_ptr()));
  __shared__ __attribute__((aligned(16))) char smem[163840];
  const unsigned LB = (unsigned)(size_t)smem;
  const unsigned tid = __builtin_amdgcn_workitem_id_x(), lane = tid & 63;
  const unsigned w = __builtin_amdgcn_readfirstlane(tid >> 6), g = w >> 2, wr = w & 3;
  const unsigned b = __builtin_amdgcn_workgroup_id_x(), xcd = b & 7, bi = b >> 3;
  const unsigned half = (xcd >> 1) & 1, cp = 4 * (xcd & 1) + (bi >> 3), rb = 8 * (xcd >> 2) + (bi & 7), rsum = (bi >> 3) + (bi & 7);
  const unsigned f2 = 32 * (xcd & 1) + bi - 8, t2 = (f2 * 37) >> 8, ci = f2 - 7 * t2, rbl = t2 & 3, rt2 = 2 * (8 * (xcd >> 1) + t2);
  const unsigned c = 7 * half + cp - 1;
  const unsigned head = 8 * half + (((half ? 0x76425310u : 0x75316420u) >> (4 * cp)) & 7);
  const bool attn = cp == 0;
  STAMP(0); CSTAMP(14);
  const unsigned wstr = attn ? QROW : FROW;
  const unsigned sG0 = half * (attn ? 448 : 1792) + (attn ? 0 : (cp - 1) * 256), sX0 = rb * 128 * XROW;
  unsigned vx0, vx1, vG, vU, vKV;
  auto voffs = [&](unsigned ln) AI {
    const unsigned r8 = ln >> 3, c8 = ln & 7, sa = (r8 >> 1) & 1, lc = c8 ^ (4 * sa);
    vx0 = (16 * w + r8) * XROW + ((c8 ^ (r8 >> 1)) << 4); vx1 = (vx0 ^ 64u) + 8 * XROW;
    const unsigned cg = attn ? lc : (c8 ^ (2 * sa)), cu = attn ? (lc >= 6 ? lc - 2 : lc) : (cg ^ 4);
    vG = (8 * w + r8) * wstr + (cg << 4); vU = (8 * w + r8) * wstr + (cu << 4);
    vKV = (8 * w + r8) * QROW + 896 * (1 + (lc >> 2)) + ((lc & 3) << 4) + ((56 * head) & ~15u);
  };
  voffs(lane);
  const unsigned lw1 = LB + w * 1024, lw2 = LB + w * 2048;
#define ROT (((0x124924u >> (2 * rsum)) & 3) * 2)
  const unsigned rot = ROT;
  const unsigned srow = 128 * rb + 64 * half + 8 * cp + w;
  unsigned vrow = lane < 56 ? lane * 16 : 0x80000000u;
  asm volatile("s_waitcnt lgkmcnt(0)" : "+s"(ka), "+s"(kb), "+v"(vx0), "+v"(vx1), "+v"(vG), "+v"(vU), "+v"(vKV), "+v"(vrow));
  const char *px = (const char*)ka[0], *pqkv = (const char*)ka[1], *pwg = (const char*)ka[2], *pwu = (const char*)ka[3], *pphi = (const char*)ka[5],
             *pwd = (const char*)ka[6], *pwo = (const char*)ka[7];
  char *ws = (char*)ka[4], *pout = (char*)kb;
  const rsrc_t rsX = mkrs(px, INSZ(2048 * XROW)), rsQ = mkrs(pqkv, INSZ(448 * QROW));
  const rsrc_t rsG = mkrs(attn ? pqkv : pwg, INSZ(448 * wstr)), rsU = mkrs(attn ? pqkv + 128 : pwu, INSZ(448 * wstr - (attn ? 128 : 0)));
  auto cidx = [&](int st) AI -> unsigned { unsigned t = rot + st; return t >= 7 ? t - 7 : t; };
  auto issueA = [&](int st, int k) AI {
    const unsigned sl = st & 3, ch = cidx(st);
    if (k == 0) dma<NTX>(lw2, sl * 16384, vx0, rsX, sX0 + ch * 128, 0);
    if (k == 1) dma<NTX>(lw2, sl * 16384 + 1024, vx1, rsX, sX0 + ch * 128, 0);
    if (k == 2) dma<NTW>(lw1, L_GU + sl * 16384, vG, rsG, sG0 + ch * 64 * wstr, 0);
    if (k == 3) dma<NTW>(lw1, L_GU + sl * 16384 + 8192, vU, rsU, sG0 + ch * 64 * wstr, 0);
    if (k == 4) dma<NTW>(lw1, L_KV + sl * 8192, vKV, rsQ, ch * 64 * QROW, 0);
  };
  const rsrc_t rsWS = mkrs(ws, WS_END);
  unsigned Ev, cpf; u32x4 xrw;
  asm volatile("buffer_load_dwordx4 %0, %1, %2, %3 offen" : "=v"(xrw) : "v"(vrow), "s"(rsX), "s"(srow * XROW) : "memory");
  asm volatile("buffer_load_dword %0, off, %1, 0 sc1" : "=v"(Ev) : "s"(rsWS) : "memory");
  {
    unsigned long long pc; asm volatile("s_getpc_b64 %0" : "=s"(pc));
    const unsigned vo = w == 0 && lane < 24 ? (bi * 24 + lane) * 128 : 0x80000000u;
    asm volatile("buffer_load_dword %0, %1, %2, 0 offen" : "=v"(cpf) : "v"(vo), "s"(mkrs((const void*)pc, CODE_BYTES)) : "memory");
  }
#pragma unroll
  for (int k = 0; k < 5; k++) issueA(0, k);
  asm volatile("s_waitcnt vmcnt(%c3)" : "+v"(xrw), "+v"(Ev), "+v"(cpf) : "i"(5 + COOP) : "memory");
#pragma unroll
  for (int k = 0; k < 5; k++) issueA(1, k);
  issueA(2, 0);
  const unsigned E = __builtin_amdgcn_readfirstlane(Ev);
  {
    float s1 = 0.f, s2 = 0.f;
#pragma unroll
    for (int i = 0; i < 4; i++) { s1 = dot2(xrw[i], 0x3f803f80u, s1); s2 = dot2(xrw[i], xrw[i], s2); }
    s1 = wavesum63(s1); s2 = wavesum63(s2);
    const float mean = s1 * (1.f / 448.f), var = __builtin_fmaxf(s2 * (1.f / 448.f) - mean * mean, 0.f);
    u32x2 gr = {pk2(-mean, __builtin_amdgcn_sqrtf(rbf(rbf(var) + 1e-5f))), E + 1};
    __builtin_amdgcn_raw_buffer_store_b64(gr, rsWS, lane == 63 ? 0u : 0x80000000u, WS_GR + srow * 8, 16);
  }

  auto prog = [&](auto at_, auto gg_) AI {
  constexpr bool AT = decltype(at_)::value; constexpr int GG = decltype(gg_)::value;
  constexpr unsigned lcstep = AT ? 224 : 128;
  unsigned lane;
  asm volatile("v_mbcnt_lo_u32_b32 %0, -1, 0\n\tv_mbcnt_hi_u32_b32 %0, -1, %0" : "=v"(lane));
  __builtin_assume(lane < 64);
  voffs(lane);
  auto issueL = [&](int hc) AI {
    const int st = hc >> 1; const unsigned ch = cidx(st);
    if (hc & 1) dma<NTW>(lw1, lc1_base(st) + 8192, vU, rsU, sG0 + ch * 64 * wstr, lcstep);
    else dma<NTW>(lw1, lc1_base(st), vG, rsG, sG0 + ch * 64 * wstr, lcstep);
  };
  auto issueG = [&](int bar, int k) AI {
    if (bar == 0) issueA(2 + k / 5, k % 5);
    else if (bar <= 3) issueA(bar + 3, k);
    else if (k < 4) issueL(4 * (bar - 4) + k);
    else if (bar == 4) dma(LB + (w & 1) * 1024, L_WPK, lane * 16, mkrs(pphi, 16 * 1568), head * 1568 + (w & 1) * 1024, 0);
    else issueL(12 + (bar - 5));
  };

  const unsigned rt = 4 * rb + wr;
  const unsigned sA2 = WS_A2 + rt * 144 * 1024 + (AT ? half * 16384 : c * 8192), vL16 = lane * 16;
  const u32x4 sent = {~0u, ~0u, ~0u, ~0u};
  auto clr = [&](int ks) AI { st16(sent, rsWS, CV, sA2, ks * 1024); };

  auto clri = [&](int q) AI { clr(GG ? (q < 6 ? 114 + q : 120 + q) : (q < 2 ? 112 + q : 118 + q)); };

  const bool hh = lane >= 32;
  const unsigned hu = lane >> 5, blk = (lane >> 4) & 1, q4 = (lane >> 2) & 3, p4 = lane & 3, ta = (q4 >> 1) & 1, l31 = lane & 31;
  const unsigned perm = wr ^ (2 * GG);
  const unsigned rowoff = (8 * hu + q4) * 128 + (perm << 11);
  const unsigned pc = (4 * blk + p4) < 6 ? (4 * blk + p4) : 6;
  auto tileaddr = [&](unsigned t) AI -> unsigned {
    if constexpr (AT) { unsigned o = 56 * t + 8 * pc; return (o >> 7) * 8192 + ((((o >> 4) & 7) ^ (4 * ta)) << 4) + (o & 8) + rowoff; }
    else return blk * 8192 + (((2 * t + (p4 >> 1)) ^ (4 * blk + 2 * ta)) << 4) + 8 * (p4 & 1) + rowoff;
  };
  constexpr int NT = !AT ? 2 : GG ? 1 : 3, TB0 = GG ? 4 - NT : 0;
  unsigned aA[3], aB[NT];
  {
    unsigned o = (head & 1) * 8 + 8 * pc;
    unsigned ak = L_KV + (((o >> 4) ^ (4 * ta)) << 4) + (o & 8) + rowoff;
    aA[0] = LB + (GG ? L_GU + tileaddr(1) : ak);
    aA[1] = LB + (GG ? L_GU + tileaddr(2) : (ak ^ 64u));
    aA[2] = LB + L_GU + tileaddr(GG ? 3 : 0);
#pragma unroll
    for (int j = 0; j < NT; j++) aB[j] = LB + tileaddr(TB0 + j);
  }
  const unsigned xrow = 32 * wr + l31;
  const unsigned aX = LB + xrow * 128 + (((2 * perm + hu) ^ ((xrow >> 1) & 7)) << 4);
  const unsigned selv = (((lane >> 4) ^ (lane >> 2)) & 1) ? 0u : 0x3f803f80u;
  const u32x4 sel4 = {selv, selv, selv, selv};
  const bf16x8 sel = __builtin_bit_cast(bf16x8, sel4);

  bf16x8 xf[28], wa[2][3], wq[8];
  f32x16 accA[3], accB[NT];
  f32x4 csA[3], csB[NT];
#pragma unroll
  for (int j = 0; j < 3; j++) { for (int i = 0; i < 16; i++) accA[j][i] = 0.f; for (int i = 0; i < 4; i++) csA[j][i] = 0.f; }
#pragma unroll
  for (int j = 0; j < NT; j++) { for (int i = 0; i < 16; i++) accB[j][i] = 0.f; for (int i = 0; i < 4; i++) csB[j][i] = 0.f; }
  u32x2 grn;

  const rsrc_t rsWD = mkrs(pwd, INSZ(1792 * XROW)), rsWO = mkrs(pwo, INSZ(448 * XROW));
  const unsigned vW2 = (lane >> 3) * XROW + (((lane & 7) ^ ((lane >> 2) & 4)) << 4) + ci * 128;
  constexpr int NS = GG ? 16 + XS : 20 - XS, NC = 6 - XS / 2, NO = 2 * NC;
#define RHO ((rbl + ci) & 3)
  const unsigned rho = RHO;
  auto t_of = [](int i) constexpr { return (0x54763210u >> (4 * ((i >> 1) & 7))) & 7; };
  constexpr int NE = GG ? 9 : 5;
  auto g1i = [](int i) constexpr { return i < 8 ? i : i == 8 ? 16 : i < 17 ? i - 1 : 17; };
  auto ksb = [&](int i) AI -> unsigned {
    if (GG) i = g1i(i);
    if (GG) return i < 16 ? 16 * wr + ((t_of(i) & 3) ^ rho) : wr;
    if (i < NO) { const unsigned cc = i % NC + rho + (XS ? 0 : rho >> 1); return wr + 8 * (cc >= NC ? cc - NC : cc); }
    return 8 * wr + 2 * (((i - NO) >> 1) ^ rho);
  };
  auto ksi = [&](int i) AI -> unsigned { if (GG) i = g1i(i); return GG ? (i < 16 ? 8 * (i & 1) + (t_of(i) & 4) : 4 * i) : i < NO ? 64 + 4 * XS + 4 * (i / NC) : 112 + (i & 1); };
  auto issueW2 = [&](int i, int hf) AI {
    if (GG == 0 && i >= NO) dma<NT2>(lw2, ring_area(i % RING) + hf * 1024, vW2, rsWO, ksb(i) * 14 * XROW, (16 * (i & 1) + 8 * hf) * XROW);
    else dma<NT2>(lw2, ring_area(i % RING) + hf * 1024, vW2, rsWD, ksb(i) * 16 * XROW, (ksi(i) * 16 + 8 * hf) * XROW);
  };

  const unsigned aCSw = LB + L_CS + l31 * 16 + wr * 4 + GG * 2048, aCSr = LB + L_CS + l31 * 16 + GG * 2048;
  auto readX = [&](int n) AI { xf[n] = ldsr<bf16x8>(aX ^ (unsigned)((n & 3) << 5), ((n >> 2) & 3) * 16384); };
  auto readA = [&](int n, int j) AI {
    const unsigned o = ((n >> 2) & 3) * ((GG == 0 && j < 2) ? 8192 : 16384);
    wa[n & 1][j] = tr2(aA[j] ^ (unsigned)((n & 3) << 11), o, o + 512);
  };
  auto readB = [&](int q) AI {
    const int n = q / NT, j = q % NT;
    const unsigned base = lc1_base(n >> 2);
    if ((NT == 3 && n < XN) ? (n & 3) == 0 : n == 24) asm volatile("" : "+v"(aB[j]));
    wq[q & 7] = tr2(aB[j] ^ (((n & 3) << 11) | (base & 0x30000u)), base & 0xffffu, (base & 0xffffu) + 512);
  };
  RowC rc; unsigned nm, denb;
  auto mkxe = [&]() AI { asm volatile("" : "+v"(nm)); u32x4 t = {nm, nm, nm, nm}; return __builtin_bit_cast(bf16x8, t); };
  auto rowconst = [&]() AI {
#pragma nounroll
    for (int it = 0; it < (1 << 16) && !ALL(grn[1] == E + 1); ++it) {
      __builtin_amdgcn_s_sleep(SLEEPN);
      asm volatile("buffer_load_dwordx2 %0, %1, %2, %3 offen sc1\n\ts_waitcnt vmcnt(0)" : "=v"(grn) : "v"(xrow * 8), "s"(rsWS), "s"(WS_GR + rb * 1024) : "memory");
    }
    const float den = U2F(grn[0] & 0xffff0000u);
    nm = hh ? 0u : __builtin_amdgcn_perm(grn[0], grn[0], 0x01000100u);
    denb = grn[0] >> 16;
    rc.r = __builtin_amdgcn_rcpf(den);
    const float k1 = -2.f * 1.4426950408889634f * 0.7978845608028654f;
    rc.K1 = k1 * rc.r; rc.K2 = (k1 * 0.044715f) * rc.r * (rc.r * rc.r); rc.Ri = den * den; rc.Lr = 1.4426950408889634f * rc.r;
  };
  waitv<7 + COOP>();
  BAR(); MEMFENCE();
  STAMP(1);
  readX(0); readA(0, 0); readA(0, 1); readA(0, 2);
  SFOR(n, 28)
    SB();
    if constexpr (n == 27) {
#pragma unroll
      for (int j = 0; j < 3; j++) ldsw<unsigned>(aCSw, j * 512, hilo(csA[j][0]));
    }
    if constexpr ((n & 3) == 3) {
      if constexpr (n == 27) WAITV(0);
      else if constexpr (n == 23) asm volatile("s_waitcnt vmcnt(10)" : "+v"(grn) :: "memory");
      else if constexpr (n == 3 || n == 15 || n == 19) WAITV(11); else WAITV(10);
      { WAITL0(); BAR(); MEMFENCE(); }
      if constexpr (n == 27) STAMP(2);
    }
    constexpr int bar = (n + 1) >> 2, ph = (n + 1) & 3;
#pragma unroll
    for (int j = 0; j < 3; j++) {
      if constexpr (n < 27) { if (j == 0) readX(n + 1); readA(n + 1, j); }
      else { wa[0][j] = ldsr<bf16x8>(aCSr, j * 512); if (AT && GG == 1 && j < 2) { readB(2 * j); readB(2 * j + 1); } }
      SB();
      accA[j] = MFMA(wa[n & 1][j], xf[n], accA[j]);
      const int slot = 3 * ph + j;
      if (bar == 0) issueG(0, slot - 2);
      else if (bar == 6) { if (slot < 5) issueG(bar, slot); }
      else if (bar <= 6) { if (slot == 0) issueG(bar, 0); if (slot == 2) issueG(bar, 1); if (slot == 5) issueG(bar, 2); if (slot == 7) issueG(bar, 3); if (slot == 10) issueG(bar, 4); }
      else if (j < 2) {
        if constexpr (AT) dma(lw2, L_FREE + j * 1024, lane * 16, mkrs(pphi, 16 * 1568), half * 8 * 1568 + w * 2048, j * 1024);
        else issueW2(0, j);
      }
      if (!NOCS && (n & 3) == 0) csA[j] = MFMA16(sel, wa[n & 1][j], csA[j]);
      if (n == 25 && j == 0) rowconst();
      if (n == 14 && j == 2) asm volatile("buffer_load_dwordx2 %0, %1, %2, %3 offen sc1" : "=v"(grn) : "v"(xrow * 8), "s"(rsWS), "s"(WS_GR + rb * 1024) : "memory");
      SB();
    }
  SEND
  { const bf16x8 xe = mkxe();
#pragma unroll
  for (int j = 0; j < 3; j++) accA[j] = MFMA(wa[0][j], xe, accA[j]); }
  asm volatile("" : "+v"(accA[0]), "+v"(accA[1]), "+v"(accA[2]));
  SB();

  const unsigned sFown = WS_FH + (rt * 32 + (AT ? 28 + 2 * half : 2 * c) + GG) * FS;
  const unsigned aWP = LB + (4 * hu + q4) * 56 + 8 * pc;
  const unsigned T12 = (E + 1) & 0xfffu, T16 = (E + 1) & 0xffffu;
  const unsigned sF = WS_FH + rt2 * 32 * FS;
  const unsigned fc = l31 >> 1;
  const bool nd1 = GG ? (fc >> 1) == wr || (fc >= 8 && fc < 8 + XS / 2) : (fc >= 8 + XS / 2 && fc < 14) || fc == 14 + (wr >> 1);
  const unsigned aIW = LB + L_PHI + xrow * 64 + ((hu ^ ((xrow >> 1) & 7)) << 3);
  SmS sm;
  unsigned aWK = aWP + L_WPK, aWQ = aWP + L_FREE;
  asm volatile("" : "+v"(aWK)); if constexpr (AT) asm volatile("" : "+v"(aWQ));
  auto kstep = [&](auto s_) AI {
    constexpr int s = decltype(s_)::value;
    if constexpr (s == 0) { sm.w0 = tr2(aWK, 0, 8 * 56); sm.w1 = tr2(aWK, 16 * 56, 24 * 56); }
    if constexpr (s == 1) sm.p = featproj2(accA[0], sm.w0, sm.w1, hh);
    if constexpr (s == 2 || s == 3) {
#pragma unroll
      for (int k = 2 * s - 4; k < 2 * s - 2; k++) {
        u32x2 vv = {pk2(accA[1][4 * k], accA[1][4 * k + 1]), pk2(accA[1][4 * k + 2], accA[1][4 * k + 3])};
        if (k == 3) vv[0] = hh ? denb : vv[0];
        ldsw<u32x2>(aIW ^ (unsigned)(k << 4), L_V - L_PHI, vv);
      }
    }
    if constexpr (s == 4) sm.bx = -max16(sm.p) * rc.Lr;
    if constexpr (s == 5) sm.bn = min16(sm.p) * rc.Lr;
    if constexpr (s >= 6 && s < 22) {
      constexpr int i = s - 6;
      sm.e[i] = __builtin_amdgcn_exp2f(__builtin_fmaf(sm.p[i], rc.Lr, sm.bx)); sm.en[i] = __builtin_amdgcn_exp2f(__builtin_fmaf(sm.p[i], -rc.Lr, sm.bn));
      if constexpr (i == 0) { sm.sp = sm.e[0]; sm.sn = sm.en[0]; } else if constexpr (i < 12) { sm.sp += sm.e[i]; sm.sn += sm.en[i]; }
      else if constexpr (i == 12) { sm.tp = sm.e[12]; sm.tn = sm.en[12]; } else { sm.tp += sm.e[i]; sm.tn += sm.en[i]; }
    }
    if constexpr (s == 22) { sm.sp = __builtin_amdgcn_rcpf(xsum(sm.sp + (hh ? 0.f : sm.tp))) * rc.r; sm.sn = __builtin_amdgcn_rcpf(xsum(sm.sn + (hh ? 0.f : sm.tn))) * rc.r; }
    if constexpr (s >= 23) {
#pragma unroll
      for (int u = (s == 29 ? 6 : s - 23); u <= (s == 29 ? 7 : s - 23); u++) {
        const int k = u >> 1;
        if (u & 1) { u32x2 t = {pk2s(sm.en[4 * k] * sm.sn, sm.en[4 * k + 1] * sm.sn), pk2s(sm.en[4 * k + 2] * sm.sn, sm.en[4 * k + 3] * sm.sn)}; ldsw<u32x2>(aIW ^ (unsigned)(k << 4), 8192, t); }
        else { u32x2 t = {pk2s(sm.e[4 * k] * sm.sp, sm.e[4 * k + 1] * sm.sp), pk2s(sm.e[4 * k + 2] * sm.sp, sm.e[4 * k + 3] * sm.sp)}; ldsw<u32x2>(aIW ^ (unsigned)(k << 4), 0, t); }
      }
    }
  };
  auto qstep = [&](auto s_, f32x16& a, unsigned lh) AI {
    constexpr int s = decltype(s_)::value;
    if constexpr (s == 0) { sm.w0 = tr2(aWQ, lh * 1568, lh * 1568 + 8 * 56); sm.w1 = tr2(aWQ, lh * 1568 + 16 * 56, lh * 1568 + 24 * 56); }
    if constexpr (s == 1) sm.p = featproj2(a, sm.w0, sm.w1, hh);
    if constexpr (s == 3) sm.bx = -max16(sm.p) * rc.Lr;
    if constexpr (s == 4) sm.bn = min16(sm.p) * rc.Lr;
    if constexpr (s >= 5 && s < 21) {
      constexpr int i = (s - 5) >> 1;
      float t0, t1;
      if constexpr ((s - 5) & 1) { t0 = __builtin_fmaf(sm.p[2 * i], -rc.Lr, sm.bn); t1 = __builtin_fmaf(sm.p[2 * i + 1], -rc.Lr, sm.bn); }
      else { t0 = __builtin_fmaf(sm.p[2 * i], rc.Lr, sm.bx); t1 = __builtin_fmaf(sm.p[2 * i + 1], rc.Lr, sm.bx); }
      a[8 * ((s - 5) & 1) + i] = U2F(pk2(__builtin_amdgcn_exp2f(t0), __builtin_amdgcn_exp2f(t1)));
      if constexpr (s == 20) asm volatile("" : "+v"(a));
    }
  };
  bf16x8 ka_[4], kb_[2][4]; f32x4 kd[2];
  auto kvstep = [&](auto s_) AI {
    constexpr int s = decltype(s_)::value;
    if constexpr (s == 0) {
      const unsigned g4 = lane >> 4, r0 = 8 * g4 + q4, sw = 4 * blk + (q4 >> 1);
      unsigned b0 = LB + L_PHI + r0 * 64 + ((p4 ^ sw) << 3);
      unsigned a0 = (b0 ^ ((wr >> 1) << 5)) + (wr & 1) * 8192;
#pragma unroll
      for (int k = 0; k < 4; k++) {
        ka_[k] = __builtin_shufflevector(trd2(a0, k * 2048), trd2(a0 ^ 16u, k * 2048 + 256), 0, 1, 2, 3, 4, 5, 6, 7);
        kb_[0][k] = __builtin_shufflevector(trd2(b0, L_V - L_PHI + k * 2048), trd2(b0 ^ 16u, L_V - L_PHI + k * 2048 + 256), 0, 1, 2, 3, 4, 5, 6, 7);
        kb_[1][k] = __builtin_shufflevector(trd2(b0 ^ 32u, L_V - L_PHI + k * 2048), trd2(b0 ^ 48u, L_V - L_PHI + k * 2048 + 256), 0, 1, 2, 3, 4, 5, 6, 7);
      }
    }
    if constexpr (s == 1) {
      f32x4 z = {0.f, 0.f, 0.f, 0.f}; kd[0] = z; kd[1] = z;
#pragma unroll
      for (int k = 0; k < 4; k++) { kd[0] = MFMA16(ka_[k], kb_[0][k], kd[0]); kd[1] = MFMA16(ka_[k], kb_[1][k], kd[1]); }
    }
    if constexpr (s == 3) {
#pragma unroll
      for (int d = 0; d < 2; d++) {
        u32x4 t;
#pragma unroll
        for (int i = 0; i < 4; i++) t[i] = (F2U(kd[d][i]) & ~0xfffu) | T12;
        st16(t, rsWS, (lane >> 4) * 512 + (lane & 15) * 16, ((head * 16 + rb) * 2 + (wr & 1)) * 4096 + (wr >> 1) * 2048, WS_KVP + d * 256);
      }
    }
  };
  auto qexp2 = [&](f32x16& a0, unsigned l0, f32x16& a1, unsigned l1) AI {
    const f32x16 p0 = featproj(a0, aWQ, l0 * 1568, hh), p1 = featproj(a1, aWQ, l1 * 1568, hh);
    a0 = dualexpP(p0, rc.Lr); a1 = dualexpP(p1, rc.Lr); asm volatile("" : "+v"(a0), "+v"(a1));
  };

  unsigned fl1 = 0;
  EpiS es;
  u32x4 pv[8];
  const unsigned vKP = l31 * 16 + hu * 8192, sKP = (rb * 32 + half) * 4096 + wr * 1024 + GG * 512, sKR = (8 * half + w) * 8192;
  auto hop1 = [&]() AI {
    waittag<16384>(pv, rsWS, vKP, sKP + WS_KVP, T12, 0xfffu);
    u32x4 o;
#pragma unroll
    for (int k = 0; k < 4; k++) {
      float sum = U2F(pv[0][k]);
#pragma unroll
      for (int i = 1; i < 8; i++) sum += U2F(pv[i][k]);
      sum = xsum(sum);
      sum = l31 == 29 ? 1.f : sum;
      if (GG == 1) sum = wr == 3 ? 0.f : sum;
      o[k] = (pk2s(sum, sum) & 0xffff0000u) | T16;
    }
    st16(o, rsWS, hh ? 0x80000000u : l31 * 16, (rb * 2 + half) * 4096 + wr * 1024 + GG * 512, WS_KVR);
    if (b == 0 && w == 4) stflag(E + 4, rsWS, WS_EPOCH);
    SB();
  };
  bf16x8 cb[NT];
  auto ldflag = [&]() AI { asm volatile("buffer_load_dword %0, %1, %2, %3 offen sc1" : "=v"(fl1) : "v"(nd1 ? lane * FS : 0x80000000u), "s"(rsWS), "s"(sF) : "memory"); };
  auto touch = [&](unsigned wo4) AI {
    const unsigned rw = (rbl * 8 + w + wo4) * 44 + lane;
    const bool dn = rw < 896;
    const unsigned vo = lane < 44 ? (dn ? ((rw >> 6) * 128 + 64 + (rw & 63)) : rw - 896) * XROW + ci * 128 : 0x80000000u;
    asm volatile("s_add_u32 m0, %0, %c5\n\ts_nop 0\n\tbuffer_load_dword %1, %3, 0 offen lds\n\tbuffer_load_dword %2, %4, 0 offen lds"
                 :: "s"(LB + w * 256), "v"(dn ? vo : 0x80000000u), "v"(dn ? 0x80000000u : vo), "s"(rsWD), "s"(rsWO), "i"(L_ST) : "memory", "scc");
  };
  auto kpath = [&]() AI {
    const f32x16 p = featproj(accA[0], aWK, 0, hh);
#pragma unroll
    for (int k = 0; k < 4; k++) {
      u32x2 vv = {pk2(accA[1][4 * k], accA[1][4 * k + 1]), pk2(accA[1][4 * k + 2], accA[1][4 * k + 3])};
      if (k == 3) vv[0] = hh ? denb : vv[0];
      ldsw<u32x2>(aIW ^ (unsigned)(k << 4), L_V - L_PHI, vv);
    }
    const f32x2 bx = dup2(-max16(p) * rc.Lr), bn = dup2(min16(p) * rc.Lr), L2 = dup2(rc.Lr);
    f32x2 ep[8], en[8];
#pragma unroll
    for (int i = 0; i < 8; i++) { ep[i] = exp2v(fma2(sub2(p, i), L2, bx)); en[i] = exp2v(fma2(sub2(p, i), -L2, bn)); }
    f32x2 sp = ((ep[0] + ep[1]) + (ep[2] + ep[3])) + (ep[4] + ep[5]), sn = ((en[0] + en[1]) + (en[2] + en[3])) + (en[4] + en[5]);
    f32x2 tp = ep[6] + ep[7], tn = en[6] + en[7];
    float a0 = sp[0], a1 = sn[0], a2 = tp[0], a3 = tn[0];
    asm("" : "+v"(a0), "+v"(a1), "+v"(a2), "+v"(a3));
    a0 += sp[1]; a2 += tp[1]; asm("" : "+v"(a0), "+v"(a2)); a1 += sn[1]; a3 += tn[1];
    const f32x2 ip = dup2(__builtin_amdgcn_rcpf(xsum(a0 + (hh ? 0.f : a2))) * rc.r), in = dup2(__builtin_amdgcn_rcpf(xsum(a1 + (hh ? 0.f : a3))) * rc.r);
#pragma unroll
    for (int k = 0; k < 4; k++) {
      const f32x2 u0 = ep[2 * k] * ip, u1 = ep[2 * k + 1] * ip, v0 = en[2 * k] * in, v1 = en[2 * k + 1] * in;
      u32x2 t0 = {pk2(u0[0], u0[1]), pk2(u1[0], u1[1])}, t1 = {pk2(v0[0], v0[1]), pk2(v1[0], v1[1])};
      ldsw<u32x2>(aIW ^ (unsigned)(k << 4), 0, t0); ldsw<u32x2>(aIW ^ (unsigned)(k << 4), 8192, t1);
    }
  };
  const unsigned sA = WS_A2 + rt2 * 144 * 1024;
  u32x4 af[NS][2];
  auto loadA1 = [&](int i, int m) AI { af[i][m] = ld16(rsWS, vL16, sA + ksb(i) * 1024, (144 * m + ksi(i)) * 1024); };
  auto stream = [&]() AI {
    SFOR(q, 28 * NT)
      constexpr int n = q / NT, j = q % NT;
      if constexpr (j == 0) {
        if constexpr (n == 27) {
#pragma unroll
          for (int t = 0; t < NT; t++) ldsw<unsigned>(aCSw, ((3 + t) & 3) * 512, hilo(csB[t][0]));
          if constexpr (AT && GG) WAITV(8);
          WAITL0(); BAR(); MEMFENCE();
        }
        else if constexpr (!AT && (n & 3) == 3) { asm volatile("s_waitcnt lgkmcnt(4)" ::: "memory"); BAR(); MEMFENCE(); }
      }
      if constexpr (q + 4 < 28 * NT) readB(q + 4);
      if constexpr (n == 27) cb[j] = ldsr<bf16x8>(aCSr, ((3 + j) & 3) * 512);
      SB();
      accB[j] = MFMA(wq[q & 7], xf[n], accB[j]);
      if constexpr (!NOCS && (n & 3) == 1) csB[j] = MFMA16(sel, wq[q & 7], csB[j]);
      if constexpr (!AT) {
        if constexpr (j == 0 && n >= 3 && n < 7) issueW2(1 + ((n - 3) >> 1), (n - 3) & 1);
        if constexpr (j == 0 && n >= 7 && n < 27 && (n & 1)) issueW2(1 + ((n + 1) >> 2), ((n + 1) >> 1) & 1);
        if constexpr (n == 27) issueW2(8, j);
        if constexpr (j == 1 && n == F1N) { waitv<(F1N < 7 ? F1N - 2 : 4 + (F1N - 5) / 2) + (TN < F1N ? 2 : 0)>(); stflag(E + 1, rsWS, sFown); }
        if constexpr (j == 1 && n == TN) touch(0);
        if constexpr (q == 2 * CK1 - 7) ldflag();
        if constexpr (q == 2 * CK1 - 1) {
          asm volatile("s_waitcnt vmcnt(%c5)" : "+v"(fl1), "+v"(accB[0]), "+v"(accB[NT - 1]), "+v"(csB[0]), "+v"(csB[NT - 1]) : "i"((CK1 & 1) ? 1 : 2) : "memory");
          const bool nde = nd1 && fc < 14;
          if (!ALL(!nde || (fl1 - E - 1u) <= 1u)) poll(rsWS, lane * FS, sF, E, nde ? 1u : 0u);
        }
        if constexpr (q >= 2 * CK1 && q < 2 * CK1 + 2 * NE) loadA1((q - 2 * CK1) >> 1, q & 1);
      } else {
        if constexpr (GG == 1 && n < 16 && (n & 1) == 0) clri(n >> 1);
        if constexpr (GG == 0 && j < 2 && n >= 1 && 2 * (n - 1) + j < 21) qstep(IntC<2 * (n - 1) + j>{}, accA[2], 0);
        if constexpr (GG == 0 && q >= 3 * H0L && q < 3 * H0L + 8) pv[q - 3 * H0L] = ld16(rsWS, vKP, sKP, WS_KVP + (q - 3 * H0L) * 16384);
        if constexpr (GG == 0 && j == 2 && n == H0C) { asm volatile("" : "+v"(accB[0]), "+v"(accB[1]), "+v"(accB[NT - 1]), "+v"(csB[0]), "+v"(csB[1]), "+v"(csB[NT - 1])); STAMP(11); hop1(); STAMP(6); }
      }
      SB();
    SEND
    const bf16x8 xe = mkxe();
#pragma unroll
    for (int t = 0; t < NT; t++) { accB[t] = MFMA(cb[t], xe, accB[t]); asm volatile("" : "+v"(accB[t])); }
    SB();
  };
  unsigned ln = vL16, aW = 0;
  auto mkaw = [&]() AI {
    asm volatile("" : "+v"(ln));
    aW = lw2 + (ln & 0x200) + ((ln & 0xc0) << 1) + ((((ln >> 3) & 0x20) | ((ln >> 1) & 0x10)) ^ ((ln >> 1) & 0x40)) + ((ln >> 1) & 8);
  };
  bf16x8 wf[NS][2];
  auto readW = [&](int i) AI {
    const unsigned ra = ring_area(i % RING), la = aW + (ra & 0x30000u);
    wf[i][0] = tr2(la, ra & 0xffffu, (ra & 0xffffu) + 1024); wf[i][1] = tr2(la ^ 64u, ra & 0xffffu, (ra & 0xffffu) + 1024);
  };
  auto tile = [&](f32x16& a, int t) AI { asm volatile("" : "+v"(a)); st16(epiP(a, rc), rsWS, vL16, sA2, t * 1024); SB(); };
  if constexpr (!AT) {
    if constexpr (GG == 0) {
      kpath(); WAITL0(); SB(); BAR(); SB(); STAMP(3);
      kvstep(IntC<0>{}); kvstep(IntC<1>{}); kvstep(IntC<3>{}); SB();
      tile(accA[2], 0);
    } else { tile(accA[0], 1); if (T2B) tile(accA[1], 2); BAR(); SB(); STAMP(3); if (!T2B) tile(accA[1], 2); tile(accA[2], 3); }
    readB(0); readB(1); readB(2); readB(3);
    STAMP(4);
    stream();
    SFOR(u, (2 * CK1 + 2 * NE > 56 ? 2 * CK1 + 2 * NE - 56 : 0)) loadA1((56 + u - 2 * CK1) >> 1, u & 1); SEND
    STAMP(5);
    mkaw();
    SFOR(i, RC) readW(i); SEND
    if constexpr (RC > 0) { WAITL0(); SB(); SFOR(i, RC) issueW2(9 + i, 0); issueW2(9 + i, 1); SEND }
    tile(accB[0], 4 + 2 * GG); tile(accB[NT - 1], 5 + 2 * GG);
  } else if constexpr (GG == 0) {
    SFOR(q, 8) clri(q); SEND
    SFOR(s, 30) kstep(s_); SEND
    WAITV(8);
    WAITL0(); SB(); BAR(); SB();
    STAMP(3);
    kvstep(IntC<0>{});
    readB(0); readB(1); readB(2); readB(3);
    kvstep(IntC<1>{}); kvstep(IntC<3>{});
    WAITV(2); stflag(E + 1, rsWS, sFown);
    STAMP(4);
    SB();
    stream();
  } else {
    stream();
    STAMP(3);
    SFOR(s, 21) qstep(s_, accA[0], 1); SEND
    WAITV(0); stflag(E + 1, rsWS, sFown);
    SFOR(s, 21) qstep(s_, accA[1], 2); SEND
    SFOR(s, 21)
      if constexpr (s < 8) pv[s] = ld16(rsWS, vKP, sKP, WS_KVP + s * 16384);
      qstep(s_, accA[2], 3);
    SEND
    STAMP(4); hop1(); STAMP(6);
    SB(); BAR(); SB();
  }

  if constexpr (AT) {
    STAMP(5);
    auto qexp = [&](f32x16& a, unsigned lh) AI { a = dualexpP(featproj(a, aWQ, lh * 1568, hh), rc.Lr); asm volatile("" : "+v"(a)); SB(); };
    if constexpr (GG == 0) qexp(accB[0], 4);
#pragma unroll
    for (int i = 0; i < 8; i++) pv[i] = ld16(rsWS, vL16, sKR, WS_KVR + i * 1024);
    if constexpr (GG == 0) { qexp(accB[1 % NT], 5); qexp(accB[NT - 1], 6); } else qexp(accB[0], 7);
    STAMP(7);
    waittag<1024>(pv, rsWS, vL16, sKR + WS_KVR, T16, 0xffffu);
    STAMP(8);
#pragma unroll
    for (int i = 0; i < 4; i++) {
      u32x4 f = {__builtin_amdgcn_perm(pv[2 * i][1], pv[2 * i][0], 0x07060302u), __builtin_amdgcn_perm(pv[2 * i][3], pv[2 * i][2], 0x07060302u),
                 __builtin_amdgcn_perm(pv[2 * i + 1][1], pv[2 * i + 1][0], 0x07060302u), __builtin_amdgcn_perm(pv[2 * i + 1][3], pv[2 * i + 1][2], 0x07060302u)};
      ldsw<u32x4>(LB + w * 4096 + vL16, i * 1024, f);
    }
    WAITL0(); BAR(); MEMFENCE();
    STAMP(10);
    f32x16 op[4], on[4];
    auto omm = [&](const f32x16& phi, unsigned lh, int z) AI {
#pragma unroll
      for (int i = 0; i < 16; i++) { op[z][i] = 0.f; on[z][i] = 0.f; }
      bf16x8 kf[4];
#pragma unroll
      for (int i = 0; i < 4; i++) kf[i] = ldsr<bf16x8>(LB + vL16, lh * 4096 + i * 1024);
#pragma unroll
      for (int i = 0; i < 2; i++) { op[z] = MFMA(kf[i], sub4(phi, i), op[z]); on[z] = MFMA(kf[2 + i], sub4(phi, 2 + i), on[z]); }
    };
    auto ofin = [&](unsigned lh, int z) AI {
      const float ia = __builtin_amdgcn_rcpf(op[z][13]), ib = __builtin_amdgcn_rcpf(on[z][13]);
      const float id = __builtin_amdgcn_rcpf(__builtin_fmaf(op[z][12], ia, __builtin_fmaf(on[z][12], ib, 1e-6f)));
      const float ca = xhi(ia * id), cb2 = xhi(ib * id);
      const f32x2 c2 = dup2(ca), d2 = dup2(cb2);
      u32x4 f0, f1;
#pragma unroll
      for (int i = 0; i < 4; i++) {
        const f32x2 u = fma2(sub2(op[z], i), c2, sub2(on[z], i) * d2), v = fma2(sub2(op[z], 4 + i), c2, sub2(on[z], 4 + i) * d2);
        f0[i] = pk2(u[0], u[1]); f1[i] = pk2(v[0], v[1]);
      }
      f1[2] = hh ? 0u : f1[2]; f1[3] = hh ? 0u : f1[3];
      st16(f0, rsWS, OV, sA2, (112 + 2 * lh) * 1024); st16(f1, rsWS, OV, sA2, (113 + 2 * lh) * 1024);
      SB();
    };
    if constexpr (GG) { omm(accA[0], 1, 0); omm(accA[1], 2, 1); SB(); ofin(1, 0); omm(accA[2], 3, 2); omm(accB[0], 7, 3); SB(); ofin(2, 1); ofin(3, 2); ofin(7, 3); }
    else { omm(accA[2], 0, 0); omm(accB[0], 4, 1); SB(); ofin(0, 0); omm(accB[1 % NT], 5, 2); omm(accB[NT - 1], 6, 3); SB(); ofin(4, 1); ofin(5, 2); ofin(6, 3); }
    STAMP(9);
  } else {
    f32x16 acc[2][2];
#pragma unroll
    for (int i = 0; i < 4; i++)
#pragma unroll
      for (int k = 0; k < 16; k++) acc[i >> 1][i & 1][k] = 0.f;
    u32x4 xr;
    const unsigned vO = (ln >> 7) * XROW + (ln & 0x70), sO = (rt2 * 32 + 8 * w) * XROW + ci * 128;
    auto mm4 = [&](int i) AI {
#pragma unroll
      for (int nn = 0; nn < 2; nn++)
#pragma unroll
        for (int mm = 0; mm < 2; mm++) acc[nn][mm] = MFMA(wf[i][nn], __builtin_bit_cast(bf16x8, af[i][mm]), acc[nn][mm]);
      SB();
    };
    STAMP(6);
    waitv<2 + 2 * RC>();
#pragma unroll
    for (int i = 0; i < NE; i++) asm volatile("" : "+v"(af[i][0]), "+v"(af[i][1]));
    SFOR(i, 9 - RC) readW(RC + i); SEND
    SB();
    SFOR(i, EB) mm4(i); SEND
    WAITV(0); stflag(E + 2, rsWS, sFown);
    WAITL0(); SB();
    BAR();
    SB(); STAMP(11);
    if constexpr (GG) __builtin_amdgcn_s_setprio(P2G1);
    constexpr int NB = (GG ? 9 : G0B) - RC;
    SFOR(i, 9)
      if constexpr (i == NB - 2) ldflag();
      if constexpr (i < NB) { issueW2(9 + RC + i, 0); issueW2(9 + RC + i, 1); SB(); }
      if constexpr (EB + i < NE) mm4(EB + i);
    SEND
    asm volatile("s_waitcnt vmcnt(4)" : "+v"(fl1) :: "memory");
    { const unsigned need = !nd1 ? 0u : fc < 14 ? 2u : 1u;
      if (!ALL(!nd1 || fl1 == E + need)) poll(rsWS, lane * FS, sF, E, need); }
    STAMP(7);
#pragma unroll
    for (int j = NE; j < NE + PD; j++) { loadA1(j, 0); loadA1(j, 1); }
    asm volatile("buffer_load_dwordx4 %0, %1, %2, %3 offen" : "=v"(xr) : "v"(vO), "s"(rsX), "s"(sO) : "memory");
    SFOR(i, 9 - RC - NB) issueW2(9 + RC + NB + i, 0); issueW2(9 + RC + NB + i, 1); SEND
    SFOR(i1, NS - NE)
      constexpr int i = NE + i1;
      asm volatile("s_waitcnt vmcnt(%c2)" : "+v"(af[i][0]), "+v"(af[i][1]) : "i"(2 * ((i + PD <= NS ? i + PD : NS) - 1 - i) + (i1 < PD && i < NO + 8 * GG ? 1 + 2 * (9 - RC - NB) : 0)) : "memory");
      if constexpr (i == 9 && i1 == 0) readW(9);
      if constexpr (i + 1 >= 9 && i + 1 < NS) readW(i + 1);
      if constexpr (GG == 0 && i >= NO) {
        auto stale = [&]() AI {
          unsigned m = UMAX(UMAX(UMAX(af[i][0][0], af[i][0][1]), UMAX(af[i][0][2], af[i][0][3])), UMAX(UMAX(af[i][1][0], af[i][1][1]), UMAX(af[i][1][2], af[i][1][3])));
          return ANY(m == ~0u);
        };
        if (__builtin_expect(stale(), 0)) {
          int it = 0;
#pragma nounroll
          do { __builtin_amdgcn_s_sleep(SLEEPR); rel1<true>(af[i][0], af[i][1], rsWS, vL16, sA + (ksb(i) + ksi(i)) * 1024); } while (++it < (1 << 14) && stale());
          SFOR(u, PD - 1) if constexpr (i + 1 + u < NS) rel1<false>(af[i + 1 + u][0], af[i + 1 + u][1], rsWS, vL16, sA + (ksb(i + 1 + u) + ksi(i + 1 + u)) * 1024); SEND
        }
      }
      if constexpr (i == 10) STAMP(8);
      if constexpr (i == 14) STAMP(9);
      SB();
      if constexpr (i + PD < NS) { loadA1(i + PD, 0); loadA1(i + PD, 1); }
      mm4(i);
    SEND
    STAMP(10);
    __builtin_amdgcn_s_setprio(0);
#pragma unroll
    for (int qd = 0; qd < 4; qd++)
#pragma unroll
      for (int k = 0; k < 4; k++) {
        const unsigned ra = ring_area(2 * qd + (k >> 1)) + (k & 1) * 1024;
        ldsw<bf16x8>(((lw2 + vL16) ^ (unsigned)(32 * (4 * (qd >> 1) + k))) + (ra & 0x30000u), ra & 0xffffu, sub4(acc[qd >> 1][qd & 1], k));
      }
    WAITL0(); BAR(); MEMFENCE();
    STAMP(12);
    const unsigned c8 = (ln >> 4) & 7, j2 = (c8 & 4) + 2 * GG + ((c8 >> 1) & 1);
    const unsigned rd = LB + ((0x51407362u >> (4 * j2)) & 7) * 16384 + (c8 & 1) * 1024 + (((8 * wr + (ln >> 7)) << 4) ^ (c8 << 5));
    asm volatile("" : "+v"(xr));
    f32x4 sum[2];
#pragma unroll
    for (int k = 0; k < 2; k++) {
      f32x4 t = {U2F(xr[2 * k] << 16), U2F(xr[2 * k] & 0xffff0000u), U2F(xr[2 * k + 1] << 16), U2F(xr[2 * k + 1] & 0xffff0000u)};
#pragma unroll
      for (int v = 0; v < 8; v++) t += ldsr<f32x4>(rd, v * 2048 + k * 512);
      sum[k] = t;
    }
    u32x4 o = {pk2(sum[0][0], sum[0][1]), pk2(sum[0][2], sum[0][3]), pk2(sum[1][0], sum[1][1]), pk2(sum[1][2], sum[1][3])};
    __builtin_amdgcn_raw_buffer_store_b128(o, mkrs(pout, 2048 * XROW), vO + sO, 0, OAUX);
  }
  STAMPS_OUT();
  };
  if (!attn && g) prog(BoolC<false>{}, IntC<1>{});
  else if (!attn) prog(BoolC<false>{}, IntC<0>{});
  else if (g) prog(BoolC<true>{}, IntC<1>{});
  else prog(BoolC<true>{}, IntC<0>{});
}

extern "C" void kernel_launch(void* const* d_in, const int* in_sizes, int n_in, void* d_out, int out_size, void* d_ws, size_t ws_size,
                              hipStream_t stream) {
  fused_kernel<<<256, 512, 0, stream>>>((const char*)d_in[0], (const char*)d_in[1], (const char*)d_in[5], (const char*)d_in[6], (char*)d_ws,
                                        (const char*)d_in[2], (const char*)d_in[7], (const char*)d_in[3], (char*)d_out);
}
```

```cpp
#include <hip/hip_runtime.h>
#include <hip/hip_bf16.h>
#include <utility>

#define DI __device__ __forceinline__
#define LAS __attribute__((address_space(3)))
typedef __attribute__((ext_vector_type(8))) short bf16x8;
typedef __attribute__((ext_vector_type(4))) short s16x4;
typedef __attribute__((ext_vector_type(2))) __bf16 bf2;
typedef __attribute__((ext_vector_type(2))) float f32x2;
typedef __attribute__((ext_vector_type(4))) float f32x4;
typedef __attribute__((ext_vector_type(16))) float f32x16;
typedef __attribute__((ext_vector_type(2))) unsigned u32x2;
typedef __attribute__((ext_vector_type(4))) unsigned u32x4;
typedef __amdgpu_buffer_rsrc_t rsrc_t;
#define F2U(x) __builtin_bit_cast(unsigned, (float)(x))
#define U2F(x) __builtin_bit_cast(float, (unsigned)(x))
#define ALL(x) (__builtin_amdgcn_ballot_w64(!(x)) == 0ull)
#define ANY(x) (__builtin_amdgcn_ballot_w64(x) != 0ull)
#define UMAX(a, b) __builtin_elementwise_max((unsigned)(a), (unsigned)(b))

constexpr unsigned XROW = 896, QROW = 2688, FROW = 3584;
constexpr unsigned FS = 128;
constexpr unsigned WS_EPOCH = 0;
constexpr unsigned WS_GR = 4096;
constexpr unsigned WS_KVP = 131072;
constexpr unsigned WS_A2 = WS_KVP + 16 * 16 * 8192;
constexpr unsigned WS_FH = WS_A2 + 64 * 144 * 1024 + 262144;
constexpr unsigned WS_KVR = WS_FH + 2048 * FS;
constexpr unsigned WS_END = WS_KVR + 16 * 8192;
constexpr unsigned L_GU = 65536, L_KV = 131072;
constexpr unsigned L_TAIL = L_KV + 3 * 8192;
constexpr unsigned L_WPK = L_TAIL;
constexpr unsigned L_ST = L_TAIL + 2048;
constexpr unsigned L_CS = L_TAIL + 4096;
constexpr unsigned L_PHI = L_GU + 32768;
constexpr unsigned L_V = L_KV + 16384;
constexpr unsigned L_FREE = 32768;

#define MFMA(a, b, c) __builtin_amdgcn_mfma_f32_32x32x16_bf16((a), (b), (c), 0, 0, 0)
#define MFMA16(a, b, c) __builtin_amdgcn_mfma_f32_16x16x32_bf16((a), (b), (c), 0, 0, 0)
#define AI __attribute__((always_inline))
#define BAR() __builtin_amdgcn_s_barrier()
#define WAITV(n) asm volatile("s_waitcnt vmcnt(" #n ")" ::: "memory")
#define WAITL0() asm volatile("s_waitcnt lgkmcnt(0)" ::: "memory")
#define SB() __builtin_amdgcn_sched_barrier(0)
#define MEMFENCE() asm volatile("" ::: "memory")
#define BARF() do { MEMFENCE(); SB(); BAR(); MEMFENCE(); SB(); } while (0)

template <int N> DI void waitv() { asm volatile("s_waitcnt vmcnt(%c0)" :: "i"(N) : "memory"); }

DI rsrc_t mkrs(const void* p, unsigned bytes) { return __builtin_amdgcn_make_buffer_rsrc((void*)p, (short)0, (int)bytes, 0x00020000); }
DI void dma(unsigned lds, unsigned limm, unsigned voff, rsrc_t rs, unsigned soff, unsigned simm) {
  unsigned t;
  asm volatile("s_add_u32 m0, %1, %c5\n\ts_add_u32 %0, %4, %c6\n\tbuffer_load_dwordx4 %2, %3, %0 offen lds"
               : "=&s"(t) : "s"(lds), "v"(voff), "s"(rs), "s"(soff), "i"(limm), "i"(simm) : "memory", "scc");
}
DI s16x4 trd2(unsigned a, unsigned off) { return __builtin_amdgcn_ds_read_tr16_b64_v4i16((LAS s16x4*)((LAS char*)(size_t)a + off)); }
DI bf16x8 tr2(unsigned a, unsigned off0, unsigned off1) {
  LAS char* p = (LAS char*)(size_t)a;
  s16x4 lo = __builtin_amdgcn_ds_read_tr16_b64_v4i16((LAS s16x4*)(p + off0));
  s16x4 hi = __builtin_amdgcn_ds_read_tr16_b64_v4i16((LAS s16x4*)(p + off1));
  return __builtin_shufflevector(lo, hi, 0, 1, 2, 3, 4, 5, 6, 7);
}
template <class T> DI T ldsr(unsigned a, unsigned off) { return *(LAS T*)((LAS char*)(size_t)a + off); }
template <class T> DI void ldsw(unsigned a, unsigned off, T v) { *(LAS T*)((LAS char*)(size_t)a + off) = v; }
DI unsigned pk2(float a, float b) { f32x2 f = {a, b}; return __builtin_bit_cast(unsigned, __builtin_convertvector(f, bf2)); }
DI float rbf(float a) { return U2F(pk2(a, a) << 16); }
DI float dot2(unsigned a, unsigned b, float c) { return __builtin_amdgcn_fdot2_f32_bf16(__builtin_bit_cast(bf2, a), __builtin_bit_cast(bf2, b), c, false); }
DI float xsum(float v) { auto r = __builtin_amdgcn_permlane32_swap(F2U(v), F2U(v), false, false); float a = U2F(r[0]); asm("" : "+v"(a)); return a + U2F(r[1]); }
DI float xmax(float v) { auto r = __builtin_amdgcn_permlane32_swap(F2U(v), F2U(v), false, false); return __builtin_fmaxf(U2F(r[0]), U2F(r[1])); }
DI float xmin(float v) { auto r = __builtin_amdgcn_permlane32_swap(F2U(v), F2U(v), false, false); return __builtin_fminf(U2F(r[0]), U2F(r[1])); }
DI float xhi(float v) { auto r = __builtin_amdgcn_permlane32_swap(F2U(v), F2U(v), false, false); return U2F(r[1]); }
DI void st16(u32x4 v, rsrc_t rs, unsigned voff, unsigned soff, unsigned simm) {
  unsigned t;
  asm volatile("s_add_u32 %0, %4, %c5\n\tbuffer_store_dwordx4 %1, %2, %3, %0 offen sc1\n\ts_nop 1" : "=&s"(t) : "v"(v), "v"(voff), "s"(rs), "s"(soff), "i"(simm) : "memory", "scc");
}
DI void stflag(unsigned v, rsrc_t rs, unsigned soff) {
  unsigned voff = (__builtin_amdgcn_mbcnt_hi(~0u, __builtin_amdgcn_mbcnt_lo(~0u, 0u)) == 0u) ? 0u : 0x80000000u;
  __builtin_amdgcn_raw_buffer_store_b32(v, rs, voff, soff, 16);
}
constexpr int SLEEPN = 2, SLEEPR = 4;
DI u32x4 ld16(rsrc_t rs, unsigned voff, unsigned soff, unsigned simm) {
  u32x4 r; unsigned t;
  asm volatile("s_add_u32 %1, %4, %c5\n\tbuffer_load_dwordx4 %0, %2, %3, %1 offen sc1" : "=v"(r), "=&s"(t) : "v"(voff), "s"(rs), "s"(soff), "i"(simm) : "memory", "scc");
  return r;
}
template <unsigned STEP> DI void ld8w(u32x4 (&p)[8], rsrc_t rs, unsigned voff, unsigned soff) {
  unsigned t;
#define L1(k) "buffer_load_dwordx4 %" #k ", %9, %10, %8 offen sc1\n\ts_add_u32 %8, %8, %c12\n\t"
  asm volatile("s_mov_b32 %8, %11\n\t" L1(0) L1(1) L1(2) L1(3) L1(4) L1(5) L1(6) L1(7) "s_waitcnt vmcnt(0)"
               : "=&v"(p[0]), "=&v"(p[1]), "=&v"(p[2]), "=&v"(p[3]), "=&v"(p[4]), "=&v"(p[5]), "=&v"(p[6]), "=&v"(p[7]), "=&s"(t)
               : "v"(voff), "s"(rs), "s"(soff), "i"(STEP) : "memory", "scc");
#undef L1
}
DI bool untag(u32x4 (&p)[8], unsigned tag, unsigned mask) {
  unsigned chk = 0;
#pragma unroll
  for (int i = 0; i < 8; i++)
#pragma unroll
    for (int k = 0; k < 4; k++) { p[i][k] ^= tag; chk |= p[i][k]; }
  return ALL((chk & mask) == 0u);
}
template <unsigned STEP> DI void waittag(u32x4 (&p)[8], rsrc_t rs, unsigned voff, unsigned soff, unsigned tag, unsigned mask) {
  asm volatile("s_waitcnt vmcnt(0)" : "+v"(p[0]), "+v"(p[1]), "+v"(p[2]), "+v"(p[3]), "+v"(p[4]), "+v"(p[5]), "+v"(p[6]), "+v"(p[7]) :: "memory");
#pragma nounroll
  for (int it = 0; !untag(p, tag, mask) && it < (1 << 16); ++it) { __builtin_amdgcn_s_sleep(SLEEPN); ld8w<STEP>(p, rs, voff, soff); }
}
template <bool WAIT> DI void rel1(u32x4& a, u32x4& b, rsrc_t rs, unsigned voff, unsigned so) {
  unsigned t;
  if constexpr (WAIT) asm volatile("buffer_load_dwordx4 %0, %3, %4, %5 offen sc1\n\ts_add_u32 %2, %5, 147456\n\tbuffer_load_dwordx4 %1, %3, %4, %2 offen sc1\n\ts_waitcnt vmcnt(0)"
                                   : "+v"(a), "+v"(b), "=&s"(t) : "v"(voff), "s"(rs), "s"(so) : "memory", "scc");
  else asm volatile("buffer_load_dwordx4 %0, %3, %4, %5 offen sc1\n\ts_add_u32 %2, %5, 147456\n\tbuffer_load_dwordx4 %1, %3, %4, %2 offen sc1"
                    : "+v"(a), "+v"(b), "=&s"(t) : "v"(voff), "s"(rs), "s"(so) : "memory", "scc");
}
DI void poll(rsrc_t rs, unsigned voff, unsigned soff, unsigned E, unsigned need) {
  voff = need ? voff : 0x80000000u;
#pragma nounroll
  for (int it = 0; it < (1 << 16); ++it) {
    unsigned v;
    asm volatile("buffer_load_dword %0, %1, %2, %3 offen sc1\n\ts_waitcnt vmcnt(0)" : "=v"(v) : "v"(voff), "s"(rs), "s"(soff) : "memory");
    bool ok = (need == 0u) || ((v - E - need) <= (3u - need));
    if (ALL(ok)) break;
    __builtin_amdgcn_s_sleep(SLEEPN);
  }
}

template <bool B> struct BoolC { static constexpr bool value = B; };
template <int I> struct IntC { static constexpr int value = I; };
template <int... I, class F> DI void sfor_impl(std::integer_sequence<int, I...>, F&& f) { (f(IntC<I>{}), ...); }
template <int N, class F> DI void sfor(F&& f) { sfor_impl(std::make_integer_sequence<int, N>{}, f); }
#define SFOR(n, N) sfor<N>([&](auto n##_) AI { constexpr int n = decltype(n##_)::value;
#define SEND });
struct RowC { float r, K1, K2, Ri, Lr; };

DI unsigned hilo(float v) { float hi = rbf(v); return pk2(hi, v - hi); }
DI float max3(float a, float b, float c) { return __builtin_fmaxf(__builtin_fmaxf(a, b), c); }
DI float min3(float a, float b, float c) { return __builtin_fminf(__builtin_fminf(a, b), c); }
DI unsigned pk2s(float a, float b) { unsigned r; asm("v_cvt_pk_bf16_f32 %0, %1, %2" : "=v"(r) : "v"(a), "v"(b)); return r; }
DI f32x2 sub2(const f32x16& v, int k) { f32x2 t = {v[2 * k], v[2 * k + 1]}; return t; }
DI f32x2 exp2v(f32x2 v) { f32x2 r = {__builtin_amdgcn_exp2f(v[0]), __builtin_amdgcn_exp2f(v[1])}; return r; }
DI f32x2 dup2(float a) { f32x2 r = {a, a}; asm("" : "+v"(r)); return r; }
DI f32x2 fma2(f32x2 a, f32x2 b, f32x2 c) { return __builtin_elementwise_fma(a, b, c); }
DI u32x4 epiP(const f32x16& a, const RowC& c) {
  u32x4 r;
  const f32x2 K1 = dup2(c.K1), K2 = dup2(c.K2), Ri = dup2(c.Ri);
#pragma unroll
  for (int i = 0; i < 4; i++) {
    const f32x2 g = sub2(a, i), d = fma2(exp2v(g * fma2(g * g, K2, K1)), Ri, Ri);
    const f32x2 s = {__builtin_amdgcn_rcpf(d[0]), __builtin_amdgcn_rcpf(d[1])}, h = (g * sub2(a, 4 + i)) * s;
    r[i] = pk2(h[0], h[1]);
  }
  return r;
}
DI f32x16 featproj2(const f32x16& a, bf16x8 w0, bf16x8 w1, bool hh) {
  u32x4 q0, q1;
#pragma unroll
  for (int i = 0; i < 4; i++) { q0[i] = pk2(a[2 * i], a[2 * i + 1]); q1[i] = pk2(a[8 + 2 * i], a[9 + 2 * i]); }
  q1[2] = hh ? 0u : q1[2]; q1[3] = hh ? 0u : q1[3];
  f32x16 z;
#pragma unroll
  for (int i = 0; i < 16; i++) z[i] = 0.f;
  z = MFMA(w0, __builtin_bit_cast(bf16x8, q0), z);
  z = MFMA(w1, __builtin_bit_cast(bf16x8, q1), z);
  return z;
}
DI f32x16 featproj(const f32x16& a, unsigned wpaddr, unsigned off, bool hh) {
  return featproj2(a, tr2(wpaddr, off, off + 8 * 56), tr2(wpaddr, off + 16 * 56, off + 24 * 56), hh);
}
DI float max16(const f32x16& p) { float m = max3(p[0], p[1], p[2]); _Pragma("unroll") for (int i = 3; i < 15; i += 2) m = max3(m, p[i], p[i + 1]); return xmax(__builtin_fmaxf(m, p[15])); }
DI float min16(const f32x16& p) { float m = min3(p[0], p[1], p[2]); _Pragma("unroll") for (int i = 3; i < 15; i += 2) m = min3(m, p[i], p[i + 1]); return xmin(__builtin_fminf(m, p[15])); }
DI f32x16 dualexpP(const f32x16& p, float Lr) {
  const f32x2 bx = dup2(-max16(p) * Lr), bn = dup2(min16(p) * Lr), L2 = dup2(Lr);
  f32x16 o;
#pragma unroll
  for (int i = 0; i < 8; i++) {
    const f32x2 ep = exp2v(fma2(sub2(p, i), L2, bx)), en = exp2v(fma2(sub2(p, i), -L2, bn));
    o[i] = U2F(pk2(ep[0], ep[1])); o[8 + i] = U2F(pk2(en[0], en[1]));
  }
  return o;
}
struct SmS { f32x16 p, e; float en[16], bx, bn, sp, sn, tp, tn; bf16x8 w0, w1; };
DI bf16x8 sub4(const f32x16& v, int k) { f32x4 t = {v[4 * k], v[4 * k + 1], v[4 * k + 2], v[4 * k + 3]}; return __builtin_bit_cast(bf16x8, t); }

DI constexpr unsigned lc1_base(int ch) { return ch == 6 ? L_KV : ((ch & 1) ? L_GU : 0u) + (ch < 2 ? 49152u : ch < 4 ? 0u : 16384u); }
template <int CTRL, int RM> DI float dppadd(float v) { return v + U2F(__builtin_amdgcn_update_dpp(0u, F2U(v), CTRL, RM, 0xf, false)); }
DI float wavesum63(float v) {
  v = dppadd<0x111, 0xf>(v); v = dppadd<0x112, 0xf>(v); v = dppadd<0x114, 0xf>(v); v = dppadd<0x118, 0xf>(v);
  v = dppadd<0x142, 0xa>(v); return dppadd<0x143, 0xc>(v);
}

constexpr int PD = 6, RING = 9, XS = 2, XN = 20, F1N = 9, TN = 10, CK1 = 20, RC = 3, EB = 3, G0B = 9, H0L = 14, H0C = 22;
constexpr unsigned CODE_BYTES = 65536;
DI constexpr unsigned ring_area(int j) { return j == 0 ? L_FREE : j == 1 ? L_PHI : lc1_base(j - 2); }

__global__ __launch_bounds__(512, 2) void fused_kernel(const char* px_, const char* pqkv_, const char* pwg_, const char* pwu_, char* ws_, const char* pphi_,
                                                       const char* pwd_, const char* pwo_, char* pout_) {
  typedef __attribute__((ext_vector_type(8))) unsigned long long u64x8;
  u64x8 ka; unsigned long long kb;
  asm volatile("s_load_dwordx16 %0, %2, 0\n\ts_load_dwordx2 %1, %2, 64" : "=&s"(ka), "=&s"(kb) : "s"(__builtin_amdgcn_kernarg_segment_ptr()));
  __shared__ __attribute__((aligned(16))) char smem[163840];
  const unsigned LB = (unsigned)(size_t)smem;
  const unsigned tid = __builtin_amdgcn_workitem_id_x(), lane = tid & 63;
  const unsigned w = __builtin_amdgcn_readfirstlane(tid >> 6), g = w >> 2, wr = w & 3;
  const unsigned b = __builtin_amdgcn_workgroup_id_x(), xcd = b & 7, bi = b >> 3;
  const unsigned half = (xcd >> 1) & 1, cp = 4 * (xcd & 1) + (bi >> 3), rb = 8 * (xcd >> 2) + (bi & 7), rsum = (bi >> 3) + (bi & 7);
  const unsigned f2 = 32 * (xcd & 1) + bi - 8, t2 = (f2 * 37) >> 8, ci = f2 - 7 * t2, rbl = t2 & 3, rt2 = 2 * (8 * (xcd >> 1) + t2);
  const unsigned c = 7 * half + cp - 1;
  const unsigned head = 8 * half + (((half ? 0x76425310u : 0x75316420u) >> (4 * cp)) & 7);
  const bool attn = cp == 0;
  const unsigned wstr = attn ? QROW : FROW;
  const unsigned sG0 = half * (attn ? 448 : 1792) + (attn ? 0 : (cp - 1) * 256), sX0 = rb * 128 * XROW;
  unsigned vx0, vx1, vG, vU, vKV;
  auto voffs = [&](unsigned ln) AI {
    const unsigned r8 = ln >> 3, c8 = ln & 7, sa = (r8 >> 1) & 1, lc = c8 ^ (4 * sa);
    vx0 = (16 * w + r8) * XROW + ((c8 ^ (r8 >> 1)) << 4); vx1 = (vx0 ^ 64u) + 8 * XROW;
    const unsigned cg = attn ? lc : (c8 ^ (2 * sa)), cu = attn ? (lc >= 6 ? lc - 2 : lc) : (cg ^ 4);
    vG = (8 * w + r8) * wstr + (cg << 4); vU = (8 * w + r8) * wstr + (cu << 4);
    vKV = (8 * w + r8) * QROW + 896 * (1 + (lc >> 2)) + ((lc & 3) << 4) + ((56 * head) & ~15u);
  };
  voffs(lane);
  const unsigned lw1 = LB + w * 1024, lw2 = LB + w * 2048;
  const unsigned rot = ((0x124924u >> (2 * rsum)) & 3) * 2;
  const unsigned srow = 128 * rb + 64 * half + 8 * cp + w;
  unsigned vrow = lane < 56 ? lane * 16 : 0x80000000u;
  asm volatile("s_waitcnt lgkmcnt(0)" : "+s"(ka), "+s"(kb), "+v"(vx0), "+v"(vx1), "+v"(vG), "+v"(vU), "+v"(vKV), "+v"(vrow));
  const char *px = (const char*)ka[0], *pqkv = (const char*)ka[1], *pwg = (const char*)ka[2], *pwu = (const char*)ka[3], *pphi = (const char*)ka[5],
             *pwd = (const char*)ka[6], *pwo = (const char*)ka[7];
  char *ws = (char*)ka[4], *pout = (char*)kb;
  const rsrc_t rsX = mkrs(px, 2048 * XROW), rsQ = mkrs(pqkv, 448 * QROW);
  const rsrc_t rsG = mkrs(attn ? pqkv : pwg, 448 * wstr), rsU = mkrs(attn ? pqkv + 128 : pwu, 448 * wstr - (attn ? 128 : 0));
  auto cidx = [&](int st) AI -> unsigned { unsigned t = rot + st; return t >= 7 ? t - 7 : t; };
  auto issueA = [&](int st, int k) AI {
    const unsigned sl = st & 3, ch = cidx(st);
    if (k == 0) dma(lw2, sl * 16384, vx0, rsX, sX0 + ch * 128, 0);
    if (k == 1) dma(lw2, sl * 16384 + 1024, vx1, rsX, sX0 + ch * 128, 0);
    if (k == 2) dma(lw1, L_GU + sl * 16384, vG, rsG, sG0 + ch * 64 * wstr, 0);
    if (k == 3) dma(lw1, L_GU + sl * 16384 + 8192, vU, rsU, sG0 + ch * 64 * wstr, 0);
    if (k == 4) dma(lw1, L_KV + sl * 8192, vKV, rsQ, ch * 64 * QROW, 0);
  };
  const rsrc_t rsWS = mkrs(ws, WS_END);
  unsigned Ev, cpf; u32x4 xrw;
  asm volatile("buffer_load_dwordx4 %0, %1, %2, %3 offen" : "=v"(xrw) : "v"(vrow), "s"(rsX), "s"(srow * XROW) : "memory");
  asm volatile("buffer_load_dword %0, off, %1, 0 sc1" : "=v"(Ev) : "s"(rsWS) : "memory");
  {
    unsigned long long pc; asm volatile("s_getpc_b64 %0" : "=s"(pc));
    const unsigned vo = w == 0 && lane < 24 ? (bi * 24 + lane) * 128 : 0x80000000u;
    asm volatile("buffer_load_dword %0, %1, %2, 0 offen" : "=v"(cpf) : "v"(vo), "s"(mkrs((const void*)pc, CODE_BYTES)) : "memory");
  }
#pragma unroll
  for (int k = 0; k < 5; k++) issueA(0, k);
  asm volatile("s_waitcnt vmcnt(5)" : "+v"(xrw), "+v"(Ev), "+v"(cpf) :: "memory");
#pragma unroll
  for (int k = 0; k < 5; k++) issueA(1, k);
  issueA(2, 0);
  const unsigned E = __builtin_amdgcn_readfirstlane(Ev);
  {
    float s1 = 0.f, s2 = 0.f;
#pragma unroll
    for (int i = 0; i < 4; i++) { s1 = dot2(xrw[i], 0x3f803f80u, s1); s2 = dot2(xrw[i], xrw[i], s2); }
    s1 = wavesum63(s1); s2 = wavesum63(s2);
    const float mean = s1 / 448.f, var = __builtin_fmaxf(s2 * (1.f / 448.f) - mean * mean, 0.f);
    u32x2 gr = {pk2(-mean, __builtin_amdgcn_sqrtf(rbf(rbf(var) + 1e-5f))), E + 1};
    __builtin_amdgcn_raw_buffer_store_b64(gr, rsWS, lane == 63 ? 0u : 0x80000000u, WS_GR + srow * 8, 16);
  }

  auto prog = [&](auto at_, auto gg_) AI {
  constexpr bool AT = decltype(at_)::value; constexpr int GG = decltype(gg_)::value;
  constexpr unsigned lcstep = AT ? 224 : 128;
  unsigned lane;
  asm volatile("v_mbcnt_lo_u32_b32 %0, -1, 0\n\tv_mbcnt_hi_u32_b32 %0, -1, %0" : "=v"(lane));
  __builtin_assume(lane < 64);
  voffs(lane);
  auto issueL = [&](int hc) AI {
    const int st = hc >> 1; const unsigned ch = cidx(st);
    if (hc & 1) dma(lw1, lc1_base(st) + 8192, vU, rsU, sG0 + ch * 64 * wstr, lcstep);
    else dma(lw1, lc1_base(st), vG, rsG, sG0 + ch * 64 * wstr, lcstep);
  };
  auto issueG = [&](int bar, int k) AI {
    if (bar == 0) issueA(2 + k / 5, k % 5);
    else if (bar <= 3) issueA(bar + 3, k);
    else if (k < 4) issueL(4 * (bar - 4) + k);
    else if (bar == 4) dma(LB + (w & 1) * 1024, L_WPK, lane * 16, mkrs(pphi, 16 * 1568), head * 1568 + (w & 1) * 1024, 0);
    else issueL(12 + (bar - 5));
  };

  const unsigned rt = 4 * rb + wr;
  const unsigned sA2 = WS_A2 + rt * 144 * 1024 + (AT ? half * 16384 : c * 8192), vL16 = lane * 16;
  const u32x4 sent = {~0u, ~0u, ~0u, ~0u};
  auto clr = [&](int ks) AI { st16(sent, rsWS, vL16, sA2, ks * 1024); };

  auto clri = [&](int q) AI { clr(GG ? (q < 6 ? 114 + q : 120 + q) : (q < 2 ? 112 + q : 118 + q)); };

  const bool hh = lane >= 32;
  const unsigned hu = lane >> 5, blk = (lane >> 4) & 1, q4 = (lane >> 2) & 3, p4 = lane & 3, ta = (q4 >> 1) & 1, l31 = lane & 31;
  const unsigned perm = wr ^ (2 * GG);
  const unsigned rowoff = (8 * hu + q4) * 128 + (perm << 11);
  const unsigned pc = (4 * blk + p4) < 6 ? (4 * blk + p4) : 6;
  auto tileaddr = [&](unsigned t) AI -> unsigned {
    if constexpr (AT) { unsigned o = 56 * t + 8 * pc; return (o >> 7) * 8192 + ((((o >> 4) & 7) ^ (4 * ta)) << 4) + (o & 8) + rowoff; }
    else return blk * 8192 + (((2 * t + (p4 >> 1)) ^ (4 * blk + 2 * ta)) << 4) + 8 * (p4 & 1) + rowoff;
  };
  constexpr int NT = !AT ? 2 : GG ? 1 : 3, TB0 = GG ? 4 - NT : 0;
  unsigned aA[3], aB[NT];
  {
    unsigned o = (head & 1) * 8 + 8 * pc;
    unsigned ak = L_KV + (((o >> 4) ^ (4 * ta)) << 4) + (o & 8) + rowoff;
    aA[0] = LB + (GG ? L_GU + tileaddr(1) : ak);
    aA[1] = LB + (GG ? L_GU + tileaddr(2) : (ak ^ 64u));
    aA[2] = LB + L_GU + tileaddr(GG ? 3 : 0);
#pragma unroll
    for (int j = 0; j < NT; j++) aB[j] = LB + tileaddr(TB0 + j);
  }
  const unsigned xrow = 32 * wr + l31;
  const unsigned aX = LB + xrow * 128 + (((2 * perm + hu) ^ ((xrow >> 1) & 7)) << 4);
  const unsigned selv = (((lane >> 4) ^ (lane >> 2)) & 1) ? 0u : 0x3f803f80u;
  const u32x4 sel4 = {selv, selv, selv, selv};
  const bf16x8 sel = __builtin_bit_cast(bf16x8, sel4);

  bf16x8 xf[28], wa[2][3], wq[8];
  f32x16 accA[3], accB[NT];
  f32x4 csA[3], csB[NT];
#pragma unroll
  for (int j = 0; j < 3; j++) { for (int i = 0; i < 16; i++) accA[j][i] = 0.f; for (int i = 0; i < 4; i++) csA[j][i] = 0.f; }
#pragma unroll
  for (int j = 0; j < NT; j++) { for (int i = 0; i < 16; i++) accB[j][i] = 0.f; for (int i = 0; i < 4; i++) csB[j][i] = 0.f; }
  u32x2 grn;

  const rsrc_t rsWD = mkrs(pwd, 1792 * XROW), rsWO = mkrs(pwo, 448 * XROW);
  const unsigned vW2 = (lane >> 3) * XROW + (((lane & 7) ^ ((lane >> 2) & 4)) << 4) + ci * 128;
  constexpr int NS = GG ? 16 + XS : 20 - XS, NC = 6 - XS / 2, NO = 2 * NC;
  const unsigned rho = (rbl + ci) & 3;
  auto t_of = [](int i) constexpr { return (0x54763210u >> (4 * ((i >> 1) & 7))) & 7; };
  constexpr int NE = GG ? 9 : 5;
  auto g1i = [](int i) constexpr { return i < 8 ? i : i == 8 ? 16 : i < 17 ? i - 1 : 17; };
  auto ksb = [&](int i) AI -> unsigned {
    if (GG) i = g1i(i);
    if (GG) return i < 16 ? 16 * wr + ((t_of(i) & 3) ^ rho) : wr;
    if (i < NO) { const unsigned cc = i % NC + rho + (XS ? 0 : rho >> 1); return wr + 8 * (cc >= NC ? cc - NC : cc); }
    return 8 * wr + 2 * (((i - NO) >> 1) ^ rho);
  };
  auto ksi = [&](int i) AI -> unsigned { if (GG) i = g1i(i); return GG ? (i < 16 ? 8 * (i & 1) + (t_of(i) & 4) : 4 * i) : i < NO ? 64 + 4 * XS + 4 * (i / NC) : 112 + (i & 1); };
  auto issueW2 = [&](int i, int hf) AI {
    if (GG == 0 && i >= NO) dma(lw2, ring_area(i % RING) + hf * 1024, vW2, rsWO, ksb(i) * 14 * XROW, (16 * (i & 1) + 8 * hf) * XROW);
    else dma(lw2, ring_area(i % RING) + hf * 1024, vW2, rsWD, ksb(i) * 16 * XROW, (ksi(i) * 16 + 8 * hf) * XROW);
  };

  const unsigned aCSw = LB + L_CS + l31 * 16 + wr * 4 + GG * 2048, aCSr = LB + L_CS + l31 * 16 + GG * 2048;
  auto readX = [&](int n) AI { xf[n] = ldsr<bf16x8>(aX ^ (unsigned)((n & 3) << 5), ((n >> 2) & 3) * 16384); };
  auto readA = [&](int n, int j) AI {
    const unsigned o = ((n >> 2) & 3) * ((GG == 0 && j < 2) ? 8192 : 16384);
    wa[n & 1][j] = tr2(aA[j] ^ (unsigned)((n & 3) << 11), o, o + 512);
  };
  auto readB = [&](int q) AI {
    const int n = q / NT, j = q % NT;
    const unsigned base = lc1_base(n >> 2);
    if ((NT == 3 && n < XN) ? (n & 3) == 0 : n == 24) asm volatile("" : "+v"(aB[j]));
    wq[q & 7] = tr2(aB[j] ^ (((n & 3) << 11) | (base & 0x30000u)), base & 0xffffu, (base & 0xffffu) + 512);
  };
  RowC rc; unsigned nm, denb;
  auto mkxe = [&]() AI { asm volatile("" : "+v"(nm)); u32x4 t = {nm, nm, nm, nm}; return __builtin_bit_cast(bf16x8, t); };
  auto rowconst = [&]() AI {
#pragma nounroll
    for (int it = 0; it < (1 << 16) && !ALL(grn[1] == E + 1); ++it) {
      __builtin_amdgcn_s_sleep(SLEEPN);
      asm volatile("buffer_load_dwordx2 %0, %1, %2, %3 offen sc1\n\ts_waitcnt vmcnt(0)" : "=v"(grn) : "v"(xrow * 8), "s"(rsWS), "s"(WS_GR + rb * 1024) : "memory");
    }
    const float den = U2F(grn[0] & 0xffff0000u);
    nm = hh ? 0u : __builtin_amdgcn_perm(grn[0], grn[0], 0x01000100u);
    denb = grn[0] >> 16;
    rc.r = __builtin_amdgcn_rcpf(den);
    const float k1 = -2.f * 1.4426950408889634f * 0.7978845608028654f;
    rc.K1 = k1 * rc.r; rc.K2 = (k1 * 0.044715f) * rc.r * (rc.r * rc.r); rc.Ri = den * den; rc.Lr = 1.4426950408889634f * rc.r;
  };
  WAITV(7);
  BAR(); MEMFENCE();
  readX(0); readA(0, 0); readA(0, 1); readA(0, 2);
  SFOR(n, 28)
    SB();
    if constexpr (n == 27) {
#pragma unroll
      for (int j = 0; j < 3; j++) ldsw<unsigned>(aCSw, j * 512, hilo(csA[j][0]));
    }
    if constexpr ((n & 3) == 3) {
      if constexpr (n == 27) WAITV(0);
      else if constexpr (n == 23) asm volatile("s_waitcnt vmcnt(10)" : "+v"(grn) :: "memory");
      else if constexpr (n == 3 || n == 15 || n == 19) WAITV(11); else WAITV(10);
      { WAITL0(); BAR(); MEMFENCE(); }
    }
    constexpr int bar = (n + 1) >> 2, ph = (n + 1) & 3;
#pragma unroll
    for (int j = 0; j < 3; j++) {
      if constexpr (n < 27) { if (j == 0) readX(n + 1); readA(n + 1, j); }
      else { wa[0][j] = ldsr<bf16x8>(aCSr, j * 512); if (AT && GG == 1 && j < 2) { readB(2 * j); readB(2 * j + 1); } }
      SB();
      accA[j] = MFMA(wa[n & 1][j], xf[n], accA[j]);
      const int slot = 3 * ph + j;
      if (bar == 0) issueG(0, slot - 2);
      else if (bar == 6) { if (slot < 5) issueG(bar, slot); }
      else if (bar <= 6) { if (slot == 0) issueG(bar, 0); if (slot == 2) issueG(bar, 1); if (slot == 5) issueG(bar, 2); if (slot == 7) issueG(bar, 3); if (slot == 10) issueG(bar, 4); }
      else if (j < 2) {
        if constexpr (AT) dma(lw2, L_FREE + j * 1024, lane * 16, mkrs(pphi, 16 * 1568), half * 8 * 1568 + w * 2048, j * 1024);
        else issueW2(0, j);
      }
      if ((n & 3) == 0) csA[j] = MFMA16(sel, wa[n & 1][j], csA[j]);
      if (n == 25 && j == 0) rowconst();
      if (n == 14 && j == 2) asm volatile("buffer_load_dwordx2 %0, %1, %2, %3 offen sc1" : "=v"(grn) : "v"(xrow * 8), "s"(rsWS), "s"(WS_GR + rb * 1024) : "memory");
      SB();
    }
  SEND
  { const bf16x8 xe = mkxe();
#pragma unroll
  for (int j = 0; j < 3; j++) accA[j] = MFMA(wa[0][j], xe, accA[j]); }
  asm volatile("" : "+v"(accA[0]), "+v"(accA[1]), "+v"(accA[2]));
  SB();

  const unsigned sFown = WS_FH + (rt * 32 + (AT ? 28 + 2 * half : 2 * c) + GG) * FS;
  const unsigned aWP = LB + (4 * hu + q4) * 56 + 8 * pc;
  const unsigned T12 = (E + 1) & 0xfffu, T16 = (E + 1) & 0xffffu;
  const unsigned sF = WS_FH + rt2 * 32 * FS;
  const unsigned fc = l31 >> 1;
  const bool nd1 = GG ? (fc >> 1) == wr || (fc >= 8 && fc < 8 + XS / 2) : (fc >= 8 + XS / 2 && fc < 14) || fc == 14 + (wr >> 1);
  const unsigned aIW = LB + L_PHI + xrow * 64 + ((hu ^ ((xrow >> 1) & 7)) << 3);
  SmS sm;
  unsigned aWK = aWP + L_WPK, aWQ = aWP + L_FREE;
  asm volatile("" : "+v"(aWK)); if constexpr (AT) asm volatile("" : "+v"(aWQ));
  auto kstep = [&](auto s_) AI {
    constexpr int s = decltype(s_)::value;
    if constexpr (s == 0) { sm.w0 = tr2(aWK, 0, 8 * 56); sm.w1 = tr2(aWK, 16 * 56, 24 * 56); }
    if constexpr (s == 1) sm.p = featproj2(accA[0], sm.w0, sm.w1, hh);
    if constexpr (s == 2 || s == 3) {
#pragma unroll
      for (int k = 2 * s - 4; k < 2 * s - 2; k++) {
        u32x2 vv = {pk2(accA[1][4 * k], accA[1][4 * k + 1]), pk2(accA[1][4 * k + 2], accA[1][4 * k + 3])};
        if (k == 3) vv[0] = hh ? denb : vv[0];
        ldsw<u32x2>(aIW ^ (unsigned)(k << 4), L_V - L_PHI, vv);
      }
    }
    if constexpr (s == 4) sm.bx = -max16(sm.p) * rc.Lr;
    if constexpr (s == 5) sm.bn = min16(sm.p) * rc.Lr;
    if constexpr (s >= 6 && s < 22) {
      constexpr int i = s - 6;
      sm.e[i] = __builtin_amdgcn_exp2f(__builtin_fmaf(sm.p[i], rc.Lr, sm.bx)); sm.en[i] = __builtin_amdgcn_exp2f(__builtin_fmaf(sm.p[i], -rc.Lr, sm.bn));
      if constexpr (i == 0) { sm.sp = sm.e[0]; sm.sn = sm.en[0]; } else if constexpr (i < 12) { sm.sp += sm.e[i]; sm.sn += sm.en[i]; }
      else if constexpr (i == 12) { sm.tp = sm.e[12]; sm.tn = sm.en[12]; } else { sm.tp += sm.e[i]; sm.tn += sm.en[i]; }
    }
    if constexpr (s == 22) { sm.sp = __builtin_amdgcn_rcpf(xsum(sm.sp + (hh ? 0.f : sm.tp))) * rc.r; sm.sn = __builtin_amdgcn_rcpf(xsum(sm.sn + (hh ? 0.f : sm.tn))) * rc.r; }
    if constexpr (s >= 23) {
#pragma unroll
      for (int u = (s == 29 ? 6 : s - 23); u <= (s == 29 ? 7 : s - 23); u++) {
        const int k = u >> 1;
        if (u & 1) { u32x2 t = {pk2s(sm.en[4 * k] * sm.sn, sm.en[4 * k + 1] * sm.sn), pk2s(sm.en[4 * k + 2] * sm.sn, sm.en[4 * k + 3] * sm.sn)}; ldsw<u32x2>(aIW ^ (unsigned)(k << 4), 8192, t); }
        else { u32x2 t = {pk2s(sm.e[4 * k] * sm.sp, sm.e[4 * k + 1] * sm.sp), pk2s(sm.e[4 * k + 2] * sm.sp, sm.e[4 * k + 3] * sm.sp)}; ldsw<u32x2>(aIW ^ (unsigned)(k << 4), 0, t); }
      }
    }
  };
  auto qstep = [&](auto s_, f32x16& a, unsigned lh) AI {
    constexpr int s = decltype(s_)::value;
    if constexpr (s == 0) { sm.w0 = tr2(aWQ, lh * 1568, lh * 1568 + 8 * 56); sm.w1 = tr2(aWQ, lh * 1568 + 16 * 56, lh * 1568 + 24 * 56); }
    if constexpr (s == 1) sm.p = featproj2(a, sm.w0, sm.w1, hh);
    if constexpr (s == 3) sm.bx = -max16(sm.p) * rc.Lr;
    if constexpr (s == 4) sm.bn = min16(sm.p) * rc.Lr;
    if constexpr (s >= 5 && s < 21) {
      constexpr int i = (s - 5) >> 1;
      float t0, t1;
      if constexpr ((s - 5) & 1) { t0 = __builtin_fmaf(sm.p[2 * i], -rc.Lr, sm.bn); t1 = __builtin_fmaf(sm.p[2 * i + 1], -rc.Lr, sm.bn); }
      else { t0 = __builtin_fmaf(sm.p[2 * i], rc.Lr, sm.bx); t1 = __builtin_fmaf(sm.p[2 * i + 1], rc.Lr, sm.bx); }
      a[8 * ((s - 5) & 1) + i] = U2F(pk2(__builtin_amdgcn_exp2f(t0), __builtin_amdgcn_exp2f(t1)));
      if constexpr (s == 20) asm volatile("" : "+v"(a));
    }
  };
  bf16x8 ka_[4], kb_[2][4]; f32x4 kd[2];
  auto kvstep = [&](auto s_) AI {
    constexpr int s = decltype(s_)::value;
    if constexpr (s == 0) {
      const unsigned g4 = lane >> 4, r0 = 8 * g4 + q4, sw = 4 * blk + (q4 >> 1);
      unsigned b0 = LB + L_PHI + r0 * 64 + ((p4 ^ sw) << 3);
      unsigned a0 = (b0 ^ ((wr >> 1) << 5)) + (wr & 1) * 8192;
#pragma unroll
      for (int k = 0; k < 4; k++) {
        ka_[k] = __builtin_shufflevector(trd2(a0, k * 2048), trd2(a0 ^ 16u, k * 2048 + 256), 0, 1, 2, 3, 4, 5, 6, 7);
        kb_[0][k] = __builtin_shufflevector(trd2(b0, L_V - L_PHI + k * 2048), trd2(b0 ^ 16u, L_V - L_PHI + k * 2048 + 256), 0, 1, 2, 3, 4, 5, 6, 7);
        kb_[1][k] = __builtin_shufflevector(trd2(b0 ^ 32u, L_V - L_PHI + k * 2048), trd2(b0 ^ 48u, L_V - L_PHI + k * 2048 + 256), 0, 1, 2, 3, 4, 5, 6, 7);
      }
    }
    if constexpr (s == 1) {
      f32x4 z = {0.f, 0.f, 0.f, 0.f}; kd[0] = z; kd[1] = z;
#pragma unroll
      for (int k = 0; k < 4; k++) { kd[0] = MFMA16(ka_[k], kb_[0][k], kd[0]); kd[1] = MFMA16(ka_[k], kb_[1][k], kd[1]); }
    }
    if constexpr (s == 3) {
#pragma unroll
      for (int d = 0; d < 2; d++) {
        u32x4 t;
#pragma unroll
        for (int i = 0; i < 4; i++) t[i] = (F2U(kd[d][i]) & ~0xfffu) | T12;
        st16(t, rsWS, (lane >> 4) * 512 + (lane & 15) * 16, ((head * 16 + rb) * 2 + (wr & 1)) * 4096 + (wr >> 1) * 2048, WS_KVP + d * 256);
      }
    }
  };

  unsigned fl1 = 0;
  u32x4 pv[8];
  const unsigned vKP = l31 * 16 + hu * 8192, sKP = (rb * 32 + half) * 4096 + wr * 1024 + GG * 512, sKR = (8 * half + w) * 8192;
  auto hop1 = [&]() AI {
    waittag<16384>(pv, rsWS, vKP, sKP + WS_KVP, T12, 0xfffu);
    u32x4 o;
#pragma unroll
    for (int k = 0; k < 4; k++) {
      float sum = U2F(pv[0][k]);
#pragma unroll
      for (int i = 1; i < 8; i++) sum += U2F(pv[i][k]);
      sum = xsum(sum);
      sum = l31 == 29 ? 1.f : sum;
      if (GG == 1) sum = wr == 3 ? 0.f : sum;
      o[k] = (pk2s(sum, sum) & 0xffff0000u) | T16;
    }
    st16(o, rsWS, hh ? 0x80000000u : l31 * 16, (rb * 2 + half) * 4096 + wr * 1024 + GG * 512, WS_KVR);
    if (b == 0 && w == 4) stflag(E + 4, rsWS, WS_EPOCH);
    SB();
  };
  bf16x8 cb[NT];
  auto ldflag = [&]() AI { asm volatile("buffer_load_dword %0, %1, %2, %3 offen sc1" : "=v"(fl1) : "v"(nd1 ? lane * FS : 0x80000000u), "s"(rsWS), "s"(sF) : "memory"); };
  auto touch = [&]() AI {
    const unsigned rw = (rbl * 8 + w) * 44 + lane;
    const bool dn = rw < 896;
    const unsigned vo = lane < 44 ? (dn ? ((rw >> 6) * 128 + 64 + (rw & 63)) : rw - 896) * XROW + ci * 128 : 0x80000000u;
    asm volatile("s_add_u32 m0, %0, %c5\n\ts_nop 0\n\tbuffer_load_dword %1, %3, 0 offen lds\n\tbuffer_load_dword %2, %4, 0 offen lds"
                 :: "s"(LB + w * 256), "v"(dn ? vo : 0x80000000u), "v"(dn ? 0x80000000u : vo), "s"(rsWD), "s"(rsWO), "i"(L_ST) : "memory", "scc");
  };
  auto kpath = [&]() AI {
    const f32x16 p = featproj(accA[0], aWK, 0, hh);
#pragma unroll
    for (int k = 0; k < 4; k++) {
      u32x2 vv = {pk2(accA[1][4 * k], accA[1][4 * k + 1]), pk2(accA[1][4 * k + 2], accA[1][4 * k + 3])};
      if (k == 3) vv[0] = hh ? denb : vv[0];
      ldsw<u32x2>(aIW ^ (unsigned)(k << 4), L_V - L_PHI, vv);
    }
    const f32x2 bx = dup2(-max16(p) * rc.Lr), bn = dup2(min16(p) * rc.Lr), L2 = dup2(rc.Lr);
    f32x2 ep[8], en[8];
#pragma unroll
    for (int i = 0; i < 8; i++) { ep[i] = exp2v(fma2(sub2(p, i), L2, bx)); en[i] = exp2v(fma2(sub2(p, i), -L2, bn)); }
    f32x2 sp = ((ep[0] + ep[1]) + (ep[2] + ep[3])) + (ep[4] + ep[5]), sn = ((en[0] + en[1]) + (en[2] + en[3])) + (en[4] + en[5]);
    f32x2 tp = ep[6] + ep[7], tn = en[6] + en[7];
    float a0 = sp[0], a1 = sn[0], a2 = tp[0], a3 = tn[0];
    asm("" : "+v"(a0), "+v"(a1), "+v"(a2), "+v"(a3));
    a0 += sp[1]; a2 += tp[1]; asm("" : "+v"(a0), "+v"(a2)); a1 += sn[1]; a3 += tn[1];
    const f32x2 ip = dup2(__builtin_amdgcn_rcpf(xsum(a0 + (hh ? 0.f : a2))) * rc.r), in = dup2(__builtin_amdgcn_rcpf(xsum(a1 + (hh ? 0.f : a3))) * rc.r);
#pragma unroll
    for (int k = 0; k < 4; k++) {
      const f32x2 u0 = ep[2 * k] * ip, u1 = ep[2 * k + 1] * ip, v0 = en[2 * k] * in, v1 = en[2 * k + 1] * in;
      u32x2 t0 = {pk2(u0[0], u0[1]), pk2(u1[0], u1[1])}, t1 = {pk2(v0[0], v0[1]), pk2(v1[0], v1[1])};
      ldsw<u32x2>(aIW ^ (unsigned)(k << 4), 0, t0); ldsw<u32x2>(aIW ^ (unsigned)(k << 4), 8192, t1);
    }
  };
  const unsigned sA = WS_A2 + rt2 * 144 * 1024;
  u32x4 af[NS][2];
  auto loadA1 = [&](int i, int m) AI { af[i][m] = ld16(rsWS, vL16, sA + ksb(i) * 1024, (144 * m + ksi(i)) * 1024); };
  auto stream = [&]() AI {
    SFOR(q, 28 * NT)
      constexpr int n = q / NT, j = q % NT;
      if constexpr (j == 0) {
        if constexpr (n == 27) {
#pragma unroll
          for (int t = 0; t < NT; t++) ldsw<unsigned>(aCSw, ((3 + t) & 3) * 512, hilo(csB[t][0]));
          if constexpr (AT && GG) WAITV(8);
          WAITL0(); BAR(); MEMFENCE();
        }
        else if constexpr (!AT && (n & 3) == 3) { asm volatile("s_waitcnt lgkmcnt(4)" ::: "memory"); BAR(); MEMFENCE(); }
      }
      if constexpr (q + 4 < 28 * NT) readB(q + 4);
      if constexpr (n == 27) cb[j] = ldsr<bf16x8>(aCSr, ((3 + j) & 3) * 512);
      SB();
      accB[j] = MFMA(wq[q & 7], xf[n], accB[j]);
      if constexpr ((n & 3) == 1) csB[j] = MFMA16(sel, wq[q & 7], csB[j]);
      if constexpr (!AT) {
        if constexpr (j == 0 && n >= 3 && n < 7) issueW2(1 + ((n - 3) >> 1), (n - 3) & 1);
        if constexpr (j == 0 && n >= 7 && n < 27 && (n & 1)) issueW2(1 + ((n + 1) >> 2), ((n + 1) >> 1) & 1);
        if constexpr (n == 27) issueW2(8, j);
        if constexpr (j == 1 && n == F1N) { waitv<(F1N < 7 ? F1N - 2 : 4 + (F1N - 5) / 2) + (TN < F1N ? 2 : 0)>(); stflag(E + 1, rsWS, sFown); }
        if constexpr (j == 1 && n == TN) touch();
        if constexpr (q == 2 * CK1 - 7) ldflag();
        if constexpr (q == 2 * CK1 - 1) {
          asm volatile("s_waitcnt vmcnt(%c5)" : "+v"(fl1), "+v"(accB[0]), "+v"(accB[NT - 1]), "+v"(csB[0]), "+v"(csB[NT - 1]) : "i"((CK1 & 1) ? 1 : 2) : "memory");
          const bool nde = nd1 && fc < 14;
          if (!ALL(!nde || (fl1 - E - 1u) <= 1u)) poll(rsWS, lane * FS, sF, E, nde ? 1u : 0u);
        }
        if constexpr (q >= 2 * CK1 && q < 2 * CK1 + 2 * NE) loadA1((q - 2 * CK1) >> 1, q & 1);
      } else {
        if constexpr (GG == 1 && n < 16 && (n & 1) == 0) clri(n >> 1);
        if constexpr (GG == 0 && j < 2 && n >= 1 && 2 * (n - 1) + j < 21) qstep(IntC<2 * (n - 1) + j>{}, accA[2], 0);
        if constexpr (GG == 0 && q >= 3 * H0L && q < 3 * H0L + 8) pv[q - 3 * H0L] = ld16(rsWS, vKP, sKP, WS_KVP + (q - 3 * H0L) * 16384);
        if constexpr (GG == 0 && j == 2 && n == H0C) { asm volatile("" : "+v"(accB[0]), "+v"(accB[1]), "+v"(accB[NT - 1]), "+v"(csB[0]), "+v"(csB[1]), "+v"(csB[NT - 1])); hop1(); }
      }
      SB();
    SEND
    const bf16x8 xe = mkxe();
#pragma unroll
    for (int t = 0; t < NT; t++) { accB[t] = MFMA(cb[t], xe, accB[t]); asm volatile("" : "+v"(accB[t])); }
    SB();
  };
  unsigned ln = vL16, aW = 0;
  auto mkaw = [&]() AI {
    asm volatile("" : "+v"(ln));
    aW = lw2 + (ln & 0x200) + ((ln & 0xc0) << 1) + ((((ln >> 3) & 0x20) | ((ln >> 1) & 0x10)) ^ ((ln >> 1) & 0x40)) + ((ln >> 1) & 8);
  };
  bf16x8 wf[NS][2];
  auto readW = [&](int i) AI {
    const unsigned ra = ring_area(i % RING), la = aW + (ra & 0x30000u);
    wf[i][0] = tr2(la, ra & 0xffffu, (ra & 0xffffu) + 1024); wf[i][1] = tr2(la ^ 64u, ra & 0xffffu, (ra & 0xffffu) + 1024);
  };
  auto tile = [&](f32x16& a, int t) AI { asm volatile("" : "+v"(a)); st16(epiP(a, rc), rsWS, vL16, sA2, t * 1024); SB(); };
  if constexpr (!AT) {
    if constexpr (GG == 0) {
      kpath(); WAITL0(); BARF();
      kvstep(IntC<0>{}); kvstep(IntC<1>{}); kvstep(IntC<3>{}); SB();
      tile(accA[2], 0);
    } else { tile(accA[0], 1); tile(accA[1], 2); BARF(); tile(accA[2], 3); }
    readB(0); readB(1); readB(2); readB(3);
    stream();
    SFOR(u, (2 * CK1 + 2 * NE > 56 ? 2 * CK1 + 2 * NE - 56 : 0)) loadA1((56 + u - 2 * CK1) >> 1, u & 1); SEND
    mkaw();
    SFOR(i, RC) readW(i); SEND
    if constexpr (RC > 0) { WAITL0(); SB(); SFOR(i, RC) issueW2(9 + i, 0); issueW2(9 + i, 1); SEND }
    tile(accB[0], 4 + 2 * GG); tile(accB[NT - 1], 5 + 2 * GG);
  } else if constexpr (GG == 0) {
    SFOR(q, 8) clri(q); SEND
    SFOR(s, 30) kstep(s_); SEND
    WAITV(8);
    WAITL0(); BARF();
    kvstep(IntC<0>{});
    readB(0); readB(1); readB(2); readB(3);
    kvstep(IntC<1>{}); kvstep(IntC<3>{});
    WAITV(2); stflag(E + 1, rsWS, sFown);
    SB();
    stream();
  } else {
    stream();
    SFOR(s, 21) qstep(s_, accA[0], 1); SEND
    WAITV(0); stflag(E + 1, rsWS, sFown);
    SFOR(s, 21) qstep(s_, accA[1], 2); SEND
    SFOR(s, 21)
      if constexpr (s < 8) pv[s] = ld16(rsWS, vKP, sKP, WS_KVP + s * 16384);
      qstep(s_, accA[2], 3);
    SEND
    hop1();
    BARF();
  }

  if constexpr (AT) {
    auto qexp = [&](f32x16& a, unsigned lh) AI { a = dualexpP(featproj(a, aWQ, lh * 1568, hh), rc.Lr); asm volatile("" : "+v"(a)); SB(); };
    if constexpr (GG == 0) qexp(accB[0], 4);
#pragma unroll
    for (int i = 0; i < 8; i++) pv[i] = ld16(rsWS, vL16, sKR, WS_KVR + i * 1024);
    if constexpr (GG == 0) { qexp(accB[1 % NT], 5); qexp(accB[NT - 1], 6); } else qexp(accB[0], 7);
    waittag<1024>(pv, rsWS, vL16, sKR + WS_KVR, T16, 0xffffu);
#pragma unroll
    for (int i = 0; i < 4; i++) {
      u32x4 f = {__builtin_amdgcn_perm(pv[2 * i][1], pv[2 * i][0], 0x07060302u), __builtin_amdgcn_perm(pv[2 * i][3], pv[2 * i][2], 0x07060302u),
                 __builtin_amdgcn_perm(pv[2 * i + 1][1], pv[2 * i + 1][0], 0x07060302u), __builtin_amdgcn_perm(pv[2 * i + 1][3], pv[2 * i + 1][2], 0x07060302u)};
      ldsw<u32x4>(LB + w * 4096 + vL16, i * 1024, f);
    }
    WAITL0(); BAR(); MEMFENCE();
    f32x16 op[4], on[4];
    auto omm = [&](const f32x16& phi, unsigned lh, int z) AI {
#pragma unroll
      for (int i = 0; i < 16; i++) { op[z][i] = 0.f; on[z][i] = 0.f; }
      bf16x8 kf[4];
#pragma unroll
      for (int i = 0; i < 4; i++) kf[i] = ldsr<bf16x8>(LB + vL16, lh * 4096 + i * 1024);
#pragma unroll
      for (int i = 0; i < 2; i++) { op[z] = MFMA(kf[i], sub4(phi, i), op[z]); on[z] = MFMA(kf[2 + i], sub4(phi, 2 + i), on[z]); }
    };
    auto ofin = [&](unsigned lh, int z) AI {
      const float ia = __builtin_amdgcn_rcpf(op[z][13]), ib = __builtin_amdgcn_rcpf(on[z][13]);
      const float id = __builtin_amdgcn_rcpf(__builtin_fmaf(op[z][12], ia, __builtin_fmaf(on[z][12], ib, 1e-6f)));
      const float ca = xhi(ia * id), cb2 = xhi(ib * id);
      const f32x2 c2 = dup2(ca), d2 = dup2(cb2);
      u32x4 f0, f1;
#pragma unroll
      for (int i = 0; i < 4; i++) {
        const f32x2 u = fma2(sub2(op[z], i), c2, sub2(on[z], i) * d2), v = fma2(sub2(op[z], 4 + i), c2, sub2(on[z], 4 + i) * d2);
        f0[i] = pk2(u[0], u[1]); f1[i] = pk2(v[0], v[1]);
      }
      f1[2] = hh ? 0u : f1[2]; f1[3] = hh ? 0u : f1[3];
      st16(f0, rsWS, vL16, sA2, (112 + 2 * lh) * 1024); st16(f1, rsWS, vL16, sA2, (113 + 2 * lh) * 1024);
      SB();
    };
    if constexpr (GG) { omm(accA[0], 1, 0); omm(accA[1], 2, 1); SB(); ofin(1, 0); omm(accA[2], 3, 2); omm(accB[0], 7, 3); SB(); ofin(2, 1); ofin(3, 2); ofin(7, 3); }
    else { omm(accA[2], 0, 0); omm(accB[0], 4, 1); SB(); ofin(0, 0); omm(accB[1 % NT], 5, 2); omm(accB[NT - 1], 6, 3); SB(); ofin(4, 1); ofin(5, 2); ofin(6, 3); }
  } else {
    f32x16 acc[2][2];
#pragma unroll
    for (int i = 0; i < 4; i++)
#pragma unroll
      for (int k = 0; k < 16; k++) acc[i >> 1][i & 1][k] = 0.f;
    u32x4 xr;
    const unsigned vO = (ln >> 7) * XROW + (ln & 0x70), sO = (rt2 * 32 + 8 * w) * XROW + ci * 128;
    auto mm4 = [&](int i) AI {
#pragma unroll
      for (int nn = 0; nn < 2; nn++)
#pragma unroll
        for (int mm = 0; mm < 2; mm++) acc[nn][mm] = MFMA(wf[i][nn], __builtin_bit_cast(bf16x8, af[i][mm]), acc[nn][mm]);
      SB();
    };
    waitv<2 + 2 * RC>();
#pragma unroll
    for (int i = 0; i < NE; i++) asm volatile("" : "+v"(af[i][0]), "+v"(af[i][1]));
    SFOR(i, 9 - RC) readW(RC + i); SEND
    SB();
    SFOR(i, EB) mm4(i); SEND
    WAITV(0); stflag(E + 2, rsWS, sFown);
    WAITL0(); SB();
    BARF();
    SB();
    if constexpr (GG) __builtin_amdgcn_s_setprio(1);
    constexpr int NB = (GG ? 9 : G0B) - RC;
    SFOR(i, 9)
      if constexpr (i == NB - 2) ldflag();
      if constexpr (i < NB) { issueW2(9 + RC + i, 0); issueW2(9 + RC + i, 1); SB(); }
      if constexpr (EB + i < NE) mm4(EB + i);
    SEND
    asm volatile("s_waitcnt vmcnt(4)" : "+v"(fl1) :: "memory");
    { const unsigned need = !nd1 ? 0u : fc < 14 ? 2u : 1u;
      if (!ALL(!nd1 || fl1 == E + need)) poll(rsWS, lane * FS, sF, E, need); }
#pragma unroll
    for (int j = NE; j < NE + PD; j++) { loadA1(j, 0); loadA1(j, 1); }
    asm volatile("buffer_load_dwordx4 %0, %1, %2, %3 offen" : "=v"(xr) : "v"(vO), "s"(rsX), "s"(sO) : "memory");
    SFOR(i, 9 - RC - NB) issueW2(9 + RC + NB + i, 0); issueW2(9 + RC + NB + i, 1); SEND
    SFOR(i1, NS - NE)
      constexpr int i = NE + i1;
      asm volatile("s_waitcnt vmcnt(%c2)" : "+v"(af[i][0]), "+v"(af[i][1]) : "i"(2 * ((i + PD <= NS ? i + PD : NS) - 1 - i) + (i1 < PD && i < NO + 8 * GG ? 1 + 2 * (9 - RC - NB) : 0)) : "memory");
      if constexpr (i == 9 && i1 == 0) readW(9);
      if constexpr (i + 1 >= 9 && i + 1 < NS) readW(i + 1);
      if constexpr (GG == 0 && i >= NO) {
        auto stale = [&]() AI {
          unsigned m = UMAX(UMAX(UMAX(af[i][0][0], af[i][0][1]), UMAX(af[i][0][2], af[i][0][3])), UMAX(UMAX(af[i][1][0], af[i][1][1]), UMAX(af[i][1][2], af[i][1][3])));
          return ANY(m == ~0u);
        };
        if (__builtin_expect(stale(), 0)) {
          int it = 0;
#pragma nounroll
          do { __builtin_amdgcn_s_sleep(SLEEPR); rel1<true>(af[i][0], af[i][1], rsWS, vL16, sA + (ksb(i) + ksi(i)) * 1024); } while (++it < (1 << 14) && stale());
          SFOR(u, PD - 1) if constexpr (i + 1 + u < NS) rel1<false>(af[i + 1 + u][0], af[i + 1 + u][1], rsWS, vL16, sA + (ksb(i + 1 + u) + ksi(i + 1 + u)) * 1024); SEND
        }
      }
      SB();
      if constexpr (i + PD < NS) { loadA1(i + PD, 0); loadA1(i + PD, 1); }
      mm4(i);
    SEND
    __builtin_amdgcn_s_setprio(0);
#pragma unroll
    for (int qd = 0; qd < 4; qd++)
#pragma unroll
      for (int k = 0; k < 4; k++) {
        const unsigned ra = ring_area(2 * qd + (k >> 1)) + (k & 1) * 1024;
        ldsw<bf16x8>(((lw2 + vL16) ^ (unsigned)(32 * (4 * (qd >> 1) + k))) + (ra & 0x30000u), ra & 0xffffu, sub4(acc[qd >> 1][qd & 1], k));
      }
    WAITL0(); BAR(); MEMFENCE();
    const unsigned c8 = (ln >> 4) & 7, j2 = (c8 & 4) + 2 * GG + ((c8 >> 1) & 1);
    const unsigned rd = LB + ((0x51407362u >> (4 * j2)) & 7) * 16384 + (c8 & 1) * 1024 + (((8 * wr + (ln >> 7)) << 4) ^ (c8 << 5));
    asm volatile("" : "+v"(xr));
    f32x4 sum[2];
#pragma unroll
    for (int k = 0; k < 2; k++) {
      f32x4 t = {U2F(xr[2 * k] << 16), U2F(xr[2 * k] & 0xffff0000u), U2F(xr[2 * k + 1] << 16), U2F(xr[2 * k + 1] & 0xffff0000u)};
#pragma unroll
      for (int v = 0; v < 8; v++) t += ldsr<f32x4>(rd, v * 2048 + k * 512);
      sum[k] = t;
    }
    u32x4 o = {pk2(sum[0][0], sum[0][1]), pk2(sum[0][2], sum[0][3]), pk2(sum[1][0], sum[1][1]), pk2(sum[1][2], sum[1][3])};
    __builtin_amdgcn_raw_buffer_store_b128(o, mkrs(pout, 2048 * XROW), vO + sO, 0, 0);
  }
  };
  if (!attn && g) prog(BoolC<false>{}, IntC<1>{});
  else if (!attn) prog(BoolC<false>{}, IntC<0>{});
  else if (g) prog(BoolC<true>{}, IntC<1>{});
  else prog(BoolC<true>{}, IntC<0>{});
}

extern "C" void kernel_launch(void* const* d_in, const int* in_sizes, int n_in, void* d_out, int out_size, void* d_ws, size_t ws_size,
                              hipStream_t stream) {
  fused_kernel<<<256, 512, 0, stream>>>((const char*)d_in[0], (const char*)d_in[1], (const char*)d_in[5], (const char*)d_in[6], (char*)d_ws,
                                        (const char*)d_in[2], (const char*)d_in[7], (const char*)d_in[3], (char*)d_out);
}
```

```cpp
#include <hip/hip_runtime.h>
#include <hip/hip_bf16.h>
#include <utility>

#define DI __device__ __forceinline__
#define LAS __attribute__((address_space(3)))
typedef __attribute__((ext_vector_type(8))) short bf16x8;
typedef __attribute__((ext_vector_type(4))) short s16x4;
typedef __attribute__((ext_vector_type(2))) __bf16 bf2;
typedef __attribute__((ext_vector_type(2))) float f32x2;
typedef __attribute__((ext_vector_type(4))) float f32x4;
typedef __attribute__((ext_vector_type(16))) float f32x16;
typedef __attribute__((ext_vector_type(2))) unsigned u32x2;
typedef __attribute__((ext_vector_type(4))) unsigned u32x4;
typedef __amdgpu_buffer_rsrc_t rsrc_t;
#define F2U(x) __builtin_bit_cast(unsigned, (float)(x))
#define U2F(x) __builtin_bit_cast(float, (unsigned)(x))
#define ALL(x) (__builtin_amdgcn_ballot_w64(!(x)) == 0ull)
#define ANY(x) (__builtin_amdgcn_ballot_w64(x) != 0ull)
#define UMAX(a, b) __builtin_elementwise_max((unsigned)(a), (unsigned)(b))

#define NTX 0
#define NTW 0
#define NT2 0
#define XS 2
#define MAP2 1
constexpr unsigned XROW = 896, QROW = 2688, FROW = 3584;
constexpr unsigned FS = 128;
constexpr unsigned WS_EPOCH = 0;
constexpr unsigned WS_GR = 4096;
constexpr unsigned WS_KVP = 131072;
constexpr unsigned WS_A2 = WS_KVP + 16 * 16 * 8192;
constexpr unsigned WS_ST = WS_A2 + 64 * 144 * 1024;
constexpr unsigned WS_FH = WS_ST + 262144;
constexpr unsigned WS_KVR = WS_FH + 2048 * FS;
constexpr unsigned WS_END = WS_KVR + 16 * 8192;
constexpr unsigned L_GU = 65536, L_KV = 131072;
constexpr unsigned L_TAIL = L_KV + 3 * 8192;
constexpr unsigned L_WPK = L_TAIL;
constexpr unsigned L_ST = L_TAIL + 2048;
constexpr unsigned L_CS = L_TAIL + 4096;
constexpr unsigned L_PHI = L_GU + 32768;
constexpr unsigned L_V = L_KV + 16384;
constexpr unsigned L_FREE = 32768;

#define MFMA(a, b, c) __builtin_amdgcn_mfma_f32_32x32x16_bf16((a), (b), (c), 0, 0, 0)
#define MFMA16(a, b, c) __builtin_amdgcn_mfma_f32_16x16x32_bf16((a), (b), (c), 0, 0, 0)
#define AI __attribute__((always_inline))
#define BAR() __builtin_amdgcn_s_barrier()
#define WAITV(n) asm volatile("s_waitcnt vmcnt(" #n ")" ::: "memory")
#define WAITL0() asm volatile("s_waitcnt lgkmcnt(0)" ::: "memory")
#define SB() __builtin_amdgcn_sched_barrier(0)
#define MEMFENCE() asm volatile("" ::: "memory")

template <int N> DI void waitv() { asm volatile("s_waitcnt vmcnt(%c0)" :: "i"(N) : "memory"); }

#define INSZ(x) (x)
DI rsrc_t mkrs(const void* p, unsigned bytes) { return __builtin_amdgcn_make_buffer_rsrc((void*)p, (short)0, (int)bytes, 0x00020000); }
template <bool NT = false> DI void dma(unsigned lds, unsigned limm, unsigned voff, rsrc_t rs, unsigned soff, unsigned simm) {
  unsigned t;
  if constexpr (NT) asm volatile("s_add_u32 m0, %1, %c5\n\ts_add_u32 %0, %4, %c6\n\tbuffer_load_dwordx4 %2, %3, %0 offen nt lds"
               : "=&s"(t) : "s"(lds), "v"(voff), "s"(rs), "s"(soff), "i"(limm), "i"(simm) : "memory", "scc");
  else asm volatile("s_add_u32 m0, %1, %c5\n\ts_add_u32 %0, %4, %c6\n\tbuffer_load_dwordx4 %2, %3, %0 offen lds"
               : "=&s"(t) : "s"(lds), "v"(voff), "s"(rs), "s"(soff), "i"(limm), "i"(simm) : "memory", "scc");
}
DI void dmat(unsigned lds, unsigned voff, rsrc_t rs) {
  asm volatile("s_mov_b32 m0, %0\n\ts_nop 0\n\tbuffer_load_dword %1, %2, 0 offen lds" :: "s"(lds), "v"(voff), "s"(rs) : "memory");
}
DI s16x4 trd(unsigned a) { return __builtin_amdgcn_ds_read_tr16_b64_v4i16((LAS s16x4*)(size_t)a); }
DI s16x4 trd2(unsigned a, unsigned off) { return __builtin_amdgcn_ds_read_tr16_b64_v4i16((LAS s16x4*)((LAS char*)(size_t)a + off)); }
DI bf16x8 tr2(unsigned a, unsigned off0, unsigned off1) {
  LAS char* p = (LAS char*)(size_t)a;
  s16x4 lo = __builtin_amdgcn_ds_read_tr16_b64_v4i16((LAS s16x4*)(p + off0));
  s16x4 hi = __builtin_amdgcn_ds_read_tr16_b64_v4i16((LAS s16x4*)(p + off1));
  return __builtin_shufflevector(lo, hi, 0, 1, 2, 3, 4, 5, 6, 7);
}
template <class T> DI T ldsr(unsigned a, unsigned off) { return *(LAS T*)((LAS char*)(size_t)a + off); }
template <class T> DI void ldsw(unsigned a, unsigned off, T v) { *(LAS T*)((LAS char*)(size_t)a + off) = v; }
DI unsigned pk2(float a, float b) { f32x2 f = {a, b}; return __builtin_bit_cast(unsigned, __builtin_convertvector(f, bf2)); }
DI float rbf(float a) { return U2F(pk2(a, a) << 16); }
DI float dot2(unsigned a, unsigned b, float c) { return __builtin_amdgcn_fdot2_f32_bf16(__builtin_bit_cast(bf2, a), __builtin_bit_cast(bf2, b), c, false); }
DI float xsum(float v) { auto r = __builtin_amdgcn_permlane32_swap(F2U(v), F2U(v), false, false); float a = U2F(r[0]); asm("" : "+v"(a)); return a + U2F(r[1]); }
DI float xmax(float v) { auto r = __builtin_amdgcn_permlane32_swap(F2U(v), F2U(v), false, false); return __builtin_fmaxf(U2F(r[0]), U2F(r[1])); }
DI float xmin(float v) { auto r = __builtin_amdgcn_permlane32_swap(F2U(v), F2U(v), false, false); return __builtin_fminf(U2F(r[0]), U2F(r[1])); }
DI float xhi(float v) { auto r = __builtin_amdgcn_permlane32_swap(F2U(v), F2U(v), false, false); return U2F(r[1]); }
#define STB "sc1"
DI void st16(u32x4 v, rsrc_t rs, unsigned voff, unsigned soff, unsigned simm) {
  unsigned t;
  asm volatile("s_add_u32 %0, %4, %c5\n\tbuffer_store_dwordx4 %1, %2, %3, %0 offen " STB "\n\ts_nop 1" : "=&s"(t) : "v"(v), "v"(voff), "s"(rs), "s"(soff), "i"(simm) : "memory", "scc");
}
DI void stflag(unsigned v, rsrc_t rs, unsigned soff) {
  unsigned voff = (__builtin_amdgcn_mbcnt_hi(~0u, __builtin_amdgcn_mbcnt_lo(~0u, 0u)) == 0u) ? 0u : 0x80000000u;
  __builtin_amdgcn_raw_buffer_store_b32(v, rs, voff, soff, 16);
}
#define XORCOND (n == 24)
#define PS 0
#define PV 0
#define KEEP2 0
#define S2N 14
#define B1N 23
#define DIST 2
#define P2G1 1
#define OAUX 0
#define RECY 0
#define POFF 99
#define H1S 0
#define H0L 14
#define H0C 22
#define Q3A 1
#define COOP 0
#define PON -2
#define XN 20
#define BAN 18
#define BBN 21
#define SL0 8
#define SLN 2
#define SLD 3
#define NSL 48
#define H1C 16
#define OV vL16
#define CV vL16
#define Q6E 0
#define TEARLY 0
#define NOCS 0
#define TPOS 0
#define PRE 0
#define LREC 0
#define CK1 20
#define EB 3
#define T2B 1
#define F1N 9
#define RC 3
#define G0B 9
#define TN 10
#define SLEEPR 4
#define SLEEPN 2
DI u32x4 ld16(rsrc_t rs, unsigned voff, unsigned soff, unsigned simm) {
  u32x4 r; unsigned t;
  asm volatile("s_add_u32 %1, %4, %c5\n\tbuffer_load_dwordx4 %0, %2, %3, %1 offen sc1" : "=v"(r), "=&s"(t) : "v"(voff), "s"(rs), "s"(soff), "i"(simm) : "memory", "scc");
  return r;
}
template <unsigned STEP> DI void ld8w(u32x4 (&p)[8], rsrc_t rs, unsigned voff, unsigned soff) {
  unsigned t;
#define L1(k) "buffer_load_dwordx4 %" #k ", %9, %10, %8 offen sc1\n\ts_add_u32 %8, %8, %c12\n\t"
  asm volatile("s_mov_b32 %8, %11\n\t" L1(0) L1(1) L1(2) L1(3) L1(4) L1(5) L1(6) L1(7) "s_waitcnt vmcnt(0)"
               : "=&v"(p[0]), "=&v"(p[1]), "=&v"(p[2]), "=&v"(p[3]), "=&v"(p[4]), "=&v"(p[5]), "=&v"(p[6]), "=&v"(p[7]), "=&s"(t)
               : "v"(voff), "s"(rs), "s"(soff), "i"(STEP) : "memory", "scc");
#undef L1
}
DI bool untag(u32x4 (&p)[8], unsigned tag, unsigned mask) {
  unsigned chk = 0;
#pragma unroll
  for (int i = 0; i < 8; i++)
#pragma unroll
    for (int k = 0; k < 4; k++) { p[i][k] ^= tag; chk |= p[i][k]; }
  return ALL((chk & mask) == 0u);
}
template <unsigned STEP> DI void waittag(u32x4 (&p)[8], rsrc_t rs, unsigned voff, unsigned soff, unsigned tag, unsigned mask) {
  asm volatile("s_waitcnt vmcnt(0)" : "+v"(p[0]), "+v"(p[1]), "+v"(p[2]), "+v"(p[3]), "+v"(p[4]), "+v"(p[5]), "+v"(p[6]), "+v"(p[7]) :: "memory");
#pragma nounroll
  for (int it = 0; !untag(p, tag, mask) && it < (1 << 16); ++it) { __builtin_amdgcn_s_sleep(SLEEPN); ld8w<STEP>(p, rs, voff, soff); }
}
template <bool WAIT> DI void rel1(u32x4& a, u32x4& b, rsrc_t rs, unsigned voff, unsigned so) {
  unsigned t;
  if constexpr (WAIT) asm volatile("buffer_load_dwordx4 %0, %3, %4, %5 offen sc1\n\ts_add_u32 %2, %5, 147456\n\tbuffer_load_dwordx4 %1, %3, %4, %2 offen sc1\n\ts_waitcnt vmcnt(0)"
                                   : "+v"(a), "+v"(b), "=&s"(t) : "v"(voff), "s"(rs), "s"(so) : "memory", "scc");
  else asm volatile("buffer_load_dwordx4 %0, %3, %4, %5 offen sc1\n\ts_add_u32 %2, %5, 147456\n\tbuffer_load_dwordx4 %1, %3, %4, %2 offen sc1"
                    : "+v"(a), "+v"(b), "=&s"(t) : "v"(voff), "s"(rs), "s"(so) : "memory", "scc");
}
DI void poll(rsrc_t rs, unsigned voff, unsigned soff, unsigned E, unsigned need) {
  voff = need ? voff : 0x80000000u;
#pragma nounroll
  for (int it = 0; it < (1 << 16); ++it) {
    unsigned v;
    asm volatile("buffer_load_dword %0, %1, %2, %3 offen sc1\n\ts_waitcnt vmcnt(0)" : "=v"(v) : "v"(voff), "s"(rs), "s"(soff) : "memory");
    bool ok = (need == 0u) || ((v - E - need) <= (3u - need));
    if (ALL(ok)) break;
    __builtin_amdgcn_s_sleep(SLEEPN);
  }
}

template <bool B> struct BoolC { static constexpr bool value = B; };
template <int I> struct IntC { static constexpr int value = I; };
template <int... I, class F> DI void sfor_impl(std::integer_sequence<int, I...>, F&& f) { (f(IntC<I>{}), ...); }
template <int N, class F> DI void sfor(F&& f) { sfor_impl(std::make_integer_sequence<int, N>{}, f); }
#define SFOR(n, N) sfor<N>([&](auto n##_) AI { constexpr int n = decltype(n##_)::value;
#define SEND });
struct RowC { float r, K1, K2, Ri, Lr; };

DI unsigned hilo(float v) { float hi = rbf(v); return pk2(hi, v - hi); }
DI float max3(float a, float b, float c) { return __builtin_fmaxf(__builtin_fmaxf(a, b), c); }
DI float min3(float a, float b, float c) { return __builtin_fminf(__builtin_fminf(a, b), c); }
DI unsigned pk2s(float a, float b) { unsigned r; asm("v_cvt_pk_bf16_f32 %0, %1, %2" : "=v"(r) : "v"(a), "v"(b)); return r; }
DI f32x2 sub2(const f32x16& v, int k) { f32x2 t = {v[2 * k], v[2 * k + 1]}; return t; }
DI f32x2 exp2v(f32x2 v) { f32x2 r = {__builtin_amdgcn_exp2f(v[0]), __builtin_amdgcn_exp2f(v[1])}; return r; }
DI f32x2 dup2(float a) { f32x2 r = {a, a}; asm("" : "+v"(r)); return r; }
DI f32x2 fma2(f32x2 a, f32x2 b, f32x2 c) { return __builtin_elementwise_fma(a, b, c); }
DI u32x4 epiP(const f32x16& a, const RowC& c) {
  u32x4 r;
  const f32x2 K1 = dup2(c.K1), K2 = dup2(c.K2), Ri = dup2(c.Ri);
#pragma unroll
  for (int i = 0; i < 4; i++) {
    const f32x2 g = sub2(a, i), d = fma2(exp2v(g * fma2(g * g, K2, K1)), Ri, Ri);
    const f32x2 s = {__builtin_amdgcn_rcpf(d[0]), __builtin_amdgcn_rcpf(d[1])}, h = (g * sub2(a, 4 + i)) * s;
    r[i] = pk2(h[0], h[1]);
  }
  return r;
}
struct EpiS { float x0, x1, m0, m1; u32x4 r; };
template <int P, int PH> DI void epiSlice(const f32x16& a, const RowC& c, EpiS& s) {
  const float g0 = a[2 * P], g1 = a[2 * P + 1];
  if constexpr (PH == 0) { s.x0 = g0 * __builtin_fmaf(g0 * g0, c.K2, c.K1); s.x1 = g1 * __builtin_fmaf(g1 * g1, c.K2, c.K1); }
  if constexpr (PH == 1) { s.x0 = __builtin_amdgcn_exp2f(s.x0); s.x1 = __builtin_amdgcn_exp2f(s.x1); s.m0 = g0 * a[8 + 2 * P]; s.m1 = g1 * a[9 + 2 * P]; }
  if constexpr (PH == 2) { s.x0 = __builtin_amdgcn_rcpf(__builtin_fmaf(s.x0, c.Ri, c.Ri)); s.x1 = __builtin_amdgcn_rcpf(__builtin_fmaf(s.x1, c.Ri, c.Ri)); }
  if constexpr (PH == 3) s.r[P] = pk2s(s.m0 * s.x0, s.m1 * s.x1);
}
DI f32x16 featproj2(const f32x16& a, bf16x8 w0, bf16x8 w1, bool hh) {
  u32x4 q0, q1;
#pragma unroll
  for (int i = 0; i < 4; i++) { q0[i] = pk2(a[2 * i], a[2 * i + 1]); q1[i] = pk2(a[8 + 2 * i], a[9 + 2 * i]); }
  q1[2] = hh ? 0u : q1[2]; q1[3] = hh ? 0u : q1[3];
  f32x16 z;
#pragma unroll
  for (int i = 0; i < 16; i++) z[i] = 0.f;
  z = MFMA(w0, __builtin_bit_cast(bf16x8, q0), z);
  z = MFMA(w1, __builtin_bit_cast(bf16x8, q1), z);
  return z;
}
DI f32x16 featproj(const f32x16& a, unsigned wpaddr, unsigned off, bool hh) {
  return featproj2(a, tr2(wpaddr, off, off + 8 * 56), tr2(wpaddr, off + 16 * 56, off + 24 * 56), hh);
}
DI float max16(const f32x16& p) { float m = max3(p[0], p[1], p[2]); _Pragma("unroll") for (int i = 3; i < 15; i += 2) m = max3(m, p[i], p[i + 1]); return xmax(__builtin_fmaxf(m, p[15])); }
DI float min16(const f32x16& p) { float m = min3(p[0], p[1], p[2]); _Pragma("unroll") for (int i = 3; i < 15; i += 2) m = min3(m, p[i], p[i + 1]); return xmin(__builtin_fminf(m, p[15])); }
DI f32x16 dualexpP(const f32x16& p, float Lr) {
  const f32x2 bx = dup2(-max16(p) * Lr), bn = dup2(min16(p) * Lr), L2 = dup2(Lr);
  f32x16 o;
#pragma unroll
  for (int i = 0; i < 8; i++) {
    const f32x2 ep = exp2v(fma2(sub2(p, i), L2, bx)), en = exp2v(fma2(sub2(p, i), -L2, bn));
    o[i] = U2F(pk2(ep[0], ep[1])); o[8 + i] = U2F(pk2(en[0], en[1]));
  }
  return o;
}
struct SmS { f32x16 p, e; float en[16], bx, bn, sp, sn, tp, tn; bf16x8 w0, w1; };
DI bf16x8 sub4(const f32x16& v, int k) { f32x4 t = {v[4 * k], v[4 * k + 1], v[4 * k + 2], v[4 * k + 3]}; return __builtin_bit_cast(bf16x8, t); }

DI constexpr unsigned lc1_base(int ch) { return ch == 6 ? L_KV : ((ch & 1) ? L_GU : 0u) + (ch < 2 ? 49152u : ch < 4 ? 0u : 16384u); }
template <int CTRL, int RM> DI float dppadd(float v) { return v + U2F(__builtin_amdgcn_update_dpp(0u, F2U(v), CTRL, RM, 0xf, false)); }
DI float wavesum63(float v) {
  v = dppadd<0x111, 0xf>(v); v = dppadd<0x112, 0xf>(v); v = dppadd<0x114, 0xf>(v); v = dppadd<0x118, 0xf>(v);
  v = dppadd<0x142, 0xa>(v); return dppadd<0x143, 0xc>(v);
}

#define STAMP(i)
#define STAMPS_OUT()
#define CSTAMP(i)

#define PDV 6
constexpr int PD = PDV, RING = 9;
constexpr int later2(int i, int NS, int GG) {
  const int younger = 2 * ((i + PD <= NS ? i + PD : NS) - 1 - i);
  int extra = i < PD ? 1 : 0;
  if (NS == 20 && (i == 10 || i == 11)) extra += 2 * (i - 9);
  return younger + extra;
}
DI constexpr unsigned ring_area(int j) { return j == 0 ? L_FREE : j == 1 ? L_PHI : lc1_base(j - 2); }
constexpr int cumG1(int s) { return s < 4 ? 0 : s < 22 ? (s - 4) / 2 : (9 + (s - 22) * 5 / 4 < 48 ? 9 + (s - 22) * 5 / 4 : 48); }

#define CODE_BYTES 65536
__global__ __launch_bounds__(512, 2) void fused_kernel(const char* px_, const char* pqkv_, const char* pwg_, const char* pwu_, char* ws_, const char* pphi_,
                                                       const char* pwd_, const char* pwo_, char* pout_) {
  typedef __attribute__((ext_vector_type(8))) unsigned long long u64x8;
  u64x8 ka; unsigned long long kb;
  asm volatile("s_load_dwordx16 %0, %2, 0\n\ts_load_dwordx2 %1, %2, 64" : "=&s"(ka), "=&s"(kb) : "s"(__builtin_amdgcn_kernarg_segment_ptr()));
  __shared__ __attribute__((aligned(16))) char smem[163840];
  const unsigned LB = (unsigned)(size_t)smem;
  const unsigned tid = __builtin_amdgcn_workitem_id_x(), lane = tid & 63;
  const unsigned w = __builtin_amdgcn_readfirstlane(tid >> 6), g = w >> 2, wr = w & 3;
  const unsigned b = __builtin_amdgcn_workgroup_id_x(), xcd = b & 7, bi = b >> 3;
  const unsigned half = (xcd >> 1) & 1, cp = 4 * (xcd & 1) + (bi >> 3), rb = 8 * (xcd >> 2) + (bi & 7), rsum = (bi >> 3) + (bi & 7);
  const unsigned f2 = 32 * (xcd & 1) + bi - 8, t2 = (f2 * 37) >> 8, ci = f2 - 7 * t2, rbl = t2 & 3, rt2 = 2 * (8 * (xcd >> 1) + t2);
  const unsigned c = 7 * half + cp - 1;
  const unsigned head = 8 * half + (((half ? 0x76425310u : 0x75316420u) >> (4 * cp)) & 7);
  const bool attn = cp == 0;
  STAMP(0); CSTAMP(14);
  const unsigned wstr = attn ? QROW : FROW;
  const unsigned sG0 = half * (attn ? 448 : 1792) + (attn ? 0 : (cp - 1) * 256), sX0 = rb * 128 * XROW;
  unsigned vx0, vx1, vG, vU, vKV;
  auto voffs = [&](unsigned ln) AI {
    const unsigned r8 = ln >> 3, c8 = ln & 7, sa = (r8 >> 1) & 1, lc = c8 ^ (4 * sa);
    vx0 = (16 * w + r8) * XROW + ((c8 ^ (r8 >> 1)) << 4); vx1 = (vx0 ^ 64u) + 8 * XROW;
    const unsigned cg = attn ? lc : (c8 ^ (2 * sa)), cu = attn ? (lc >= 6 ? lc - 2 : lc) : (cg ^ 4);
    vG = (8 * w + r8) * wstr + (cg << 4); vU = (8 * w + r8) * wstr + (cu << 4);
    vKV = (8 * w + r8) * QROW + 896 * (1 + (lc >> 2)) + ((lc & 3) << 4) + ((56 * head) & ~15u);
  };
  voffs(lane);
  const unsigned lw1 = LB + w * 1024, lw2 = LB + w * 2048;
#define ROT (((0x124924u >> (2 * rsum)) & 3) * 2)
  const unsigned rot = ROT;
  const unsigned srow = 128 * rb + 64 * half + 8 * cp + w;
  unsigned vrow = lane < 56 ? lane * 16 : 0x80000000u;
  asm volatile("s_waitcnt lgkmcnt(0)" : "+s"(ka), "+s"(kb), "+v"(vx0), "+v"(vx1), "+v"(vG), "+v"(vU), "+v"(vKV), "+v"(vrow));
  const char *px = (const char*)ka[0], *pqkv = (const char*)ka[1], *pwg = (const char*)ka[2], *pwu = (const char*)ka[3], *pphi = (const char*)ka[5],
             *pwd = (const char*)ka[6], *pwo = (const char*)ka[7];
  char *ws = (char*)ka[4], *pout = (char*)kb;
  const rsrc_t rsX = mkrs(px, INSZ(2048 * XROW)), rsQ = mkrs(pqkv, INSZ(448 * QROW));
  const rsrc_t rsG = mkrs(attn ? pqkv : pwg, INSZ(448 * wstr)), rsU = mkrs(attn ? pqkv + 128 : pwu, INSZ(448 * wstr - (attn ? 128 : 0)));
  auto cidx = [&](int st) AI -> unsigned { if (st == 0) return 0u; unsigned t = rot + st; return t >= 7 ? t - 6 : t; };
  auto issueA = [&](int st, int k) AI {
    const unsigned sl = st & 3, ch = cidx(st);
    if (k == 0) dma<NTX>(lw2, sl * 16384, vx0, rsX, sX0 + ch * 128, 0);
    if (k == 1) dma<NTX>(lw2, sl * 16384 + 1024, vx1, rsX, sX0 + ch * 128, 0);
    if (k == 2) dma<NTW>(lw1, L_GU + sl * 16384, vG, rsG, sG0 + ch * 64 * wstr, 0);
    if (k == 3) dma<NTW>(lw1, L_GU + sl * 16384 + 8192, vU, rsU, sG0 + ch * 64 * wstr, 0);
    if (k == 4) dma<NTW>(lw1, L_KV + sl * 8192, vKV, rsQ, ch * 64 * QROW, 0);
  };
  const rsrc_t rsWS = mkrs(ws, WS_END);
  unsigned Ev, cpf; u32x4 xrw;
  asm volatile("buffer_load_dwordx4 %0, %1, %2, %3 offen" : "=v"(xrw) : "v"(vrow), "s"(rsX), "s"(srow * XROW) : "memory");
  asm volatile("buffer_load_dword %0, off, %1, 0 sc1" : "=v"(Ev) : "s"(rsWS) : "memory");
  {
    unsigned long long pc; asm volatile("s_getpc_b64 %0" : "=s"(pc));
    const unsigned vo = w == 0 && lane < 24 ? (bi * 24 + lane) * 128 : 0x80000000u;
    asm volatile("buffer_load_dword %0, %1, %2, 0 offen" : "=v"(cpf) : "v"(vo), "s"(mkrs((const void*)pc, CODE_BYTES)) : "memory");
  }
#pragma unroll
  for (int k = 0; k < 5; k++) issueA(0, k);
  BAR();
#pragma unroll
  for (int k = 0; k < 5; k++) issueA(1, k);
  issueA(2, 0);
  asm volatile("s_waitcnt vmcnt(11)" : "+v"(xrw), "+v"(Ev), "+v"(cpf) :: "memory");
  const unsigned E = __builtin_amdgcn_readfirstlane(Ev);
  {
    float s1 = 0.f, s2 = 0.f;
#pragma unroll
    for (int i = 0; i < 4; i++) { s1 = dot2(xrw[i], 0x3f803f80u, s1); s2 = dot2(xrw[i], xrw[i], s2); }
    s1 = wavesum63(s1); s2 = wavesum63(s2);
    const float mean = s1 / 448.f, var = __builtin_fmaxf(s2 * (1.f / 448.f) - mean * mean, 0.f);
    u32x2 gr = {pk2(-mean, __builtin_amdgcn_sqrtf(rbf(rbf(var) + 1e-5f))), E + 1};
    __builtin_amdgcn_raw_buffer_store_b64(gr, rsWS, lane == 63 ? 0u : 0x80000000u, WS_GR + srow * 8, 16);
  }

  auto prog = [&](auto at_, auto gg_) AI {
  constexpr bool AT = decltype(at_)::value; constexpr int GG = decltype(gg_)::value;
  constexpr unsigned lcstep = AT ? 224 : 128;
  unsigned lane;
  asm volatile("v_mbcnt_lo_u32_b32 %0, -1, 0\n\tv_mbcnt_hi_u32_b32 %0, -1, %0" : "=v"(lane));
  __builtin_assume(lane < 64);
  voffs(lane);
  auto issueL = [&](int hc) AI {
    const int st = hc >> 1; const unsigned ch = cidx(st);
    if (hc & 1) dma<NTW>(lw1, lc1_base(st) + 8192, vU, rsU, sG0 + ch * 64 * wstr, lcstep);
    else dma<NTW>(lw1, lc1_base(st), vG, rsG, sG0 + ch * 64 * wstr, lcstep);
  };
  auto issueG = [&](int bar, int k) AI {
    if (bar == 0) issueA(2 + k / 5, k % 5);
    else if (bar <= 3) issueA(bar + 3, k);
    else if (k < 4) issueL(4 * (bar - 4) + k);
    else if (bar == 4) dma(LB + (w & 1) * 1024, L_WPK, lane * 16, mkrs(pphi, 16 * 1568), head * 1568 + (w & 1) * 1024, 0);
    else issueL(12 + (bar - 5));
  };

  const unsigned rt = 4 * rb + wr;
  const unsigned sA2 = WS_A2 + rt * 144 * 1024 + (AT ? half * 16384 : c * 8192), vL16 = lane * 16;
  const u32x4 sent = {~0u, ~0u, ~0u, ~0u};
  auto clr = [&](int ks) AI { st16(sent, rsWS, CV, sA2, ks * 1024); };

  auto clri = [&](int q) AI { clr(GG ? (q < 6 ? 114 + q : 120 + q) : (q < 2 ? 112 + q : 118 + q)); };

  const bool hh = lane >= 32;
  const unsigned hu = lane >> 5, blk = (lane >> 4) & 1, q4 = (lane >> 2) & 3, p4 = lane & 3, ta = (q4 >> 1) & 1, l31 = lane & 31;
  const unsigned perm = wr ^ (2 * GG);
  const unsigned rowoff = (8 * hu + q4) * 128 + (perm << 11);
  const unsigned pc = (4 * blk + p4) < 6 ? (4 * blk + p4) : 6;
  auto tileaddr = [&](unsigned t) AI -> unsigned {
    if constexpr (AT) { unsigned o = 56 * t + 8 * pc; return (o >> 7) * 8192 + ((((o >> 4) & 7) ^ (4 * ta)) << 4) + (o & 8) + rowoff; }
    else return blk * 8192 + (((2 * t + (p4 >> 1)) ^ (4 * blk + 2 * ta)) << 4) + 8 * (p4 & 1) + rowoff;
  };
  constexpr int NT = !AT ? 2 : GG ? 1 : 3, TB0 = GG ? 4 - NT : 0;
  unsigned aA[3], aB[NT];
  {
    unsigned o = (head & 1) * 8 + 8 * pc;
    unsigned ak = L_KV + (((o >> 4) ^ (4 * ta)) << 4) + (o & 8) + rowoff;
    aA[0] = LB + (GG ? L_GU + tileaddr(1) : ak);
    aA[1] = LB + (GG ? L_GU + tileaddr(2) : (ak ^ 64u));
    aA[2] = LB + L_GU + tileaddr(GG ? 3 : 0);
#pragma unroll
    for (int j = 0; j < NT; j++) aB[j] = LB + tileaddr(TB0 + j);
  }
  const unsigned xrow = 32 * wr + l31;
  const unsigned aX = LB + xrow * 128 + (((2 * perm + hu) ^ ((xrow >> 1) & 7)) << 4);
  const unsigned selv = (((lane >> 4) ^ (lane >> 2)) & 1) ? 0u : 0x3f803f80u;
  const u32x4 sel4 = {selv, selv, selv, selv};
  const bf16x8 sel = __builtin_bit_cast(bf16x8, sel4);

  bf16x8 xf[28], wa[2][3], wq[8];
  f32x16 accA[3], accB[NT];
  f32x4 csA[3], csB[NT];
#pragma unroll
  for (int j = 0; j < 3; j++) { for (int i = 0; i < 16; i++) accA[j][i] = 0.f; for (int i = 0; i < 4; i++) csA[j][i] = 0.f; }
#pragma unroll
  for (int j = 0; j < NT; j++) { for (int i = 0; i < 16; i++) accB[j][i] = 0.f; for (int i = 0; i < 4; i++) csB[j][i] = 0.f; }
  u32x2 grn;

  const rsrc_t rsWD = mkrs(pwd, INSZ(1792 * XROW)), rsWO = mkrs(pwo, INSZ(448 * XROW));
  const unsigned vW2 = (lane >> 3) * XROW + (((lane & 7) ^ ((lane >> 2) & 4)) << 4) + ci * 128;
  constexpr int NS = GG ? 16 + XS : 20 - XS, NC = 6 - XS / 2, NO = 2 * NC;
#define RHO ((rbl + ci) & 3)
  const unsigned rho = RHO;
  auto t_of = [](int i) constexpr { return (0x54763210u >> (4 * ((i >> 1) & 7))) & 7; };
  constexpr int NE = GG ? 9 : 5;
  auto g1i = [](int i) constexpr { return i < 8 ? i : i == 8 ? 16 : i < 17 ? i - 1 : 17; };
  auto ksb = [&](int i) AI -> unsigned {
    if (GG) i = g1i(i);
    if (GG) return i < 16 ? 16 * wr + ((t_of(i) & 3) ^ rho) : wr;
    if (i < NO) { const unsigned cc = i % NC + rho + (XS ? 0 : rho >> 1); return wr + 8 * (cc >= NC ? cc - NC : cc); }
    return 8 * wr + 2 * (((i - NO) >> 1) ^ rho);
  };
  auto ksi = [&](int i) AI -> unsigned { if (GG) i = g1i(i); return GG ? (i < 16 ? 8 * (i & 1) + (t_of(i) & 4) : 4 * i) : i < NO ? 64 + 4 * XS + 4 * (i / NC) : 112 + (i & 1); };
  auto issueW2 = [&](int i, int hf) AI {
    if (GG == 0 && i >= NO) dma<NT2>(lw2, ring_area(i % RING) + hf * 1024, vW2, rsWO, ksb(i) * 14 * XROW, (16 * (i & 1) + 8 * hf) * XROW);
    else dma<NT2>(lw2, ring_area(i % RING) + hf * 1024, vW2, rsWD, ksb(i) * 16 * XROW, (ksi(i) * 16 + 8 * hf) * XROW);
  };

  const unsigned aCSw = LB + L_CS + l31 * 16 + wr * 4 + GG * 2048, aCSr = LB + L_CS + l31 * 16 + GG * 2048;
  auto readX = [&](int n) AI { xf[n] = ldsr<bf16x8>(aX ^ (unsigned)((n & 3) << 5), ((n >> 2) & 3) * 16384); };
  auto readA = [&](int n, int j) AI {
    const unsigned o = ((n >> 2) & 3) * ((GG == 0 && j < 2) ? 8192 : 16384);
    wa[n & 1][j] = tr2(aA[j] ^ (unsigned)((n & 3) << 11), o, o + 512);
  };
  auto readB = [&](int q) AI {
    const int n = q / NT, j = q % NT;
    const unsigned base = lc1_base(n >> 2);
    if ((NT == 3 && n < XN) ? (n & 3) == 0 : n == 24) asm volatile("" : "+v"(aB[j]));
    wq[q & 7] = tr2(aB[j] ^ (((n & 3) << 11) | (base & 0x30000u)), base & 0xffffu, (base & 0xffffu) + 512);
  };
  RowC rc; unsigned nm, denb;
  auto mkxe = [&]() AI { asm volatile("" : "+v"(nm)); u32x4 t = {nm, nm, nm, nm}; return __builtin_bit_cast(bf16x8, t); };
  auto rowconst = [&]() AI {
#pragma nounroll
    for (int it = 0; it < (1 << 16) && !ALL(grn[1] == E + 1); ++it) {
      __builtin_amdgcn_s_sleep(SLEEPN);
      asm volatile("buffer_load_dwordx2 %0, %1, %2, %3 offen sc1\n\ts_waitcnt vmcnt(0)" : "=v"(grn) : "v"(xrow * 8), "s"(rsWS), "s"(WS_GR + rb * 1024) : "memory");
    }
    const float den = U2F(grn[0] & 0xffff0000u);
    nm = hh ? 0u : __builtin_amdgcn_perm(grn[0], grn[0], 0x01000100u);
    denb = grn[0] >> 16;
    rc.r = __builtin_amdgcn_rcpf(den);
    const float k1 = -2.f * 1.4426950408889634f * 0.7978845608028654f;
    rc.K1 = k1 * rc.r; rc.K2 = (k1 * 0.044715f) * rc.r * (rc.r * rc.r); rc.Ri = den * den; rc.Lr = 1.4426950408889634f * rc.r;
  };
  waitv<7 + COOP>();
  BAR(); MEMFENCE();
  STAMP(1);
  readX(0); readA(0, 0); readA(0, 1); readA(0, 2);
  SFOR(n, 28)
    SB();
    if constexpr (n == 27) {
#pragma unroll
      for (int j = 0; j < 3; j++) ldsw<unsigned>(aCSw, j * 512, hilo(csA[j][0]));
    }
    if constexpr ((n & 3) == 3) {
      if constexpr (n == 27) WAITV(0);
      else if constexpr (n == 23) asm volatile("s_waitcnt vmcnt(10)" : "+v"(grn) :: "memory");
      else if constexpr (n == 3 || n == 15 || n == 19) WAITV(11); else WAITV(10);
      { WAITL0(); BAR(); MEMFENCE(); }
      if constexpr (n == 27) STAMP(2);
    }
    constexpr int bar = (n + 1) >> 2, ph = (n + 1) & 3;
#pragma unroll
    for (int j = 0; j < 3; j++) {
      if constexpr (n < 27) { if (j == 0) readX(n + 1); readA(n + 1, j); }
      else { wa[0][j] = ldsr<bf16x8>(aCSr, j * 512); if (AT && GG == 1 && j < 2) { readB(2 * j); readB(2 * j + 1); } }
      SB();
      accA[j] = MFMA(wa[n & 1][j], xf[n], accA[j]);
      const int slot = 3 * ph + j;
      if (bar == 0) issueG(0, slot - 2);
      else if (bar == 6) { if (slot < 5) issueG(bar, slot); }
      else if (bar <= 6) { if (slot == 0) issueG(bar, 0); if (slot == 2) issueG(bar, 1); if (slot == 5) issueG(bar, 2); if (slot == 7) issueG(bar, 3); if (slot == 10) issueG(bar, 4); }
      else if (j < 2) {
        if constexpr (AT) dma(lw2, L_FREE + j * 1024, lane * 16, mkrs(pphi, 16 * 1568), half * 8 * 1568 + w * 2048, j * 1024);
        else issueW2(0, j);
      }
      if (!NOCS && (n & 3) == 0) csA[j] = MFMA16(sel, wa[n & 1][j], csA[j]);
      if (n == 25 && j == 0) rowconst();
      if (n == 14 && j == 2) asm volatile("buffer_load_dwordx2 %0, %1, %2, %3 offen sc1" : "=v"(grn) : "v"(xrow * 8), "s"(rsWS), "s"(WS_GR + rb * 1024) : "memory");
      SB();
    }
  SEND
  { const bf16x8 xe = mkxe();
#pragma unroll
  for (int j = 0; j < 3; j++) accA[j] = MFMA(wa[0][j], xe, accA[j]); }
  asm volatile("" : "+v"(accA[0]), "+v"(accA[1]), "+v"(accA[2]));
  SB();

  const unsigned sFown = WS_FH + (rt * 32 + (AT ? 28 + 2 * half : 2 * c) + GG) * FS;
  const unsigned aWP = LB + (4 * hu + q4) * 56 + 8 * pc;
  const unsigned T12 = (E + 1) & 0xfffu, T16 = (E + 1) & 0xffffu;
  const unsigned sF = WS_FH + rt2 * 32 * FS;
  const unsigned fc = l31 >> 1;
  const bool nd1 = GG ? (fc >> 1) == wr || (fc >= 8 && fc < 8 + XS / 2) : (fc >= 8 + XS / 2 && fc < 14) || fc == 14 + (wr >> 1);
  const unsigned aIW = LB + L_PHI + xrow * 64 + ((hu ^ ((xrow >> 1) & 7)) << 3);
  SmS sm;
  unsigned aWK = aWP + L_WPK, aWQ = aWP + L_FREE;
  asm volatile("" : "+v"(aWK)); if constexpr (AT) asm volatile("" : "+v"(aWQ));
  auto kstep = [&](auto s_) AI {
    constexpr int s = decltype(s_)::value;
    if constexpr (s == 0) { sm.w0 = tr2(aWK, 0, 8 * 56); sm.w1 = tr2(aWK, 16 * 56, 24 * 56); }
    if constexpr (s == 1) sm.p = featproj2(accA[0], sm.w0, sm.w1, hh);
    if constexpr (s == 2 || s == 3) {
#pragma unroll
      for (int k = 2 * s - 4; k < 2 * s - 2; k++) {
        u32x2 vv = {pk2(accA[1][4 * k], accA[1][4 * k + 1]), pk2(accA[1][4 * k + 2], accA[1][4 * k + 3])};
        if (k == 3) vv[0] = hh ? denb : vv[0];
        ldsw<u32x2>(aIW ^ (unsigned)(k << 4), L_V - L_PHI, vv);
      }
    }
    if constexpr (s == 4) sm.bx = -max16(sm.p) * rc.Lr;
    if constexpr (s == 5) sm.bn = min16(sm.p) * rc.Lr;
    if constexpr (s >= 6 && s < 22) {
      constexpr int i = s - 6;
      sm.e[i] = __builtin_amdgcn_exp2f(__builtin_fmaf(sm.p[i], rc.Lr, sm.bx)); sm.en[i] = __builtin_amdgcn_exp2f(__builtin_fmaf(sm.p[i], -rc.Lr, sm.bn));
      if constexpr (i == 0) { sm.sp = sm.e[0]; sm.sn = sm.en[0]; } else if constexpr (i < 12) { sm.sp += sm.e[i]; sm.sn += sm.en[i]; }
      else if constexpr (i == 12) { sm.tp = sm.e[12]; sm.tn = sm.en[12]; } else { sm.tp += sm.e[i]; sm.tn += sm.en[i]; }
    }
    if constexpr (s == 22) { sm.sp = __builtin_amdgcn_rcpf(xsum(sm.sp + (hh ? 0.f : sm.tp))) * rc.r; sm.sn = __builtin_amdgcn_rcpf(xsum(sm.sn + (hh ? 0.f : sm.tn))) * rc.r; }
    if constexpr (s >= 23) {
#pragma unroll
      for (int u = (s == 29 ? 6 : s - 23); u <= (s == 29 ? 7 : s - 23); u++) {
        const int k = u >> 1;
        if (u & 1) { u32x2 t = {pk2s(sm.en[4 * k] * sm.sn, sm.en[4 * k + 1] * sm.sn), pk2s(sm.en[4 * k + 2] * sm.sn, sm.en[4 * k + 3] * sm.sn)}; ldsw<u32x2>(aIW ^ (unsigned)(k << 4), 8192, t); }
        else { u32x2 t = {pk2s(sm.e[4 * k] * sm.sp, sm.e[4 * k + 1] * sm.sp), pk2s(sm.e[4 * k + 2] * sm.sp, sm.e[4 * k + 3] * sm.sp)}; ldsw<u32x2>(aIW ^ (unsigned)(k << 4), 0, t); }
      }
    }
  };
  auto qstep = [&](auto s_, f32x16& a, unsigned lh) AI {
    constexpr int s = decltype(s_)::value;
    if constexpr (s == 0) { sm.w0 = tr2(aWQ, lh * 1568, lh * 1568 + 8 * 56); sm.w1 = tr2(aWQ, lh * 1568 + 16 * 56, lh * 1568 + 24 * 56); }
    if constexpr (s == 1) sm.p = featproj2(a, sm.w0, sm.w1, hh);
    if constexpr (s == 3) sm.bx = -max16(sm.p) * rc.Lr;
    if constexpr (s == 4) sm.bn = min16(sm.p) * rc.Lr;
    if constexpr (s >= 5 && s < 21) {
      constexpr int i = (s - 5) >> 1;
      float t0, t1;
      if constexpr ((s - 5) & 1) { t0 = __builtin_fmaf(sm.p[2 * i], -rc.Lr, sm.bn); t1 = __builtin_fmaf(sm.p[2 * i + 1], -rc.Lr, sm.bn); }
      else { t0 = __builtin_fmaf(sm.p[2 * i], rc.Lr, sm.bx); t1 = __builtin_fmaf(sm.p[2 * i + 1], rc.Lr, sm.bx); }
      a[8 * ((s - 5) & 1) + i] = U2F(pk2(__builtin_amdgcn_exp2f(t0), __builtin_amdgcn_exp2f(t1)));
      if constexpr (s == 20) asm volatile("" : "+v"(a));
    }
  };
  bf16x8 ka_[4], kb_[2][4]; f32x4 kd[2];
  auto kvstep = [&](auto s_) AI {
    constexpr int s = decltype(s_)::value;
    if constexpr (s == 0) {
      const unsigned g4 = lane >> 4, r0 = 8 * g4 + q4, sw = 4 * blk + (q4 >> 1);
      unsigned b0 = LB + L_PHI + r0 * 64 + ((p4 ^ sw) << 3);
      unsigned a0 = (b0 ^ ((wr >> 1) << 5)) + (wr & 1) * 8192;
#pragma unroll
      for (int k = 0; k < 4; k++) {
        ka_[k] = __builtin_shufflevector(trd2(a0, k * 2048), trd2(a0 ^ 16u, k * 2048 + 256), 0, 1, 2, 3, 4, 5, 6, 7);
        kb_[0][k] = __builtin_shufflevector(trd2(b0, L_V - L_PHI + k * 2048), trd2(b0 ^ 16u, L_V - L_PHI + k * 2048 + 256), 0, 1, 2, 3, 4, 5, 6, 7);
        kb_[1][k] = __builtin_shufflevector(trd2(b0 ^ 32u, L_V - L_PHI + k * 2048), trd2(b0 ^ 48u, L_V - L_PHI + k * 2048 + 256), 0, 1, 2, 3, 4, 5, 6, 7);
      }
    }
    if constexpr (s == 1) {
      f32x4 z = {0.f, 0.f, 0.f, 0.f}; kd[0] = z; kd[1] = z;
#pragma unroll
      for (int k = 0; k < 4; k++) { kd[0] = MFMA16(ka_[k], kb_[0][k], kd[0]); kd[1] = MFMA16(ka_[k], kb_[1][k], kd[1]); }
    }
    if constexpr (s == 3) {
#pragma unroll
      for (int d = 0; d < 2; d++) {
        u32x4 t;
#pragma unroll
        for (int i = 0; i < 4; i++) t[i] = (F2U(kd[d][i]) & ~0xfffu) | T12;
        st16(t, rsWS, (lane >> 4) * 512 + (lane & 15) * 16, ((head * 16 + rb) * 2 + (wr & 1)) * 4096 + (wr >> 1) * 2048, WS_KVP + d * 256);
      }
    }
  };
  auto qexp2 = [&](f32x16& a0, unsigned l0, f32x16& a1, unsigned l1) AI {
    const f32x16 p0 = featproj(a0, aWQ, l0 * 1568, hh), p1 = featproj(a1, aWQ, l1 * 1568, hh);
    a0 = dualexpP(p0, rc.Lr); a1 = dualexpP(p1, rc.Lr); asm volatile("" : "+v"(a0), "+v"(a1));
  };

  unsigned fl1 = 0;
  EpiS es;
  u32x4 pv[8];
  const unsigned vKP = l31 * 16 + hu * 8192, sKP = (rb * 32 + half) * 4096 + wr * 1024 + GG * 512, sKR = (8 * half + w) * 8192;
  auto hop1 = [&]() AI {
    waittag<16384>(pv, rsWS, vKP, sKP + WS_KVP, T12, 0xfffu);
    u32x4 o;
#pragma unroll
    for (int k = 0; k < 4; k++) {
      float sum = U2F(pv[0][k]);
#pragma unroll
      for (int i = 1; i < 8; i++) sum += U2F(pv[i][k]);
      sum = xsum(sum);
      sum = l31 == 29 ? 1.f : sum;
      if (GG == 1) sum = wr == 3 ? 0.f : sum;
      o[k] = (pk2s(sum, sum) & 0xffff0000u) | T16;
    }
    st16(o, rsWS, hh ? 0x80000000u : l31 * 16, (rb * 2 + half) * 4096 + wr * 1024 + GG * 512, WS_KVR);
    if (b == 0 && w == 4) stflag(E + 4, rsWS, WS_EPOCH);
    SB();
  };
  bf16x8 cb[NT];
  auto ldflag = [&]() AI { asm volatile("buffer_load_dword %0, %1, %2, %3 offen sc1" : "=v"(fl1) : "v"(nd1 ? lane * FS : 0x80000000u), "s"(rsWS), "s"(sF) : "memory"); };
  auto touch = [&](unsigned wo4) AI {
    const unsigned rw = (rbl * 8 + w + wo4) * 44 + lane;
    const bool dn = rw < 896;
    const unsigned vo = lane < 44 ? (dn ? ((rw >> 6) * 128 + 64 + (rw & 63)) : rw - 896) * XROW + ci * 128 : 0x80000000u;
    asm volatile("s_add_u32 m0, %0, %c5\n\ts_nop 0\n\tbuffer_load_dword %1, %3, 0 offen lds\n\tbuffer_load_dword %2, %4, 0 offen lds"
                 :: "s"(LB + w * 256), "v"(dn ? vo : 0x80000000u), "v"(dn ? 0x80000000u : vo), "s"(rsWD), "s"(rsWO), "i"(L_ST) : "memory", "scc");
  };
  auto kpath = [&]() AI {
    const f32x16 p = featproj(accA[0], aWK, 0, hh);
#pragma unroll
    for (int k = 0; k < 4; k++) {
      u32x2 vv = {pk2(accA[1][4 * k], accA[1][4 * k + 1]), pk2(accA[1][4 * k + 2], accA[1][4 * k + 3])};
      if (k == 3) vv[0] = hh ? denb : vv[0];
      ldsw<u32x2>(aIW ^ (unsigned)(k << 4), L_V - L_PHI, vv);
    }
    const f32x2 bx = dup2(-max16(p) * rc.Lr), bn = dup2(min16(p) * rc.Lr), L2 = dup2(rc.Lr);
    f32x2 ep[8], en[8];
#pragma unroll
    for (int i = 0; i < 8; i++) { ep[i] = exp2v(fma2(sub2(p, i), L2, bx)); en[i] = exp2v(fma2(sub2(p, i), -L2, bn)); }
    f32x2 sp = ((ep[0] + ep[1]) + (ep[2] + ep[3])) + (ep[4] + ep[5]), sn = ((en[0] + en[1]) + (en[2] + en[3])) + (en[4] + en[5]);
    f32x2 tp = ep[6] + ep[7], tn = en[6] + en[7];
    float a0 = sp[0], a1 = sn[0], a2 = tp[0], a3 = tn[0];
    asm("" : "+v"(a0), "+v"(a1), "+v"(a2), "+v"(a3));
    a0 += sp[1]; a2 += tp[1]; asm("" : "+v"(a0), "+v"(a2)); a1 += sn[1]; a3 += tn[1];
    const f32x2 ip = dup2(__builtin_amdgcn_rcpf(xsum(a0 + (hh ? 0.f : a2))) * rc.r), in = dup2(__builtin_amdgcn_rcpf(xsum(a1 + (hh ? 0.f : a3))) * rc.r);
#pragma unroll
    for (int k = 0; k < 4; k++) {
      const f32x2 u0 = ep[2 * k] * ip, u1 = ep[2 * k + 1] * ip, v0 = en[2 * k] * in, v1 = en[2 * k + 1] * in;
      u32x2 t0 = {pk2(u0[0], u0[1]), pk2(u1[0], u1[1])}, t1 = {pk2(v0[0], v0[1]), pk2(v1[0], v1[1])};
      ldsw<u32x2>(aIW ^ (unsigned)(k << 4), 0, t0); ldsw<u32x2>(aIW ^ (unsigned)(k << 4), 8192, t1);
    }
  };
  const unsigned sA = WS_A2 + rt2 * 144 * 1024;
  u32x4 af[NS][2];
  auto loadA1 = [&](int i, int m) AI { af[i][m] = ld16(rsWS, vL16, sA + ksb(i) * 1024, (144 * m + ksi(i)) * 1024); };
  auto stream = [&]() AI {
    SFOR(q, 28 * NT)
      constexpr int n = q / NT, j = q % NT;
      if constexpr (j == 0) {
        if constexpr (n == 27) {
#pragma unroll
          for (int t = 0; t < NT; t++) ldsw<unsigned>(aCSw, ((3 + t) & 3) * 512, hilo(csB[t][0]));
          if constexpr (AT && GG) WAITV(8);
          WAITL0(); BAR(); MEMFENCE();
        }
        else if constexpr (!AT && (n & 3) == 3) { asm volatile("s_waitcnt lgkmcnt(4)" ::: "memory"); BAR(); MEMFENCE(); }
      }
      if constexpr (q + 4 < 28 * NT) readB(q + 4);
      if constexpr (n == 27) cb[j] = ldsr<bf16x8>(aCSr, ((3 + j) & 3) * 512);
      SB();
      accB[j] = MFMA(wq[q & 7], xf[n], accB[j]);
      if constexpr (!NOCS && (n & 3) == 1) csB[j] = MFMA16(sel, wq[q & 7], csB[j]);
      if constexpr (!AT) {
        if constexpr (j == 0 && n >= 3 && n < 7) issueW2(1 + ((n - 3) >> 1), (n - 3) & 1);
        if constexpr (j == 0 && n >= 7 && n < 27 && (n & 1)) issueW2(1 + ((n + 1) >> 2), ((n + 1) >> 1) & 1);
        if constexpr (n == 27) issueW2(8, j);
        if constexpr (j == 1 && n == F1N) { waitv<(F1N < 7 ? F1N - 2 : 4 + (F1N - 5) / 2) + (TN < F1N ? 2 : 0)>(); stflag(E + 1, rsWS, sFown); }
        if constexpr (j == 1 && n == TN) touch(0);
        if constexpr (q == 2 * CK1 - 7) ldflag();
        if constexpr (q == 2 * CK1 - 1) {
          asm volatile("s_waitcnt vmcnt(%c5)" : "+v"(fl1), "+v"(accB[0]), "+v"(accB[NT - 1]), "+v"(csB[0]), "+v"(csB[NT - 1]) : "i"((CK1 & 1) ? 1 : 2) : "memory");
          const bool nde = nd1 && fc < 14;
          if (!ALL(!nde || (fl1 - E - 1u) <= 1u)) poll(rsWS, lane * FS, sF, E, nde ? 1u : 0u);
        }
        if constexpr (q >= 2 * CK1 && q < 2 * CK1 + 2 * NE) loadA1((q - 2 * CK1) >> 1, q & 1);
      } else {
        if constexpr (GG == 1 && n < 16 && (n & 1) == 0) clri(n >> 1);
        if constexpr (GG == 0 && j < 2 && n >= 1 && 2 * (n - 1) + j < 21) qstep(IntC<2 * (n - 1) + j>{}, accA[2], 0);
        if constexpr (GG == 0 && q >= 3 * H0L && q < 3 * H0L + 8) pv[q - 3 * H0L] = ld16(rsWS, vKP, sKP, WS_KVP + (q - 3 * H0L) * 16384);
        if constexpr (GG == 0 && j == 2 && n == H0C) { asm volatile("" : "+v"(accB[0]), "+v"(accB[1]), "+v"(accB[NT - 1]), "+v"(csB[0]), "+v"(csB[1]), "+v"(csB[NT - 1])); STAMP(11); hop1(); STAMP(6); }
      }
      SB();
    SEND
    const bf16x8 xe = mkxe();
#pragma unroll
    for (int t = 0; t < NT; t++) { accB[t] = MFMA(cb[t], xe, accB[t]); asm volatile("" : "+v"(accB[t])); }
    SB();
  };
  unsigned ln = vL16, aW = 0;
  auto mkaw = [&]() AI {
    asm volatile("" : "+v"(ln));
    aW = lw2 + (ln & 0x200) + ((ln & 0xc0) << 1) + ((((ln >> 3) & 0x20) | ((ln >> 1) & 0x10)) ^ ((ln >> 1) & 0x40)) + ((ln >> 1) & 8);
  };
  bf16x8 wf[NS][2];
  auto readW = [&](int i) AI {
    const unsigned ra = ring_area(i % RING), la = aW + (ra & 0x30000u);
    wf[i][0] = tr2(la, ra & 0xffffu, (ra & 0xffffu) + 1024); wf[i][1] = tr2(la ^ 64u, ra & 0xffffu, (ra & 0xffffu) + 1024);
  };
  auto tile = [&](f32x16& a, int t) AI { asm volatile("" : "+v"(a)); st16(epiP(a, rc), rsWS, vL16, sA2, t * 1024); SB(); };
  if constexpr (!AT) {
    if constexpr (GG == 0) {
      kpath(); WAITL0(); SB(); BAR(); SB(); STAMP(3);
      kvstep(IntC<0>{}); kvstep(IntC<1>{}); kvstep(IntC<3>{}); SB();
      tile(accA[2], 0);
    } else { tile(accA[0], 1); if (T2B) tile(accA[1], 2); BAR(); SB(); STAMP(3); if (!T2B) tile(accA[1], 2); tile(accA[2], 3); }
    readB(0); readB(1); readB(2); readB(3);
    STAMP(4);
    stream();
    SFOR(u, (2 * CK1 + 2 * NE > 56 ? 2 * CK1 + 2 * NE - 56 : 0)) loadA1((56 + u - 2 * CK1) >> 1, u & 1); SEND
    STAMP(5);
    mkaw();
    SFOR(i, RC) readW(i); SEND
    if constexpr (RC > 0) { WAITL0(); SB(); SFOR(i, RC) issueW2(9 + i, 0); issueW2(9 + i, 1); SEND }
    tile(accB[0], 4 + 2 * GG); tile(accB[NT - 1], 5 + 2 * GG);
  } else if constexpr (GG == 0) {
    SFOR(q, 8) clri(q); SEND
    SFOR(s, 30) kstep(s_); SEND
    WAITV(8);
    WAITL0(); SB(); BAR(); SB();
    STAMP(3);
    kvstep(IntC<0>{});
    readB(0); readB(1); readB(2); readB(3);
    kvstep(IntC<1>{}); kvstep(IntC<3>{});
    WAITV(2); stflag(E + 1, rsWS, sFown);
    STAMP(4);
    SB();
    stream();
  } else {
    stream();
    STAMP(3);
    SFOR(s, 21) qstep(s_, accA[0], 1); SEND
    WAITV(0); stflag(E + 1, rsWS, sFown);
    SFOR(s, 21) qstep(s_, accA[1], 2); SEND
    SFOR(s, 21)
      if constexpr (s < 8) pv[s] = ld16(rsWS, vKP, sKP, WS_KVP + s * 16384);
      qstep(s_, accA[2], 3);
    SEND
    STAMP(4); hop1(); STAMP(6);
    SB(); BAR(); SB();
  }

  if constexpr (AT) {
    STAMP(5);
    auto qexp = [&](f32x16& a, unsigned lh) AI { a = dualexpP(featproj(a, aWQ, lh * 1568, hh), rc.Lr); asm volatile("" : "+v"(a)); SB(); };
    if constexpr (GG == 0) qexp(accB[0], 4);
#pragma unroll
    for (int i = 0; i < 8; i++) pv[i] = ld16(rsWS, vL16, sKR, WS_KVR + i * 1024);
    if constexpr (GG == 0) { qexp(accB[1 % NT], 5); qexp(accB[NT - 1], 6); } else qexp(accB[0], 7);
    STAMP(7);
    waittag<1024>(pv, rsWS, vL16, sKR + WS_KVR, T16, 0xffffu);
    STAMP(8);
#pragma unroll
    for (int i = 0; i < 4; i++) {
      u32x4 f = {__builtin_amdgcn_perm(pv[2 * i][1], pv[2 * i][0], 0x07060302u), __builtin_amdgcn_perm(pv[2 * i][3], pv[2 * i][2], 0x07060302u),
                 __builtin_amdgcn_perm(pv[2 * i + 1][1], pv[2 * i + 1][0], 0x07060302u), __builtin_amdgcn_perm(pv[2 * i + 1][3], pv[2 * i + 1][2], 0x07060302u)};
      ldsw<u32x4>(LB + w * 4096 + vL16, i * 1024, f);
    }
    WAITL0(); BAR(); MEMFENCE();
    STAMP(10);
    f32x16 op[4], on[4];
    auto omm = [&](const f32x16& phi, unsigned lh, int z) AI {
#pragma unroll
      for (int i = 0; i < 16; i++) { op[z][i] = 0.f; on[z][i] = 0.f; }
      bf16x8 kf[4];
#pragma unroll
      for (int i = 0; i < 4; i++) kf[i] = ldsr<bf16x8>(LB + vL16, lh * 4096 + i * 1024);
#pragma unroll
      for (int i = 0; i < 2; i++) { op[z] = MFMA(kf[i], sub4(phi, i), op[z]); on[z] = MFMA(kf[2 + i], sub4(phi, 2 + i), on[z]); }
    };
    auto ofin = [&](unsigned lh, int z) AI {
      const float ia = __builtin_amdgcn_rcpf(op[z][13]), ib = __builtin_amdgcn_rcpf(on[z][13]);
      const float id = __builtin_amdgcn_rcpf(__builtin_fmaf(op[z][12], ia, __builtin_fmaf(on[z][12], ib, 1e-6f)));
      const float ca = xhi(ia * id), cb2 = xhi(ib * id);
      const f32x2 c2 = dup2(ca), d2 = dup2(cb2);
      u32x4 f0, f1;
#pragma unroll
      for (int i = 0; i < 4; i++) {
        const f32x2 u = fma2(sub2(op[z], i), c2, sub2(on[z], i) * d2), v = fma2(sub2(op[z], 4 + i), c2, sub2(on[z], 4 + i) * d2);
        f0[i] = pk2(u[0], u[1]); f1[i] = pk2(v[0], v[1]);
      }
      f1[2] = hh ? 0u : f1[2]; f1[3] = hh ? 0u : f1[3];
      st16(f0, rsWS, OV, sA2, (112 + 2 * lh) * 1024); st16(f1, rsWS, OV, sA2, (113 + 2 * lh) * 1024);
      SB();
    };
    if constexpr (GG) { omm(accA[0], 1, 0); omm(accA[1], 2, 1); SB(); ofin(1, 0); omm(accA[2], 3, 2); omm(accB[0], 7, 3); SB(); ofin(2, 1); ofin(3, 2); ofin(7, 3); }
    else { omm(accA[2], 0, 0); omm(accB[0], 4, 1); SB(); ofin(0, 0); omm(accB[1 % NT], 5, 2); omm(accB[NT - 1], 6, 3); SB(); ofin(4, 1); ofin(5, 2); ofin(6, 3); }
    STAMP(9);
  } else {
    f32x16 acc[2][2];
#pragma unroll
    for (int i = 0; i < 4; i++)
#pragma unroll
      for (int k = 0; k < 16; k++) acc[i >> 1][i & 1][k] = 0.f;
    u32x4 xr;
    const unsigned vO = (ln >> 7) * XROW + (ln & 0x70), sO = (rt2 * 32 + 8 * w) * XROW + ci * 128;
    auto mm4 = [&](int i) AI {
#pragma unroll
      for (int nn = 0; nn < 2; nn++)
#pragma unroll
        for (int mm = 0; mm < 2; mm++) acc[nn][mm] = MFMA(wf[i][nn], __builtin_bit_cast(bf16x8, af[i][mm]), acc[nn][mm]);
      SB();
    };
    STAMP(6);
    waitv<2 + 2 * RC>();
#pragma unroll
    for (int i = 0; i < NE; i++) asm volatile("" : "+v"(af[i][0]), "+v"(af[i][1]));
    SFOR(i, 9 - RC) readW(RC + i); SEND
    SB();
    SFOR(i, EB) mm4(i); SEND
    WAITV(0); stflag(E + 2, rsWS, sFown);
    WAITL0(); SB();
    BAR();
    SB(); STAMP(11);
    if constexpr (GG) __builtin_amdgcn_s_setprio(P2G1);
    constexpr int NB = (GG ? 9 : G0B) - RC;
    SFOR(i, 9)
      if constexpr (i == NB - 2) ldflag();
      if constexpr (i < NB) { issueW2(9 + RC + i, 0); issueW2(9 + RC + i, 1); SB(); }
      if constexpr (EB + i < NE) mm4(EB + i);
    SEND
    asm volatile("s_waitcnt vmcnt(4)" : "+v"(fl1) :: "memory");
    { const unsigned need = !nd1 ? 0u : fc < 14 ? 2u : 1u;
      if (!ALL(!nd1 || fl1 == E + need)) poll(rsWS, lane * FS, sF, E, need); }
    STAMP(7);
#pragma unroll
    for (int j = NE; j < NE + PD; j++) { loadA1(j, 0); loadA1(j, 1); }
    asm volatile("buffer_load_dwordx4 %0, %1, %2, %3 offen" : "=v"(xr) : "v"(vO), "s"(rsX), "s"(sO) : "memory");
    SFOR(i, 9 - RC - NB) issueW2(9 + RC + NB + i, 0); issueW2(9 + RC + NB + i, 1); SEND
    SFOR(i1, NS - NE)
      constexpr int i = NE + i1;
      asm volatile("s_waitcnt vmcnt(%c2)" : "+v"(af[i][0]), "+v"(af[i][1]) : "i"(2 * ((i + PD <= NS ? i + PD : NS) - 1 - i) + (i1 < PD && i < NO + 8 * GG ? 1 + 2 * (9 - RC - NB) : 0)) : "memory");
      if constexpr (i == 9 && i1 == 0) readW(9);
      if constexpr (i + 1 >= 9 && i + 1 < NS) readW(i + 1);
      if constexpr (GG == 0 && i >= NO) {
        auto stale = [&]() AI {
          unsigned m = UMAX(UMAX(UMAX(af[i][0][0], af[i][0][1]), UMAX(af[i][0][2], af[i][0][3])), UMAX(UMAX(af[i][1][0], af[i][1][1]), UMAX(af[i][1][2], af[i][1][3])));
          return ANY(m == ~0u);
        };
        if (__builtin_expect(stale(), 0)) {
          int it = 0;
#pragma nounroll
          do { __builtin_amdgcn_s_sleep(SLEEPR); rel1<true>(af[i][0], af[i][1], rsWS, vL16, sA + (ksb(i) + ksi(i)) * 1024); } while (++it < (1 << 14) && stale());
          SFOR(u, PD - 1) if constexpr (i + 1 + u < NS) rel1<false>(af[i + 1 + u][0], af[i + 1 + u][1], rsWS, vL16, sA + (ksb(i + 1 + u) + ksi(i + 1 + u)) * 1024); SEND
        }
      }
      if constexpr (i == 10) STAMP(8);
      if constexpr (i == 14) STAMP(9);
      SB();
      if constexpr (i + PD < NS) { loadA1(i + PD, 0); loadA1(i + PD, 1); }
      mm4(i);
    SEND
    STAMP(10);
    __builtin_amdgcn_s_setprio(0);
#pragma unroll
    for (int qd = 0; qd < 4; qd++)
#pragma unroll
      for (int k = 0; k < 4; k++) {
        const unsigned ra = ring_area(2 * qd + (k >> 1)) + (k & 1) * 1024;
        ldsw<bf16x8>(((lw2 + vL16) ^ (unsigned)(32 * (4 * (qd >> 1) + k))) + (ra & 0x30000u), ra & 0xffffu, sub4(acc[qd >> 1][qd & 1], k));
      }
    WAITL0(); BAR(); MEMFENCE();
    STAMP(12);
    const unsigned c8 = (ln >> 4) & 7, j2 = (c8 & 4) + 2 * GG + ((c8 >> 1) & 1);
    const unsigned rd = LB + ((0x51407362u >> (4 * j2)) & 7) * 16384 + (c8 & 1) * 1024 + (((8 * wr + (ln >> 7)) << 4) ^ (c8 << 5));
    asm volatile("" : "+v"(xr));
    f32x4 sum[2];
#pragma unroll
    for (int k = 0; k < 2; k++) {
      f32x4 t = {U2F(xr[2 * k] << 16), U2F(xr[2 * k] & 0xffff0000u), U2F(xr[2 * k + 1] << 16), U2F(xr[2 * k + 1] & 0xffff0000u)};
#pragma unroll
      for (int v = 0; v < 8; v++) t += ldsr<f32x4>(rd, v * 2048 + k * 512);
      sum[k] = t;
    }
    u32x4 o = {pk2(sum[0][0], sum[0][1]), pk2(sum[0][2], sum[0][3]), pk2(sum[1][0], sum[1][1]), pk2(sum[1][2], sum[1][3])};
    __builtin_amdgcn_raw_buffer_store_b128(o, mkrs(pout, 2048 * XROW), vO + sO, 0, OAUX);
  }
  STAMPS_OUT();
  };
  if (!attn && g) prog(BoolC<false>{}, IntC<1>{});
  else if (!attn) prog(BoolC<false>{}, IntC<0>{});
  else if (g) prog(BoolC<true>{}, IntC<1>{});
  else prog(BoolC<true>{}, IntC<0>{});
}

extern "C" void kernel_launch(void* const* d_in, const int* in_sizes, int n_in, void* d_out, int out_size, void* d_ws, size_t ws_size,
                              hipStream_t stream) {
  fused_kernel<<<256, 512, 0, stream>>>((const char*)d_in[0], (const char*)d_in[1], (const char*)d_in[5], (const char*)d_in[6], (char*)d_ws,
                                        (const char*)d_in[2], (const char*)d_in[7], (const char*)d_in[3], (char*)d_out);
}
```

```cpp
#define TMODE 3
#include <hip/hip_runtime.h>
#include <hip/hip_bf16.h>
#include <utility>

#define DI __device__ __forceinline__
#define LAS __attribute__((address_space(3)))
typedef __attribute__((ext_vector_type(8))) short bf16x8;
typedef __attribute__((ext_vector_type(4))) short s16x4;
typedef __attribute__((ext_vector_type(2))) __bf16 bf2;
typedef __attribute__((ext_vector_type(2))) float f32x2;
typedef __attribute__((ext_vector_type(4))) float f32x4;
typedef __attribute__((ext_vector_type(16))) float f32x16;
typedef __attribute__((ext_vector_type(2))) unsigned u32x2;
typedef __attribute__((ext_vector_type(4))) unsigned u32x4;
typedef __amdgpu_buffer_rsrc_t rsrc_t;
#define F2U(x) __builtin_bit_cast(unsigned, (float)(x))
#define U2F(x) __builtin_bit_cast(float, (unsigned)(x))
#define ALL(x) (__builtin_amdgcn_ballot_w64(!(x)) == 0ull)
#define ANY(x) (__builtin_amdgcn_ballot_w64(x) != 0ull)
#define UMAX(a, b) __builtin_elementwise_max((unsigned)(a), (unsigned)(b))

#define NTX 0
#define NTW 0
#define NT2 0
#define XS 2
#define MAP2 1
constexpr unsigned XROW = 896, QROW = 2688, FROW = 3584;
constexpr unsigned FS = 128;
constexpr unsigned WS_EPOCH = 0;
constexpr unsigned WS_GR = 4096;
constexpr unsigned WS_KVP = 131072;
constexpr unsigned WS_A2 = WS_KVP + 16 * 16 * 8192;
constexpr unsigned WS_ST = WS_A2 + 64 * 144 * 1024;
constexpr unsigned WS_FH = WS_ST + 262144;
constexpr unsigned WS_KVR = WS_FH + 2048 * FS;
constexpr unsigned WS_END = WS_KVR + 16 * 8192;
constexpr unsigned L_GU = 65536, L_KV = 131072;
constexpr unsigned L_TAIL = L_KV + 3 * 8192;
constexpr unsigned L_WPK = L_TAIL;
constexpr unsigned L_ST = L_TAIL + 2048;
constexpr unsigned L_CS = L_TAIL + 4096;
constexpr unsigned L_PHI = L_GU + 32768;
constexpr unsigned L_V = L_KV + 16384;
constexpr unsigned L_FREE = 32768;

#define MFMA(a, b, c) __builtin_amdgcn_mfma_f32_32x32x16_bf16((a), (b), (c), 0, 0, 0)
#define MFMA16(a, b, c) __builtin_amdgcn_mfma_f32_16x16x32_bf16((a), (b), (c), 0, 0, 0)
#define AI __attribute__((always_inline))
#define BAR() __builtin_amdgcn_s_barrier()
#define WAITV(n) asm volatile("s_waitcnt vmcnt(" #n ")" ::: "memory")
#define WAITL0() asm volatile("s_waitcnt lgkmcnt(0)" ::: "memory")
#define SB() __builtin_amdgcn_sched_barrier(0)
#define MEMFENCE() asm volatile("" ::: "memory")

template <int N> DI void waitv() { asm volatile("s_waitcnt vmcnt(%c0)" :: "i"(N) : "memory"); }

#define INSZ(x) (x)
DI rsrc_t mkrs(const void* p, unsigned bytes) { return __builtin_amdgcn_make_buffer_rsrc((void*)p, (short)0, (int)bytes, 0x00020000); }
template <bool NT = false> DI void dma(unsigned lds, unsigned limm, unsigned voff, rsrc_t rs, unsigned soff, unsigned simm) {
  unsigned t;
  if constexpr (NT) asm volatile("s_add_u32 m0, %1, %c5\n\ts_add_u32 %0, %4, %c6\n\tbuffer_load_dwordx4 %2, %3, %0 offen nt lds"
               : "=&s"(t) : "s"(lds), "v"(voff), "s"(rs), "s"(soff), "i"(limm), "i"(simm) : "memory", "scc");
  else asm volatile("s_add_u32 m0, %1, %c5\n\ts_add_u32 %0, %4, %c6\n\tbuffer_load_dwordx4 %2, %3, %0 offen lds"
               : "=&s"(t) : "s"(lds), "v"(voff), "s"(rs), "s"(soff), "i"(limm), "i"(simm) : "memory", "scc");
}
DI void dmat(unsigned lds, unsigned voff, rsrc_t rs) {
  asm volatile("s_mov_b32 m0, %0\n\ts_nop 0\n\tbuffer_load_dword %1, %2, 0 offen lds" :: "s"(lds), "v"(voff), "s"(rs) : "memory");
}
DI s16x4 trd(unsigned a) { return __builtin_amdgcn_ds_read_tr16_b64_v4i16((LAS s16x4*)(size_t)a); }
DI s16x4 trd2(unsigned a, unsigned off) { return __builtin_amdgcn_ds_read_tr16_b64_v4i16((LAS s16x4*)((LAS char*)(size_t)a + off)); }
DI bf16x8 tr2(unsigned a, unsigned off0, unsigned off1) {
  LAS char* p = (LAS char*)(size_t)a;
  s16x4 lo = __builtin_amdgcn_ds_read_tr16_b64_v4i16((LAS s16x4*)(p + off0));
  s16x4 hi = __builtin_amdgcn_ds_read_tr16_b64_v4i16((LAS s16x4*)(p + off1));
  return __builtin_shufflevector(lo, hi, 0, 1, 2, 3, 4, 5, 6, 7);
}
template <class T> DI T ldsr(unsigned a, unsigned off) { return *(LAS T*)((LAS char*)(size_t)a + off); }
template <class T> DI void ldsw(unsigned a, unsigned off, T v) { *(LAS T*)((LAS char*)(size_t)a + off) = v; }
DI unsigned pk2(float a, float b) { f32x2 f = {a, b}; return __builtin_bit_cast(unsigned, __builtin_convertvector(f, bf2)); }
DI float rbf(float a) { return U2F(pk2(a, a) << 16); }
DI float dot2(unsigned a, unsigned b, float c) { return __builtin_amdgcn_fdot2_f32_bf16(__builtin_bit_cast(bf2, a), __builtin_bit_cast(bf2, b), c, false); }
DI float xsum(float v) { auto r = __builtin_amdgcn_permlane32_swap(F2U(v), F2U(v), false, false); float a = U2F(r[0]); asm("" : "+v"(a)); return a + U2F(r[1]); }
DI float xmax(float v) { auto r = __builtin_amdgcn_permlane32_swap(F2U(v), F2U(v), false, false); return __builtin_fmaxf(U2F(r[0]), U2F(r[1])); }
DI float xmin(float v) { auto r = __builtin_amdgcn_permlane32_swap(F2U(v), F2U(v), false, false); return __builtin_fminf(U2F(r[0]), U2F(r[1])); }
DI float xhi(float v) { auto r = __builtin_amdgcn_permlane32_swap(F2U(v), F2U(v), false, false); return U2F(r[1]); }
#define STB "sc1"
DI void st16(u32x4 v, rsrc_t rs, unsigned voff, unsigned soff, unsigned simm) {
  unsigned t;
  asm volatile("s_add_u32 %0, %4, %c5\n\tbuffer_store_dwordx4 %1, %2, %3, %0 offen " STB "\n\ts_nop 1" : "=&s"(t) : "v"(v), "v"(voff), "s"(rs), "s"(soff), "i"(simm) : "memory", "scc");
}
DI void stflag(unsigned v, rsrc_t rs, unsigned soff) {
  unsigned voff = (__builtin_amdgcn_mbcnt_hi(~0u, __builtin_amdgcn_mbcnt_lo(~0u, 0u)) == 0u) ? 0u : 0x80000000u;
  __builtin_amdgcn_raw_buffer_store_b32(v, rs, voff, soff, 16);
}
#define XORCOND (n == 24)
#define PS 0
#define PV 0
#define KEEP2 0
#define S2N 14
#define B1N 23
#define DIST 2
#define P2G1 1
#define OAUX 0
#define RECY 0
#define POFF 99
#define H1S 0
#define H0L 14
#define H0C 22
#define Q3A 1
#define COOP 0
#define PON -2
#define XN 20
#define BAN 18
#define BBN 21
#define SL0 8
#define SLN 2
#define SLD 3
#define NSL 48
#define H1C 16
#define OV vL16
#define CV vL16
#define Q6E 0
#define TEARLY 0
#define NOCS 0
#define TPOS 0
#define PRE 0
#define LREC 0
#define CK1 20
#define EB 3
#define T2B 1
#define F1N 9
#define RC 3
#define G0B 9
#define TN 10
#define SLEEPR 4
#define SLEEPN 2
DI u32x4 ld16(rsrc_t rs, unsigned voff, unsigned soff, unsigned simm) {
  u32x4 r; unsigned t;
  asm volatile("s_add_u32 %1, %4, %c5\n\tbuffer_load_dwordx4 %0, %2, %3, %1 offen sc1" : "=v"(r), "=&s"(t) : "v"(voff), "s"(rs), "s"(soff), "i"(simm) : "memory", "scc");
  return r;
}
template <unsigned STEP> DI void ld8w(u32x4 (&p)[8], rsrc_t rs, unsigned voff, unsigned soff) {
  unsigned t;
#define L1(k) "buffer_load_dwordx4 %" #k ", %9, %10, %8 offen sc1\n\ts_add_u32 %8, %8, %c12\n\t"
  asm volatile("s_mov_b32 %8, %11\n\t" L1(0) L1(1) L1(2) L1(3) L1(4) L1(5) L1(6) L1(7) "s_waitcnt vmcnt(0)"
               : "=&v"(p[0]), "=&v"(p[1]), "=&v"(p[2]), "=&v"(p[3]), "=&v"(p[4]), "=&v"(p[5]), "=&v"(p[6]), "=&v"(p[7]), "=&s"(t)
               : "v"(voff), "s"(rs), "s"(soff), "i"(STEP) : "memory", "scc");
#undef L1
}
DI bool untag(u32x4 (&p)[8], unsigned tag, unsigned mask) {
  unsigned chk = 0;
#pragma unroll
  for (int i = 0; i < 8; i++)
#pragma unroll
    for (int k = 0; k < 4; k++) { p[i][k] ^= tag; chk |= p[i][k]; }
  return ALL((chk & mask) == 0u);
}
template <unsigned STEP> DI void waittag(u32x4 (&p)[8], rsrc_t rs, unsigned voff, unsigned soff, unsigned tag, unsigned mask) {
  asm volatile("s_waitcnt vmcnt(0)" : "+v"(p[0]), "+v"(p[1]), "+v"(p[2]), "+v"(p[3]), "+v"(p[4]), "+v"(p[5]), "+v"(p[6]), "+v"(p[7]) :: "memory");
#pragma nounroll
  for (int it = 0; !untag(p, tag, mask) && it < (1 << 16); ++it) { __builtin_amdgcn_s_sleep(SLEEPN); ld8w<STEP>(p, rs, voff, soff); }
}
template <bool WAIT> DI void rel1(u32x4& a, u32x4& b, rsrc_t rs, unsigned voff, unsigned so) {
  unsigned t;
  if constexpr (WAIT) asm volatile("buffer_load_dwordx4 %0, %3, %4, %5 offen sc1\n\ts_add_u32 %2, %5, 147456\n\tbuffer_load_dwordx4 %1, %3, %4, %2 offen sc1\n\ts_waitcnt vmcnt(0)"
                                   : "+v"(a), "+v"(b), "=&s"(t) : "v"(voff), "s"(rs), "s"(so) : "memory", "scc");
  else asm volatile("buffer_load_dwordx4 %0, %3, %4, %5 offen sc1\n\ts_add_u32 %2, %5, 147456\n\tbuffer_load_dwordx4 %1, %3, %4, %2 offen sc1"
                    : "+v"(a), "+v"(b), "=&s"(t) : "v"(voff), "s"(rs), "s"(so) : "memory", "scc");
}
DI void poll(rsrc_t rs, unsigned voff, unsigned soff, unsigned E, unsigned need) {
  voff = need ? voff : 0x80000000u;
#pragma nounroll
  for (int it = 0; it < (1 << 16); ++it) {
    unsigned v;
    asm volatile("buffer_load_dword %0, %1, %2, %3 offen sc1\n\ts_waitcnt vmcnt(0)" : "=v"(v) : "v"(voff), "s"(rs), "s"(soff) : "memory");
    bool ok = (need == 0u) || ((v - E - need) <= (3u - need));
    if (ALL(ok)) break;
    __builtin_amdgcn_s_sleep(SLEEPN);
  }
}

template <bool B> struct BoolC { static constexpr bool value = B; };
template <int I> struct IntC { static constexpr int value = I; };
template <int... I, class F> DI void sfor_impl(std::integer_sequence<int, I...>, F&& f) { (f(IntC<I>{}), ...); }
template <int N, class F> DI void sfor(F&& f) { sfor_impl(std::make_integer_sequence<int, N>{}, f); }
#define SFOR(n, N) sfor<N>([&](auto n##_) AI { constexpr int n = decltype(n##_)::value;
#define SEND });
struct RowC { float r, K1, K2, Ri, Lr; };

DI unsigned hilo(float v) { float hi = rbf(v); return pk2(hi, v - hi); }
DI float max3(float a, float b, float c) { return __builtin_fmaxf(__builtin_fmaxf(a, b), c); }
DI float min3(float a, float b, float c) { return __builtin_fminf(__builtin_fminf(a, b), c); }
DI unsigned pk2s(float a, float b) { unsigned r; asm("v_cvt_pk_bf16_f32 %0, %1, %2" : "=v"(r) : "v"(a), "v"(b)); return r; }
DI f32x2 sub2(const f32x16& v, int k) { f32x2 t = {v[2 * k], v[2 * k + 1]}; return t; }
DI f32x2 exp2v(f32x2 v) { f32x2 r = {__builtin_amdgcn_exp2f(v[0]), __builtin_amdgcn_exp2f(v[1])}; return r; }
DI f32x2 dup2(float a) { f32x2 r = {a, a}; asm("" : "+v"(r)); return r; }
DI f32x2 fma2(f32x2 a, f32x2 b, f32x2 c) { return __builtin_elementwise_fma(a, b, c); }
DI u32x4 epiP(const f32x16& a, const RowC& c) {
  u32x4 r;
  const f32x2 K1 = dup2(c.K1), K2 = dup2(c.K2), Ri = dup2(c.Ri);
#pragma unroll
  for (int i = 0; i < 4; i++) {
    const f32x2 g = sub2(a, i), d = fma2(exp2v(g * fma2(g * g, K2, K1)), Ri, Ri);
    const f32x2 s = {__builtin_amdgcn_rcpf(d[0]), __builtin_amdgcn_rcpf(d[1])}, h = (g * sub2(a, 4 + i)) * s;
    r[i] = pk2(h[0], h[1]);
  }
  return r;
}
struct EpiS { float x0, x1, m0, m1; u32x4 r; };
template <int P, int PH> DI void epiSlice(const f32x16& a, const RowC& c, EpiS& s) {
  const float g0 = a[2 * P], g1 = a[2 * P + 1];
  if constexpr (PH == 0) { s.x0 = g0 * __builtin_fmaf(g0 * g0, c.K2, c.K1); s.x1 = g1 * __builtin_fmaf(g1 * g1, c.K2, c.K1); }
  if constexpr (PH == 1) { s.x0 = __builtin_amdgcn_exp2f(s.x0); s.x1 = __builtin_amdgcn_exp2f(s.x1); s.m0 = g0 * a[8 + 2 * P]; s.m1 = g1 * a[9 + 2 * P]; }
  if constexpr (PH == 2) { s.x0 = __builtin_amdgcn_rcpf(__builtin_fmaf(s.x0, c.Ri, c.Ri)); s.x1 = __builtin_amdgcn_rcpf(__builtin_fmaf(s.x1, c.Ri, c.Ri)); }
  if constexpr (PH == 3) s.r[P] = pk2s(s.m0 * s.x0, s.m1 * s.x1);
}
DI f32x16 featproj2(const f32x16& a, bf16x8 w0, bf16x8 w1, bool hh) {
  u32x4 q0, q1;
#pragma unroll
  for (int i = 0; i < 4; i++) { q0[i] = pk2(a[2 * i], a[2 * i + 1]); q1[i] = pk2(a[8 + 2 * i], a[9 + 2 * i]); }
  q1[2] = hh ? 0u : q1[2]; q1[3] = hh ? 0u : q1[3];
  f32x16 z;
#pragma unroll
  for (int i = 0; i < 16; i++) z[i] = 0.f;
  z = MFMA(w0, __builtin_bit_cast(bf16x8, q0), z);
  z = MFMA(w1, __builtin_bit_cast(bf16x8, q1), z);
  return z;
}
DI f32x16 featproj(const f32x16& a, unsigned wpaddr, unsigned off, bool hh) {
  return featproj2(a, tr2(wpaddr, off, off + 8 * 56), tr2(wpaddr, off + 16 * 56, off + 24 * 56), hh);
}
DI float max16(const f32x16& p) { float m = max3(p[0], p[1], p[2]); _Pragma("unroll") for (int i = 3; i < 15; i += 2) m = max3(m, p[i], p[i + 1]); return xmax(__builtin_fmaxf(m, p[15])); }
DI float min16(const f32x16& p) { float m = min3(p[0], p[1], p[2]); _Pragma("unroll") for (int i = 3; i < 15; i += 2) m = min3(m, p[i], p[i + 1]); return xmin(__builtin_fminf(m, p[15])); }
DI f32x16 dualexpP(const f32x16& p, float Lr) {
  const f32x2 bx = dup2(-max16(p) * Lr), bn = dup2(min16(p) * Lr), L2 = dup2(Lr);
  f32x16 o;
#pragma unroll
  for (int i = 0; i < 8; i++) {
    const f32x2 ep = exp2v(fma2(sub2(p, i), L2, bx)), en = exp2v(fma2(sub2(p, i), -L2, bn));
    o[i] = U2F(pk2(ep[0], ep[1])); o[8 + i] = U2F(pk2(en[0], en[1]));
  }
  return o;
}
struct SmS { f32x16 p, e; float en[16], bx, bn, sp, sn, tp, tn; bf16x8 w0, w1; };
DI bf16x8 sub4(const f32x16& v, int k) { f32x4 t = {v[4 * k], v[4 * k + 1], v[4 * k + 2], v[4 * k + 3]}; return __builtin_bit_cast(bf16x8, t); }

DI constexpr unsigned lc1_base(int ch) { return ch == 6 ? L_KV : ((ch & 1) ? L_GU : 0u) + (ch < 2 ? 49152u : ch < 4 ? 0u : 16384u); }
template <int CTRL, int RM> DI float dppadd(float v) { return v + U2F(__builtin_amdgcn_update_dpp(0u, F2U(v), CTRL, RM, 0xf, false)); }
DI float wavesum63(float v) {
  v = dppadd<0x111, 0xf>(v); v = dppadd<0x112, 0xf>(v); v = dppadd<0x114, 0xf>(v); v = dppadd<0x118, 0xf>(v);
  v = dppadd<0x142, 0xa>(v); return dppadd<0x143, 0xc>(v);
}

#define STAMP(i)
#define STAMPS_OUT()
#define CSTAMP(i)

#define PDV 6
constexpr int PD = PDV, RING = 9;
constexpr int later2(int i, int NS, int GG) {
  const int younger = 2 * ((i + PD <= NS ? i + PD : NS) - 1 - i);
  int extra = i < PD ? 1 : 0;
  if (NS == 20 && (i == 10 || i == 11)) extra += 2 * (i - 9);
  return younger + extra;
}
DI constexpr unsigned ring_area(int j) { return j == 0 ? L_FREE : j == 1 ? L_PHI : lc1_base(j - 2); }
constexpr int cumG1(int s) { return s < 4 ? 0 : s < 22 ? (s - 4) / 2 : (9 + (s - 22) * 5 / 4 < 48 ? 9 + (s - 22) * 5 / 4 : 48); }

#define CODE_BYTES 65536
__global__ __launch_bounds__(512, 2) void fused_kernel(const char* px_, const char* pqkv_, const char* pwg_, const char* pwu_, char* ws_, const char* pphi_,
                                                       const char* pwd_, const char* pwo_, char* pout_) {
  typedef __attribute__((ext_vector_type(8))) unsigned long long u64x8;
  u64x8 ka; unsigned long long kb;
  asm volatile("s_load_dwordx16 %0, %2, 0\n\ts_load_dwordx2 %1, %2, 64" : "=&s"(ka), "=&s"(kb) : "s"(__builtin_amdgcn_kernarg_segment_ptr()));
  __shared__ __attribute__((aligned(16))) char smem[163840];
  const unsigned LB = (unsigned)(size_t)smem;
  const unsigned tid = __builtin_amdgcn_workitem_id_x(), lane = tid & 63;
  const unsigned w = __builtin_amdgcn_readfirstlane(tid >> 6), g = w >> 2, wr = w & 3;
  const unsigned b = __builtin_amdgcn_workgroup_id_x(), xcd = b & 7, bi = b >> 3;
  const unsigned half = (xcd >> 1) & 1, cp = 4 * (xcd & 1) + (bi >> 3), rb = 8 * (xcd >> 2) + (bi & 7), rsum = (bi >> 3) + (bi & 7);
  const unsigned f2 = 32 * (xcd & 1) + bi - 8, t2 = (f2 * 37) >> 8, ci = f2 - 7 * t2, rbl = t2 & 3, rt2 = 2 * (8 * (xcd >> 1) + t2);
  const unsigned c = 7 * half + cp - 1;
  const unsigned head = 8 * half + (((half ? 0x76425310u : 0x75316420u) >> (4 * cp)) & 7);
  const bool attn = cp == 0;
  STAMP(0); CSTAMP(14);
  const unsigned wstr = attn ? QROW : FROW;
  const unsigned sG0 = half * (attn ? 448 : 1792) + (attn ? 0 : (cp - 1) * 256), sX0 = rb * 128 * XROW;
  unsigned vx0, vx1, vG, vU, vKV;
  auto voffs = [&](unsigned ln) AI {
    const unsigned r8 = ln >> 3, c8 = ln & 7, sa = (r8 >> 1) & 1, lc = c8 ^ (4 * sa);
    vx0 = (16 * w + r8) * XROW + ((c8 ^ (r8 >> 1)) << 4); vx1 = (vx0 ^ 64u) + 8 * XROW;
    const unsigned cg = attn ? lc : (c8 ^ (2 * sa)), cu = attn ? (lc >= 6 ? lc - 2 : lc) : (cg ^ 4);
    vG = (8 * w + r8) * wstr + (cg << 4); vU = (8 * w + r8) * wstr + (cu << 4);
    vKV = (8 * w + r8) * QROW + 896 * (1 + (lc >> 2)) + ((lc & 3) << 4) + ((56 * head) & ~15u);
  };
  voffs(lane);
  const unsigned lw1 = LB + w * 1024, lw2 = LB + w * 2048;
#define ROT (((0x124924u >> (2 * rsum)) & 3) * 2)
  const unsigned rot = ROT;
  const unsigned srow = 128 * rb + 64 * half + 8 * cp + w;
  unsigned vrow = lane < 56 ? lane * 16 : 0x80000000u;
  asm volatile("s_waitcnt lgkmcnt(0)" : "+s"(ka), "+s"(kb), "+v"(vx0), "+v"(vx1), "+v"(vG), "+v"(vU), "+v"(vKV), "+v"(vrow));
  const char *px = (const char*)ka[0], *pqkv = (const char*)ka[1], *pwg = (const char*)ka[2], *pwu = (const char*)ka[3], *pphi = (const char*)ka[5],
             *pwd = (const char*)ka[6], *pwo = (const char*)ka[7];
  char *ws = (char*)ka[4], *pout = (char*)kb;
  const rsrc_t rsX = mkrs(px, INSZ(2048 * XROW)), rsQ = mkrs(pqkv, INSZ(448 * QROW));
  const rsrc_t rsG = mkrs(attn ? pqkv : pwg, INSZ(448 * wstr)), rsU = mkrs(attn ? pqkv + 128 : pwu, INSZ(448 * wstr - (attn ? 128 : 0)));
  auto cidx = [&](int st) AI -> unsigned { unsigned t = rot + st; return t >= 7 ? t - 7 : t; };
  auto issueA = [&](int st, int k) AI {
    const unsigned sl = st & 3, ch = cidx(st);
    if (k == 0) dma<NTX>(lw2, sl * 16384, vx0, rsX, sX0 + ch * 128, 0);
    if (k == 1) dma<NTX>(lw2, sl * 16384 + 1024, vx1, rsX, sX0 + ch * 128, 0);
    if (k == 2) dma<NTW>(lw1, L_GU + sl * 16384, vG, rsG, sG0 + ch * 64 * wstr, 0);
    if (k == 3) dma<NTW>(lw1, L_GU + sl * 16384 + 8192, vU, rsU, sG0 + ch * 64 * wstr, 0);
    if (k == 4) dma<NTW>(lw1, L_KV + sl * 8192, vKV, rsQ, ch * 64 * QROW, 0);
  };
  const rsrc_t rsWS = mkrs(ws, WS_END);
  unsigned Ev, cpf; u32x4 xrw;
  asm volatile("buffer_load_dwordx4 %0, %1, %2, %3 offen" : "=v"(xrw) : "v"(vrow), "s"(rsX), "s"(srow * XROW) : "memory");
  asm volatile("buffer_load_dword %0, off, %1, 0 sc1" : "=v"(Ev) : "s"(rsWS) : "memory");
  {
    unsigned long long pc; asm volatile("s_getpc_b64 %0" : "=s"(pc));
    const unsigned vo = w == 0 && lane < 24 ? (bi * 24 + lane) * 128 : 0x80000000u;
    asm volatile("buffer_load_dword %0, %1, %2, 0 offen" : "=v"(cpf) : "v"(vo), "s"(mkrs((const void*)pc, CODE_BYTES)) : "memory");
  }
#pragma unroll
  for (int k = 0; k < 5; k++) issueA(0, k);
  asm volatile("s_waitcnt vmcnt(%c3)" : "+v"(xrw), "+v"(Ev), "+v"(cpf) : "i"(5 + COOP) : "memory");
#pragma unroll
  for (int k = 0; k < 5; k++) issueA(1, k);
  issueA(2, 0);
  const unsigned E = __builtin_amdgcn_readfirstlane(Ev);
  {
    float s1 = 0.f, s2 = 0.f;
#pragma unroll
    for (int i = 0; i < 4; i++) { s1 = dot2(xrw[i], 0x3f803f80u, s1); s2 = dot2(xrw[i], xrw[i], s2); }
    s1 = wavesum63(s1); s2 = wavesum63(s2);
    const float mean = s1 / 448.f, var = __builtin_fmaxf(s2 * (1.f / 448.f) - mean * mean, 0.f);
    u32x2 gr = {pk2(-mean, __builtin_amdgcn_sqrtf(rbf(rbf(var) + 1e-5f))), E + 1};
    __builtin_amdgcn_raw_buffer_store_b64(gr, rsWS, lane == 63 ? 0u : 0x80000000u, WS_GR + srow * 8, 16);
  }

  auto prog = [&](auto at_, auto gg_) AI {
  constexpr bool AT = decltype(at_)::value; constexpr int GG = decltype(gg_)::value;
  constexpr unsigned lcstep = AT ? 224 : 128;
  unsigned lane;
  asm volatile("v_mbcnt_lo_u32_b32 %0, -1, 0\n\tv_mbcnt_hi_u32_b32 %0, -1, %0" : "=v"(lane));
  __builtin_assume(lane < 64);
  voffs(lane);
  auto issueL = [&](int hc) AI {
    const int st = hc >> 1; const unsigned ch = cidx(st);
    if (hc & 1) dma<NTW>(lw1, lc1_base(st) + 8192, vU, rsU, sG0 + ch * 64 * wstr, lcstep);
    else dma<NTW>(lw1, lc1_base(st), vG, rsG, sG0 + ch * 64 * wstr, lcstep);
  };
  auto issueG = [&](int bar, int k) AI {
    if (bar == 0) issueA(2 + k / 5, k % 5);
    else if (bar <= 3) issueA(bar + 3, k);
    else if (k < 4) issueL(4 * (bar - 4) + k);
    else if (bar == 4) dma(LB + (w & 1) * 1024, L_WPK, lane * 16, mkrs(pphi, 16 * 1568), head * 1568 + (w & 1) * 1024, 0);
    else issueL(12 + (bar - 5));
  };

  const unsigned rt = 4 * rb + wr;
  const unsigned sA2 = WS_A2 + rt * 144 * 1024 + (AT ? half * 16384 : c * 8192), vL16 = lane * 16;
  const u32x4 sent = {~0u, ~0u, ~0u, ~0u};
  auto clr = [&](int ks) AI { st16(sent, rsWS, CV, sA2, ks * 1024); };

  auto clri = [&](int q) AI { clr(GG ? (q < 6 ? 114 + q : 120 + q) : (q < 2 ? 112 + q : 118 + q)); };

  const bool hh = lane >= 32;
  const unsigned hu = lane >> 5, blk = (lane >> 4) & 1, q4 = (lane >> 2) & 3, p4 = lane & 3, ta = (q4 >> 1) & 1, l31 = lane & 31;
  const unsigned perm = wr ^ (2 * GG);
  const unsigned rowoff = (8 * hu + q4) * 128 + (perm << 11);
  const unsigned pc = (4 * blk + p4) < 6 ? (4 * blk + p4) : 6;
  auto tileaddr = [&](unsigned t) AI -> unsigned {
    if constexpr (AT) { unsigned o = 56 * t + 8 * pc; return (o >> 7) * 8192 + ((((o >> 4) & 7) ^ (4 * ta)) << 4) + (o & 8) + rowoff; }
    else return blk * 8192 + (((2 * t + (p4 >> 1)) ^ (4 * blk + 2 * ta)) << 4) + 8 * (p4 & 1) + rowoff;
  };
  constexpr int NT = !AT ? 2 : GG ? 1 : 3, TB0 = GG ? 4 - NT : 0;
  unsigned aA[3], aB[NT];
  {
    unsigned o = (head & 1) * 8 + 8 * pc;
    unsigned ak = L_KV + (((o >> 4) ^ (4 * ta)) << 4) + (o & 8) + rowoff;
    aA[0] = LB + (GG ? L_GU + tileaddr(1) : ak);
    aA[1] = LB + (GG ? L_GU + tileaddr(2) : (ak ^ 64u));
    aA[2] = LB + L_GU + tileaddr(GG ? 3 : 0);
#pragma unroll
    for (int j = 0; j < NT; j++) aB[j] = LB + tileaddr(TB0 + j);
  }
  const unsigned xrow = 32 * wr + l31;
  const unsigned aX = LB + xrow * 128 + (((2 * perm + hu) ^ ((xrow >> 1) & 7)) << 4);
  const unsigned selv = (((lane >> 4) ^ (lane >> 2)) & 1) ? 0u : 0x3f803f80u;
  const u32x4 sel4 = {selv, selv, selv, selv};
  const bf16x8 sel = __builtin_bit_cast(bf16x8, sel4);

  bf16x8 xf[28], wa[2][3], wq[8];
  f32x16 accA[3], accB[NT];
  f32x4 csA[3], csB[NT];
#pragma unroll
  for (int j = 0; j < 3; j++) { for (int i = 0; i < 16; i++) accA[j][i] = 0.f; for (int i = 0; i < 4; i++) csA[j][i] = 0.f; }
#pragma unroll
  for (int j = 0; j < NT; j++) { for (int i = 0; i < 16; i++) accB[j][i] = 0.f; for (int i = 0; i < 4; i++) csB[j][i] = 0.f; }
  u32x2 grn;

  const rsrc_t rsWD = mkrs(pwd, INSZ(1792 * XROW)), rsWO = mkrs(pwo, INSZ(448 * XROW));
  const unsigned vW2 = (lane >> 3) * XROW + (((lane & 7) ^ ((lane >> 2) & 4)) << 4) + ci * 128;
  constexpr int NS = GG ? 16 + XS : 20 - XS, NC = 6 - XS / 2, NO = 2 * NC;
#define RHO ((rbl + ci) & 3)
  const unsigned rho = RHO;
  auto t_of = [](int i) constexpr { return (0x54763210u >> (4 * ((i >> 1) & 7))) & 7; };
  constexpr int NE = GG ? 9 : 5;
  auto g1i = [](int i) constexpr { return i < 8 ? i : i == 8 ? 16 : i < 17 ? i - 1 : 17; };
  auto ksb = [&](int i) AI -> unsigned {
    if (GG) i = g1i(i);
    if (GG) return i < 16 ? 16 * wr + ((t_of(i) & 3) ^ rho) : wr;
    if (i < NO) { const unsigned cc = i % NC + rho + (XS ? 0 : rho >> 1); return wr + 8 * (cc >= NC ? cc - NC : cc); }
    return 8 * wr + 2 * (((i - NO) >> 1) ^ rho);
  };
  auto ksi = [&](int i) AI -> unsigned { if (GG) i = g1i(i); return GG ? (i < 16 ? 8 * (i & 1) + (t_of(i) & 4) : 4 * i) : i < NO ? 64 + 4 * XS + 4 * (i / NC) : 112 + (i & 1); };
  auto issueW2 = [&](int i, int hf) AI {
    if (GG == 0 && i >= NO) dma<NT2>(lw2, ring_area(i % RING) + hf * 1024, vW2, rsWO, ksb(i) * 14 * XROW, (16 * (i & 1) + 8 * hf) * XROW);
    else dma<NT2>(lw2, ring_area(i % RING) + hf * 1024, vW2, rsWD, ksb(i) * 16 * XROW, (ksi(i) * 16 + 8 * hf) * XROW);
  };

  const unsigned aCSw = LB + L_CS + l31 * 16 + wr * 4 + GG * 2048, aCSr = LB + L_CS + l31 * 16 + GG * 2048;
  auto readX = [&](int n) AI { xf[n] = ldsr<bf16x8>(aX ^ (unsigned)((n & 3) << 5), ((n >> 2) & 3) * 16384); };
  auto readA = [&](int n, int j) AI {
    const unsigned o = ((n >> 2) & 3) * ((GG == 0 && j < 2) ? 8192 : 16384);
    wa[n & 1][j] = tr2(aA[j] ^ (unsigned)((n & 3) << 11), o, o + 512);
  };
  auto readB = [&](int q) AI {
    const int n = q / NT, j = q % NT;
    const unsigned base = lc1_base(n >> 2);
    if ((NT == 3 && n < XN) ? (n & 3) == 0 : n == 24) asm volatile("" : "+v"(aB[j]));
    wq[q & 7] = tr2(aB[j] ^ (((n & 3) << 11) | (base & 0x30000u)), base & 0xffffu, (base & 0xffffu) + 512);
  };
  RowC rc; unsigned nm, denb;
  auto mkxe = [&]() AI { asm volatile("" : "+v"(nm)); u32x4 t = {nm, nm, nm, nm}; return __builtin_bit_cast(bf16x8, t); };
  auto rowconst = [&]() AI {
#pragma nounroll
    for (int it = 0; it < (1 << 16) && !ALL(grn[1] == E + 1); ++it) {
      __builtin_amdgcn_s_sleep(SLEEPN);
      asm volatile("buffer_load_dwordx2 %0, %1, %2, %3 offen sc1\n\ts_waitcnt vmcnt(0)" : "=v"(grn) : "v"(xrow * 8), "s"(rsWS), "s"(WS_GR + rb * 1024) : "memory");
    }
    const float den = U2F(grn[0] & 0xffff0000u);
    nm = hh ? 0u : __builtin_amdgcn_perm(grn[0], grn[0], 0x01000100u);
    denb = grn[0] >> 16;
    rc.r = __builtin_amdgcn_rcpf(den);
    const float k1 = -2.f * 1.4426950408889634f * 0.7978845608028654f;
    rc.K1 = k1 * rc.r; rc.K2 = (k1 * 0.044715f) * rc.r * (rc.r * rc.r); rc.Ri = den * den; rc.Lr = 1.4426950408889634f * rc.r;
  };
  waitv<7 + COOP>();
  BAR(); MEMFENCE();
  STAMP(1);
  readX(0); readA(0, 0); readA(0, 1); readA(0, 2);
  SFOR(n, 28)
    SB();
    if constexpr (n == 27) {
#pragma unroll
      for (int j = 0; j < 3; j++) ldsw<unsigned>(aCSw, j * 512, hilo(csA[j][0]));
    }
    if constexpr ((n & 3) == 3) {
      if constexpr (n == 27) WAITV(0);
      else if constexpr (n == 23) asm volatile("s_waitcnt vmcnt(10)" : "+v"(grn) :: "memory");
      else if constexpr (n == 3 || n == 15 || n == 19) WAITV(11); else WAITV(10);
      { WAITL0(); BAR(); MEMFENCE(); }
      if constexpr (n == 27) STAMP(2);
    }
    constexpr int bar = (n + 1) >> 2, ph = (n + 1) & 3;
#pragma unroll
    for (int j = 0; j < 3; j++) {
      if constexpr (n < 27) { if (j == 0) readX(n + 1); readA(n + 1, j); }
      else { wa[0][j] = ldsr<bf16x8>(aCSr, j * 512); if (AT && GG == 1 && j < 2) { readB(2 * j); readB(2 * j + 1); } }
      SB();
      accA[j] = MFMA(wa[n & 1][j], xf[n], accA[j]);
      const int slot = 3 * ph + j;
      if (bar == 0) issueG(0, slot - 2);
      else if (bar == 6) { if (slot < 5) issueG(bar, slot); }
      else if (bar <= 6) { if (slot == 0) issueG(bar, 0); if (slot == 2) issueG(bar, 1); if (slot == 5) issueG(bar, 2); if (slot == 7) issueG(bar, 3); if (slot == 10) issueG(bar, 4); }
      else if (j < 2) {
        if constexpr (AT) dma(lw2, L_FREE + j * 1024, lane * 16, mkrs(pphi, 16 * 1568), half * 8 * 1568 + w * 2048, j * 1024);
        else issueW2(0, j);
      }
      if (!NOCS && (n & 3) == 0) csA[j] = MFMA16(sel, wa[n & 1][j], csA[j]);
      if (n == 25 && j == 0) rowconst();
      if (n == 14 && j == 2) asm volatile("buffer_load_dwordx2 %0, %1, %2, %3 offen sc1" : "=v"(grn) : "v"(xrow * 8), "s"(rsWS), "s"(WS_GR + rb * 1024) : "memory");
      SB();
    }
  SEND
  { const bf16x8 xe = mkxe();
#pragma unroll
  for (int j = 0; j < 3; j++) accA[j] = MFMA(wa[0][j], xe, accA[j]); }
  asm volatile("" : "+v"(accA[0]), "+v"(accA[1]), "+v"(accA[2]));
  SB();

  const unsigned sFown = WS_FH + (rt * 32 + (AT ? 28 + 2 * half : 2 * c) + GG) * FS;
  const unsigned aWP = LB + (4 * hu + q4) * 56 + 8 * pc;
  const unsigned T12 = (E + 1) & 0xfffu, T16 = (E + 1) & 0xffffu;
  const unsigned sF = WS_FH + rt2 * 32 * FS;
  const unsigned fc = l31 >> 1;
  const bool nd1 = GG ? (fc >> 1) == wr || (fc >= 8 && fc < 8 + XS / 2) : (fc >= 8 + XS / 2 && fc < 14) || fc == 14 + (wr >> 1);
  const unsigned aIW = LB + L_PHI + xrow * 64 + ((hu ^ ((xrow >> 1) & 7)) << 3);
  SmS sm;
  unsigned aWK = aWP + L_WPK, aWQ = aWP + L_FREE;
  asm volatile("" : "+v"(aWK)); if constexpr (AT) asm volatile("" : "+v"(aWQ));
  auto kstep = [&](auto s_) AI {
    constexpr int s = decltype(s_)::value;
    if constexpr (s == 0) { sm.w0 = tr2(aWK, 0, 8 * 56); sm.w1 = tr2(aWK, 16 * 56, 24 * 56); }
    if constexpr (s == 1) sm.p = featproj2(accA[0], sm.w0, sm.w1, hh);
    if constexpr (s == 2 || s == 3) {
#pragma unroll
      for (int k = 2 * s - 4; k < 2 * s - 2; k++) {
        u32x2 vv = {pk2(accA[1][4 * k], accA[1][4 * k + 1]), pk2(accA[1][4 * k + 2], accA[1][4 * k + 3])};
        if (k == 3) vv[0] = hh ? denb : vv[0];
        ldsw<u32x2>(aIW ^ (unsigned)(k << 4), L_V - L_PHI, vv);
      }
    }
    if constexpr (s == 4) sm.bx = -max16(sm.p) * rc.Lr;
    if constexpr (s == 5) sm.bn = min16(sm.p) * rc.Lr;
    if constexpr (s >= 6 && s < 22) {
      constexpr int i = s - 6;
      sm.e[i] = __builtin_amdgcn_exp2f(__builtin_fmaf(sm.p[i], rc.Lr, sm.bx)); sm.en[i] = __builtin_amdgcn_exp2f(__builtin_fmaf(sm.p[i], -rc.Lr, sm.bn));
      if constexpr (i == 0) { sm.sp = sm.e[0]; sm.sn = sm.en[0]; } else if constexpr (i < 12) { sm.sp += sm.e[i]; sm.sn += sm.en[i]; }
      else if constexpr (i == 12) { sm.tp = sm.e[12]; sm.tn = sm.en[12]; } else { sm.tp += sm.e[i]; sm.tn += sm.en[i]; }
    }
    if constexpr (s == 22) { sm.sp = __builtin_amdgcn_rcpf(xsum(sm.sp + (hh ? 0.f : sm.tp))) * rc.r; sm.sn = __builtin_amdgcn_rcpf(xsum(sm.sn + (hh ? 0.f : sm.tn))) * rc.r; }
    if constexpr (s >= 23) {
#pragma unroll
      for (int u = (s == 29 ? 6 : s - 23); u <= (s == 29 ? 7 : s - 23); u++) {
        const int k = u >> 1;
        if (u & 1) { u32x2 t = {pk2s(sm.en[4 * k] * sm.sn, sm.en[4 * k + 1] * sm.sn), pk2s(sm.en[4 * k + 2] * sm.sn, sm.en[4 * k + 3] * sm.sn)}; ldsw<u32x2>(aIW ^ (unsigned)(k << 4), 8192, t); }
        else { u32x2 t = {pk2s(sm.e[4 * k] * sm.sp, sm.e[4 * k + 1] * sm.sp), pk2s(sm.e[4 * k + 2] * sm.sp, sm.e[4 * k + 3] * sm.sp)}; ldsw<u32x2>(aIW ^ (unsigned)(k << 4), 0, t); }
      }
    }
  };
  auto qstep = [&](auto s_, f32x16& a, unsigned lh) AI {
    constexpr int s = decltype(s_)::value;
    if constexpr (s == 0) { sm.w0 = tr2(aWQ, lh * 1568, lh * 1568 + 8 * 56); sm.w1 = tr2(aWQ, lh * 1568 + 16 * 56, lh * 1568 + 24 * 56); }
    if constexpr (s == 1) sm.p = featproj2(a, sm.w0, sm.w1, hh);
    if constexpr (s == 3) sm.bx = -max16(sm.p) * rc.Lr;
    if constexpr (s == 4) sm.bn = min16(sm.p) * rc.Lr;
    if constexpr (s >= 5 && s < 21) {
      constexpr int i = (s - 5) >> 1;
      float t0, t1;
      if constexpr ((s - 5) & 1) { t0 = __builtin_fmaf(sm.p[2 * i], -rc.Lr, sm.bn); t1 = __builtin_fmaf(sm.p[2 * i + 1], -rc.Lr, sm.bn); }
      else { t0 = __builtin_fmaf(sm.p[2 * i], rc.Lr, sm.bx); t1 = __builtin_fmaf(sm.p[2 * i + 1], rc.Lr, sm.bx); }
      a[8 * ((s - 5) & 1) + i] = U2F(pk2(__builtin_amdgcn_exp2f(t0), __builtin_amdgcn_exp2f(t1)));
      if constexpr (s == 20) asm volatile("" : "+v"(a));
    }
  };
  bf16x8 ka_[4], kb_[2][4]; f32x4 kd[2];
  auto kvstep = [&](auto s_) AI {
    constexpr int s = decltype(s_)::value;
    if constexpr (s == 0) {
      const unsigned g4 = lane >> 4, r0 = 8 * g4 + q4, sw = 4 * blk + (q4 >> 1);
      unsigned b0 = LB + L_PHI + r0 * 64 + ((p4 ^ sw) << 3);
      unsigned a0 = (b0 ^ ((wr >> 1) << 5)) + (wr & 1) * 8192;
#pragma unroll
      for (int k = 0; k < 4; k++) {
        ka_[k] = __builtin_shufflevector(trd2(a0, k * 2048), trd2(a0 ^ 16u, k * 2048 + 256), 0, 1, 2, 3, 4, 5, 6, 7);
        kb_[0][k] = __builtin_shufflevector(trd2(b0, L_V - L_PHI + k * 2048), trd2(b0 ^ 16u, L_V - L_PHI + k * 2048 + 256), 0, 1, 2, 3, 4, 5, 6, 7);
        kb_[1][k] = __builtin_shufflevector(trd2(b0 ^ 32u, L_V - L_PHI + k * 2048), trd2(b0 ^ 48u, L_V - L_PHI + k * 2048 + 256), 0, 1, 2, 3, 4, 5, 6, 7);
      }
    }
    if constexpr (s == 1) {
      f32x4 z = {0.f, 0.f, 0.f, 0.f}; kd[0] = z; kd[1] = z;
#pragma unroll
      for (int k = 0; k < 4; k++) { kd[0] = MFMA16(ka_[k], kb_[0][k], kd[0]); kd[1] = MFMA16(ka_[k], kb_[1][k], kd[1]); }
    }
    if constexpr (s == 3) {
#pragma unroll
      for (int d = 0; d < 2; d++) {
        u32x4 t;
#pragma unroll
        for (int i = 0; i < 4; i++) t[i] = (F2U(kd[d][i]) & ~0xfffu) | T12;
        st16(t, rsWS, (lane >> 4) * 512 + (lane & 15) * 16, ((head * 16 + rb) * 2 + (wr & 1)) * 4096 + (wr >> 1) * 2048, WS_KVP + d * 256);
      }
    }
  };
  auto qexp2 = [&](f32x16& a0, unsigned l0, f32x16& a1, unsigned l1) AI {
    const f32x16 p0 = featproj(a0, aWQ, l0 * 1568, hh), p1 = featproj(a1, aWQ, l1 * 1568, hh);
    a0 = dualexpP(p0, rc.Lr); a1 = dualexpP(p1, rc.Lr); asm volatile("" : "+v"(a0), "+v"(a1));
  };

  unsigned fl1 = 0;
  EpiS es;
  u32x4 pv[8];
  const unsigned vKP = l31 * 16 + hu * 8192, sKP = (rb * 32 + half) * 4096 + wr * 1024 + GG * 512, sKR = (8 * half + w) * 8192;
  auto hop1 = [&]() AI {
    waittag<16384>(pv, rsWS, vKP, sKP + WS_KVP, T12, 0xfffu);
    u32x4 o;
#pragma unroll
    for (int k = 0; k < 4; k++) {
      float sum = U2F(pv[0][k]);
#pragma unroll
      for (int i = 1; i < 8; i++) sum += U2F(pv[i][k]);
      sum = xsum(sum);
      sum = l31 == 29 ? 1.f : sum;
      if (GG == 1) sum = wr == 3 ? 0.f : sum;
      o[k] = (pk2s(sum, sum) & 0xffff0000u) | T16;
    }
    st16(o, rsWS, hh ? 0x80000000u : l31 * 16, (rb * 2 + half) * 4096 + wr * 1024 + GG * 512, WS_KVR);
    if (b == 0 && w == 4) stflag(E + 4, rsWS, WS_EPOCH);
    SB();
  };
  bf16x8 cb[NT];
  auto ldflag = [&]() AI { asm volatile("buffer_load_dword %0, %1, %2, %3 offen sc1" : "=v"(fl1) : "v"(nd1 ? lane * FS : 0x80000000u), "s"(rsWS), "s"(sF) : "memory"); };
  auto touch = [&](unsigned wo4) AI {
    const unsigned rw = (rbl * 8 + w + wo4) * 44 + lane;
    const bool dn = rw < 896;
    const unsigned vo = lane < 44 ? (dn ? ((rw >> 6) * 128 + 64 + (rw & 63)) : rw - 896) * XROW + ci * 128 : 0x80000000u;
    asm volatile("s_add_u32 m0, %0, %c5\n\ts_nop 0\n\tbuffer_load_dword %1, %3, 0 offen lds\n\tbuffer_load_dword %2, %4, 0 offen lds"
                 :: "s"(LB + w * 256), "v"(dn ? vo : 0x80000000u), "v"(dn ? 0x80000000u : vo), "s"(rsWD), "s"(rsWO), "i"(L_ST) : "memory", "scc");
  };
  auto kpath = [&]() AI {
    const f32x16 p = featproj(accA[0], aWK, 0, hh);
#pragma unroll
    for (int k = 0; k < 4; k++) {
      u32x2 vv = {pk2(accA[1][4 * k], accA[1][4 * k + 1]), pk2(accA[1][4 * k + 2], accA[1][4 * k + 3])};
      if (k == 3) vv[0] = hh ? denb : vv[0];
      ldsw<u32x2>(aIW ^ (unsigned)(k << 4), L_V - L_PHI, vv);
    }
    const f32x2 bx = dup2(-max16(p) * rc.Lr), bn = dup2(min16(p) * rc.Lr), L2 = dup2(rc.Lr);
    f32x2 ep[8], en[8];
#pragma unroll
    for (int i = 0; i < 8; i++) { ep[i] = exp2v(fma2(sub2(p, i), L2, bx)); en[i] = exp2v(fma2(sub2(p, i), -L2, bn)); }
    f32x2 sp = ((ep[0] + ep[1]) + (ep[2] + ep[3])) + (ep[4] + ep[5]), sn = ((en[0] + en[1]) + (en[2] + en[3])) + (en[4] + en[5]);
    f32x2 tp = ep[6] + ep[7], tn = en[6] + en[7];
    float a0 = sp[0], a1 = sn[0], a2 = tp[0], a3 = tn[0];
    asm("" : "+v"(a0), "+v"(a1), "+v"(a2), "+v"(a3));
    a0 += sp[1]; a2 += tp[1]; asm("" : "+v"(a0), "+v"(a2)); a1 += sn[1]; a3 += tn[1];
    const f32x2 ip = dup2(__builtin_amdgcn_rcpf(xsum(a0 + (hh ? 0.f : a2))) * rc.r), in = dup2(__builtin_amdgcn_rcpf(xsum(a1 + (hh ? 0.f : a3))) * rc.r);
#pragma unroll
    for (int k = 0; k < 4; k++) {
      const f32x2 u0 = ep[2 * k] * ip, u1 = ep[2 * k + 1] * ip, v0 = en[2 * k] * in, v1 = en[2 * k + 1] * in;
      u32x2 t0 = {pk2(u0[0], u0[1]), pk2(u1[0], u1[1])}, t1 = {pk2(v0[0], v0[1]), pk2(v1[0], v1[1])};
      ldsw<u32x2>(aIW ^ (unsigned)(k << 4), 0, t0); ldsw<u32x2>(aIW ^ (unsigned)(k << 4), 8192, t1);
    }
  };
  const unsigned sA = WS_A2 + rt2 * 144 * 1024;
  u32x4 af[NS][2];
  auto loadA1 = [&](int i, int m) AI { af[i][m] = ld16(rsWS, vL16, sA + ksb(i) * 1024, (144 * m + ksi(i)) * 1024); };
  auto stream = [&]() AI {
    SFOR(q, 28 * NT)
      constexpr int n = q / NT, j = q % NT;
      if constexpr (j == 0) {
        if constexpr (n == 27) {
#pragma unroll
          for (int t = 0; t < NT; t++) ldsw<unsigned>(aCSw, ((3 + t) & 3) * 512, hilo(csB[t][0]));
          if constexpr (AT && GG) WAITV(8);
          WAITL0(); BAR(); MEMFENCE();
        }
        else if constexpr (!AT && (n & 3) == 3) { asm volatile("s_waitcnt lgkmcnt(4)" ::: "memory"); BAR(); MEMFENCE(); }
      }
      if constexpr (q + 4 < 28 * NT) readB(q + 4);
      if constexpr (n == 27) cb[j] = ldsr<bf16x8>(aCSr, ((3 + j) & 3) * 512);
      SB();
      accB[j] = MFMA(wq[q & 7], xf[n], accB[j]);
      if constexpr (!NOCS && (n & 3) == 1) csB[j] = MFMA16(sel, wq[q & 7], csB[j]);
      if constexpr (!AT) {
        if constexpr (j == 0 && n >= 3 && n < 7) issueW2(1 + ((n - 3) >> 1), (n - 3) & 1);
        if constexpr (j == 0 && n >= 7 && n < 27 && (n & 1)) issueW2(1 + ((n + 1) >> 2), ((n + 1) >> 1) & 1);
        if constexpr (n == 27) issueW2(8, j);
        if constexpr (j == 1 && n == F1N) { waitv<(F1N < 7 ? F1N - 2 : 4 + (F1N - 5) / 2) + (TN < F1N && (TMODE == 0 || (TMODE == 3 && GG == 0)) ? 2 : 0)>(); stflag(E + 1, rsWS, sFown); }
        if constexpr (j == 1 && n == TN && (TMODE == 0 || (TMODE == 3 && GG == 0))) touch(0);
        if constexpr (q == 2 * CK1 - 7) ldflag();
        if constexpr (q == 2 * CK1 - 1) {
          asm volatile("s_waitcnt vmcnt(%c5)" : "+v"(fl1), "+v"(accB[0]), "+v"(accB[NT - 1]), "+v"(csB[0]), "+v"(csB[NT - 1]) : "i"((CK1 & 1) ? 1 : 2) : "memory");
          const bool nde = nd1 && fc < 14;
          if (!ALL(!nde || (fl1 - E - 1u) <= 1u)) poll(rsWS, lane * FS, sF, E, nde ? 1u : 0u);
        }
        if constexpr (q >= 2 * CK1 && q < 2 * CK1 + 2 * NE) loadA1((q - 2 * CK1) >> 1, q & 1);
      } else {
        if constexpr (GG == 1 && n < 16 && (n & 1) == 0) clri(n >> 1);
        if constexpr (GG == 0 && j < 2 && n >= 1 && 2 * (n - 1) + j < 21) qstep(IntC<2 * (n - 1) + j>{}, accA[2], 0);
        if constexpr (GG == 0 && q >= 3 * H0L && q < 3 * H0L + 8) pv[q - 3 * H0L] = ld16(rsWS, vKP, sKP, WS_KVP + (q - 3 * H0L) * 16384);
        if constexpr (GG == 0 && j == 2 && n == H0C) { asm volatile("" : "+v"(accB[0]), "+v"(accB[1]), "+v"(accB[NT - 1]), "+v"(csB[0]), "+v"(csB[1]), "+v"(csB[NT - 1])); STAMP(11); hop1(); STAMP(6); }
      }
      SB();
    SEND
    const bf16x8 xe = mkxe();
#pragma unroll
    for (int t = 0; t < NT; t++) { accB[t] = MFMA(cb[t], xe, accB[t]); asm volatile("" : "+v"(accB[t])); }
    SB();
  };
  unsigned ln = vL16, aW = 0;
  auto mkaw = [&]() AI {
    asm volatile("" : "+v"(ln));
    aW = lw2 + (ln & 0x200) + ((ln & 0xc0) << 1) + ((((ln >> 3) & 0x20) | ((ln >> 1) & 0x10)) ^ ((ln >> 1) & 0x40)) + ((ln >> 1) & 8);
  };
  bf16x8 wf[NS][2];
  auto readW = [&](int i) AI {
    const unsigned ra = ring_area(i % RING), la = aW + (ra & 0x30000u);
    wf[i][0] = tr2(la, ra & 0xffffu, (ra & 0xffffu) + 1024); wf[i][1] = tr2(la ^ 64u, ra & 0xffffu, (ra & 0xffffu) + 1024);
  };
  auto tile = [&](f32x16& a, int t) AI { asm volatile("" : "+v"(a)); st16(epiP(a, rc), rsWS, vL16, sA2, t * 1024); SB(); };
  if constexpr (!AT) {
    if constexpr (GG == 0) {
      if (TMODE == 2) touch(0);
      kpath(); WAITL0(); SB(); BAR(); SB(); STAMP(3);
      kvstep(IntC<0>{}); kvstep(IntC<1>{}); kvstep(IntC<3>{}); SB();
      if (TMODE == 1) touch(0);
      tile(accA[2], 0);
    } else { if (TMODE >= 1 && TMODE <= 3) touch(0); tile(accA[0], 1); if (T2B) tile(accA[1], 2); BAR(); SB(); STAMP(3); if (!T2B) tile(accA[1], 2); tile(accA[2], 3); }
    readB(0); readB(1); readB(2); readB(3);
    STAMP(4);
    stream();
    SFOR(u, (2 * CK1 + 2 * NE > 56 ? 2 * CK1 + 2 * NE - 56 : 0)) loadA1((56 + u - 2 * CK1) >> 1, u & 1); SEND
    STAMP(5);
    mkaw();
    SFOR(i, RC) readW(i); SEND
    if constexpr (RC > 0) { WAITL0(); SB(); SFOR(i, RC) issueW2(9 + i, 0); issueW2(9 + i, 1); SEND }
    tile(accB[0], 4 + 2 * GG); tile(accB[NT - 1], 5 + 2 * GG);
  } else if constexpr (GG == 0) {
    SFOR(q, 8) clri(q); SEND
    SFOR(s, 30) kstep(s_); SEND
    WAITV(8);
    WAITL0(); SB(); BAR(); SB();
    STAMP(3);
    kvstep(IntC<0>{});
    readB(0); readB(1); readB(2); readB(3);
    kvstep(IntC<1>{}); kvstep(IntC<3>{});
    WAITV(2); stflag(E + 1, rsWS, sFown);
    STAMP(4);
    SB();
    stream();
  } else {
    stream();
    STAMP(3);
    SFOR(s, 21) qstep(s_, accA[0], 1); SEND
    WAITV(0); stflag(E + 1, rsWS, sFown);
    SFOR(s, 21) qstep(s_, accA[1], 2); SEND
    SFOR(s, 21)
      if constexpr (s < 8) pv[s] = ld16(rsWS, vKP, sKP, WS_KVP + s * 16384);
      qstep(s_, accA[2], 3);
    SEND
    STAMP(4); hop1(); STAMP(6);
    SB(); BAR(); SB();
  }

  if constexpr (AT) {
    STAMP(5);
    auto qexp = [&](f32x16& a, unsigned lh) AI { a = dualexpP(featproj(a, aWQ, lh * 1568, hh), rc.Lr); asm volatile("" : "+v"(a)); SB(); };
    if constexpr (GG == 0) qexp(accB[0], 4);
#pragma unroll
    for (int i = 0; i < 8; i++) pv[i] = ld16(rsWS, vL16, sKR, WS_KVR + i * 1024);
    if constexpr (GG == 0) { qexp(accB[1 % NT], 5); qexp(accB[NT - 1], 6); } else qexp(accB[0], 7);
    STAMP(7);
    waittag<1024>(pv, rsWS, vL16, sKR + WS_KVR, T16, 0xffffu);
    STAMP(8);
#pragma unroll
    for (int i = 0; i < 4; i++) {
      u32x4 f = {__builtin_amdgcn_perm(pv[2 * i][1], pv[2 * i][0], 0x07060302u), __builtin_amdgcn_perm(pv[2 * i][3], pv[2 * i][2], 0x07060302u),
                 __builtin_amdgcn_perm(pv[2 * i + 1][1], pv[2 * i + 1][0], 0x07060302u), __builtin_amdgcn_perm(pv[2 * i + 1][3], pv[2 * i + 1][2], 0x07060302u)};
      ldsw<u32x4>(LB + w * 4096 + vL16, i * 1024, f);
    }
    WAITL0(); BAR(); MEMFENCE();
    STAMP(10);
    f32x16 op[4], on[4];
    auto omm = [&](const f32x16& phi, unsigned lh, int z) AI {
#pragma unroll
      for (int i = 0; i < 16; i++) { op[z][i] = 0.f; on[z][i] = 0.f; }
      bf16x8 kf[4];
#pragma unroll
      for (int i = 0; i < 4; i++) kf[i] = ldsr<bf16x8>(LB + vL16, lh * 4096 + i * 1024);
#pragma unroll
      for (int i = 0; i < 2; i++) { op[z] = MFMA(kf[i], sub4(phi, i), op[z]); on[z] = MFMA(kf[2 + i], sub4(phi, 2 + i), on[z]); }
    };
    auto ofin = [&](unsigned lh, int z) AI {
      const float ia = __builtin_amdgcn_rcpf(op[z][13]), ib = __builtin_amdgcn_rcpf(on[z][13]);
      const float id = __builtin_amdgcn_rcpf(__builtin_fmaf(op[z][12], ia, __builtin_fmaf(on[z][12], ib, 1e-6f)));
      const float ca = xhi(ia * id), cb2 = xhi(ib * id);
      const f32x2 c2 = dup2(ca), d2 = dup2(cb2);
      u32x4 f0, f1;
#pragma unroll
      for (int i = 0; i < 4; i++) {
        const f32x2 u = fma2(sub2(op[z], i), c2, sub2(on[z], i) * d2), v = fma2(sub2(op[z], 4 + i), c2, sub2(on[z], 4 + i) * d2);
        f0[i] = pk2(u[0], u[1]); f1[i] = pk2(v[0], v[1]);
      }
      f1[2] = hh ? 0u : f1[2]; f1[3] = hh ? 0u : f1[3];
      st16(f0, rsWS, OV, sA2, (112 + 2 * lh) * 1024); st16(f1, rsWS, OV, sA2, (113 + 2 * lh) * 1024);
      SB();
    };
    if constexpr (GG) { omm(accA[0], 1, 0); omm(accA[1], 2, 1); SB(); ofin(1, 0); omm(accA[2], 3, 2); omm(accB[0], 7, 3); SB(); ofin(2, 1); ofin(3, 2); ofin(7, 3); }
    else { omm(accA[2], 0, 0); omm(accB[0], 4, 1); SB(); ofin(0, 0); omm(accB[1 % NT], 5, 2); omm(accB[NT - 1], 6, 3); SB(); ofin(4, 1); ofin(5, 2); ofin(6, 3); }
    STAMP(9);
  } else {
    f32x16 acc[2][2];
#pragma unroll
    for (int i = 0; i < 4; i++)
#pragma unroll
      for (int k = 0; k < 16; k++) acc[i >> 1][i & 1][k] = 0.f;
    u32x4 xr;
    const unsigned vO = (ln >> 7) * XROW + (ln & 0x70), sO = (rt2 * 32 + 8 * w) * XROW + ci * 128;
    auto mm4 = [&](int i) AI {
#pragma unroll
      for (int nn = 0; nn < 2; nn++)
#pragma unroll
        for (int mm = 0; mm < 2; mm++) acc[nn][mm] = MFMA(wf[i][nn], __builtin_bit_cast(bf16x8, af[i][mm]), acc[nn][mm]);
      SB();
    };
    STAMP(6);
    waitv<2 + 2 * RC>();
#pragma unroll
    for (int i = 0; i < NE; i++) asm volatile("" : "+v"(af[i][0]), "+v"(af[i][1]));
    SFOR(i, 9 - RC) readW(RC + i); SEND
    SB();
    SFOR(i, EB) mm4(i); SEND
    WAITV(0); stflag(E + 2, rsWS, sFown);
    WAITL0(); SB();
    BAR();
    SB(); STAMP(11);
    if constexpr (GG) __builtin_amdgcn_s_setprio(P2G1);
    constexpr int NB = (GG ? 9 : G0B) - RC;
    SFOR(i, 9)
      if constexpr (i == NB - 2) ldflag();
      if constexpr (i < NB) { issueW2(9 + RC + i, 0); issueW2(9 + RC + i, 1); SB(); }
      if constexpr (EB + i < NE) mm4(EB + i);
    SEND
    asm volatile("s_waitcnt vmcnt(4)" : "+v"(fl1) :: "memory");
    { const unsigned need = !nd1 ? 0u : fc < 14 ? 2u : 1u;
      if (!ALL(!nd1 || fl1 == E + need)) poll(rsWS, lane * FS, sF, E, need); }
    STAMP(7);
#pragma unroll
    for (int j = NE; j < NE + PD; j++) { loadA1(j, 0); loadA1(j, 1); }
    asm volatile("buffer_load_dwordx4 %0, %1, %2, %3 offen" : "=v"(xr) : "v"(vO), "s"(rsX), "s"(sO) : "memory");
    SFOR(i, 9 - RC - NB) issueW2(9 + RC + NB + i, 0); issueW2(9 + RC + NB + i, 1); SEND
    SFOR(i1, NS - NE)
      constexpr int i = NE + i1;
      asm volatile("s_waitcnt vmcnt(%c2)" : "+v"(af[i][0]), "+v"(af[i][1]) : "i"(2 * ((i + PD <= NS ? i + PD : NS) - 1 - i) + (i1 < PD && i < NO + 8 * GG ? 1 + 2 * (9 - RC - NB) : 0)) : "memory");
      if constexpr (i == 9 && i1 == 0) readW(9);
      if constexpr (i + 1 >= 9 && i + 1 < NS) readW(i + 1);
      if constexpr (GG == 0 && i >= NO) {
        auto stale = [&]() AI {
          unsigned m = UMAX(UMAX(UMAX(af[i][0][0], af[i][0][1]), UMAX(af[i][0][2], af[i][0][3])), UMAX(UMAX(af[i][1][0], af[i][1][1]), UMAX(af[i][1][2], af[i][1][3])));
          return ANY(m == ~0u);
        };
        if (__builtin_expect(stale(), 0)) {
          int it = 0;
#pragma nounroll
          do { __builtin_amdgcn_s_sleep(SLEEPR); rel1<true>(af[i][0], af[i][1], rsWS, vL16, sA + (ksb(i) + ksi(i)) * 1024); } while (++it < (1 << 14) && stale());
          SFOR(u, PD - 1) if constexpr (i + 1 + u < NS) rel1<false>(af[i + 1 + u][0], af[i + 1 + u][1], rsWS, vL16, sA + (ksb(i + 1 + u) + ksi(i + 1 + u)) * 1024); SEND
        }
      }
      if constexpr (i == 10) STAMP(8);
      if constexpr (i == 14) STAMP(9);
      SB();
      if constexpr (i + PD < NS) { loadA1(i + PD, 0); loadA1(i + PD, 1); }
      mm4(i);
    SEND
    STAMP(10);
    __builtin_amdgcn_s_setprio(0);
#pragma unroll
    for (int qd = 0; qd < 4; qd++)
#pragma unroll
      for (int k = 0; k < 4; k++) {
        const unsigned ra = ring_area(2 * qd + (k >> 1)) + (k & 1) * 1024;
        ldsw<bf16x8>(((lw2 + vL16) ^ (unsigned)(32 * (4 * (qd >> 1) + k))) + (ra & 0x30000u), ra & 0xffffu, sub4(acc[qd >> 1][qd & 1], k));
      }
    WAITL0(); BAR(); MEMFENCE();
    STAMP(12);
    const unsigned c8 = (ln >> 4) & 7, j2 = (c8 & 4) + 2 * GG + ((c8 >> 1) & 1);
    const unsigned rd = LB + ((0x51407362u >> (4 * j2)) & 7) * 16384 + (c8 & 1) * 1024 + (((8 * wr + (ln >> 7)) << 4) ^ (c8 << 5));
    asm volatile("" : "+v"(xr));
    f32x4 sum[2];
#pragma unroll
    for (int k = 0; k < 2; k++) {
      f32x4 t = {U2F(xr[2 * k] << 16), U2F(xr[2 * k] & 0xffff0000u), U2F(xr[2 * k + 1] << 16), U2F(xr[2 * k + 1] & 0xffff0000u)};
#pragma unroll
      for (int v = 0; v < 8; v++) t += ldsr<f32x4>(rd, v * 2048 + k * 512);
      sum[k] = t;
    }
    u32x4 o = {pk2(sum[0][0], sum[0][1]), pk2(sum[0][2], sum[0][3]), pk2(sum[1][0], sum[1][1]), pk2(sum[1][2], sum[1][3])};
    __builtin_amdgcn_raw_buffer_store_b128(o, mkrs(pout, 2048 * XROW), vO + sO, 0, OAUX);
  }
  STAMPS_OUT();
  };
  if (!attn && g) prog(BoolC<false>{}, IntC<1>{});
  else if (!attn) prog(BoolC<false>{}, IntC<0>{});
  else if (g) prog(BoolC<true>{}, IntC<1>{});
  else prog(BoolC<true>{}, IntC<0>{});
}

extern "C" void kernel_launch(void* const* d_in, const int* in_sizes, int n_in, void* d_out, int out_size, void* d_ws, size_t ws_size,
                              hipStream_t stream) {
  fused_kernel<<<256, 512, 0, stream>>>((const char*)d_in[0], (const char*)d_in[1], (const char*)d_in[5], (const char*)d_in[6], (char*)d_ws,
                                        (const char*)d_in[2], (const char*)d_in[7], (const char*)d_in[3], (char*)d_out);
}
```

```cpp
#define CTN 16
#include <hip/hip_runtime.h>
#include <hip/hip_bf16.h>
#include <utility>

#define DI __device__ __forceinline__
#define LAS __attribute__((address_space(3)))
typedef __attribute__((ext_vector_type(8))) short bf16x8;
typedef __attribute__((ext_vector_type(4))) short s16x4;
typedef __attribute__((ext_vector_type(2))) __bf16 bf2;
typedef __attribute__((ext_vector_type(2))) float f32x2;
typedef __attribute__((ext_vector_type(4))) float f32x4;
typedef __attribute__((ext_vector_type(16))) float f32x16;
typedef __attribute__((ext_vector_type(2))) unsigned u32x2;
typedef __attribute__((ext_vector_type(4))) unsigned u32x4;
typedef __amdgpu_buffer_rsrc_t rsrc_t;
#define F2U(x) __builtin_bit_cast(unsigned, (float)(x))
#define U2F(x) __builtin_bit_cast(float, (unsigned)(x))
#define ALL(x) (__builtin_amdgcn_ballot_w64(!(x)) == 0ull)
#define ANY(x) (__builtin_amdgcn_ballot_w64(x) != 0ull)
#define UMAX(a, b) __builtin_elementwise_max((unsigned)(a), (unsigned)(b))

#define NTX 0
#define NTW 0
#define NT2 0
#define XS 2
#define MAP2 1
constexpr unsigned XROW = 896, QROW = 2688, FROW = 3584;
constexpr unsigned FS = 128;
constexpr unsigned WS_EPOCH = 0;
constexpr unsigned WS_GR = 4096;
constexpr unsigned WS_KVP = 131072;
constexpr unsigned WS_A2 = WS_KVP + 16 * 16 * 8192;
constexpr unsigned WS_ST = WS_A2 + 64 * 144 * 1024;
constexpr unsigned WS_FH = WS_ST + 262144;
constexpr unsigned WS_KVR = WS_FH + 2048 * FS;
constexpr unsigned WS_END = WS_KVR + 16 * 8192;
constexpr unsigned L_GU = 65536, L_KV = 131072;
constexpr unsigned L_TAIL = L_KV + 3 * 8192;
constexpr unsigned L_WPK = L_TAIL;
constexpr unsigned L_ST = L_TAIL + 2048;
constexpr unsigned L_CS = L_TAIL + 4096;
constexpr unsigned L_PHI = L_GU + 32768;
constexpr unsigned L_V = L_KV + 16384;
constexpr unsigned L_FREE = 32768;

#define MFMA(a, b, c) __builtin_amdgcn_mfma_f32_32x32x16_bf16((a), (b), (c), 0, 0, 0)
#define MFMA16(a, b, c) __builtin_amdgcn_mfma_f32_16x16x32_bf16((a), (b), (c), 0, 0, 0)
#define AI __attribute__((always_inline))
#define BAR() __builtin_amdgcn_s_barrier()
#define WAITV(n) asm volatile("s_waitcnt vmcnt(" #n ")" ::: "memory")
#define WAITL0() asm volatile("s_waitcnt lgkmcnt(0)" ::: "memory")
#define SB() __builtin_amdgcn_sched_barrier(0)
#define MEMFENCE() asm volatile("" ::: "memory")

template <int N> DI void waitv() { asm volatile("s_waitcnt vmcnt(%c0)" :: "i"(N) : "memory"); }

#define INSZ(x) (x)
DI rsrc_t mkrs(const void* p, unsigned bytes) { return __builtin_amdgcn_make_buffer_rsrc((void*)p, (short)0, (int)bytes, 0x00020000); }
template <bool NT = false> DI void dma(unsigned lds, unsigned limm, unsigned voff, rsrc_t rs, unsigned soff, unsigned simm) {
  unsigned t;
  if constexpr (NT) asm volatile("s_add_u32 m0, %1, %c5\n\ts_add_u32 %0, %4, %c6\n\tbuffer_load_dwordx4 %2, %3, %0 offen nt lds"
               : "=&s"(t) : "s"(lds), "v"(voff), "s"(rs), "s"(soff), "i"(limm), "i"(simm) : "memory", "scc");
  else asm volatile("s_add_u32 m0, %1, %c5\n\ts_add_u32 %0, %4, %c6\n\tbuffer_load_dwordx4 %2, %3, %0 offen lds"
               : "=&s"(t) : "s"(lds), "v"(voff), "s"(rs), "s"(soff), "i"(limm), "i"(simm) : "memory", "scc");
}
DI void dmat(unsigned lds, unsigned voff, rsrc_t rs) {
  asm volatile("s_mov_b32 m0, %0\n\ts_nop 0\n\tbuffer_load_dword %1, %2, 0 offen lds" :: "s"(lds), "v"(voff), "s"(rs) : "memory");
}
DI s16x4 trd(unsigned a) { return __builtin_amdgcn_ds_read_tr16_b64_v4i16((LAS s16x4*)(size_t)a); }
DI s16x4 trd2(unsigned a, unsigned off) { return __builtin_amdgcn_ds_read_tr16_b64_v4i16((LAS s16x4*)((LAS char*)(size_t)a + off)); }
DI bf16x8 tr2(unsigned a, unsigned off0, unsigned off1) {
  LAS char* p = (LAS char*)(size_t)a;
  s16x4 lo = __builtin_amdgcn_ds_read_tr16_b64_v4i16((LAS s16x4*)(p + off0));
  s16x4 hi = __builtin_amdgcn_ds_read_tr16_b64_v4i16((LAS s16x4*)(p + off1));
  return __builtin_shufflevector(lo, hi, 0, 1, 2, 3, 4, 5, 6, 7);
}
template <class T> DI T ldsr(unsigned a, unsigned off) { return *(LAS T*)((LAS char*)(size_t)a + off); }
template <class T> DI void ldsw(unsigned a, unsigned off, T v) { *(LAS T*)((LAS char*)(size_t)a + off) = v; }
DI unsigned pk2(float a, float b) { f32x2 f = {a, b}; return __builtin_bit_cast(unsigned, __builtin_convertvector(f, bf2)); }
DI float rbf(float a) { return U2F(pk2(a, a) << 16); }
DI float dot2(unsigned a, unsigned b, float c) { return __builtin_amdgcn_fdot2_f32_bf16(__builtin_bit_cast(bf2, a), __builtin_bit_cast(bf2, b), c, false); }
DI float xsum(float v) { auto r = __builtin_amdgcn_permlane32_swap(F2U(v), F2U(v), false, false); float a = U2F(r[0]); asm("" : "+v"(a)); return a + U2F(r[1]); }
DI float xmax(float v) { auto r = __builtin_amdgcn_permlane32_swap(F2U(v), F2U(v), false, false); return __builtin_fmaxf(U2F(r[0]), U2F(r[1])); }
DI float xmin(float v) { auto r = __builtin_amdgcn_permlane32_swap(F2U(v), F2U(v), false, false); return __builtin_fminf(U2F(r[0]), U2F(r[1])); }
DI float xhi(float v) { auto r = __builtin_amdgcn_permlane32_swap(F2U(v), F2U(v), false, false); return U2F(r[1]); }
#define STB "sc1"
DI void st16(u32x4 v, rsrc_t rs, unsigned voff, unsigned soff, unsigned simm) {
  unsigned t;
  asm volatile("s_add_u32 %0, %4, %c5\n\tbuffer_store_dwordx4 %1, %2, %3, %0 offen " STB "\n\ts_nop 1" : "=&s"(t) : "v"(v), "v"(voff), "s"(rs), "s"(soff), "i"(simm) : "memory", "scc");
}
DI void stflag(unsigned v, rsrc_t rs, unsigned soff) {
  unsigned voff = (__builtin_amdgcn_mbcnt_hi(~0u, __builtin_amdgcn_mbcnt_lo(~0u, 0u)) == 0u) ? 0u : 0x80000000u;
  __builtin_amdgcn_raw_buffer_store_b32(v, rs, voff, soff, 16);
}
#define XORCOND (n == 24)
#define PS 0
#define PV 0
#define KEEP2 0
#define S2N 14
#define B1N 23
#define DIST 2
#define P2G1 1
#define OAUX 0
#define RECY 0
#define POFF 99
#define H1S 0
#define H0L 14
#define H0C 22
#define Q3A 1
#define COOP 0
#define PON -2
#define XN 20
#define BAN 18
#define BBN 21
#define SL0 8
#define SLN 2
#define SLD 3
#define NSL 48
#define H1C 16
#define OV vL16
#define CV vL16
#define Q6E 0
#define TEARLY 0
#define NOCS 0
#define TPOS 0
#define PRE 0
#define LREC 0
#define CK1 20
#define EB 3
#define T2B 1
#define F1N 9
#define RC 3
#define G0B 9
#define TN 10
#define TMODE 3
#define SLEEPR 4
#define SLEEPN 2
DI u32x4 ld16(rsrc_t rs, unsigned voff, unsigned soff, unsigned simm) {
  u32x4 r; unsigned t;
  asm volatile("s_add_u32 %1, %4, %c5\n\tbuffer_load_dwordx4 %0, %2, %3, %1 offen sc1" : "=v"(r), "=&s"(t) : "v"(voff), "s"(rs), "s"(soff), "i"(simm) : "memory", "scc");
  return r;
}
template <unsigned STEP> DI void ld8w(u32x4 (&p)[8], rsrc_t rs, unsigned voff, unsigned soff) {
  unsigned t;
#define L1(k) "buffer_load_dwordx4 %" #k ", %9, %10, %8 offen sc1\n\ts_add_u32 %8, %8, %c12\n\t"
  asm volatile("s_mov_b32 %8, %11\n\t" L1(0) L1(1) L1(2) L1(3) L1(4) L1(5) L1(6) L1(7) "s_waitcnt vmcnt(0)"
               : "=&v"(p[0]), "=&v"(p[1]), "=&v"(p[2]), "=&v"(p[3]), "=&v"(p[4]), "=&v"(p[5]), "=&v"(p[6]), "=&v"(p[7]), "=&s"(t)
               : "v"(voff), "s"(rs), "s"(soff), "i"(STEP) : "memory", "scc");
#undef L1
}
DI bool untag(u32x4 (&p)[8], unsigned tag, unsigned mask) {
  unsigned chk = 0;
#pragma unroll
  for (int i = 0; i < 8; i++)
#pragma unroll
    for (int k = 0; k < 4; k++) { p[i][k] ^= tag; chk |= p[i][k]; }
  return ALL((chk & mask) == 0u);
}
template <unsigned STEP> DI void waittag(u32x4 (&p)[8], rsrc_t rs, unsigned voff, unsigned soff, unsigned tag, unsigned mask) {
  asm volatile("s_waitcnt vmcnt(0)" : "+v"(p[0]), "+v"(p[1]), "+v"(p[2]), "+v"(p[3]), "+v"(p[4]), "+v"(p[5]), "+v"(p[6]), "+v"(p[7]) :: "memory");
#pragma nounroll
  for (int it = 0; !untag(p, tag, mask) && it < (1 << 16); ++it) { __builtin_amdgcn_s_sleep(SLEEPN); ld8w<STEP>(p, rs, voff, soff); }
}
template <bool WAIT> DI void rel1(u32x4& a, u32x4& b, rsrc_t rs, unsigned voff, unsigned so) {
  unsigned t;
  if constexpr (WAIT) asm volatile("buffer_load_dwordx4 %0, %3, %4, %5 offen sc1\n\ts_add_u32 %2, %5, 147456\n\tbuffer_load_dwordx4 %1, %3, %4, %2 offen sc1\n\ts_waitcnt vmcnt(0)"
                                   : "+v"(a), "+v"(b), "=&s"(t) : "v"(voff), "s"(rs), "s"(so) : "memory", "scc");
  else asm volatile("buffer_load_dwordx4 %0, %3, %4, %5 offen sc1\n\ts_add_u32 %2, %5, 147456\n\tbuffer_load_dwordx4 %1, %3, %4, %2 offen sc1"
                    : "+v"(a), "+v"(b), "=&s"(t) : "v"(voff), "s"(rs), "s"(so) : "memory", "scc");
}
DI void poll(rsrc_t rs, unsigned voff, unsigned soff, unsigned E, unsigned need) {
  voff = need ? voff : 0x80000000u;
#pragma nounroll
  for (int it = 0; it < (1 << 16); ++it) {
    unsigned v;
    asm volatile("buffer_load_dword %0, %1, %2, %3 offen sc1\n\ts_waitcnt vmcnt(0)" : "=v"(v) : "v"(voff), "s"(rs), "s"(soff) : "memory");
    bool ok = (need == 0u) || ((v - E - need) <= (3u - need));
    if (ALL(ok)) break;
    __builtin_amdgcn_s_sleep(SLEEPN);
  }
}

template <bool B> struct BoolC { static constexpr bool value = B; };
template <int I> struct IntC { static constexpr int value = I; };
template <int... I, class F> DI void sfor_impl(std::integer_sequence<int, I...>, F&& f) { (f(IntC<I>{}), ...); }
template <int N, class F> DI void sfor(F&& f) { sfor_impl(std::make_integer_sequence<int, N>{}, f); }
#define SFOR(n, N) sfor<N>([&](auto n##_) AI { constexpr int n = decltype(n##_)::value;
#define SEND });
struct RowC { float r, K1, K2, Ri, Lr; };

DI unsigned hilo(float v) { float hi = rbf(v); return pk2(hi, v - hi); }
DI float max3(float a, float b, float c) { return __builtin_fmaxf(__builtin_fmaxf(a, b), c); }
DI float min3(float a, float b, float c) { return __builtin_fminf(__builtin_fminf(a, b), c); }
DI unsigned pk2s(float a, float b) { unsigned r; asm("v_cvt_pk_bf16_f32 %0, %1, %2" : "=v"(r) : "v"(a), "v"(b)); return r; }
DI f32x2 sub2(const f32x16& v, int k) { f32x2 t = {v[2 * k], v[2 * k + 1]}; return t; }
DI f32x2 exp2v(f32x2 v) { f32x2 r = {__builtin_amdgcn_exp2f(v[0]), __builtin_amdgcn_exp2f(v[1])}; return r; }
DI f32x2 dup2(float a) { f32x2 r = {a, a}; asm("" : "+v"(r)); return r; }
DI f32x2 fma2(f32x2 a, f32x2 b, f32x2 c) { return __builtin_elementwise_fma(a, b, c); }
DI u32x4 epiP(const f32x16& a, const RowC& c) {
  u32x4 r;
  const f32x2 K1 = dup2(c.K1), K2 = dup2(c.K2), Ri = dup2(c.Ri);
#pragma unroll
  for (int i = 0; i < 4; i++) {
    const f32x2 g = sub2(a, i), d = fma2(exp2v(g * fma2(g * g, K2, K1)), Ri, Ri);
    const f32x2 s = {__builtin_amdgcn_rcpf(d[0]), __builtin_amdgcn_rcpf(d[1])}, h = (g * sub2(a, 4 + i)) * s;
    r[i] = pk2(h[0], h[1]);
  }
  return r;
}
struct EpiS { float x0, x1, m0, m1; u32x4 r; };
template <int P, int PH> DI void epiSlice(const f32x16& a, const RowC& c, EpiS& s) {
  const float g0 = a[2 * P], g1 = a[2 * P + 1];
  if constexpr (PH == 0) { s.x0 = g0 * __builtin_fmaf(g0 * g0, c.K2, c.K1); s.x1 = g1 * __builtin_fmaf(g1 * g1, c.K2, c.K1); }
  if constexpr (PH == 1) { s.x0 = __builtin_amdgcn_exp2f(s.x0); s.x1 = __builtin_amdgcn_exp2f(s.x1); s.m0 = g0 * a[8 + 2 * P]; s.m1 = g1 * a[9 + 2 * P]; }
  if constexpr (PH == 2) { s.x0 = __builtin_amdgcn_rcpf(__builtin_fmaf(s.x0, c.Ri, c.Ri)); s.x1 = __builtin_amdgcn_rcpf(__builtin_fmaf(s.x1, c.Ri, c.Ri)); }
  if constexpr (PH == 3) s.r[P] = pk2s(s.m0 * s.x0, s.m1 * s.x1);
}
DI f32x16 featproj2(const f32x16& a, bf16x8 w0, bf16x8 w1, bool hh) {
  u32x4 q0, q1;
#pragma unroll
  for (int i = 0; i < 4; i++) { q0[i] = pk2(a[2 * i], a[2 * i + 1]); q1[i] = pk2(a[8 + 2 * i], a[9 + 2 * i]); }
  q1[2] = hh ? 0u : q1[2]; q1[3] = hh ? 0u : q1[3];
  f32x16 z;
#pragma unroll
  for (int i = 0; i < 16; i++) z[i] = 0.f;
  z = MFMA(w0, __builtin_bit_cast(bf16x8, q0), z);
  z = MFMA(w1, __builtin_bit_cast(bf16x8, q1), z);
  return z;
}
DI f32x16 featproj(const f32x16& a, unsigned wpaddr, unsigned off, bool hh) {
  return featproj2(a, tr2(wpaddr, off, off + 8 * 56), tr2(wpaddr, off + 16 * 56, off + 24 * 56), hh);
}
DI float max16(const f32x16& p) { float m = max3(p[0], p[1], p[2]); _Pragma("unroll") for (int i = 3; i < 15; i += 2) m = max3(m, p[i], p[i + 1]); return xmax(__builtin_fmaxf(m, p[15])); }
DI float min16(const f32x16& p) { float m = min3(p[0], p[1], p[2]); _Pragma("unroll") for (int i = 3; i < 15; i += 2) m = min3(m, p[i], p[i + 1]); return xmin(__builtin_fminf(m, p[15])); }
DI f32x16 dualexpP(const f32x16& p, float Lr) {
  const f32x2 bx = dup2(-max16(p) * Lr), bn = dup2(min16(p) * Lr), L2 = dup2(Lr);
  f32x16 o;
#pragma unroll
  for (int i = 0; i < 8; i++) {
    const f32x2 ep = exp2v(fma2(sub2(p, i), L2, bx)), en = exp2v(fma2(sub2(p, i), -L2, bn));
    o[i] = U2F(pk2(ep[0], ep[1])); o[8 + i] = U2F(pk2(en[0], en[1]));
  }
  return o;
}
struct SmS { f32x16 p, e; float en[16], bx, bn, sp, sn, tp, tn; bf16x8 w0, w1; };
DI bf16x8 sub4(const f32x16& v, int k) { f32x4 t = {v[4 * k], v[4 * k + 1], v[4 * k + 2], v[4 * k + 3]}; return __builtin_bit_cast(bf16x8, t); }

DI constexpr unsigned lc1_base(int ch) { return ch == 6 ? L_KV : ((ch & 1) ? L_GU : 0u) + (ch < 2 ? 49152u : ch < 4 ? 0u : 16384u); }
template <int CTRL, int RM> DI float dppadd(float v) { return v + U2F(__builtin_amdgcn_update_dpp(0u, F2U(v), CTRL, RM, 0xf, false)); }
DI float wavesum63(float v) {
  v = dppadd<0x111, 0xf>(v); v = dppadd<0x112, 0xf>(v); v = dppadd<0x114, 0xf>(v); v = dppadd<0x118, 0xf>(v);
  v = dppadd<0x142, 0xa>(v); return dppadd<0x143, 0xc>(v);
}

#define STAMP(i)
#define STAMPS_OUT()
#define CSTAMP(i)

#define PDV 6
constexpr int PD = PDV, RING = 9;
constexpr int later2(int i, int NS, int GG) {
  const int younger = 2 * ((i + PD <= NS ? i + PD : NS) - 1 - i);
  int extra = i < PD ? 1 : 0;
  if (NS == 20 && (i == 10 || i == 11)) extra += 2 * (i - 9);
  return younger + extra;
}
DI constexpr unsigned ring_area(int j) { return j == 0 ? L_FREE : j == 1 ? L_PHI : lc1_base(j - 2); }
constexpr int cumG1(int s) { return s < 4 ? 0 : s < 22 ? (s - 4) / 2 : (9 + (s - 22) * 5 / 4 < 48 ? 9 + (s - 22) * 5 / 4 : 48); }

#define CODE_BYTES 65536
__global__ __launch_bounds__(512, 2) void fused_kernel(const char* px_, const char* pqkv_, const char* pwg_, const char* pwu_, char* ws_, const char* pphi_,
                                                       const char* pwd_, const char* pwo_, char* pout_) {
  typedef __attribute__((ext_vector_type(8))) unsigned long long u64x8;
  u64x8 ka; unsigned long long kb;
  asm volatile("s_load_dwordx16 %0, %2, 0\n\ts_load_dwordx2 %1, %2, 64" : "=&s"(ka), "=&s"(kb) : "s"(__builtin_amdgcn_kernarg_segment_ptr()));
  __shared__ __attribute__((aligned(16))) char smem[163840];
  const unsigned LB = (unsigned)(size_t)smem;
  const unsigned tid = __builtin_amdgcn_workitem_id_x(), lane = tid & 63;
  const unsigned w = __builtin_amdgcn_readfirstlane(tid >> 6), g = w >> 2, wr = w & 3;
  const unsigned b = __builtin_amdgcn_workgroup_id_x(), xcd = b & 7, bi = b >> 3;
  const unsigned half = (xcd >> 1) & 1, cp = 4 * (xcd & 1) + (bi >> 3), rb = 8 * (xcd >> 2) + (bi & 7), rsum = (bi >> 3) + (bi & 7);
  const unsigned f2 = 32 * (xcd & 1) + bi - 8, t2 = (f2 * 37) >> 8, ci = f2 - 7 * t2, rbl = t2 & 3, rt2 = 2 * (8 * (xcd >> 1) + t2);
  const unsigned c = 7 * half + cp - 1;
  const unsigned head = 8 * half + (((half ? 0x76425310u : 0x75316420u) >> (4 * cp)) & 7);
  const bool attn = cp == 0;
  STAMP(0); CSTAMP(14);
  const unsigned wstr = attn ? QROW : FROW;
  const unsigned sG0 = half * (attn ? 448 : 1792) + (attn ? 0 : (cp - 1) * 256), sX0 = rb * 128 * XROW;
  unsigned vx0, vx1, vG, vU, vKV;
  auto voffs = [&](unsigned ln) AI {
    const unsigned r8 = ln >> 3, c8 = ln & 7, sa = (r8 >> 1) & 1, lc = c8 ^ (4 * sa);
    vx0 = (16 * w + r8) * XROW + ((c8 ^ (r8 >> 1)) << 4); vx1 = (vx0 ^ 64u) + 8 * XROW;
    const unsigned cg = attn ? lc : (c8 ^ (2 * sa)), cu = attn ? (lc >= 6 ? lc - 2 : lc) : (cg ^ 4);
    vG = (8 * w + r8) * wstr + (cg << 4); vU = (8 * w + r8) * wstr + (cu << 4);
    vKV = (8 * w + r8) * QROW + 896 * (1 + (lc >> 2)) + ((lc & 3) << 4) + ((56 * head) & ~15u);
  };
  voffs(lane);
  const unsigned lw1 = LB + w * 1024, lw2 = LB + w * 2048;
#define ROT (((0x124924u >> (2 * rsum)) & 3) * 2)
  const unsigned rot = ROT;
  const unsigned srow = 128 * rb + 64 * half + 8 * cp + w;
  unsigned vrow = lane < 56 ? lane * 16 : 0x80000000u;
  asm volatile("s_waitcnt lgkmcnt(0)" : "+s"(ka), "+s"(kb), "+v"(vx0), "+v"(vx1), "+v"(vG), "+v"(vU), "+v"(vKV), "+v"(vrow));
  const char *px = (const char*)ka[0], *pqkv = (const char*)ka[1], *pwg = (const char*)ka[2], *pwu = (const char*)ka[3], *pphi = (const char*)ka[5],
             *pwd = (const char*)ka[6], *pwo = (const char*)ka[7];
  char *ws = (char*)ka[4], *pout = (char*)kb;
  const rsrc_t rsX = mkrs(px, INSZ(2048 * XROW)), rsQ = mkrs(pqkv, INSZ(448 * QROW));
  const rsrc_t rsG = mkrs(attn ? pqkv : pwg, INSZ(448 * wstr)), rsU = mkrs(attn ? pqkv + 128 : pwu, INSZ(448 * wstr - (attn ? 128 : 0)));
  auto cidx = [&](int st) AI -> unsigned { unsigned t = rot + st; return t >= 7 ? t - 7 : t; };
  auto issueA = [&](int st, int k) AI {
    const unsigned sl = st & 3, ch = cidx(st);
    if (k == 0) dma<NTX>(lw2, sl * 16384, vx0, rsX, sX0 + ch * 128, 0);
    if (k == 1) dma<NTX>(lw2, sl * 16384 + 1024, vx1, rsX, sX0 + ch * 128, 0);
    if (k == 2) dma<NTW>(lw1, L_GU + sl * 16384, vG, rsG, sG0 + ch * 64 * wstr, 0);
    if (k == 3) dma<NTW>(lw1, L_GU + sl * 16384 + 8192, vU, rsU, sG0 + ch * 64 * wstr, 0);
    if (k == 4) dma<NTW>(lw1, L_KV + sl * 8192, vKV, rsQ, ch * 64 * QROW, 0);
  };
  const rsrc_t rsWS = mkrs(ws, WS_END);
  unsigned Ev, cpf; u32x4 xrw;
  asm volatile("buffer_load_dwordx4 %0, %1, %2, %3 offen" : "=v"(xrw) : "v"(vrow), "s"(rsX), "s"(srow * XROW) : "memory");
  asm volatile("buffer_load_dword %0, off, %1, 0 sc1" : "=v"(Ev) : "s"(rsWS) : "memory");
  {
    unsigned long long pc; asm volatile("s_getpc_b64 %0" : "=s"(pc));
    const unsigned vo = w == 0 && lane < 24 ? (bi * 24 + lane) * 128 : 0x80000000u;
    asm volatile("buffer_load_dword %0, %1, %2, 0 offen" : "=v"(cpf) : "v"(vo), "s"(mkrs((const void*)pc, CODE_BYTES)) : "memory");
  }
#pragma unroll
  for (int k = 0; k < 5; k++) issueA(0, k);
  asm volatile("s_waitcnt vmcnt(%c3)" : "+v"(xrw), "+v"(Ev), "+v"(cpf) : "i"(5 + COOP) : "memory");
#pragma unroll
  for (int k = 0; k < 5; k++) issueA(1, k);
  issueA(2, 0);
  const unsigned E = __builtin_amdgcn_readfirstlane(Ev);
  {
    float s1 = 0.f, s2 = 0.f;
#pragma unroll
    for (int i = 0; i < 4; i++) { s1 = dot2(xrw[i], 0x3f803f80u, s1); s2 = dot2(xrw[i], xrw[i], s2); }
    s1 = wavesum63(s1); s2 = wavesum63(s2);
    const float mean = s1 / 448.f, var = __builtin_fmaxf(s2 * (1.f / 448.f) - mean * mean, 0.f);
    u32x2 gr = {pk2(-mean, __builtin_amdgcn_sqrtf(rbf(rbf(var) + 1e-5f))), E + 1};
    __builtin_amdgcn_raw_buffer_store_b64(gr, rsWS, lane == 63 ? 0u : 0x80000000u, WS_GR + srow * 8, 16);
  }

  auto prog = [&](auto at_, auto gg_) AI {
  constexpr bool AT = decltype(at_)::value; constexpr int GG = decltype(gg_)::value;
  constexpr unsigned lcstep = AT ? 224 : 128;
  unsigned lane;
  asm volatile("v_mbcnt_lo_u32_b32 %0, -1, 0\n\tv_mbcnt_hi_u32_b32 %0, -1, %0" : "=v"(lane));
  __builtin_assume(lane < 64);
  voffs(lane);
  auto issueL = [&](int hc) AI {
    const int st = hc >> 1; const unsigned ch = cidx(st);
    if (hc & 1) dma<NTW>(lw1, lc1_base(st) + 8192, vU, rsU, sG0 + ch * 64 * wstr, lcstep);
    else dma<NTW>(lw1, lc1_base(st), vG, rsG, sG0 + ch * 64 * wstr, lcstep);
  };
  auto issueG = [&](int bar, int k) AI {
    if (bar == 0) issueA(2 + k / 5, k % 5);
    else if (bar <= 3) issueA(bar + 3, k);
    else if (k < 4) issueL(4 * (bar - 4) + k);
    else if (bar == 4) dma(LB + (w & 1) * 1024, L_WPK, lane * 16, mkrs(pphi, 16 * 1568), head * 1568 + (w & 1) * 1024, 0);
    else issueL(12 + (bar - 5));
  };

  const unsigned rt = 4 * rb + wr;
  const unsigned sA2 = WS_A2 + rt * 144 * 1024 + (AT ? half * 16384 : c * 8192), vL16 = lane * 16;
  const u32x4 sent = {~0u, ~0u, ~0u, ~0u};
  auto clr = [&](int ks) AI { st16(sent, rsWS, CV, sA2, ks * 1024); };

  auto clri = [&](int q) AI { clr(GG ? (q < 6 ? 114 + q : 120 + q) : (q < 2 ? 112 + q : 118 + q)); };

  const bool hh = lane >= 32;
  const unsigned hu = lane >> 5, blk = (lane >> 4) & 1, q4 = (lane >> 2) & 3, p4 = lane & 3, ta = (q4 >> 1) & 1, l31 = lane & 31;
  const unsigned perm = wr ^ (2 * GG);
  const unsigned rowoff = (8 * hu + q4) * 128 + (perm << 11);
  const unsigned pc = (4 * blk + p4) < 6 ? (4 * blk + p4) : 6;
  auto tileaddr = [&](unsigned t) AI -> unsigned {
    if constexpr (AT) { unsigned o = 56 * t + 8 * pc; return (o >> 7) * 8192 + ((((o >> 4) & 7) ^ (4 * ta)) << 4) + (o & 8) + rowoff; }
    else return blk * 8192 + (((2 * t + (p4 >> 1)) ^ (4 * blk + 2 * ta)) << 4) + 8 * (p4 & 1) + rowoff;
  };
  constexpr int NT = !AT ? 2 : GG ? 1 : 3, TB0 = GG ? 4 - NT : 0;
  unsigned aA[3], aB[NT];
  {
    unsigned o = (head & 1) * 8 + 8 * pc;
    unsigned ak = L_KV + (((o >> 4) ^ (4 * ta)) << 4) + (o & 8) + rowoff;
    aA[0] = LB + (GG ? L_GU + tileaddr(1) : ak);
    aA[1] = LB + (GG ? L_GU + tileaddr(2) : (ak ^ 64u));
    aA[2] = LB + L_GU + tileaddr(GG ? 3 : 0);
#pragma unroll
    for (int j = 0; j < NT; j++) aB[j] = LB + tileaddr(TB0 + j);
  }
  const unsigned xrow = 32 * wr + l31;
  const unsigned aX = LB + xrow * 128 + (((2 * perm + hu) ^ ((xrow >> 1) & 7)) << 4);
  const unsigned selv = (((lane >> 4) ^ (lane >> 2)) & 1) ? 0u : 0x3f803f80u;
  const u32x4 sel4 = {selv, selv, selv, selv};
  const bf16x8 sel = __builtin_bit_cast(bf16x8, sel4);

  bf16x8 xf[28], wa[2][3], wq[8];
  f32x16 accA[3], accB[NT];
  f32x4 csA[3], csB[NT];
#pragma unroll
  for (int j = 0; j < 3; j++) { for (int i = 0; i < 16; i++) accA[j][i] = 0.f; for (int i = 0; i < 4; i++) csA[j][i] = 0.f; }
#pragma unroll
  for (int j = 0; j < NT; j++) { for (int i = 0; i < 16; i++) accB[j][i] = 0.f; for (int i = 0; i < 4; i++) csB[j][i] = 0.f; }
  u32x2 grn;

  const rsrc_t rsWD = mkrs(pwd, INSZ(1792 * XROW)), rsWO = mkrs(pwo, INSZ(448 * XROW));
  const unsigned vW2 = (lane >> 3) * XROW + (((lane & 7) ^ ((lane >> 2) & 4)) << 4) + ci * 128;
  constexpr int NS = GG ? 16 + XS : 20 - XS, NC = 6 - XS / 2, NO = 2 * NC;
#define RHO ((rbl + ci) & 3)
  const unsigned rho = RHO;
  auto t_of = [](int i) constexpr { return (0x54763210u >> (4 * ((i >> 1) & 7))) & 7; };
  constexpr int NE = GG ? 9 : 5;
  auto g1i = [](int i) constexpr { return i < 8 ? i : i == 8 ? 16 : i < 17 ? i - 1 : 17; };
  auto ksb = [&](int i) AI -> unsigned {
    if (GG) i = g1i(i);
    if (GG) return i < 16 ? 16 * wr + ((t_of(i) & 3) ^ rho) : wr;
    if (i < NO) { const unsigned cc = i % NC + rho + (XS ? 0 : rho >> 1); return wr + 8 * (cc >= NC ? cc - NC : cc); }
    return 8 * wr + 2 * (((i - NO) >> 1) ^ rho);
  };
  auto ksi = [&](int i) AI -> unsigned { if (GG) i = g1i(i); return GG ? (i < 16 ? 8 * (i & 1) + (t_of(i) & 4) : 4 * i) : i < NO ? 64 + 4 * XS + 4 * (i / NC) : 112 + (i & 1); };
  auto issueW2 = [&](int i, int hf) AI {
    if (GG == 0 && i >= NO) dma<NT2>(lw2, ring_area(i % RING) + hf * 1024, vW2, rsWO, ksb(i) * 14 * XROW, (16 * (i & 1) + 8 * hf) * XROW);
    else dma<NT2>(lw2, ring_area(i % RING) + hf * 1024, vW2, rsWD, ksb(i) * 16 * XROW, (ksi(i) * 16 + 8 * hf) * XROW);
  };

  const unsigned aCSw = LB + L_CS + l31 * 16 + wr * 4 + GG * 2048, aCSr = LB + L_CS + l31 * 16 + GG * 2048;
  auto readX = [&](int n) AI { xf[n] = ldsr<bf16x8>(aX ^ (unsigned)((n & 3) << 5), ((n >> 2) & 3) * 16384); };
  auto readA = [&](int n, int j) AI {
    const unsigned o = ((n >> 2) & 3) * ((GG == 0 && j < 2) ? 8192 : 16384);
    wa[n & 1][j] = tr2(aA[j] ^ (unsigned)((n & 3) << 11), o, o + 512);
  };
  auto readB = [&](int q) AI {
    const int n = q / NT, j = q % NT;
    const unsigned base = lc1_base(n >> 2);
    if ((NT == 3 && n < XN) ? (n & 3) == 0 : n == 24) asm volatile("" : "+v"(aB[j]));
    wq[q & 7] = tr2(aB[j] ^ (((n & 3) << 11) | (base & 0x30000u)), base & 0xffffu, (base & 0xffffu) + 512);
  };
  RowC rc; unsigned nm, denb;
  auto mkxe = [&]() AI { asm volatile("" : "+v"(nm)); u32x4 t = {nm, nm, nm, nm}; return __builtin_bit_cast(bf16x8, t); };
  auto rowconst = [&]() AI {
#pragma nounroll
    for (int it = 0; it < (1 << 16) && !ALL(grn[1] == E + 1); ++it) {
      __builtin_amdgcn_s_sleep(SLEEPN);
      asm volatile("buffer_load_dwordx2 %0, %1, %2, %3 offen sc1\n\ts_waitcnt vmcnt(0)" : "=v"(grn) : "v"(xrow * 8), "s"(rsWS), "s"(WS_GR + rb * 1024) : "memory");
    }
    const float den = U2F(grn[0] & 0xffff0000u);
    nm = hh ? 0u : __builtin_amdgcn_perm(grn[0], grn[0], 0x01000100u);
    denb = grn[0] >> 16;
    rc.r = __builtin_amdgcn_rcpf(den);
    const float k1 = -2.f * 1.4426950408889634f * 0.7978845608028654f;
    rc.K1 = k1 * rc.r; rc.K2 = (k1 * 0.044715f) * rc.r * (rc.r * rc.r); rc.Ri = den * den; rc.Lr = 1.4426950408889634f * rc.r;
  };
  waitv<7 + COOP>();
  BAR(); MEMFENCE();
  STAMP(1);
  readX(0); readA(0, 0); readA(0, 1); readA(0, 2);
  SFOR(n, 28)
    SB();
    if constexpr (n == 27) {
#pragma unroll
      for (int j = 0; j < 3; j++) ldsw<unsigned>(aCSw, j * 512, hilo(csA[j][0]));
    }
    if constexpr ((n & 3) == 3) {
      if constexpr (n == 27) WAITV(0);
      else if constexpr (n == 23) asm volatile("s_waitcnt vmcnt(%c1)" : "+v"(grn) : "i"(10 + (CTN >= 15 && CTN < 23)) : "memory");
      else if constexpr (n == 19) waitv<11 + (CTN >= 15 && CTN < 19)>();
      else if constexpr (n == 3 || n == 15) WAITV(11); else WAITV(10);
      { WAITL0(); BAR(); MEMFENCE(); }
      if constexpr (n == 27) STAMP(2);
    }
    constexpr int bar = (n + 1) >> 2, ph = (n + 1) & 3;
#pragma unroll
    for (int j = 0; j < 3; j++) {
      if constexpr (n < 27) { if (j == 0) readX(n + 1); readA(n + 1, j); }
      else { wa[0][j] = ldsr<bf16x8>(aCSr, j * 512); if (AT && GG == 1 && j < 2) { readB(2 * j); readB(2 * j + 1); } }
      SB();
      accA[j] = MFMA(wa[n & 1][j], xf[n], accA[j]);
      const int slot = 3 * ph + j;
      if (bar == 0) issueG(0, slot - 2);
      else if (bar == 6) { if (slot < 5) issueG(bar, slot); }
      else if (bar <= 6) { if (slot == 0) issueG(bar, 0); if (slot == 2) issueG(bar, 1); if (slot == 5) issueG(bar, 2); if (slot == 7) issueG(bar, 3); if (slot == 10) issueG(bar, 4); }
      else if (j < 2) {
        if constexpr (AT) dma(lw2, L_FREE + j * 1024, lane * 16, mkrs(pphi, 16 * 1568), half * 8 * 1568 + w * 2048, j * 1024);
        else issueW2(0, j);
      }
      if (!NOCS && (n & 3) == 0) csA[j] = MFMA16(sel, wa[n & 1][j], csA[j]);
      if (n == CTN && j == 1) {
        const unsigned per = (w & 1) ? 13 : 49;
        dmat(LB + L_ST + w * 256, (w >> 1) == 3 && lane < per ? (b * per + lane) * 128 : 0x80000000u, mkrs((w & 1) ? pwo : pwd, ((w & 1) ? 448 : 1792) * XROW));
      }
      if (n == 25 && j == 0) rowconst();
      if (n == 14 && j == 2) asm volatile("buffer_load_dwordx2 %0, %1, %2, %3 offen sc1" : "=v"(grn) : "v"(xrow * 8), "s"(rsWS), "s"(WS_GR + rb * 1024) : "memory");
      SB();
    }
  SEND
  { const bf16x8 xe = mkxe();
#pragma unroll
  for (int j = 0; j < 3; j++) accA[j] = MFMA(wa[0][j], xe, accA[j]); }
  asm volatile("" : "+v"(accA[0]), "+v"(accA[1]), "+v"(accA[2]));
  SB();

  const unsigned sFown = WS_FH + (rt * 32 + (AT ? 28 + 2 * half : 2 * c) + GG) * FS;
  const unsigned aWP = LB + (4 * hu + q4) * 56 + 8 * pc;
  const unsigned T12 = (E + 1) & 0xfffu, T16 = (E + 1) & 0xffffu;
  const unsigned sF = WS_FH + rt2 * 32 * FS;
  const unsigned fc = l31 >> 1;
  const bool nd1 = GG ? (fc >> 1) == wr || (fc >= 8 && fc < 8 + XS / 2) : (fc >= 8 + XS / 2 && fc < 14) || fc == 14 + (wr >> 1);
  const unsigned aIW = LB + L_PHI + xrow * 64 + ((hu ^ ((xrow >> 1) & 7)) << 3);
  SmS sm;
  unsigned aWK = aWP + L_WPK, aWQ = aWP + L_FREE;
  asm volatile("" : "+v"(aWK)); if constexpr (AT) asm volatile("" : "+v"(aWQ));
  auto kstep = [&](auto s_) AI {
    constexpr int s = decltype(s_)::value;
    if constexpr (s == 0) { sm.w0 = tr2(aWK, 0, 8 * 56); sm.w1 = tr2(aWK, 16 * 56, 24 * 56); }
    if constexpr (s == 1) sm.p = featproj2(accA[0], sm.w0, sm.w1, hh);
    if constexpr (s == 2 || s == 3) {
#pragma unroll
      for (int k = 2 * s - 4; k < 2 * s - 2; k++) {
        u32x2 vv = {pk2(accA[1][4 * k], accA[1][4 * k + 1]), pk2(accA[1][4 * k + 2], accA[1][4 * k + 3])};
        if (k == 3) vv[0] = hh ? denb : vv[0];
        ldsw<u32x2>(aIW ^ (unsigned)(k << 4), L_V - L_PHI, vv);
      }
    }
    if constexpr (s == 4) sm.bx = -max16(sm.p) * rc.Lr;
    if constexpr (s == 5) sm.bn = min16(sm.p) * rc.Lr;
    if constexpr (s >= 6 && s < 22) {
      constexpr int i = s - 6;
      sm.e[i] = __builtin_amdgcn_exp2f(__builtin_fmaf(sm.p[i], rc.Lr, sm.bx)); sm.en[i] = __builtin_amdgcn_exp2f(__builtin_fmaf(sm.p[i], -rc.Lr, sm.bn));
      if constexpr (i == 0) { sm.sp = sm.e[0]; sm.sn = sm.en[0]; } else if constexpr (i < 12) { sm.sp += sm.e[i]; sm.sn += sm.en[i]; }
      else if constexpr (i == 12) { sm.tp = sm.e[12]; sm.tn = sm.en[12]; } else { sm.tp += sm.e[i]; sm.tn += sm.en[i]; }
    }
    if constexpr (s == 22) { sm.sp = __builtin_amdgcn_rcpf(xsum(sm.sp + (hh ? 0.f : sm.tp))) * rc.r; sm.sn = __builtin_amdgcn_rcpf(xsum(sm.sn + (hh ? 0.f : sm.tn))) * rc.r; }
    if constexpr (s >= 23) {
#pragma unroll
      for (int u = (s == 29 ? 6 : s - 23); u <= (s == 29 ? 7 : s - 23); u++) {
        const int k = u >> 1;
        if (u & 1) { u32x2 t = {pk2s(sm.en[4 * k] * sm.sn, sm.en[4 * k + 1] * sm.sn), pk2s(sm.en[4 * k + 2] * sm.sn, sm.en[4 * k + 3] * sm.sn)}; ldsw<u32x2>(aIW ^ (unsigned)(k << 4), 8192, t); }
        else { u32x2 t = {pk2s(sm.e[4 * k] * sm.sp, sm.e[4 * k + 1] * sm.sp), pk2s(sm.e[4 * k + 2] * sm.sp, sm.e[4 * k + 3] * sm.sp)}; ldsw<u32x2>(aIW ^ (unsigned)(k << 4), 0, t); }
      }
    }
  };
  auto qstep = [&](auto s_, f32x16& a, unsigned lh) AI {
    constexpr int s = decltype(s_)::value;
    if constexpr (s == 0) { sm.w0 = tr2(aWQ, lh * 1568, lh * 1568 + 8 * 56); sm.w1 = tr2(aWQ, lh * 1568 + 16 * 56, lh * 1568 + 24 * 56); }
    if constexpr (s == 1) sm.p = featproj2(a, sm.w0, sm.w1, hh);
    if constexpr (s == 3) sm.bx = -max16(sm.p) * rc.Lr;
    if constexpr (s == 4) sm.bn = min16(sm.p) * rc.Lr;
    if constexpr (s >= 5 && s < 21) {
      constexpr int i = (s - 5) >> 1;
      float t0, t1;
      if constexpr ((s - 5) & 1) { t0 = __builtin_fmaf(sm.p[2 * i], -rc.Lr, sm.bn); t1 = __builtin_fmaf(sm.p[2 * i + 1], -rc.Lr, sm.bn); }
      else { t0 = __builtin_fmaf(sm.p[2 * i], rc.Lr, sm.bx); t1 = __builtin_fmaf(sm.p[2 * i + 1], rc.Lr, sm.bx); }
      a[8 * ((s - 5) & 1) + i] = U2F(pk2(__builtin_amdgcn_exp2f(t0), __builtin_amdgcn_exp2f(t1)));
      if constexpr (s == 20) asm volatile("" : "+v"(a));
    }
  };
  bf16x8 ka_[4], kb_[2][4]; f32x4 kd[2];
  auto kvstep = [&](auto s_) AI {
    constexpr int s = decltype(s_)::value;
    if constexpr (s == 0) {
      const unsigned g4 = lane >> 4, r0 = 8 * g4 + q4, sw = 4 * blk + (q4 >> 1);
      unsigned b0 = LB + L_PHI + r0 * 64 + ((p4 ^ sw) << 3);
      unsigned a0 = (b0 ^ ((wr >> 1) << 5)) + (wr & 1) * 8192;
#pragma unroll
      for (int k = 0; k < 4; k++) {
        ka_[k] = __builtin_shufflevector(trd2(a0, k * 2048), trd2(a0 ^ 16u, k * 2048 + 256), 0, 1, 2, 3, 4, 5, 6, 7);
        kb_[0][k] = __builtin_shufflevector(trd2(b0, L_V - L_PHI + k * 2048), trd2(b0 ^ 16u, L_V - L_PHI + k * 2048 + 256), 0, 1, 2, 3, 4, 5, 6, 7);
        kb_[1][k] = __builtin_shufflevector(trd2(b0 ^ 32u, L_V - L_PHI + k * 2048), trd2(b0 ^ 48u, L_V - L_PHI + k * 2048 + 256), 0, 1, 2, 3, 4, 5, 6, 7);
      }
    }
    if constexpr (s == 1) {
      f32x4 z = {0.f, 0.f, 0.f, 0.f}; kd[0] = z; kd[1] = z;
#pragma unroll
      for (int k = 0; k < 4; k++) { kd[0] = MFMA16(ka_[k], kb_[0][k], kd[0]); kd[1] = MFMA16(ka_[k], kb_[1][k], kd[1]); }
    }
    if constexpr (s == 3) {
#pragma unroll
      for (int d = 0; d < 2; d++) {
        u32x4 t;
#pragma unroll
        for (int i = 0; i < 4; i++) t[i] = (F2U(kd[d][i]) & ~0xfffu) | T12;
        st16(t, rsWS, (lane >> 4) * 512 + (lane & 15) * 16, ((head * 16 + rb) * 2 + (wr & 1)) * 4096 + (wr >> 1) * 2048, WS_KVP + d * 256);
      }
    }
  };
  auto qexp2 = [&](f32x16& a0, unsigned l0, f32x16& a1, unsigned l1) AI {
    const f32x16 p0 = featproj(a0, aWQ, l0 * 1568, hh), p1 = featproj(a1, aWQ, l1 * 1568, hh);
    a0 = dualexpP(p0, rc.Lr); a1 = dualexpP(p1, rc.Lr); asm volatile("" : "+v"(a0), "+v"(a1));
  };

  unsigned fl1 = 0;
  EpiS es;
  u32x4 pv[8];
  const unsigned vKP = l31 * 16 + hu * 8192, sKP = (rb * 32 + half) * 4096 + wr * 1024 + GG * 512, sKR = (8 * half + w) * 8192;
  auto hop1 = [&]() AI {
    waittag<16384>(pv, rsWS, vKP, sKP + WS_KVP, T12, 0xfffu);
    u32x4 o;
#pragma unroll
    for (int k = 0; k < 4; k++) {
      float sum = U2F(pv[0][k]);
#pragma unroll
      for (int i = 1; i < 8; i++) sum += U2F(pv[i][k]);
      sum = xsum(sum);
      sum = l31 == 29 ? 1.f : sum;
      if (GG == 1) sum = wr == 3 ? 0.f : sum;
      o[k] = (pk2s(sum, sum) & 0xffff0000u) | T16;
    }
    st16(o, rsWS, hh ? 0x80000000u : l31 * 16, (rb * 2 + half) * 4096 + wr * 1024 + GG * 512, WS_KVR);
    if (b == 0 && w == 4) stflag(E + 4, rsWS, WS_EPOCH);
    SB();
  };
  bf16x8 cb[NT];
  auto ldflag = [&]() AI { asm volatile("buffer_load_dword %0, %1, %2, %3 offen sc1" : "=v"(fl1) : "v"(nd1 ? lane * FS : 0x80000000u), "s"(rsWS), "s"(sF) : "memory"); };
  auto touch = [&](unsigned wo4) AI {
    const unsigned rw = (rbl * 8 + w + wo4) * 44 + lane;
    const bool dn = rw < 896;
    const unsigned vo = lane < 44 ? (dn ? ((rw >> 6) * 128 + 64 + (rw & 63)) : rw - 896) * XROW + ci * 128 : 0x80000000u;
    asm volatile("s_add_u32 m0, %0, %c5\n\ts_nop 0\n\tbuffer_load_dword %1, %3, 0 offen lds\n\tbuffer_load_dword %2, %4, 0 offen lds"
                 :: "s"(LB + w * 256), "v"(dn ? vo : 0x80000000u), "v"(dn ? 0x80000000u : vo), "s"(rsWD), "s"(rsWO), "i"(L_ST) : "memory", "scc");
  };
  auto kpath = [&]() AI {
    const f32x16 p = featproj(accA[0], aWK, 0, hh);
#pragma unroll
    for (int k = 0; k < 4; k++) {
      u32x2 vv = {pk2(accA[1][4 * k], accA[1][4 * k + 1]), pk2(accA[1][4 * k + 2], accA[1][4 * k + 3])};
      if (k == 3) vv[0] = hh ? denb : vv[0];
      ldsw<u32x2>(aIW ^ (unsigned)(k << 4), L_V - L_PHI, vv);
    }
    const f32x2 bx = dup2(-max16(p) * rc.Lr), bn = dup2(min16(p) * rc.Lr), L2 = dup2(rc.Lr);
    f32x2 ep[8], en[8];
#pragma unroll
    for (int i = 0; i < 8; i++) { ep[i] = exp2v(fma2(sub2(p, i), L2, bx)); en[i] = exp2v(fma2(sub2(p, i), -L2, bn)); }
    f32x2 sp = ((ep[0] + ep[1]) + (ep[2] + ep[3])) + (ep[4] + ep[5]), sn = ((en[0] + en[1]) + (en[2] + en[3])) + (en[4] + en[5]);
    f32x2 tp = ep[6] + ep[7], tn = en[6] + en[7];
    float a0 = sp[0], a1 = sn[0], a2 = tp[0], a3 = tn[0];
    asm("" : "+v"(a0), "+v"(a1), "+v"(a2), "+v"(a3));
    a0 += sp[1]; a2 += tp[1]; asm("" : "+v"(a0), "+v"(a2)); a1 += sn[1]; a3 += tn[1];
    const f32x2 ip = dup2(__builtin_amdgcn_rcpf(xsum(a0 + (hh ? 0.f : a2))) * rc.r), in = dup2(__builtin_amdgcn_rcpf(xsum(a1 + (hh ? 0.f : a3))) * rc.r);
#pragma unroll
    for (int k = 0; k < 4; k++) {
      const f32x2 u0 = ep[2 * k] * ip, u1 = ep[2 * k + 1] * ip, v0 = en[2 * k] * in, v1 = en[2 * k + 1] * in;
      u32x2 t0 = {pk2(u0[0], u0[1]), pk2(u1[0], u1[1])}, t1 = {pk2(v0[0], v0[1]), pk2(v1[0], v1[1])};
      ldsw<u32x2>(aIW ^ (unsigned)(k << 4), 0, t0); ldsw<u32x2>(aIW ^ (unsigned)(k << 4), 8192, t1);
    }
  };
  const unsigned sA = WS_A2 + rt2 * 144 * 1024;
  u32x4 af[NS][2];
  auto loadA1 = [&](int i, int m) AI { af[i][m] = ld16(rsWS, vL16, sA + ksb(i) * 1024, (144 * m + ksi(i)) * 1024); };
  auto stream = [&]() AI {
    SFOR(q, 28 * NT)
      constexpr int n = q / NT, j = q % NT;
      if constexpr (j == 0) {
        if constexpr (n == 27) {
#pragma unroll
          for (int t = 0; t < NT; t++) ldsw<unsigned>(aCSw, ((3 + t) & 3) * 512, hilo(csB[t][0]));
          if constexpr (AT && GG) WAITV(8);
          WAITL0(); BAR(); MEMFENCE();
        }
        else if constexpr (!AT && (n & 3) == 3) { asm volatile("s_waitcnt lgkmcnt(4)" ::: "memory"); BAR(); MEMFENCE(); }
      }
      if constexpr (q + 4 < 28 * NT) readB(q + 4);
      if constexpr (n == 27) cb[j] = ldsr<bf16x8>(aCSr, ((3 + j) & 3) * 512);
      SB();
      accB[j] = MFMA(wq[q & 7], xf[n], accB[j]);
      if constexpr (!NOCS && (n & 3) == 1) csB[j] = MFMA16(sel, wq[q & 7], csB[j]);
      if constexpr (!AT) {
        if constexpr (j == 0 && n >= 3 && n < 7) issueW2(1 + ((n - 3) >> 1), (n - 3) & 1);
        if constexpr (j == 0 && n >= 7 && n < 27 && (n & 1)) issueW2(1 + ((n + 1) >> 2), ((n + 1) >> 1) & 1);
        if constexpr (n == 27) issueW2(8, j);
        if constexpr (j == 1 && n == F1N) { waitv<(F1N < 7 ? F1N - 2 : 4 + (F1N - 5) / 2) + (TN < F1N && (TMODE == 0 || (TMODE == 3 && GG == 0)) ? 2 : 0)>(); stflag(E + 1, rsWS, sFown); }
        if constexpr (j == 1 && n == TN && (TMODE == 0 || (TMODE == 3 && GG == 0))) touch(0);
        if constexpr (q == 2 * CK1 - 7) ldflag();
        if constexpr (q == 2 * CK1 - 1) {
          asm volatile("s_waitcnt vmcnt(%c5)" : "+v"(fl1), "+v"(accB[0]), "+v"(accB[NT - 1]), "+v"(csB[0]), "+v"(csB[NT - 1]) : "i"((CK1 & 1) ? 1 : 2) : "memory");
          const bool nde = nd1 && fc < 14;
          if (!ALL(!nde || (fl1 - E - 1u) <= 1u)) poll(rsWS, lane * FS, sF, E, nde ? 1u : 0u);
        }
        if constexpr (q >= 2 * CK1 && q < 2 * CK1 + 2 * NE) loadA1((q - 2 * CK1) >> 1, q & 1);
      } else {
        if constexpr (GG == 1 && n < 16 && (n & 1) == 0) clri(n >> 1);
        if constexpr (GG == 0 && j < 2 && n >= 1 && 2 * (n - 1) + j < 21) qstep(IntC<2 * (n - 1) + j>{}, accA[2], 0);
        if constexpr (GG == 0 && q >= 3 * H0L && q < 3 * H0L + 8) pv[q - 3 * H0L] = ld16(rsWS, vKP, sKP, WS_KVP + (q - 3 * H0L) * 16384);
        if constexpr (GG == 0 && j == 2 && n == H0C) { asm volatile("" : "+v"(accB[0]), "+v"(accB[1]), "+v"(accB[NT - 1]), "+v"(csB[0]), "+v"(csB[1]), "+v"(csB[NT - 1])); STAMP(11); hop1(); STAMP(6); }
      }
      SB();
    SEND
    const bf16x8 xe = mkxe();
#pragma unroll
    for (int t = 0; t < NT; t++) { accB[t] = MFMA(cb[t], xe, accB[t]); asm volatile("" : "+v"(accB[t])); }
    SB();
  };
  unsigned ln = vL16, aW = 0;
  auto mkaw = [&]() AI {
    asm volatile("" : "+v"(ln));
    aW = lw2 + (ln & 0x200) + ((ln & 0xc0) << 1) + ((((ln >> 3) & 0x20) | ((ln >> 1) & 0x10)) ^ ((ln >> 1) & 0x40)) + ((ln >> 1) & 8);
  };
  bf16x8 wf[NS][2];
  auto readW = [&](int i) AI {
    const unsigned ra = ring_area(i % RING), la = aW + (ra & 0x30000u);
    wf[i][0] = tr2(la, ra & 0xffffu, (ra & 0xffffu) + 1024); wf[i][1] = tr2(la ^ 64u, ra & 0xffffu, (ra & 0xffffu) + 1024);
  };
  auto tile = [&](f32x16& a, int t) AI { asm volatile("" : "+v"(a)); st16(epiP(a, rc), rsWS, vL16, sA2, t * 1024); SB(); };
  if constexpr (!AT) {
    if constexpr (GG == 0) {
      if (TMODE == 2) touch(0);
      kpath(); WAITL0(); SB(); BAR(); SB(); STAMP(3);
      kvstep(IntC<0>{}); kvstep(IntC<1>{}); kvstep(IntC<3>{}); SB();
      if (TMODE == 1) touch(0);
      tile(accA[2], 0);
    } else { if (TMODE >= 1 && TMODE <= 3) touch(0); tile(accA[0], 1); if (T2B) tile(accA[1], 2); BAR(); SB(); STAMP(3); if (!T2B) tile(accA[1], 2); tile(accA[2], 3); }
    readB(0); readB(1); readB(2); readB(3);
    STAMP(4);
    stream();
    SFOR(u, (2 * CK1 + 2 * NE > 56 ? 2 * CK1 + 2 * NE - 56 : 0)) loadA1((56 + u - 2 * CK1) >> 1, u & 1); SEND
    STAMP(5);
    mkaw();
    SFOR(i, RC) readW(i); SEND
    if constexpr (RC > 0) { WAITL0(); SB(); SFOR(i, RC) issueW2(9 + i, 0); issueW2(9 + i, 1); SEND }
    tile(accB[0], 4 + 2 * GG); tile(accB[NT - 1], 5 + 2 * GG);
  } else if constexpr (GG == 0) {
    SFOR(q, 8) clri(q); SEND
    SFOR(s, 30) kstep(s_); SEND
    WAITV(8);
    WAITL0(); SB(); BAR(); SB();
    STAMP(3);
    kvstep(IntC<0>{});
    readB(0); readB(1); readB(2); readB(3);
    kvstep(IntC<1>{}); kvstep(IntC<3>{});
    WAITV(2); stflag(E + 1, rsWS, sFown);
    STAMP(4);
    SB();
    stream();
  } else {
    stream();
    STAMP(3);
    SFOR(s, 21) qstep(s_, accA[0], 1); SEND
    WAITV(0); stflag(E + 1, rsWS, sFown);
    SFOR(s, 21) qstep(s_, accA[1], 2); SEND
    SFOR(s, 21)
      if constexpr (s < 8) pv[s] = ld16(rsWS, vKP, sKP, WS_KVP + s * 16384);
      qstep(s_, accA[2], 3);
    SEND
    STAMP(4); hop1(); STAMP(6);
    SB(); BAR(); SB();
  }

  if constexpr (AT) {
    STAMP(5);
    auto qexp = [&](f32x16& a, unsigned lh) AI { a = dualexpP(featproj(a, aWQ, lh * 1568, hh), rc.Lr); asm volatile("" : "+v"(a)); SB(); };
    if constexpr (GG == 0) qexp(accB[0], 4);
#pragma unroll
    for (int i = 0; i < 8; i++) pv[i] = ld16(rsWS, vL16, sKR, WS_KVR + i * 1024);
    if constexpr (GG == 0) { qexp(accB[1 % NT], 5); qexp(accB[NT - 1], 6); } else qexp(accB[0], 7);
    STAMP(7);
    waittag<1024>(pv, rsWS, vL16, sKR + WS_KVR, T16, 0xffffu);
    STAMP(8);
#pragma unroll
    for (int i = 0; i < 4; i++) {
      u32x4 f = {__builtin_amdgcn_perm(pv[2 * i][1], pv[2 * i][0], 0x07060302u), __builtin_amdgcn_perm(pv[2 * i][3], pv[2 * i][2], 0x07060302u),
                 __builtin_amdgcn_perm(pv[2 * i + 1][1], pv[2 * i + 1][0], 0x07060302u), __builtin_amdgcn_perm(pv[2 * i + 1][3], pv[2 * i + 1][2], 0x07060302u)};
      ldsw<u32x4>(LB + w * 4096 + vL16, i * 1024, f);
    }
    WAITL0(); BAR(); MEMFENCE();
    STAMP(10);
    f32x16 op[4], on[4];
    auto omm = [&](const f32x16& phi, unsigned lh, int z) AI {
#pragma unroll
      for (int i = 0; i < 16; i++) { op[z][i] = 0.f; on[z][i] = 0.f; }
      bf16x8 kf[4];
#pragma unroll
      for (int i = 0; i < 4; i++) kf[i] = ldsr<bf16x8>(LB + vL16, lh * 4096 + i * 1024);
#pragma unroll
      for (int i = 0; i < 2; i++) { op[z] = MFMA(kf[i], sub4(phi, i), op[z]); on[z] = MFMA(kf[2 + i], sub4(phi, 2 + i), on[z]); }
    };
    auto ofin = [&](unsigned lh, int z) AI {
      const float ia = __builtin_amdgcn_rcpf(op[z][13]), ib = __builtin_amdgcn_rcpf(on[z][13]);
      const float id = __builtin_amdgcn_rcpf(__builtin_fmaf(op[z][12], ia, __builtin_fmaf(on[z][12], ib, 1e-6f)));
      const float ca = xhi(ia * id), cb2 = xhi(ib * id);
      const f32x2 c2 = dup2(ca), d2 = dup2(cb2);
      u32x4 f0, f1;
#pragma unroll
      for (int i = 0; i < 4; i++) {
        const f32x2 u = fma2(sub2(op[z], i), c2, sub2(on[z], i) * d2), v = fma2(sub2(op[z], 4 + i), c2, sub2(on[z], 4 + i) * d2);
        f0[i] = pk2(u[0], u[1]); f1[i] = pk2(v[0], v[1]);
      }
      f1[2] = hh ? 0u : f1[2]; f1[3] = hh ? 0u : f1[3];
      st16(f0, rsWS, OV, sA2, (112 + 2 * lh) * 1024); st16(f1, rsWS, OV, sA2, (113 + 2 * lh) * 1024);
      SB();
    };
    if constexpr (GG) { omm(accA[0], 1, 0); omm(accA[1], 2, 1); SB(); ofin(1, 0); omm(accA[2], 3, 2); omm(accB[0], 7, 3); SB(); ofin(2, 1); ofin(3, 2); ofin(7, 3); }
    else { omm(accA[2], 0, 0); omm(accB[0], 4, 1); SB(); ofin(0, 0); omm(accB[1 % NT], 5, 2); omm(accB[NT - 1], 6, 3); SB(); ofin(4, 1); ofin(5, 2); ofin(6, 3); }
    STAMP(9);
  } else {
    f32x16 acc[2][2];
#pragma unroll
    for (int i = 0; i < 4; i++)
#pragma unroll
      for (int k = 0; k < 16; k++) acc[i >> 1][i & 1][k] = 0.f;
    u32x4 xr;
    const unsigned vO = (ln >> 7) * XROW + (ln & 0x70), sO = (rt2 * 32 + 8 * w) * XROW + ci * 128;
    auto mm4 = [&](int i) AI {
#pragma unroll
      for (int nn = 0; nn < 2; nn++)
#pragma unroll
        for (int mm = 0; mm < 2; mm++) acc[nn][mm] = MFMA(wf[i][nn], __builtin_bit_cast(bf16x8, af[i][mm]), acc[nn][mm]);
      SB();
    };
    STAMP(6);
    waitv<2 + 2 * RC>();
#pragma unroll
    for (int i = 0; i < NE; i++) asm volatile("" : "+v"(af[i][0]), "+v"(af[i][1]));
    SFOR(i, 9 - RC) readW(RC + i); SEND
    SB();
    SFOR(i, EB) mm4(i); SEND
    WAITV(0); stflag(E + 2, rsWS, sFown);
    WAITL0(); SB();
    BAR();
    SB(); STAMP(11);
    if constexpr (GG) __builtin_amdgcn_s_setprio(P2G1);
    constexpr int NB = (GG ? 9 : G0B) - RC;
    SFOR(i, 9)
      if constexpr (i == NB - 2) ldflag();
      if constexpr (i < NB) { issueW2(9 + RC + i, 0); issueW2(9 + RC + i, 1); SB(); }
      if constexpr (EB + i < NE) mm4(EB + i);
    SEND
    asm volatile("s_waitcnt vmcnt(4)" : "+v"(fl1) :: "memory");
    { const unsigned need = !nd1 ? 0u : fc < 14 ? 2u : 1u;
      if (!ALL(!nd1 || fl1 == E + need)) poll(rsWS, lane * FS, sF, E, need); }
    STAMP(7);
#pragma unroll
    for (int j = NE; j < NE + PD; j++) { loadA1(j, 0); loadA1(j, 1); }
    asm volatile("buffer_load_dwordx4 %0, %1, %2, %3 offen" : "=v"(xr) : "v"(vO), "s"(rsX), "s"(sO) : "memory");
    SFOR(i, 9 - RC - NB) issueW2(9 + RC + NB + i, 0); issueW2(9 + RC + NB + i, 1); SEND
    SFOR(i1, NS - NE)
      constexpr int i = NE + i1;
      asm volatile("s_waitcnt vmcnt(%c2)" : "+v"(af[i][0]), "+v"(af[i][1]) : "i"(2 * ((i + PD <= NS ? i + PD : NS) - 1 - i) + (i1 < PD && i < NO + 8 * GG ? 1 + 2 * (9 - RC - NB) : 0)) : "memory");
      if constexpr (i == 9 && i1 == 0) readW(9);
      if constexpr (i + 1 >= 9 && i + 1 < NS) readW(i + 1);
      if constexpr (GG == 0 && i >= NO) {
        auto stale = [&]() AI {
          unsigned m = UMAX(UMAX(UMAX(af[i][0][0], af[i][0][1]), UMAX(af[i][0][2], af[i][0][3])), UMAX(UMAX(af[i][1][0], af[i][1][1]), UMAX(af[i][1][2], af[i][1][3])));
          return ANY(m == ~0u);
        };
        if (__builtin_expect(stale(), 0)) {
          int it = 0;
#pragma nounroll
          do { __builtin_amdgcn_s_sleep(SLEEPR); rel1<true>(af[i][0], af[i][1], rsWS, vL16, sA + (ksb(i) + ksi(i)) * 1024); } while (++it < (1 << 14) && stale());
          SFOR(u, PD - 1) if constexpr (i + 1 + u < NS) rel1<false>(af[i + 1 + u][0], af[i + 1 + u][1], rsWS, vL16, sA + (ksb(i + 1 + u) + ksi(i + 1 + u)) * 1024); SEND
        }
      }
      if constexpr (i == 10) STAMP(8);
      if constexpr (i == 14) STAMP(9);
      SB();
      if constexpr (i + PD < NS) { loadA1(i + PD, 0); loadA1(i + PD, 1); }
      mm4(i);
    SEND
    STAMP(10);
    __builtin_amdgcn_s_setprio(0);
#pragma unroll
    for (int qd = 0; qd < 4; qd++)
#pragma unroll
      for (int k = 0; k < 4; k++) {
        const unsigned ra = ring_area(2 * qd + (k >> 1)) + (k & 1) * 1024;
        ldsw<bf16x8>(((lw2 + vL16) ^ (unsigned)(32 * (4 * (qd >> 1) + k))) + (ra & 0x30000u), ra & 0xffffu, sub4(acc[qd >> 1][qd & 1], k));
      }
    WAITL0(); BAR(); MEMFENCE();
    STAMP(12);
    const unsigned c8 = (ln >> 4) & 7, j2 = (c8 & 4) + 2 * GG + ((c8 >> 1) & 1);
    const unsigned rd = LB + ((0x51407362u >> (4 * j2)) & 7) * 16384 + (c8 & 1) * 1024 + (((8 * wr + (ln >> 7)) << 4) ^ (c8 << 5));
    asm volatile("" : "+v"(xr));
    f32x4 sum[2];
#pragma unroll
    for (int k = 0; k < 2; k++) {
      f32x4 t = {U2F(xr[2 * k] << 16), U2F(xr[2 * k] & 0xffff0000u), U2F(xr[2 * k + 1] << 16), U2F(xr[2 * k + 1] & 0xffff0000u)};
#pragma unroll
      for (int v = 0; v < 8; v++) t += ldsr<f32x4>(rd, v * 2048 + k * 512);
      sum[k] = t;
    }
    u32x4 o = {pk2(sum[0][0], sum[0][1]), pk2(sum[0][2], sum[0][3]), pk2(sum[1][0], sum[1][1]), pk2(sum[1][2], sum[1][3])};
    __builtin_amdgcn_raw_buffer_store_b128(o, mkrs(pout, 2048 * XROW), vO + sO, 0, OAUX);
  }
  STAMPS_OUT();
  };
  if (!attn && g) prog(BoolC<false>{}, IntC<1>{});
  else if (!attn) prog(BoolC<false>{}, IntC<0>{});
  else if (g) prog(BoolC<true>{}, IntC<1>{});
  else prog(BoolC<true>{}, IntC<0>{});
}

extern "C" void kernel_launch(void* const* d_in, const int* in_sizes, int n_in, void* d_out, int out_size, void* d_ws, size_t ws_size,
                              hipStream_t stream) {
  fused_kernel<<<256, 512, 0, stream>>>((const char*)d_in[0], (const char*)d_in[1], (const char*)d_in[5], (const char*)d_in[6], (char*)d_ws,
                                        (const char*)d_in[2], (const char*)d_in[7], (const char*)d_in[3], (char*)d_out);
}
```

```cpp
#include <hip/hip_runtime.h>
#include <hip/hip_bf16.h>
#include <utility>

#define DI __device__ __forceinline__
#define LAS __attribute__((address_space(3)))
typedef __attribute__((ext_vector_type(8))) short bf16x8;
typedef __attribute__((ext_vector_type(4))) short s16x4;
typedef __attribute__((ext_vector_type(2))) __bf16 bf2;
typedef __attribute__((ext_vector_type(2))) float f32x2;
typedef __attribute__((ext_vector_type(4))) float f32x4;
typedef __attribute__((ext_vector_type(16))) float f32x16;
typedef __attribute__((ext_vector_type(2))) unsigned u32x2;
typedef __attribute__((ext_vector_type(4))) unsigned u32x4;
typedef __amdgpu_buffer_rsrc_t rsrc_t;
#define F2U(x) __builtin_bit_cast(unsigned, (float)(x))
#define U2F(x) __builtin_bit_cast(float, (unsigned)(x))
#define ALL(x) (__builtin_amdgcn_ballot_w64(!(x)) == 0ull)
#define ANY(x) (__builtin_amdgcn_ballot_w64(x) != 0ull)
#define UMAX(a, b) __builtin_elementwise_max((unsigned)(a), (unsigned)(b))

constexpr unsigned XROW = 896, QROW = 2688, FROW = 3584;
constexpr unsigned FS = 128;
constexpr unsigned WS_EPOCH = 0;
constexpr unsigned WS_GR = 4096;
constexpr unsigned WS_KVP = 131072;
constexpr unsigned WS_A2 = WS_KVP + 16 * 16 * 8192;
constexpr unsigned WS_FH = WS_A2 + 64 * 144 * 1024 + 262144;
constexpr unsigned WS_KVR = WS_FH + 2048 * FS;
constexpr unsigned WS_END = WS_KVR + 16 * 8192;
constexpr unsigned L_GU = 65536, L_KV = 131072;
constexpr unsigned L_TAIL = L_KV + 3 * 8192;
constexpr unsigned L_WPK = L_TAIL;
constexpr unsigned L_ST = L_TAIL + 2048;
constexpr unsigned L_CS = L_TAIL + 4096;
constexpr unsigned L_PHI = L_GU + 32768;
constexpr unsigned L_V = L_KV + 16384;
constexpr unsigned L_FREE = 32768;

#define MFMA(a, b, c) __builtin_amdgcn_mfma_f32_32x32x16_bf16((a), (b), (c), 0, 0, 0)
#define MFMA16(a, b, c) __builtin_amdgcn_mfma_f32_16x16x32_bf16((a), (b), (c), 0, 0, 0)
#define AI __attribute__((always_inline))
#define BAR() __builtin_amdgcn_s_barrier()
#define WAITV(n) asm volatile("s_waitcnt vmcnt(" #n ")" ::: "memory")
#define WAITL0() asm volatile("s_waitcnt lgkmcnt(0)" ::: "memory")
#define SB() __builtin_amdgcn_sched_barrier(0)
#define MEMFENCE() asm volatile("" ::: "memory")
#define BARF() do { MEMFENCE(); SB(); BAR(); MEMFENCE(); SB(); } while (0)

template <int N> DI void waitv() { asm volatile("s_waitcnt vmcnt(%c0)" :: "i"(N) : "memory"); }

DI rsrc_t mkrs(const void* p, unsigned bytes) { return __builtin_amdgcn_make_buffer_rsrc((void*)p, (short)0, (int)bytes, 0x00020000); }
DI void dma(unsigned lds, unsigned limm, unsigned voff, rsrc_t rs, unsigned soff, unsigned simm) {
  unsigned t;
  asm volatile("s_add_u32 m0, %1, %c5\n\ts_add_u32 %0, %4, %c6\n\tbuffer_load_dwordx4 %2, %3, %0 offen lds"
               : "=&s"(t) : "s"(lds), "v"(voff), "s"(rs), "s"(soff), "i"(limm), "i"(simm) : "memory", "scc");
}
DI void dmat(unsigned lds, unsigned voff, rsrc_t rs) {
  asm volatile("s_mov_b32 m0, %0\n\ts_nop 0\n\tbuffer_load_dword %1, %2, 0 offen lds" :: "s"(lds), "v"(voff), "s"(rs) : "memory");
}
DI s16x4 trd2(unsigned a, unsigned off) { return __builtin_amdgcn_ds_read_tr16_b64_v4i16((LAS s16x4*)((LAS char*)(size_t)a + off)); }
DI bf16x8 tr2(unsigned a, unsigned off0, unsigned off1) {
  LAS char* p = (LAS char*)(size_t)a;
  s16x4 lo = __builtin_amdgcn_ds_read_tr16_b64_v4i16((LAS s16x4*)(p + off0));
  s16x4 hi = __builtin_amdgcn_ds_read_tr16_b64_v4i16((LAS s16x4*)(p + off1));
  return __builtin_shufflevector(lo, hi, 0, 1, 2, 3, 4, 5, 6, 7);
}
template <class T> DI T ldsr(unsigned a, unsigned off) { return *(LAS T*)((LAS char*)(size_t)a + off); }
template <class T> DI void ldsw(unsigned a, unsigned off, T v) { *(LAS T*)((LAS char*)(size_t)a + off) = v; }
DI unsigned pk2(float a, float b) { f32x2 f = {a, b}; return __builtin_bit_cast(unsigned, __builtin_convertvector(f, bf2)); }
DI float rbf(float a) { return U2F(pk2(a, a) << 16); }
DI float dot2(unsigned a, unsigned b, float c) { return __builtin_amdgcn_fdot2_f32_bf16(__builtin_bit_cast(bf2, a), __builtin_bit_cast(bf2, b), c, false); }
DI float xsum(float v) { auto r = __builtin_amdgcn_permlane32_swap(F2U(v), F2U(v), false, false); float a = U2F(r[0]); asm("" : "+v"(a)); return a + U2F(r[1]); }
DI float xmax(float v) { auto r = __builtin_amdgcn_permlane32_swap(F2U(v), F2U(v), false, false); return __builtin_fmaxf(U2F(r[0]), U2F(r[1])); }
DI float xmin(float v) { auto r = __builtin_amdgcn_permlane32_swap(F2U(v), F2U(v), false, false); return __builtin_fminf(U2F(r[0]), U2F(r[1])); }
DI float xhi(float v) { auto r = __builtin_amdgcn_permlane32_swap(F2U(v), F2U(v), false, false); return U2F(r[1]); }
DI void st16(u32x4 v, rsrc_t rs, unsigned voff, unsigned soff, unsigned simm) {
  unsigned t;
  asm volatile("s_add_u32 %0, %4, %c5\n\tbuffer_store_dwordx4 %1, %2, %3, %0 offen sc1\n\ts_nop 1" : "=&s"(t) : "v"(v), "v"(voff), "s"(rs), "s"(soff), "i"(simm) : "memory", "scc");
}
DI void stflag(unsigned v, rsrc_t rs, unsigned soff) {
  unsigned voff = (__builtin_amdgcn_mbcnt_hi(~0u, __builtin_amdgcn_mbcnt_lo(~0u, 0u)) == 0u) ? 0u : 0x80000000u;
  __builtin_amdgcn_raw_buffer_store_b32(v, rs, voff, soff, 16);
}
constexpr int SLEEPN = 2, SLEEPR = 1;
DI u32x4 ld16(rsrc_t rs, unsigned voff, unsigned soff, unsigned simm) {
  u32x4 r; unsigned t;
  asm volatile("s_add_u32 %1, %4, %c5\n\tbuffer_load_dwordx4 %0, %2, %3, %1 offen sc1" : "=v"(r), "=&s"(t) : "v"(voff), "s"(rs), "s"(soff), "i"(simm) : "memory", "scc");
  return r;
}
template <unsigned STEP> DI void ld8w(u32x4 (&p)[8], rsrc_t rs, unsigned voff, unsigned soff) {
  unsigned t;
#define L1(k) "buffer_load_dwordx4 %" #k ", %9, %10, %8 offen sc1\n\ts_add_u32 %8, %8, %c12\n\t"
  asm volatile("s_mov_b32 %8, %11\n\t" L1(0) L1(1) L1(2) L1(3) L1(4) L1(5) L1(6) L1(7) "s_waitcnt vmcnt(0)"
               : "=&v"(p[0]), "=&v"(p[1]), "=&v"(p[2]), "=&v"(p[3]), "=&v"(p[4]), "=&v"(p[5]), "=&v"(p[6]), "=&v"(p[7]), "=&s"(t)
               : "v"(voff), "s"(rs), "s"(soff), "i"(STEP) : "memory", "scc");
#undef L1
}
DI bool untag(u32x4 (&p)[8], unsigned tag, unsigned mask) {
  unsigned chk = 0;
#pragma unroll
  for (int i = 0; i < 8; i++)
#pragma unroll
    for (int k = 0; k < 4; k++) { p[i][k] ^= tag; chk |= p[i][k]; }
  return ALL((chk & mask) == 0u);
}
template <unsigned STEP> DI void waittag(u32x4 (&p)[8], rsrc_t rs, unsigned voff, unsigned soff, unsigned tag, unsigned mask) {
  asm volatile("s_waitcnt vmcnt(0)" : "+v"(p[0]), "+v"(p[1]), "+v"(p[2]), "+v"(p[3]), "+v"(p[4]), "+v"(p[5]), "+v"(p[6]), "+v"(p[7]) :: "memory");
#pragma nounroll
  for (int it = 0; !untag(p, tag, mask) && it < (1 << 16); ++it) { __builtin_amdgcn_s_sleep(SLEEPN); ld8w<STEP>(p, rs, voff, soff); }
}
template <bool WAIT> DI void rel1(u32x4& a, u32x4& b, rsrc_t rs, unsigned voff, unsigned so) {
  unsigned t;
  if constexpr (WAIT) asm volatile("buffer_load_dwordx4 %0, %3, %4, %5 offen sc1\n\ts_add_u32 %2, %5, 147456\n\tbuffer_load_dwordx4 %1, %3, %4, %2 offen sc1\n\ts_waitcnt vmcnt(0)"
                                   : "+v"(a), "+v"(b), "=&s"(t) : "v"(voff), "s"(rs), "s"(so) : "memory", "scc");
  else asm volatile("buffer_load_dwordx4 %0, %3, %4, %5 offen sc1\n\ts_add_u32 %2, %5, 147456\n\tbuffer_load_dwordx4 %1, %3, %4, %2 offen sc1"
                    : "+v"(a), "+v"(b), "=&s"(t) : "v"(voff), "s"(rs), "s"(so) : "memory", "scc");
}
DI void poll(rsrc_t rs, unsigned voff, unsigned soff, unsigned E, unsigned need) {
  voff = need ? voff : 0x80000000u;
#pragma nounroll
  for (int it = 0; it < (1 << 16); ++it) {
    unsigned v;
    asm volatile("buffer_load_dword %0, %1, %2, %3 offen sc1\n\ts_waitcnt vmcnt(0)" : "=v"(v) : "v"(voff), "s"(rs), "s"(soff) : "memory");
    bool ok = (need == 0u) || ((v - E - need) <= (3u - need));
    if (ALL(ok)) break;
    __builtin_amdgcn_s_sleep(SLEEPN);
  }
}

template <bool B> struct BoolC { static constexpr bool value = B; };
template <int I> struct IntC { static constexpr int value = I; };
template <int... I, class F> DI void sfor_impl(std::integer_sequence<int, I...>, F&& f) { (f(IntC<I>{}), ...); }
template <int N, class F> DI void sfor(F&& f) { sfor_impl(std::make_integer_sequence<int, N>{}, f); }
#define SFOR(n, N) sfor<N>([&](auto n##_) AI { constexpr int n = decltype(n##_)::value;
#define SEND });
struct RowC { float r, K1, K2, Ri, Lr; };

DI unsigned hilo(float v) { float hi = rbf(v); return pk2(hi, v - hi); }
DI float max3(float a, float b, float c) { return __builtin_fmaxf(__builtin_fmaxf(a, b), c); }
DI float min3(float a, float b, float c) { return __builtin_fminf(__builtin_fminf(a, b), c); }
DI unsigned pk2s(float a, float b) { unsigned r; asm("v_cvt_pk_bf16_f32 %0, %1, %2" : "=v"(r) : "v"(a), "v"(b)); return r; }
DI f32x2 sub2(const f32x16& v, int k) { f32x2 t = {v[2 * k], v[2 * k + 1]}; return t; }
DI f32x2 exp2v(f32x2 v) { f32x2 r = {__builtin_amdgcn_exp2f(v[0]), __builtin_amdgcn_exp2f(v[1])}; return r; }
DI f32x2 dup2(float a) { f32x2 r = {a, a}; asm("" : "+v"(r)); return r; }
DI f32x2 fma2(f32x2 a, f32x2 b, f32x2 c) { return __builtin_elementwise_fma(a, b, c); }
DI u32x4 epiP(const f32x16& a, const RowC& c) {
  u32x4 r;
  const f32x2 K1 = dup2(c.K1), K2 = dup2(c.K2), Ri = dup2(c.Ri);
#pragma unroll
  for (int i = 0; i < 4; i++) {
    const f32x2 g = sub2(a, i), d = fma2(exp2v(g * fma2(g * g, K2, K1)), Ri, Ri);
    const f32x2 s = {__builtin_amdgcn_rcpf(d[0]), __builtin_amdgcn_rcpf(d[1])}, h = (g * sub2(a, 4 + i)) * s;
    r[i] = pk2(h[0], h[1]);
  }
  return r;
}
DI f32x16 featproj2(const f32x16& a, bf16x8 w0, bf16x8 w1, bool hh) {
  u32x4 q0, q1;
#pragma unroll
  for (int i = 0; i < 4; i++) { q0[i] = pk2(a[2 * i], a[2 * i + 1]); q1[i] = pk2(a[8 + 2 * i], a[9 + 2 * i]); }
  q1[2] = hh ? 0u : q1[2]; q1[3] = hh ? 0u : q1[3];
  f32x16 z;
#pragma unroll
  for (int i = 0; i < 16; i++) z[i] = 0.f;
  z = MFMA(w0, __builtin_bit_cast(bf16x8, q0), z);
  z = MFMA(w1, __builtin_bit_cast(bf16x8, q1), z);
  return z;
}
DI f32x16 featproj(const f32x16& a, unsigned wpaddr, unsigned off, bool hh) {
  return featproj2(a, tr2(wpaddr, off, off + 8 * 56), tr2(wpaddr, off + 16 * 56, off + 24 * 56), hh);
}
DI float max16(const f32x16& p) { float m = max3(p[0], p[1], p[2]); _Pragma("unroll") for (int i = 3; i < 15; i += 2) m = max3(m, p[i], p[i + 1]); return xmax(__builtin_fmaxf(m, p[15])); }
DI float min16(const f32x16& p) { float m = min3(p[0], p[1], p[2]); _Pragma("unroll") for (int i = 3; i < 15; i += 2) m = min3(m, p[i], p[i + 1]); return xmin(__builtin_fminf(m, p[15])); }
DI f32x16 dualexpP(const f32x16& p, float Lr) {
  const f32x2 bx = dup2(-max16(p) * Lr), bn = dup2(min16(p) * Lr), L2 = dup2(Lr);
  f32x16 o;
#pragma unroll
  for (int i = 0; i < 8; i++) {
    const f32x2 ep = exp2v(fma2(sub2(p, i), L2, bx)), en = exp2v(fma2(sub2(p, i), -L2, bn));
    o[i] = U2F(pk2(ep[0], ep[1])); o[8 + i] = U2F(pk2(en[0], en[1]));
  }
  return o;
}
struct SmS { f32x16 p, e; float en[16], bx, bn, sp, sn, tp, tn; bf16x8 w0, w1; };
DI bf16x8 sub4(const f32x16& v, int k) { f32x4 t = {v[4 * k], v[4 * k + 1], v[4 * k + 2], v[4 * k + 3]}; return __builtin_bit_cast(bf16x8, t); }

DI constexpr unsigned lc1_base(int ch) { return ch == 6 ? L_KV : ((ch & 1) ? L_GU : 0u) + (ch < 2 ? 49152u : ch < 4 ? 0u : 16384u); }
template <int CTRL, int RM> DI float dppadd(float v) { return v + U2F(__builtin_amdgcn_update_dpp(0u, F2U(v), CTRL, RM, 0xf, false)); }
DI float wavesum63(float v) {
  v = dppadd<0x111, 0xf>(v); v = dppadd<0x112, 0xf>(v); v = dppadd<0x114, 0xf>(v); v = dppadd<0x118, 0xf>(v);
  v = dppadd<0x142, 0xa>(v); return dppadd<0x143, 0xc>(v);
}

constexpr int PD = 6, RING = 9, XS = 2, XN = 20, CTN = 16, F1N = 7, CK1 = 24, RC = 3, EB = 4, G0B = 9, H0L = 14, H0C = 22;
constexpr unsigned CODE_BYTES = 65536;
DI constexpr unsigned ring_area(int j) { return j == 0 ? L_FREE : j == 1 ? L_PHI : lc1_base(j - 2); }

__global__ __launch_bounds__(512, 2) void fused_kernel(const char* px_, const char* pqkv_, const char* pwg_, const char* pwu_, char* ws_, const char* pphi_,
                                                       const char* pwd_, const char* pwo_, char* pout_) {
  typedef __attribute__((ext_vector_type(8))) unsigned long long u64x8;
  u64x8 ka; unsigned long long kb;
  asm volatile("s_load_dwordx16 %0, %2, 0\n\ts_load_dwordx2 %1, %2, 64" : "=&s"(ka), "=&s"(kb) : "s"(__builtin_amdgcn_kernarg_segment_ptr()));
  __shared__ __attribute__((aligned(16))) char smem[163840];
  const unsigned LB = (unsigned)(size_t)smem;
  const unsigned tid = __builtin_amdgcn_workitem_id_x(), lane = tid & 63;
  const unsigned w = __builtin_amdgcn_readfirstlane(tid >> 6), g = w >> 2, wr = w & 3;
  const unsigned b = __builtin_amdgcn_workgroup_id_x(), xcd = b & 7, bi = b >> 3;
  const unsigned half = (xcd >> 1) & 1, cp = 4 * (xcd & 1) + (bi >> 3), rb = 8 * (xcd >> 2) + (bi & 7), rsum = (bi >> 3) + (bi & 7);
  const unsigned f2 = 32 * (xcd & 1) + bi - 8, t2 = (f2 * 37) >> 8, ci = f2 - 7 * t2, rbl = t2 & 3, rt2 = 2 * (8 * (xcd >> 1) + t2);
  const unsigned c = 7 * half + cp - 1;
  const unsigned head = 8 * half + (((half ? 0x76425310u : 0x75316420u) >> (4 * cp)) & 7);
  const bool attn = cp == 0;
  const unsigned wstr = attn ? QROW : FROW;
  const unsigned sG0 = half * (attn ? 448 : 1792) + (attn ? 0 : (cp - 1) * 256), sX0 = rb * 128 * XROW;
  unsigned vx0, vx1, vG, vU, vKV;
  auto voffs = [&](unsigned ln) AI {
    const unsigned r8 = ln >> 3, c8 = ln & 7, sa = (r8 >> 1) & 1, lc = c8 ^ (4 * sa);
    vx0 = (16 * w + r8) * XROW + ((c8 ^ (r8 >> 1)) << 4); vx1 = (vx0 ^ 64u) + 8 * XROW;
    const unsigned cg = attn ? lc : (c8 ^ (2 * sa)), cu = attn ? (lc >= 6 ? lc - 2 : lc) : (cg ^ 4);
    vG = (8 * w + r8) * wstr + (cg << 4); vU = (8 * w + r8) * wstr + (cu << 4);
    vKV = (8 * w + r8) * QROW + 896 * (1 + (lc >> 2)) + ((lc & 3) << 4) + ((56 * head) & ~15u);
  };
  voffs(lane);
  const unsigned lw1 = LB + w * 1024, lw2 = LB + w * 2048;
  const unsigned rot = ((0x124924u >> (2 * rsum)) & 3) * 2;
  const unsigned srow = 128 * rb + 64 * half + 8 * cp + w;
  unsigned vrow = lane < 56 ? lane * 16 : 0x80000000u;
  asm volatile("s_waitcnt lgkmcnt(0)" : "+s"(ka), "+s"(kb), "+v"(vx0), "+v"(vx1), "+v"(vG), "+v"(vU), "+v"(vKV), "+v"(vrow));
  const char *px = (const char*)ka[0], *pqkv = (const char*)ka[1], *pwg = (const char*)ka[2], *pwu = (const char*)ka[3], *pphi = (const char*)ka[5],
             *pwd = (const char*)ka[6], *pwo = (const char*)ka[7];
  char *ws = (char*)ka[4], *pout = (char*)kb;
  const rsrc_t rsX = mkrs(px, 2048 * XROW), rsQ = mkrs(pqkv, 448 * QROW);
  const rsrc_t rsG = mkrs(attn ? pqkv : pwg, 448 * wstr), rsU = mkrs(attn ? pqkv + 128 : pwu, 448 * wstr - (attn ? 128 : 0));
  auto cidx = [&](int st) AI -> unsigned { unsigned t = rot + st; return t >= 7 ? t - 7 : t; };
  auto issueA = [&](int st, int k) AI {
    const unsigned sl = st & 3, ch = cidx(st);
    if (k == 0) dma(lw2, sl * 16384, vx0, rsX, sX0 + ch * 128, 0);
    if (k == 1) dma(lw2, sl * 16384 + 1024, vx1, rsX, sX0 + ch * 128, 0);
    if (k == 2) dma(lw1, L_GU + sl * 16384, vG, rsG, sG0 + ch * 64 * wstr, 0);
    if (k == 3) dma(lw1, L_GU + sl * 16384 + 8192, vU, rsU, sG0 + ch * 64 * wstr, 0);
    if (k == 4) dma(lw1, L_KV + sl * 8192, vKV, rsQ, ch * 64 * QROW, 0);
  };
  const rsrc_t rsWS = mkrs(ws, WS_END);
  unsigned Ev, cpf; u32x4 xrw;
  asm volatile("buffer_load_dwordx4 %0, %1, %2, %3 offen" : "=v"(xrw) : "v"(vrow), "s"(rsX), "s"(srow * XROW) : "memory");
  asm volatile("buffer_load_dword %0, off, %1, 0 sc1" : "=v"(Ev) : "s"(rsWS) : "memory");
  {
    unsigned long long pc; asm volatile("s_getpc_b64 %0" : "=s"(pc));
    const unsigned vo = w == 0 && lane < 24 ? (bi * 24 + lane) * 128 : 0x80000000u;
    asm volatile("buffer_load_dword %0, %1, %2, 0 offen" : "=v"(cpf) : "v"(vo), "s"(mkrs((const void*)pc, CODE_BYTES)) : "memory");
  }
#pragma unroll
  for (int k = 0; k < 5; k++) issueA(0, k);
  asm volatile("s_waitcnt vmcnt(5)" : "+v"(xrw), "+v"(Ev), "+v"(cpf) :: "memory");
#pragma unroll
  for (int k = 0; k < 5; k++) issueA(1, k);
  issueA(2, 0);
  const unsigned E = __builtin_amdgcn_readfirstlane(Ev);
  {
    float s1 = 0.f, s2 = 0.f;
#pragma unroll
    for (int i = 0; i < 4; i++) { s1 = dot2(xrw[i], 0x3f803f80u, s1); s2 = dot2(xrw[i], xrw[i], s2); }
    s1 = wavesum63(s1); s2 = wavesum63(s2);
    const float mean = s1 / 448.f, var = __builtin_fmaxf(s2 * (1.f / 448.f) - mean * mean, 0.f);
    u32x2 gr = {pk2(-mean, __builtin_amdgcn_sqrtf(rbf(rbf(var) + 1e-5f))), E + 1};
    __builtin_amdgcn_raw_buffer_store_b64(gr, rsWS, lane == 63 ? 0u : 0x80000000u, WS_GR + srow * 8, 16);
  }

  auto prog = [&](auto at_, auto gg_) AI {
  constexpr bool AT = decltype(at_)::value; constexpr int GG = decltype(gg_)::value;
  constexpr unsigned lcstep = AT ? 224 : 128;
  unsigned lane;
  asm volatile("v_mbcnt_lo_u32_b32 %0, -1, 0\n\tv_mbcnt_hi_u32_b32 %0, -1, %0" : "=v"(lane));
  __builtin_assume(lane < 64);
  voffs(lane);
  auto issueL = [&](int hc) AI {
    const int st = hc >> 1; const unsigned ch = cidx(st);
    if (hc & 1) dma(lw1, lc1_base(st) + 8192, vU, rsU, sG0 + ch * 64 * wstr, lcstep);
    else dma(lw1, lc1_base(st), vG, rsG, sG0 + ch * 64 * wstr, lcstep);
  };
  auto issueG = [&](int bar, int k) AI {
    if (bar == 0) issueA(2 + k / 5, k % 5);
    else if (bar <= 3) issueA(bar + 3, k);
    else if (k < 4) issueL(4 * (bar - 4) + k);
    else if (bar == 4) dma(LB + (w & 1) * 1024, L_WPK, lane * 16, mkrs(pphi, 16 * 1568), head * 1568 + (w & 1) * 1024, 0);
    else issueL(12 + (bar - 5));
  };

  const unsigned rt = 4 * rb + wr;
  const unsigned sA2 = WS_A2 + rt * 144 * 1024 + (AT ? half * 16384 : c * 8192), vL16 = lane * 16;
  const u32x4 sent = {~0u, ~0u, ~0u, ~0u};
  auto clr = [&](int ks) AI { st16(sent, rsWS, vL16, sA2, ks * 1024); };

  auto clri = [&](int q) AI { clr(GG ? (q < 6 ? 114 + q : 120 + q) : (q < 2 ? 112 + q : 118 + q)); };

  const bool hh = lane >= 32;
  const unsigned hu = lane >> 5, blk = (lane >> 4) & 1, q4 = (lane >> 2) & 3, p4 = lane & 3, ta = (q4 >> 1) & 1, l31 = lane & 31;
  const unsigned perm = wr ^ (2 * GG);
  const unsigned rowoff = (8 * hu + q4) * 128 + (perm << 11);
  const unsigned pc = (4 * blk + p4) < 6 ? (4 * blk + p4) : 6;
  auto tileaddr = [&](unsigned t) AI -> unsigned {
    if constexpr (AT) { unsigned o = 56 * t + 8 * pc; return (o >> 7) * 8192 + ((((o >> 4) & 7) ^ (4 * ta)) << 4) + (o & 8) + rowoff; }
    else return blk * 8192 + (((2 * t + (p4 >> 1)) ^ (4 * blk + 2 * ta)) << 4) + 8 * (p4 & 1) + rowoff;
  };
  constexpr int NT = !AT ? 2 : GG ? 1 : 3, TB0 = GG ? 4 - NT : 0;
  unsigned aA[3], aB[NT];
  {
    unsigned o = (head & 1) * 8 + 8 * pc;
    unsigned ak = L_KV + (((o >> 4) ^ (4 * ta)) << 4) + (o & 8) + rowoff;
    aA[0] = LB + (GG ? L_GU + tileaddr(1) : ak);
    aA[1] = LB + (GG ? L_GU + tileaddr(2) : (ak ^ 64u));
    aA[2] = LB + L_GU + tileaddr(GG ? 3 : 0);
#pragma unroll
    for (int j = 0; j < NT; j++) aB[j] = LB + tileaddr(TB0 + j);
  }
  const unsigned xrow = 32 * wr + l31;
  const unsigned aX = LB + xrow * 128 + (((2 * perm + hu) ^ ((xrow >> 1) & 7)) << 4);
  const unsigned selv = (((lane >> 4) ^ (lane >> 2)) & 1) ? 0u : 0x3f803f80u;
  const u32x4 sel4 = {selv, selv, selv, selv};
  const bf16x8 sel = __builtin_bit_cast(bf16x8, sel4);

  bf16x8 xf[28], wa[2][3], wq[8];
  f32x16 accA[3], accB[NT];
  f32x4 csA[3], csB[NT];
#pragma unroll
  for (int j = 0; j < 3; j++) { for (int i = 0; i < 16; i++) accA[j][i] = 0.f; for (int i = 0; i < 4; i++) csA[j][i] = 0.f; }
#pragma unroll
  for (int j = 0; j < NT; j++) { for (int i = 0; i < 16; i++) accB[j][i] = 0.f; for (int i = 0; i < 4; i++) csB[j][i] = 0.f; }
  u32x2 grn;

  const rsrc_t rsWD = mkrs(pwd, 1792 * XROW), rsWO = mkrs(pwo, 448 * XROW);
  const unsigned vW2 = (lane >> 3) * XROW + (((lane & 7) ^ ((lane >> 2) & 4)) << 4) + ci * 128;
  constexpr int NS = GG ? 16 + XS : 20 - XS, NC = 6 - XS / 2, NO = 2 * NC;
  const unsigned rho = (rbl + ci) & 3;
  auto t_of = [](int i) constexpr { return (0x54763210u >> (4 * ((i >> 1) & 7))) & 7; };
  constexpr int NE = GG ? 9 : 5;
  auto g1i = [](int i) constexpr { return i < 8 ? i : i == 8 ? 16 : i < 17 ? i - 1 : 17; };
  auto ksb = [&](int i) AI -> unsigned {
    if (GG) i = g1i(i);
    if (GG) return i < 16 ? 16 * wr + ((t_of(i) & 3) ^ rho) : wr;
    if (i < NO) { const unsigned cc = i % NC + rho + (XS ? 0 : rho >> 1); return wr + 8 * (cc >= NC ? cc - NC : cc); }
    return 8 * wr + 2 * ((i - NO) >> 1);
  };
  auto ksi = [&](int i) AI -> unsigned { if (GG) i = g1i(i); return GG ? (i < 16 ? 8 * (i & 1) + (t_of(i) & 4) : 4 * i) : i < NO ? 64 + 4 * XS + 4 * (i / NC) : 112 + (i & 1); };
  auto issueW2 = [&](int i, int hf) AI {
    if (GG == 0 && i >= NO) dma(lw2, ring_area(i % RING) + hf * 1024, vW2, rsWO, ksb(i) * 14 * XROW, (16 * (i & 1) + 8 * hf) * XROW);
    else dma(lw2, ring_area(i % RING) + hf * 1024, vW2, rsWD, ksb(i) * 16 * XROW, (ksi(i) * 16 + 8 * hf) * XROW);
  };

  const unsigned aCSw = LB + L_CS + l31 * 16 + wr * 4 + GG * 2048, aCSr = LB + L_CS + l31 * 16 + GG * 2048;
  auto readX = [&](int n) AI { xf[n] = ldsr<bf16x8>(aX ^ (unsigned)((n & 3) << 5), ((n >> 2) & 3) * 16384); };
  auto readA = [&](int n, int j) AI {
    const unsigned o = ((n >> 2) & 3) * ((GG == 0 && j < 2) ? 8192 : 16384);
    wa[n & 1][j] = tr2(aA[j] ^ (unsigned)((n & 3) << 11), o, o + 512);
  };
  auto readB = [&](int q) AI {
    const int n = q / NT, j = q % NT;
    const unsigned base = lc1_base(n >> 2);
    if ((NT == 3 && n < XN) ? (n & 3) == 0 : n == 24) asm volatile("" : "+v"(aB[j]));
    wq[q & 7] = tr2(aB[j] ^ (((n & 3) << 11) | (base & 0x30000u)), base & 0xffffu, (base & 0xffffu) + 512);
  };
  RowC rc; unsigned nm, denb;
  auto mkxe = [&]() AI { asm volatile("" : "+v"(nm)); u32x4 t = {nm, nm, nm, nm}; return __builtin_bit_cast(bf16x8, t); };
  auto rowconst = [&]() AI {
#pragma nounroll
    for (int it = 0; it < (1 << 16) && !ALL(grn[1] == E + 1); ++it) {
      __builtin_amdgcn_s_sleep(SLEEPN);
      asm volatile("buffer_load_dwordx2 %0, %1, %2, %3 offen sc1\n\ts_waitcnt vmcnt(0)" : "=v"(grn) : "v"(xrow * 8), "s"(rsWS), "s"(WS_GR + rb * 1024) : "memory");
    }
    const float den = U2F(grn[0] & 0xffff0000u);
    nm = hh ? 0u : __builtin_amdgcn_perm(grn[0], grn[0], 0x01000100u);
    denb = grn[0] >> 16;
    rc.r = __builtin_amdgcn_rcpf(den);
    const float k1 = -2.f * 1.4426950408889634f * 0.7978845608028654f;
    rc.K1 = k1 * rc.r; rc.K2 = (k1 * 0.044715f) * rc.r * (rc.r * rc.r); rc.Ri = den * den; rc.Lr = 1.4426950408889634f * rc.r;
  };
  WAITV(7);
  BAR(); MEMFENCE();
  readX(0); readA(0, 0); readA(0, 1); readA(0, 2);
  SFOR(n, 28)
    SB();
    if constexpr (n == 27) {
#pragma unroll
      for (int j = 0; j < 3; j++) ldsw<unsigned>(aCSw, j * 512, hilo(csA[j][0]));
    }
    if constexpr ((n & 3) == 3) {
      if constexpr (n == 27) WAITV(0);
      else if constexpr (n == 23) asm volatile("s_waitcnt vmcnt(%c1)" : "+v"(grn) : "i"(10 + (CTN >= 15 && CTN < 23)) : "memory");
      else if constexpr (n == 19) waitv<11 + (CTN >= 15 && CTN < 19)>();
      else if constexpr (n == 3 || n == 15) WAITV(11); else WAITV(10);
      { WAITL0(); BAR(); MEMFENCE(); }
    }
    constexpr int bar = (n + 1) >> 2, ph = (n + 1) & 3;
#pragma unroll
    for (int j = 0; j < 3; j++) {
      if constexpr (n < 27) { if (j == 0) readX(n + 1); readA(n + 1, j); }
      else { wa[0][j] = ldsr<bf16x8>(aCSr, j * 512); if (AT && GG == 1 && j < 2) { readB(2 * j); readB(2 * j + 1); } }
      SB();
      accA[j] = MFMA(wa[n & 1][j], xf[n], accA[j]);
      const int slot = 3 * ph + j;
      if (bar == 0) issueG(0, slot - 2);
      else if (bar == 6) { if (slot < 5) issueG(bar, slot); }
      else if (bar <= 6) { if (slot == 0) issueG(bar, 0); if (slot == 2) issueG(bar, 1); if (slot == 5) issueG(bar, 2); if (slot == 7) issueG(bar, 3); if (slot == 10) issueG(bar, 4); }
      else if (j < 2) {
        if constexpr (AT) dma(lw2, L_FREE + j * 1024, lane * 16, mkrs(pphi, 16 * 1568), half * 8 * 1568 + w * 2048, j * 1024);
        else issueW2(0, j);
      }
      if ((n & 3) == 0) csA[j] = MFMA16(sel, wa[n & 1][j], csA[j]);
      if (n == CTN && j == 1) {
        const unsigned per = (w & 1) ? 13 : 49;
        dmat(LB + L_ST + w * 256, (w >> 1) == 3 && lane < per ? (b * per + lane) * 128 : 0x80000000u, mkrs((w & 1) ? pwo : pwd, ((w & 1) ? 448 : 1792) * XROW));
      }
      if (n == 25 && j == 0) rowconst();
      if (n == 14 && j == 2) asm volatile("buffer_load_dwordx2 %0, %1, %2, %3 offen sc1" : "=v"(grn) : "v"(xrow * 8), "s"(rsWS), "s"(WS_GR + rb * 1024) : "memory");
      SB();
    }
  SEND
  { const bf16x8 xe = mkxe();
#pragma unroll
  for (int j = 0; j < 3; j++) accA[j] = MFMA(wa[0][j], xe, accA[j]); }
  asm volatile("" : "+v"(accA[0]), "+v"(accA[1]), "+v"(accA[2]));
  SB();

  const unsigned sFown = WS_FH + (rt * 32 + (AT ? 28 + 2 * half : 2 * c) + GG) * FS;
  const unsigned aWP = LB + (4 * hu + q4) * 56 + 8 * pc;
  const unsigned T12 = (E + 1) & 0xfffu, T16 = (E + 1) & 0xffffu;
  const unsigned sF = WS_FH + rt2 * 32 * FS;
  const unsigned fc = l31 >> 1;
  const bool nd1 = GG ? (fc >> 1) == wr || (fc >= 8 && fc < 8 + XS / 2) : (fc >= 8 + XS / 2 && fc < 14) || fc == 14 + (wr >> 1);
  const unsigned aIW = LB + L_PHI + xrow * 64 + ((hu ^ ((xrow >> 1) & 7)) << 3);
  SmS sm;
  unsigned aWK = aWP + L_WPK, aWQ = aWP + L_FREE;
  asm volatile("" : "+v"(aWK)); if constexpr (AT) asm volatile("" : "+v"(aWQ));
  auto kstep = [&](auto s_) AI {
    constexpr int s = decltype(s_)::value;
    if constexpr (s == 0) { sm.w0 = tr2(aWK, 0, 8 * 56); sm.w1 = tr2(aWK, 16 * 56, 24 * 56); }
    if constexpr (s == 1) sm.p = featproj2(accA[0], sm.w0, sm.w1, hh);
    if constexpr (s == 2 || s == 3) {
#pragma unroll
      for (int k = 2 * s - 4; k < 2 * s - 2; k++) {
        u32x2 vv = {pk2(accA[1][4 * k], accA[1][4 * k + 1]), pk2(accA[1][4 * k + 2], accA[1][4 * k + 3])};
        if (k == 3) vv[0] = hh ? denb : vv[0];
        ldsw<u32x2>(aIW ^ (unsigned)(k << 4), L_V - L_PHI, vv);
      }
    }
    if constexpr (s == 4) sm.bx = -max16(sm.p) * rc.Lr;
    if constexpr (s == 5) sm.bn = min16(sm.p) * rc.Lr;
    if constexpr (s >= 6 && s < 22) {
      constexpr int i = s - 6;
      sm.e[i] = __builtin_amdgcn_exp2f(__builtin_fmaf(sm.p[i], rc.Lr, sm.bx)); sm.en[i] = __builtin_amdgcn_exp2f(__builtin_fmaf(sm.p[i], -rc.Lr, sm.bn));
      if constexpr (i == 0) { sm.sp = sm.e[0]; sm.sn = sm.en[0]; } else if constexpr (i < 12) { sm.sp += sm.e[i]; sm.sn += sm.en[i]; }
      else if constexpr (i == 12) { sm.tp = sm.e[12]; sm.tn = sm.en[12]; } else { sm.tp += sm.e[i]; sm.tn += sm.en[i]; }
    }
    if constexpr (s == 22) { sm.sp = __builtin_amdgcn_rcpf(xsum(sm.sp + (hh ? 0.f : sm.tp))) * rc.r; sm.sn = __builtin_amdgcn_rcpf(xsum(sm.sn + (hh ? 0.f : sm.tn))) * rc.r; }
    if constexpr (s >= 23) {
#pragma unroll
      for (int u = (s == 29 ? 6 : s - 23); u <= (s == 29 ? 7 : s - 23); u++) {
        const int k = u >> 1;
        if (u & 1) { u32x2 t = {pk2s(sm.en[4 * k] * sm.sn, sm.en[4 * k + 1] * sm.sn), pk2s(sm.en[4 * k + 2] * sm.sn, sm.en[4 * k + 3] * sm.sn)}; ldsw<u32x2>(aIW ^ (unsigned)(k << 4), 8192, t); }
        else { u32x2 t = {pk2s(sm.e[4 * k] * sm.sp, sm.e[4 * k + 1] * sm.sp), pk2s(sm.e[4 * k + 2] * sm.sp, sm.e[4 * k + 3] * sm.sp)}; ldsw<u32x2>(aIW ^ (unsigned)(k << 4), 0, t); }
      }
    }
  };
  auto qstep = [&](auto s_, f32x16& a, unsigned lh) AI {
    constexpr int s = decltype(s_)::value;
    if constexpr (s == 0) { sm.w0 = tr2(aWQ, lh * 1568, lh * 1568 + 8 * 56); sm.w1 = tr2(aWQ, lh * 1568 + 16 * 56, lh * 1568 + 24 * 56); }
    if constexpr (s == 1) sm.p = featproj2(a, sm.w0, sm.w1, hh);
    if constexpr (s == 3) sm.bx = -max16(sm.p) * rc.Lr;
    if constexpr (s == 4) sm.bn = min16(sm.p) * rc.Lr;
    if constexpr (s >= 5 && s < 21) {
      constexpr int i = (s - 5) >> 1;
      float t0, t1;
      if constexpr ((s - 5) & 1) { t0 = __builtin_fmaf(sm.p[2 * i], -rc.Lr, sm.bn); t1 = __builtin_fmaf(sm.p[2 * i + 1], -rc.Lr, sm.bn); }
      else { t0 = __builtin_fmaf(sm.p[2 * i], rc.Lr, sm.bx); t1 = __builtin_fmaf(sm.p[2 * i + 1], rc.Lr, sm.bx); }
      a[8 * ((s - 5) & 1) + i] = U2F(pk2(__builtin_amdgcn_exp2f(t0), __builtin_amdgcn_exp2f(t1)));
      if constexpr (s == 20) asm volatile("" : "+v"(a));
    }
  };
  bf16x8 ka_[4], kb_[2][4]; f32x4 kd[2];
  auto kvstep = [&](auto s_) AI {
    constexpr int s = decltype(s_)::value;
    if constexpr (s == 0) {
      const unsigned g4 = lane >> 4, r0 = 8 * g4 + q4, sw = 4 * blk + (q4 >> 1);
      unsigned b0 = LB + L_PHI + r0 * 64 + ((p4 ^ sw) << 3);
      unsigned a0 = (b0 ^ ((wr >> 1) << 5)) + (wr & 1) * 8192;
#pragma unroll
      for (int k = 0; k < 4; k++) {
        ka_[k] = __builtin_shufflevector(trd2(a0, k * 2048), trd2(a0 ^ 16u, k * 2048 + 256), 0, 1, 2, 3, 4, 5, 6, 7);
        kb_[0][k] = __builtin_shufflevector(trd2(b0, L_V - L_PHI + k * 2048), trd2(b0 ^ 16u, L_V - L_PHI + k * 2048 + 256), 0, 1, 2, 3, 4, 5, 6, 7);
        kb_[1][k] = __builtin_shufflevector(trd2(b0 ^ 32u, L_V - L_PHI + k * 2048), trd2(b0 ^ 48u, L_V - L_PHI + k * 2048 + 256), 0, 1, 2, 3, 4, 5, 6, 7);
      }
    }
    if constexpr (s == 1) {
      f32x4 z = {0.f, 0.f, 0.f, 0.f}; kd[0] = z; kd[1] = z;
#pragma unroll
      for (int k = 0; k < 4; k++) { kd[0] = MFMA16(ka_[k], kb_[0][k], kd[0]); kd[1] = MFMA16(ka_[k], kb_[1][k], kd[1]); }
    }
    if constexpr (s == 3) {
#pragma unroll
      for (int d = 0; d < 2; d++) {
        u32x4 t;
#pragma unroll
        for (int i = 0; i < 4; i++) t[i] = (F2U(kd[d][i]) & ~0xfffu) | T12;
        st16(t, rsWS, (lane >> 4) * 512 + (lane & 15) * 16, ((head * 16 + rb) * 2 + (wr & 1)) * 4096 + (wr >> 1) * 2048, WS_KVP + d * 256);
      }
    }
  };

  unsigned fl1 = 0;
  u32x4 pv[8];
  const unsigned vKP = l31 * 16 + hu * 8192, sKP = (rb * 32 + half) * 4096 + wr * 1024 + GG * 512, sKR = (8 * half + w) * 8192;
  auto hop1 = [&]() AI {
    waittag<16384>(pv, rsWS, vKP, sKP + WS_KVP, T12, 0xfffu);
    u32x4 o;
#pragma unroll
    for (int k = 0; k < 4; k++) {
      float sum = U2F(pv[0][k]);
#pragma unroll
      for (int i = 1; i < 8; i++) sum += U2F(pv[i][k]);
      sum = xsum(sum);
      sum = l31 == 29 ? 1.f : sum;
      if (GG == 1) sum = wr == 3 ? 0.f : sum;
      o[k] = (pk2s(sum, sum) & 0xffff0000u) | T16;
    }
    st16(o, rsWS, hh ? 0x80000000u : l31 * 16, (rb * 2 + half) * 4096 + wr * 1024 + GG * 512, WS_KVR);
    if (b == 0 && w == 4) stflag(E + 4, rsWS, WS_EPOCH);
    SB();
  };
  bf16x8 cb[NT];
  auto ldflag = [&]() AI { asm volatile("buffer_load_dword %0, %1, %2, %3 offen sc1" : "=v"(fl1) : "v"(nd1 ? lane * FS : 0x80000000u), "s"(rsWS), "s"(sF) : "memory"); };
  auto kpath = [&]() AI {
    const f32x16 p = featproj(accA[0], aWK, 0, hh);
#pragma unroll
    for (int k = 0; k < 4; k++) {
      u32x2 vv = {pk2(accA[1][4 * k], accA[1][4 * k + 1]), pk2(accA[1][4 * k + 2], accA[1][4 * k + 3])};
      if (k == 3) vv[0] = hh ? denb : vv[0];
      ldsw<u32x2>(aIW ^ (unsigned)(k << 4), L_V - L_PHI, vv);
    }
    const f32x2 bx = dup2(-max16(p) * rc.Lr), bn = dup2(min16(p) * rc.Lr), L2 = dup2(rc.Lr);
    f32x2 ep[8], en[8];
#pragma unroll
    for (int i = 0; i < 8; i++) { ep[i] = exp2v(fma2(sub2(p, i), L2, bx)); en[i] = exp2v(fma2(sub2(p, i), -L2, bn)); }
    f32x2 sp = ((ep[0] + ep[1]) + (ep[2] + ep[3])) + (ep[4] + ep[5]), sn = ((en[0] + en[1]) + (en[2] + en[3])) + (en[4] + en[5]);
    f32x2 tp = ep[6] + ep[7], tn = en[6] + en[7];
    float a0 = sp[0], a1 = sn[0], a2 = tp[0], a3 = tn[0];
    asm("" : "+v"(a0), "+v"(a1), "+v"(a2), "+v"(a3));
    a0 += sp[1]; a2 += tp[1]; asm("" : "+v"(a0), "+v"(a2)); a1 += sn[1]; a3 += tn[1];
    const f32x2 ip = dup2(__builtin_amdgcn_rcpf(xsum(a0 + (hh ? 0.f : a2))) * rc.r), in = dup2(__builtin_amdgcn_rcpf(xsum(a1 + (hh ? 0.f : a3))) * rc.r);
#pragma unroll
    for (int k = 0; k < 4; k++) {
      const f32x2 u0 = ep[2 * k] * ip, u1 = ep[2 * k + 1] * ip, v0 = en[2 * k] * in, v1 = en[2 * k + 1] * in;
      u32x2 t0 = {pk2(u0[0], u0[1]), pk2(u1[0], u1[1])}, t1 = {pk2(v0[0], v0[1]), pk2(v1[0], v1[1])};
      ldsw<u32x2>(aIW ^ (unsigned)(k << 4), 0, t0); ldsw<u32x2>(aIW ^ (unsigned)(k << 4), 8192, t1);
    }
  };
  const unsigned sA = WS_A2 + rt2 * 144 * 1024;
  u32x4 af[NS][2];
  auto loadA1 = [&](int i, int m) AI { af[i][m] = ld16(rsWS, vL16, sA + ksb(i) * 1024, (144 * m + ksi(i)) * 1024); };
  auto stream = [&]() AI {
    SFOR(q, 28 * NT)
      constexpr int n = q / NT, j = q % NT;
      if constexpr (j == 0) {
        if constexpr (n == 27) {
#pragma unroll
          for (int t = 0; t < NT; t++) ldsw<unsigned>(aCSw, ((3 + t) & 3) * 512, hilo(csB[t][0]));
          if constexpr (AT && GG) WAITV(8);
          WAITL0(); BAR(); MEMFENCE();
        }
        else if constexpr (!AT && (n & 3) == 3) { asm volatile("s_waitcnt lgkmcnt(4)" ::: "memory"); BAR(); MEMFENCE(); }
      }
      if constexpr (q + 4 < 28 * NT) readB(q + 4);
      if constexpr (n == 27) cb[j] = ldsr<bf16x8>(aCSr, ((3 + j) & 3) * 512);
      SB();
      accB[j] = MFMA(wq[q & 7], xf[n], accB[j]);
      if constexpr ((n & 3) == 1) csB[j] = MFMA16(sel, wq[q & 7], csB[j]);
      if constexpr (!AT) {
        if constexpr (j == 0 && n >= 3 && n < 7) issueW2(1 + ((n - 3) >> 1), (n - 3) & 1);
        if constexpr (j == 0 && n >= 7 && n < 27 && (n & 1)) issueW2(1 + ((n + 1) >> 2), ((n + 1) >> 1) & 1);
        if constexpr (n == 27) issueW2(8, j);
        if constexpr (j == 1 && n == F1N) { waitv<(F1N < 7 ? F1N - 2 : 4 + (F1N - 5) / 2)>(); stflag(E + 1, rsWS, sFown); }
        if constexpr (q == 2 * CK1 - 7) ldflag();
        if constexpr (q == 2 * CK1 - 1) {
          asm volatile("s_waitcnt vmcnt(%c5)" : "+v"(fl1), "+v"(accB[0]), "+v"(accB[NT - 1]), "+v"(csB[0]), "+v"(csB[NT - 1]) : "i"((CK1 & 1) ? 1 : 2) : "memory");
          const bool nde = nd1 && fc < 14;
          if (!ALL(!nde || (fl1 - E - 1u) <= 1u)) poll(rsWS, lane * FS, sF, E, nde ? 1u : 0u);
        }
        if constexpr (q >= 2 * CK1 && q < 2 * CK1 + 2 * NE) loadA1((q - 2 * CK1) >> 1, q & 1);
      } else {
        if constexpr (GG == 1 && n < 16 && (n & 1) == 0) clri(n >> 1);
        if constexpr (GG == 0 && j < 2 && n >= 1 && 2 * (n - 1) + j < 21) qstep(IntC<2 * (n - 1) + j>{}, accA[2], 0);
        if constexpr (GG == 0 && q >= 3 * H0L && q < 3 * H0L + 8) pv[q - 3 * H0L] = ld16(rsWS, vKP, sKP, WS_KVP + (q - 3 * H0L) * 16384);
        if constexpr (GG == 0 && j == 2 && n == H0C) { asm volatile("" : "+v"(accB[0]), "+v"(accB[1]), "+v"(accB[NT - 1]), "+v"(csB[0]), "+v"(csB[1]), "+v"(csB[NT - 1])); hop1(); }
      }
      SB();
    SEND
    const bf16x8 xe = mkxe();
#pragma unroll
    for (int t = 0; t < NT; t++) { accB[t] = MFMA(cb[t], xe, accB[t]); asm volatile("" : "+v"(accB[t])); }
    SB();
  };
  unsigned ln = vL16, aW = 0;
  auto mkaw = [&]() AI {
    asm volatile("" : "+v"(ln));
    aW = lw2 + (ln & 0x200) + ((ln & 0xc0) << 1) + ((((ln >> 3) & 0x20) | ((ln >> 1) & 0x10)) ^ ((ln >> 1) & 0x40)) + ((ln >> 1) & 8);
  };
  bf16x8 wf[NS][2];
  auto readW = [&](int i) AI {
    const unsigned ra = ring_area(i % RING), la = aW + (ra & 0x30000u);
    wf[i][0] = tr2(la, ra & 0xffffu, (ra & 0xffffu) + 1024); wf[i][1] = tr2(la ^ 64u, ra & 0xffffu, (ra & 0xffffu) + 1024);
  };
  constexpr int NL = 2 * CK1 + 2 * NE > 56 ? 2 * CK1 + 2 * NE - 56 : 0;
  auto tile = [&](f32x16& a, int t) AI { asm volatile("" : "+v"(a)); st16(epiP(a, rc), rsWS, vL16, sA2, t * 1024); SB(); };
  if constexpr (!AT) {
    if constexpr (GG == 0) {
      kpath(); WAITL0(); BARF();
      kvstep(IntC<0>{}); kvstep(IntC<1>{}); kvstep(IntC<3>{}); SB();
      tile(accA[2], 0);
    } else { tile(accA[0], 1); tile(accA[1], 2); BARF(); tile(accA[2], 3); }
    readB(0); readB(1); readB(2); readB(3);
    stream();
    auto rest = [&]() AI { SFOR(u, NL) loadA1((56 + u - 2 * CK1) >> 1, u & 1); SEND };
    auto recy = [&]() AI { if constexpr (RC > 0) { WAITL0(); SB(); SFOR(i, RC) issueW2(9 + i, 0); issueW2(9 + i, 1); SEND } };
    mkaw();
    SFOR(i, RC) readW(i); SEND
    tile(accB[0], 4 + 2 * GG);
    recy();
    tile(accB[NT - 1], 5 + 2 * GG);
    rest();
  } else if constexpr (GG == 0) {
    SFOR(q, 8) clri(q); SEND
    SFOR(s, 30) kstep(s_); SEND
    WAITV(8);
    WAITL0(); BARF();
    kvstep(IntC<0>{});
    readB(0); readB(1); readB(2); readB(3);
    kvstep(IntC<1>{}); kvstep(IntC<3>{});
    WAITV(2); stflag(E + 1, rsWS, sFown);
    SB();
    stream();
  } else {
    stream();
    SFOR(s, 21) qstep(s_, accA[0], 1); SEND
    WAITV(0); stflag(E + 1, rsWS, sFown);
    SFOR(s, 21) qstep(s_, accA[1], 2); SEND
    SFOR(s, 21)
      if constexpr (s < 8) pv[s] = ld16(rsWS, vKP, sKP, WS_KVP + s * 16384);
      qstep(s_, accA[2], 3);
    SEND
    hop1();
    BARF();
  }

  if constexpr (AT) {
    auto qexp = [&](f32x16& a, unsigned lh) AI { a = dualexpP(featproj(a, aWQ, lh * 1568, hh), rc.Lr); asm volatile("" : "+v"(a)); SB(); };
    if constexpr (GG == 0) qexp(accB[0], 4);
#pragma unroll
    for (int i = 0; i < 8; i++) pv[i] = ld16(rsWS, vL16, sKR, WS_KVR + i * 1024);
    if constexpr (GG == 0) { qexp(accB[1 % NT], 5); qexp(accB[NT - 1], 6); } else qexp(accB[0], 7);
    waittag<1024>(pv, rsWS, vL16, sKR + WS_KVR, T16, 0xffffu);
#pragma unroll
    for (int i = 0; i < 4; i++) {
      u32x4 f = {__builtin_amdgcn_perm(pv[2 * i][1], pv[2 * i][0], 0x07060302u), __builtin_amdgcn_perm(pv[2 * i][3], pv[2 * i][2], 0x07060302u),
                 __builtin_amdgcn_perm(pv[2 * i + 1][1], pv[2 * i + 1][0], 0x07060302u), __builtin_amdgcn_perm(pv[2 * i + 1][3], pv[2 * i + 1][2], 0x07060302u)};
      ldsw<u32x4>(LB + w * 4096 + vL16, i * 1024, f);
    }
    WAITL0(); BAR(); MEMFENCE();
    f32x16 op[4], on[4];
    auto omm = [&](const f32x16& phi, unsigned lh, int z) AI {
#pragma unroll
      for (int i = 0; i < 16; i++) { op[z][i] = 0.f; on[z][i] = 0.f; }
      bf16x8 kf[4];
#pragma unroll
      for (int i = 0; i < 4; i++) kf[i] = ldsr<bf16x8>(LB + vL16, lh * 4096 + i * 1024);
#pragma unroll
      for (int i = 0; i < 2; i++) { op[z] = MFMA(kf[i], sub4(phi, i), op[z]); on[z] = MFMA(kf[2 + i], sub4(phi, 2 + i), on[z]); }
    };
    auto ofin = [&](unsigned lh, int z) AI {
      const float ia = __builtin_amdgcn_rcpf(op[z][13]), ib = __builtin_amdgcn_rcpf(on[z][13]);
      const float id = __builtin_amdgcn_rcpf(__builtin_fmaf(op[z][12], ia, __builtin_fmaf(on[z][12], ib, 1e-6f)));
      const float ca = xhi(ia * id), cb2 = xhi(ib * id);
      const f32x2 c2 = dup2(ca), d2 = dup2(cb2);
      u32x4 f0, f1;
#pragma unroll
      for (int i = 0; i < 4; i++) {
        const f32x2 u = fma2(sub2(op[z], i), c2, sub2(on[z], i) * d2), v = fma2(sub2(op[z], 4 + i), c2, sub2(on[z], 4 + i) * d2);
        f0[i] = pk2(u[0], u[1]); f1[i] = pk2(v[0], v[1]);
      }
      f1[2] = hh ? 0u : f1[2]; f1[3] = hh ? 0u : f1[3];
      st16(f0, rsWS, vL16, sA2, (112 + 2 * lh) * 1024); st16(f1, rsWS, vL16, sA2, (113 + 2 * lh) * 1024);
      SB();
    };
    if constexpr (GG) { omm(accA[0], 1, 0); omm(accA[1], 2, 1); SB(); ofin(1, 0); omm(accA[2], 3, 2); omm(accB[0], 7, 3); SB(); ofin(2, 1); ofin(3, 2); ofin(7, 3); }
    else { omm(accA[2], 0, 0); omm(accB[0], 4, 1); SB(); ofin(0, 0); omm(accB[1 % NT], 5, 2); omm(accB[NT - 1], 6, 3); SB(); ofin(4, 1); ofin(5, 2); ofin(6, 3); }
  } else {
    f32x16 acc[2][2];
#pragma unroll
    for (int i = 0; i < 4; i++)
#pragma unroll
      for (int k = 0; k < 16; k++) acc[i >> 1][i & 1][k] = 0.f;
    u32x4 xr;
    const unsigned vO = (ln >> 7) * XROW + (ln & 0x70), sO = (rt2 * 32 + 8 * w) * XROW + ci * 128;
    auto mm4 = [&](int i) AI {
#pragma unroll
      for (int nn = 0; nn < 2; nn++)
#pragma unroll
        for (int mm = 0; mm < 2; mm++) acc[nn][mm] = MFMA(wf[i][nn], __builtin_bit_cast(bf16x8, af[i][mm]), acc[nn][mm]);
      SB();
    };
    constexpr int NI = NE - NL / 2;
    waitv<2 + 2 * RC + NL>();
#pragma unroll
    for (int i = 0; i < NI; i++) asm volatile("" : "+v"(af[i][0]), "+v"(af[i][1]));
    SFOR(i, 9 - RC) readW(RC + i); SEND
    SB();
    SFOR(i, EB) mm4(i); SEND
    waitv<NL>(); stflag(E + 2, rsWS, sFown);
    WAITV(1);
#pragma unroll
    for (int i = NI; i < NE; i++) asm volatile("" : "+v"(af[i][0]), "+v"(af[i][1]));
    WAITL0(); SB();
    BARF();
    SB();
    if constexpr (GG) __builtin_amdgcn_s_setprio(1);
    constexpr int NB = (GG ? 9 : G0B) - RC;
    SFOR(i, 9)
      if constexpr (i == NB - 2) ldflag();
      if constexpr (i < NB) { issueW2(9 + RC + i, 0); issueW2(9 + RC + i, 1); SB(); }
      if constexpr (EB + i < NE) mm4(EB + i);
    SEND
    asm volatile("s_waitcnt vmcnt(4)" : "+v"(fl1) :: "memory");
    { const unsigned need = !nd1 ? 0u : fc < 14 ? 2u : 1u;
      if (!ALL(!nd1 || fl1 == E + need)) poll(rsWS, lane * FS, sF, E, need); }
#pragma unroll
    for (int j = NE; j < NE + PD; j++) { loadA1(j, 0); loadA1(j, 1); }
    asm volatile("buffer_load_dwordx4 %0, %1, %2, %3 offen" : "=v"(xr) : "v"(vO), "s"(rsX), "s"(sO) : "memory");
    SFOR(i, 9 - RC - NB) issueW2(9 + RC + NB + i, 0); issueW2(9 + RC + NB + i, 1); SEND
    SFOR(i1, NS - NE)
      constexpr int i = NE + i1;
      asm volatile("s_waitcnt vmcnt(%c2)" : "+v"(af[i][0]), "+v"(af[i][1]) : "i"(2 * ((i + PD <= NS ? i + PD : NS) - 1 - i) + (i1 < PD && i < NO + 8 * GG ? 1 + 2 * (9 - RC - NB) : 0)) : "memory");
      if constexpr (i == 9 && i1 == 0) readW(9);
      if constexpr (i + 1 >= 9 && i + 1 < NS) readW(i + 1);
      if constexpr (GG == 0 && i >= NO) {
        auto stale = [&]() AI {
          unsigned m = UMAX(UMAX(UMAX(af[i][0][0], af[i][0][1]), UMAX(af[i][0][2], af[i][0][3])), UMAX(UMAX(af[i][1][0], af[i][1][1]), UMAX(af[i][1][2], af[i][1][3])));
          return ANY(m == ~0u);
        };
        if (__builtin_expect(stale(), 0)) {
          int it = 0;
#pragma nounroll
          do {
            __builtin_amdgcn_s_sleep(SLEEPR);
            { SFOR(u, PD - 1) if constexpr (i + 1 + u < NS) rel1<false>(af[i + 1 + u][0], af[i + 1 + u][1], rsWS, vL16, sA + (ksb(i + 1 + u) + ksi(i + 1 + u)) * 1024); SEND }
            rel1<true>(af[i][0], af[i][1], rsWS, vL16, sA + (ksb(i) + ksi(i)) * 1024);
          } while (++it < (1 << 14) && stale());
        }
      }
      SB();
      if constexpr (i + PD < NS) { loadA1(i + PD, 0); loadA1(i + PD, 1); }
      mm4(i);
    SEND
    __builtin_amdgcn_s_setprio(0);
#pragma unroll
    for (int qd = 0; qd < 4; qd++)
#pragma unroll
      for (int k = 0; k < 4; k++) {
        const unsigned ra = ring_area(2 * qd + (k >> 1)) + (k & 1) * 1024;
        ldsw<bf16x8>(((lw2 + vL16) ^ (unsigned)(32 * (4 * (qd >> 1) + k))) + (ra & 0x30000u), ra & 0xffffu, sub4(acc[qd >> 1][qd & 1], k));
      }
    WAITL0(); BAR(); MEMFENCE();
    const unsigned c8 = (ln >> 4) & 7, j2 = (c8 & 4) + 2 * GG + ((c8 >> 1) & 1);
    const unsigned rd = LB + ((0x51407362u >> (4 * j2)) & 7) * 16384 + (c8 & 1) * 1024 + (((8 * wr + (ln >> 7)) << 4) ^ (c8 << 5));
    asm volatile("" : "+v"(xr));
    f32x4 sum[2];
#pragma unroll
    for (int k = 0; k < 2; k++) {
      f32x4 t = {U2F(xr[2 * k] << 16), U2F(xr[2 * k] & 0xffff0000u), U2F(xr[2 * k + 1] << 16), U2F(xr[2 * k + 1] & 0xffff0000u)};
#pragma unroll
      for (int v = 0; v < 8; v++) t += ldsr<f32x4>(rd, v * 2048 + k * 512);
      sum[k] = t;
    }
    u32x4 o = {pk2(sum[0][0], sum[0][1]), pk2(sum[0][2], sum[0][3]), pk2(sum[1][0], sum[1][1]), pk2(sum[1][2], sum[1][3])};
    __builtin_amdgcn_raw_buffer_store_b128(o, mkrs(pout, 2048 * XROW), vO + sO, 0, 0);
  }
  };
  if (!attn && g) prog(BoolC<false>{}, IntC<1>{});
  else if (!attn) prog(BoolC<false>{}, IntC<0>{});
  else if (g) prog(BoolC<true>{}, IntC<1>{});
  else prog(BoolC<true>{}, IntC<0>{});
}

extern "C" void kernel_launch(void* const* d_in, const int* in_sizes, int n_in, void* d_out, int out_size, void* d_ws, size_t ws_size,
                              hipStream_t stream) {
  fused_kernel<<<256, 512, 0, stream>>>((const char*)d_in[0], (const char*)d_in[1], (const char*)d_in[5], (const char*)d_in[6], (char*)d_ws,
                                        (const char*)d_in[2], (const char*)d_in[7], (const char*)d_in[3], (char*)d_out);
}
```

```cpp
#include <hip/hip_runtime.h>
#include <hip/hip_bf16.h>
#include <utility>

#define DI __device__ __forceinline__
#define LAS __attribute__((address_space(3)))
typedef __attribute__((ext_vector_type(8))) short bf16x8;
typedef __attribute__((ext_vector_type(4))) short s16x4;
typedef __attribute__((ext_vector_type(2))) __bf16 bf2;
typedef __attribute__((ext_vector_type(2))) float f32x2;
typedef __attribute__((ext_vector_type(4))) float f32x4;
typedef __attribute__((ext_vector_type(16))) float f32x16;
typedef __attribute__((ext_vector_type(2))) unsigned u32x2;
typedef __attribute__((ext_vector_type(4))) unsigned u32x4;
typedef __amdgpu_buffer_rsrc_t rsrc_t;
#define F2U(x) __builtin_bit_cast(unsigned, (float)(x))
#define U2F(x) __builtin_bit_cast(float, (unsigned)(x))
#define ALL(x) (__builtin_amdgcn_ballot_w64(!(x)) == 0ull)
#define ANY(x) (__builtin_amdgcn_ballot_w64(x) != 0ull)
#define UMAX(a, b) __builtin_elementwise_max((unsigned)(a), (unsigned)(b))

constexpr unsigned XROW = 896, QROW = 2688, FROW = 3584;
constexpr unsigned FS = 128;
constexpr unsigned WS_EPOCH = 0;
constexpr unsigned WS_GR = 4096;
constexpr unsigned WS_KVP = 131072;
constexpr unsigned WS_A2 = WS_KVP + 16 * 16 * 8192;
constexpr unsigned WS_FH = WS_A2 + 64 * 144 * 1024 + 262144;
constexpr unsigned WS_KVR = WS_FH + 2048 * FS;
constexpr unsigned WS_END = WS_KVR + 16 * 8192;
constexpr unsigned L_GU = 65536, L_KV = 131072;
constexpr unsigned L_TAIL = L_KV + 3 * 8192;
constexpr unsigned L_WPK = L_TAIL;
constexpr unsigned L_ST = L_TAIL + 2048;
constexpr unsigned L_CS = L_TAIL + 4096;
constexpr unsigned L_PHI = L_GU + 32768;
constexpr unsigned L_V = L_KV + 16384;
constexpr unsigned L_FREE = 32768;

#define MFMA(a, b, c) __builtin_amdgcn_mfma_f32_32x32x16_bf16((a), (b), (c), 0, 0, 0)
#define MFMA16(a, b, c) __builtin_amdgcn_mfma_f32_16x16x32_bf16((a), (b), (c), 0, 0, 0)
#define AI __attribute__((always_inline))
#define BAR() __builtin_amdgcn_s_barrier()
#define WAITV(n) asm volatile("s_waitcnt vmcnt(" #n ")" ::: "memory")
#define WAITL0() asm volatile("s_waitcnt lgkmcnt(0)" ::: "memory")
#define SB() __builtin_amdgcn_sched_barrier(0)
#define MEMFENCE() asm volatile("" ::: "memory")
#define BARF() do { MEMFENCE(); SB(); BAR(); MEMFENCE(); SB(); } while (0)

template <int N> DI void waitv() { asm volatile("s_waitcnt vmcnt(%c0)" :: "i"(N) : "memory"); }

DI rsrc_t mkrs(const void* p, unsigned bytes) { return __builtin_amdgcn_make_buffer_rsrc((void*)p, (short)0, (int)bytes, 0x00020000); }
DI void dma(unsigned lds, unsigned limm, unsigned voff, rsrc_t rs, unsigned soff, unsigned simm) {
  unsigned t;
  asm volatile("s_add_u32 m0, %1, %c5\n\ts_add_u32 %0, %4, %c6\n\tbuffer_load_dwordx4 %2, %3, %0 offen lds"
               : "=&s"(t) : "s"(lds), "v"(voff), "s"(rs), "s"(soff), "i"(limm), "i"(simm) : "memory", "scc");
}
DI void dmat(unsigned lds, unsigned voff, rsrc_t rs) {
  asm volatile("s_mov_b32 m0, %0\n\ts_nop 0\n\tbuffer_load_dword %1, %2, 0 offen lds" :: "s"(lds), "v"(voff), "s"(rs) : "memory");
}
DI s16x4 trd2(unsigned a, unsigned off) { return __builtin_amdgcn_ds_read_tr16_b64_v4i16((LAS s16x4*)((LAS char*)(size_t)a + off)); }
DI bf16x8 tr2(unsigned a, unsigned off0, unsigned off1) {
  LAS char* p = (LAS char*)(size_t)a;
  s16x4 lo = __builtin_amdgcn_ds_read_tr16_b64_v4i16((LAS s16x4*)(p + off0));
  s16x4 hi = __builtin_amdgcn_ds_read_tr16_b64_v4i16((LAS s16x4*)(p + off1));
  return __builtin_shufflevector(lo, hi, 0, 1, 2, 3, 4, 5, 6, 7);
}
template <class T> DI T ldsr(unsigned a, unsigned off) { return *(LAS T*)((LAS char*)(size_t)a + off); }
template <class T> DI void ldsw(unsigned a, unsigned off, T v) { *(LAS T*)((LAS char*)(size_t)a + off) = v; }
DI unsigned pk2(float a, float b) { f32x2 f = {a, b}; return __builtin_bit_cast(unsigned, __builtin_convertvector(f, bf2)); }
DI float rbf(float a) { return U2F(pk2(a, a) << 16); }
DI float dot2(unsigned a, unsigned b, float c) { return __builtin_amdgcn_fdot2_f32_bf16(__builtin_bit_cast(bf2, a), __builtin_bit_cast(bf2, b), c, false); }
DI float xsum(float v) { auto r = __builtin_amdgcn_permlane32_swap(F2U(v), F2U(v), false, false); float a = U2F(r[0]); asm("" : "+v"(a)); return a + U2F(r[1]); }
DI float xmax(float v) { auto r = __builtin_amdgcn_permlane32_swap(F2U(v), F2U(v), false, false); return __builtin_fmaxf(U2F(r[0]), U2F(r[1])); }
DI float xmin(float v) { auto r = __builtin_amdgcn_permlane32_swap(F2U(v), F2U(v), false, false); return __builtin_fminf(U2F(r[0]), U2F(r[1])); }
DI float xhi(float v) { auto r = __builtin_amdgcn_permlane32_swap(F2U(v), F2U(v), false, false); return U2F(r[1]); }
DI void st16(u32x4 v, rsrc_t rs, unsigned voff, unsigned soff, unsigned simm) {
  unsigned t;
  asm volatile("s_add_u32 %0, %4, %c5\n\tbuffer_store_dwordx4 %1, %2, %3, %0 offen sc1\n\ts_nop 1" : "=&s"(t) : "v"(v), "v"(voff), "s"(rs), "s"(soff), "i"(simm) : "memory", "scc");
}
DI void stflag(unsigned v, rsrc_t rs, unsigned soff) {
  unsigned voff = (__builtin_amdgcn_mbcnt_hi(~0u, __builtin_amdgcn_mbcnt_lo(~0u, 0u)) == 0u) ? 0u : 0x80000000u;
  __builtin_amdgcn_raw_buffer_store_b32(v, rs, voff, soff, 16);
}
constexpr int SLEEPN = 2, SLEEPR = 1;
DI u32x4 ld16(rsrc_t rs, unsigned voff, unsigned soff, unsigned simm) {
  u32x4 r; unsigned t;
  asm volatile("s_add_u32 %1, %4, %c5\n\tbuffer_load_dwordx4 %0, %2, %3, %1 offen sc1" : "=v"(r), "=&s"(t) : "v"(voff), "s"(rs), "s"(soff), "i"(simm) : "memory", "scc");
  return r;
}
template <unsigned STEP> DI void ld8w(u32x4 (&p)[8], rsrc_t rs, unsigned voff, unsigned soff) {
  unsigned t;
#define L1(k) "buffer_load_dwordx4 %" #k ", %9, %10, %8 offen sc1\n\ts_add_u32 %8, %8, %c12\n\t"
  asm volatile("s_mov_b32 %8, %11\n\t" L1(0) L1(1) L1(2) L1(3) L1(4) L1(5) L1(6) L1(7) "s_waitcnt vmcnt(0)"
               : "=&v"(p[0]), "=&v"(p[1]), "=&v"(p[2]), "=&v"(p[3]), "=&v"(p[4]), "=&v"(p[5]), "=&v"(p[6]), "=&v"(p[7]), "=&s"(t)
               : "v"(voff), "s"(rs), "s"(soff), "i"(STEP) : "memory", "scc");
#undef L1
}
DI bool untag(u32x4 (&p)[8], unsigned tag, unsigned mask) {
  unsigned chk = 0;
#pragma unroll
  for (int i = 0; i < 8; i++)
#pragma unroll
    for (int k = 0; k < 4; k++) { p[i][k] ^= tag; chk |= p[i][k]; }
  return ALL((chk & mask) == 0u);
}
template <unsigned STEP> DI void waittag(u32x4 (&p)[8], rsrc_t rs, unsigned voff, unsigned soff, unsigned tag, unsigned mask) {
  asm volatile("s_waitcnt vmcnt(0)" : "+v"(p[0]), "+v"(p[1]), "+v"(p[2]), "+v"(p[3]), "+v"(p[4]), "+v"(p[5]), "+v"(p[6]), "+v"(p[7]) :: "memory");
#pragma nounroll
  for (int it = 0; !untag(p, tag, mask) && it < (1 << 16); ++it) { __builtin_amdgcn_s_sleep(SLEEPN); ld8w<STEP>(p, rs, voff, soff); }
}
template <bool WAIT> DI void rel1(u32x4& a, u32x4& b, rsrc_t rs, unsigned voff, unsigned so) {
  unsigned t;
  if constexpr (WAIT) asm volatile("buffer_load_dwordx4 %0, %3, %4, %5 offen sc1\n\ts_add_u32 %2, %5, 147456\n\tbuffer_load_dwordx4 %1, %3, %4, %2 offen sc1\n\ts_waitcnt vmcnt(0)"
                                   : "+v"(a), "+v"(b), "=&s"(t) : "v"(voff), "s"(rs), "s"(so) : "memory", "scc");
  else asm volatile("buffer_load_dwordx4 %0, %3, %4, %5 offen sc1\n\ts_add_u32 %2, %5, 147456\n\tbuffer_load_dwordx4 %1, %3, %4, %2 offen sc1"
                    : "+v"(a), "+v"(b), "=&s"(t) : "v"(voff), "s"(rs), "s"(so) : "memory", "scc");
}
DI void poll(rsrc_t rs, unsigned voff, unsigned soff, unsigned E, unsigned need) {
  voff = need ? voff : 0x80000000u;
#pragma nounroll
  for (int it = 0; it < (1 << 16); ++it) {
    unsigned v;
    asm volatile("buffer_load_dword %0, %1, %2, %3 offen sc1\n\ts_waitcnt vmcnt(0)" : "=v"(v) : "v"(voff), "s"(rs), "s"(soff) : "memory");
    bool ok = (need == 0u) || ((v - E - need) <= (3u - need));
    if (ALL(ok)) break;
    __builtin_amdgcn_s_sleep(SLEEPN);
  }
}

template <bool B> struct BoolC { static constexpr bool value = B; };
template <int I> struct IntC { static constexpr int value = I; };
template <int... I, class F> DI void sfor_impl(std::integer_sequence<int, I...>, F&& f) { (f(IntC<I>{}), ...); }
template <int N, class F> DI void sfor(F&& f) { sfor_impl(std::make_integer_sequence<int, N>{}, f); }
#define SFOR(n, N) sfor<N>([&](auto n##_) AI { constexpr int n = decltype(n##_)::value;
#define SEND });
struct RowC { float r, K1, K2, Ri, Lr; };

DI unsigned hilo(float v) { float hi = rbf(v); return pk2(hi, v - hi); }
DI float max3(float a, float b, float c) { return __builtin_fmaxf(__builtin_fmaxf(a, b), c); }
DI float min3(float a, float b, float c) { return __builtin_fminf(__builtin_fminf(a, b), c); }
DI unsigned pk2s(float a, float b) { unsigned r; asm("v_cvt_pk_bf16_f32 %0, %1, %2" : "=v"(r) : "v"(a), "v"(b)); return r; }
DI f32x2 sub2(const f32x16& v, int k) { f32x2 t = {v[2 * k], v[2 * k + 1]}; return t; }
DI f32x2 exp2v(f32x2 v) { f32x2 r = {__builtin_amdgcn_exp2f(v[0]), __builtin_amdgcn_exp2f(v[1])}; return r; }
DI f32x2 dup2(float a) { f32x2 r = {a, a}; asm("" : "+v"(r)); return r; }
DI f32x2 fma2(f32x2 a, f32x2 b, f32x2 c) { return __builtin_elementwise_fma(a, b, c); }
DI u32x4 epiP(const f32x16& a, const RowC& c) {
  u32x4 r;
  const f32x2 K1 = dup2(c.K1), K2 = dup2(c.K2), Ri = dup2(c.Ri);
#pragma unroll
  for (int i = 0; i < 4; i++) {
    const f32x2 g = sub2(a, i), d = fma2(exp2v(g * fma2(g * g, K2, K1)), Ri, Ri);
    const f32x2 s = {__builtin_amdgcn_rcpf(d[0]), __builtin_amdgcn_rcpf(d[1])}, h = (g * sub2(a, 4 + i)) * s;
    r[i] = pk2(h[0], h[1]);
  }
  return r;
}
DI f32x16 featproj2(const f32x16& a, bf16x8 w0, bf16x8 w1, bool hh) {
  u32x4 q0, q1;
#pragma unroll
  for (int i = 0; i < 4; i++) { q0[i] = pk2(a[2 * i], a[2 * i + 1]); q1[i] = pk2(a[8 + 2 * i], a[9 + 2 * i]); }
  q1[2] = hh ? 0u : q1[2]; q1[3] = hh ? 0u : q1[3];
  f32x16 z;
#pragma unroll
  for (int i = 0; i < 16; i++) z[i] = 0.f;
  z = MFMA(w0, __builtin_bit_cast(bf16x8, q0), z);
  z = MFMA(w1, __builtin_bit_cast(bf16x8, q1), z);
  return z;
}
DI f32x16 featproj(const f32x16& a, unsigned wpaddr, unsigned off, bool hh) {
  return featproj2(a, tr2(wpaddr, off, off + 8 * 56), tr2(wpaddr, off + 16 * 56, off + 24 * 56), hh);
}
DI float max16(const f32x16& p) { float m = max3(p[0], p[1], p[2]); _Pragma("unroll") for (int i = 3; i < 15; i += 2) m = max3(m, p[i], p[i + 1]); return xmax(__builtin_fmaxf(m, p[15])); }
DI float min16(const f32x16& p) { float m = min3(p[0], p[1], p[2]); _Pragma("unroll") for (int i = 3; i < 15; i += 2) m = min3(m, p[i], p[i + 1]); return xmin(__builtin_fminf(m, p[15])); }
DI f32x16 dualexpP(const f32x16& p, float Lr) {
  const f32x2 bx = dup2(-max16(p) * Lr), bn = dup2(min16(p) * Lr), L2 = dup2(Lr);
  f32x16 o;
#pragma unroll
  for (int i = 0; i < 8; i++) {
    const f32x2 ep = exp2v(fma2(sub2(p, i), L2, bx)), en = exp2v(fma2(sub2(p, i), -L2, bn));
    o[i] = U2F(pk2(ep[0], ep[1])); o[8 + i] = U2F(pk2(en[0], en[1]));
  }
  return o;
}
struct SmS { f32x16 p, e; float en[16], bx, bn, sp, sn, tp, tn; bf16x8 w0, w1; };
DI bf16x8 sub4(const f32x16& v, int k) { f32x4 t = {v[4 * k], v[4 * k + 1], v[4 * k + 2], v[4 * k + 3]}; return __builtin_bit_cast(bf16x8, t); }

DI constexpr unsigned lc1_base(int ch) { return ch == 6 ? L_KV : ((ch & 1) ? L_GU : 0u) + (ch < 2 ? 49152u : ch < 4 ? 0u : 16384u); }
template <int CTRL, int RM> DI float dppadd(float v) { return v + U2F(__builtin_amdgcn_update_dpp(0u, F2U(v), CTRL, RM, 0xf, false)); }
DI float wavesum63(float v) {
  v = dppadd<0x111, 0xf>(v); v = dppadd<0x112, 0xf>(v); v = dppadd<0x114, 0xf>(v); v = dppadd<0x118, 0xf>(v);
  v = dppadd<0x142, 0xa>(v); return dppadd<0x143, 0xc>(v);
}

constexpr int PD = 6, RING = 9, XS = 2, XN = 20, CTN = 16, EA = 2, F1N = 6, CK1 = 24, EB = 3, H0L = 14, H0C = 22;
constexpr unsigned CODE_BYTES = 65536;
DI constexpr unsigned ring_area(int j) { return j == 0 ? L_FREE : j == 1 ? L_PHI : lc1_base(j - 2); }

__global__ __launch_bounds__(512, 2) void fused_kernel(const char* px_, const char* pqkv_, const char* pwg_, const char* pwu_, char* ws_, const char* pphi_,
                                                       const char* pwd_, const char* pwo_, char* pout_) {
  typedef __attribute__((ext_vector_type(8))) unsigned long long u64x8;
  u64x8 ka; unsigned long long kb;
  asm volatile("s_load_dwordx16 %0, %2, 0\n\ts_load_dwordx2 %1, %2, 64" : "=&s"(ka), "=&s"(kb) : "s"(__builtin_amdgcn_kernarg_segment_ptr()));
  __shared__ __attribute__((aligned(16))) char smem[163840];
  const unsigned LB = (unsigned)(size_t)smem;
  const unsigned tid = __builtin_amdgcn_workitem_id_x(), lane = tid & 63;
  const unsigned w = __builtin_amdgcn_readfirstlane(tid >> 6), g = w >> 2, wr = w & 3;
  const unsigned b = __builtin_amdgcn_workgroup_id_x(), xcd = b & 7, bi = b >> 3;
  const unsigned half = (xcd >> 1) & 1, cp = 4 * (xcd & 1) + (bi >> 3), rb = 8 * (xcd >> 2) + (bi & 7), rsum = (bi >> 3) + (bi & 7);
  const unsigned f2 = 32 * (xcd & 1) + bi - 8, t2 = (f2 * 37) >> 8, ci = f2 - 7 * t2, rbl = t2 & 3, rt2 = 2 * (8 * (xcd >> 1) + t2);
  const unsigned c = 7 * half + cp - 1;
  const unsigned head = 8 * half + (((half ? 0x76425310u : 0x75316420u) >> (4 * cp)) & 7);
  const bool attn = cp == 0;
  const unsigned wstr = attn ? QROW : FROW;
  const unsigned sG0 = half * (attn ? 448 : 1792) + (attn ? 0 : (cp - 1) * 256), sX0 = rb * 128 * XROW;
  unsigned vx0, vx1, vG, vU, vKV;
  auto voffs = [&](unsigned ln) AI {
    const unsigned r8 = ln >> 3, c8 = ln & 7, sa = (r8 >> 1) & 1, lc = c8 ^ (4 * sa);
    vx0 = (16 * w + r8) * XROW + ((c8 ^ (r8 >> 1)) << 4); vx1 = (vx0 ^ 64u) + 8 * XROW;
    const unsigned cg = attn ? lc : (c8 ^ (2 * sa)), cu = attn ? (lc >= 6 ? lc - 2 : lc) : (cg ^ 4);
    vG = (8 * w + r8) * wstr + (cg << 4); vU = (8 * w + r8) * wstr + (cu << 4);
    vKV = (8 * w + r8) * QROW + 896 * (1 + (lc >> 2)) + ((lc & 3) << 4) + ((56 * head) & ~15u);
  };
  voffs(lane);
  const unsigned lw1 = LB + w * 1024, lw2 = LB + w * 2048;
  const unsigned rot = (rsum & 3) * 2;
  const unsigned srow = 128 * rb + 64 * half + 8 * cp + w;
  unsigned vrow = lane < 56 ? lane * 16 : 0x80000000u;
  asm volatile("s_waitcnt lgkmcnt(0)" : "+s"(ka), "+s"(kb), "+v"(vx0), "+v"(vx1), "+v"(vG), "+v"(vU), "+v"(vKV), "+v"(vrow));
  const char *px = (const char*)ka[0], *pqkv = (const char*)ka[1], *pwg = (const char*)ka[2], *pwu = (const char*)ka[3], *pphi = (const char*)ka[5],
             *pwd = (const char*)ka[6], *pwo = (const char*)ka[7];
  char *ws = (char*)ka[4], *pout = (char*)kb;
  const rsrc_t rsX = mkrs(px, 2048 * XROW), rsQ = mkrs(pqkv, 448 * QROW);
  const rsrc_t rsG = mkrs(attn ? pqkv : pwg, 448 * wstr), rsU = mkrs(attn ? pqkv + 128 : pwu, 448 * wstr - (attn ? 128 : 0));
  auto cidx = [&](int st) AI -> unsigned { unsigned t = rot + st; return t >= 7 ? t - 7 : t; };
  auto issueA = [&](int st, int k) AI {
    const unsigned sl = st & 3, ch = cidx(st);
    if (k == 0) dma(lw2, sl * 16384, vx0, rsX, sX0 + ch * 128, 0);
    if (k == 1) dma(lw2, sl * 16384 + 1024, vx1, rsX, sX0 + ch * 128, 0);
    if (k == 2) dma(lw1, L_GU + sl * 16384, vG, rsG, sG0 + ch * 64 * wstr, 0);
    if (k == 3) dma(lw1, L_GU + sl * 16384 + 8192, vU, rsU, sG0 + ch * 64 * wstr, 0);
    if (k == 4) dma(lw1, L_KV + sl * 8192, vKV, rsQ, ch * 64 * QROW, 0);
  };
  const rsrc_t rsWS = mkrs(ws, WS_END);
  unsigned Ev, cpf; u32x4 xrw;
  asm volatile("buffer_load_dwordx4 %0, %1, %2, %3 offen" : "=v"(xrw) : "v"(vrow), "s"(rsX), "s"(srow * XROW) : "memory");
  asm volatile("buffer_load_dword %0, off, %1, 0 sc1" : "=v"(Ev) : "s"(rsWS) : "memory");
  {
    unsigned long long pc; asm volatile("s_getpc_b64 %0" : "=s"(pc));
    const unsigned vo = w == 0 && lane < 24 ? (bi * 24 + lane) * 128 : 0x80000000u;
    asm volatile("buffer_load_dword %0, %1, %2, 0 offen" : "=v"(cpf) : "v"(vo), "s"(mkrs((const void*)pc, CODE_BYTES)) : "memory");
  }
#pragma unroll
  for (int k = 0; k < 5; k++) issueA(0, k);
  asm volatile("s_waitcnt vmcnt(5)" : "+v"(xrw), "+v"(Ev), "+v"(cpf) :: "memory");
#pragma unroll
  for (int k = 0; k < 5; k++) issueA(1, k);
  issueA(2, 0);
  const unsigned E = __builtin_amdgcn_readfirstlane(Ev);
  {
    float s1 = 0.f, s2 = 0.f;
#pragma unroll
    for (int i = 0; i < 4; i++) { s1 = dot2(xrw[i], 0x3f803f80u, s1); s2 = dot2(xrw[i], xrw[i], s2); }
    s1 = wavesum63(s1); s2 = wavesum63(s2);
    const float mean = s1 / 448.f, var = __builtin_fmaxf(s2 * (1.f / 448.f) - mean * mean, 0.f);
    u32x2 gr = {pk2(-mean, __builtin_amdgcn_sqrtf(rbf(rbf(var) + 1e-5f))), E + 1};
    __builtin_amdgcn_raw_buffer_store_b64(gr, rsWS, lane == 63 ? 0u : 0x80000000u, WS_GR + srow * 8, 16);
  }

  auto prog = [&](auto at_, auto gg_) AI {
  constexpr bool AT = decltype(at_)::value; constexpr int GG = decltype(gg_)::value;
  constexpr unsigned lcstep = AT ? 224 : 128;
  unsigned lane;
  asm volatile("v_mbcnt_lo_u32_b32 %0, -1, 0\n\tv_mbcnt_hi_u32_b32 %0, -1, %0" : "=v"(lane));
  __builtin_assume(lane < 64);
  voffs(lane);
  auto issueL = [&](int hc) AI {
    const int st = hc >> 1; const unsigned ch = cidx(st);
    if (hc & 1) dma(lw1, lc1_base(st) + 8192, vU, rsU, sG0 + ch * 64 * wstr, lcstep);
    else dma(lw1, lc1_base(st), vG, rsG, sG0 + ch * 64 * wstr, lcstep);
  };
  auto issueG = [&](int bar, int k) AI {
    if (bar == 0) issueA(2 + k / 5, k % 5);
    else if (bar <= 3) issueA(bar + 3, k);
    else if (k < 4) issueL(4 * (bar - 4) + k);
    else if (bar == 4) dma(LB + (w & 1) * 1024, L_WPK, lane * 16, mkrs(pphi, 16 * 1568), head * 1568 + (w & 1) * 1024, 0);
    else issueL(12 + (bar - 5));
  };

  const unsigned rt = 4 * rb + wr;
  const unsigned sA2 = WS_A2 + rt * 144 * 1024 + (AT ? half * 16384 : c * 8192), vL16 = lane * 16;
  const u32x4 sent = {~0u, ~0u, ~0u, ~0u};
  auto clr = [&](int ks) AI { st16(sent, rsWS, vL16, sA2, ks * 1024); };

  auto clri = [&](int q) AI { clr(GG ? (q < 6 ? 114 + q : 120 + q) : (q < 2 ? 112 + q : 118 + q)); };

  const bool hh = lane >= 32;
  const unsigned hu = lane >> 5, blk = (lane >> 4) & 1, q4 = (lane >> 2) & 3, p4 = lane & 3, ta = (q4 >> 1) & 1, l31 = lane & 31;
  const unsigned perm = wr ^ (2 * GG);
  const unsigned rowoff = (8 * hu + q4) * 128 + (perm << 11);
  const unsigned pc = (4 * blk + p4) < 6 ? (4 * blk + p4) : 6;
  auto tileaddr = [&](unsigned t) AI -> unsigned {
    if constexpr (AT) { unsigned o = 56 * t + 8 * pc; return (o >> 7) * 8192 + ((((o >> 4) & 7) ^ (4 * ta)) << 4) + (o & 8) + rowoff; }
    else return blk * 8192 + (((2 * t + (p4 >> 1)) ^ (4 * blk + 2 * ta)) << 4) + 8 * (p4 & 1) + rowoff;
  };
  constexpr int NT = !AT ? 2 : GG ? 1 : 3, TB0 = GG ? 4 - NT : 0;
  unsigned aA[3], aB[NT];
  {
    unsigned o = (head & 1) * 8 + 8 * pc;
    unsigned ak = L_KV + (((o >> 4) ^ (4 * ta)) << 4) + (o & 8) + rowoff;
    aA[0] = LB + (GG ? L_GU + tileaddr(1) : ak);
    aA[1] = LB + (GG ? L_GU + tileaddr(2) : (ak ^ 64u));
    aA[2] = LB + L_GU + tileaddr(GG ? 3 : 0);
#pragma unroll
    for (int j = 0; j < NT; j++) aB[j] = LB + tileaddr(TB0 + j);
  }
  const unsigned xrow = 32 * wr + l31;
  const unsigned aX = LB + xrow * 128 + (((2 * perm + hu) ^ ((xrow >> 1) & 7)) << 4);
  const unsigned selv = (((lane >> 4) ^ (lane >> 2)) & 1) ? 0u : 0x3f803f80u;
  const u32x4 sel4 = {selv, selv, selv, selv};
  const bf16x8 sel = __builtin_bit_cast(bf16x8, sel4);

  bf16x8 xf[28], wa[2][3], wq[8];
  f32x16 accA[3], accB[NT];
  f32x4 csA[3], csB[NT];
#pragma unroll
  for (int j = 0; j < 3; j++) { for (int i = 0; i < 16; i++) accA[j][i] = 0.f; for (int i = 0; i < 4; i++) csA[j][i] = 0.f; }
#pragma unroll
  for (int j = 0; j < NT; j++) { for (int i = 0; i < 16; i++) accB[j][i] = 0.f; for (int i = 0; i < 4; i++) csB[j][i] = 0.f; }
  u32x2 grn;

  const rsrc_t rsWD = mkrs(pwd, 1792 * XROW), rsWO = mkrs(pwo, 448 * XROW);
  const unsigned vW2 = (lane >> 3) * XROW + (((lane & 7) ^ ((lane >> 2) & 4)) << 4) + ci * 128;
  constexpr int NS = GG ? 16 + XS : 20 - XS, NC = 6 - XS / 2, NO = 2 * NC;
  const unsigned rho = (rbl + ci) & 3;
  auto t_of = [](int i) constexpr { return (0x54763210u >> (4 * ((i >> 1) & 7))) & 7; };
  constexpr int NE = GG ? 9 : 5;
  auto g1i = [](int i) constexpr { return i < 8 ? i : i == 8 ? 16 : i < 17 ? i - 1 : 17; };
  auto ksb = [&](int i) AI -> unsigned {
    if (GG) i = g1i(i);
    if (GG) return i < 16 ? 16 * wr + ((t_of(i) & 3) ^ rho) : wr;
    if (i < NO) { const unsigned cc = i % NC + rho + (XS ? 0 : rho >> 1); return wr + 8 * (cc >= NC ? cc - NC : cc); }
    return 8 * wr + 2 * ((i - NO) >> 1);
  };
  auto ksi = [&](int i) AI -> unsigned { if (GG) i = g1i(i); return GG ? (i < 16 ? 8 * (i & 1) + (t_of(i) & 4) : 4 * i) : i < NO ? 64 + 4 * XS + 4 * (i / NC) : 112 + (i & 1); };
  auto issueW2 = [&](int i, int hf) AI {
    if (GG == 0 && i >= NO) dma(lw2, ring_area(i < EA ? 0 : (i - EA) % RING) + hf * 1024, vW2, rsWO, ksb(i) * 14 * XROW, (16 * (i & 1) + 8 * hf) * XROW);
    else dma(lw2, ring_area(i < EA ? 0 : (i - EA) % RING) + hf * 1024, vW2, rsWD, ksb(i) * 16 * XROW, (ksi(i) * 16 + 8 * hf) * XROW);
  };

  const unsigned aCSw = LB + L_CS + l31 * 16 + wr * 4 + GG * 2048, aCSr = LB + L_CS + l31 * 16 + GG * 2048;
  auto readX = [&](int n) AI { xf[n] = ldsr<bf16x8>(aX ^ (unsigned)((n & 3) << 5), ((n >> 2) & 3) * 16384); };
  auto readA = [&](int n, int j) AI {
    const unsigned o = ((n >> 2) & 3) * ((GG == 0 && j < 2) ? 8192 : 16384);
    wa[n & 1][j] = tr2(aA[j] ^ (unsigned)((n & 3) << 11), o, o + 512);
  };
  auto readB = [&](int q) AI {
    const int n = q / NT, j = q % NT;
    const unsigned base = lc1_base(n >> 2);
    if ((NT == 3 && n < XN) ? (n & 3) == 0 : n == 24) asm volatile("" : "+v"(aB[j]));
    wq[q & 7] = tr2(aB[j] ^ (((n & 3) << 11) | (base & 0x30000u)), base & 0xffffu, (base & 0xffffu) + 512);
  };
  RowC rc; unsigned nm, denb;
  auto mkxe = [&]() AI { asm volatile("" : "+v"(nm)); u32x4 t = {nm, nm, nm, nm}; return __builtin_bit_cast(bf16x8, t); };
  auto rowconst = [&]() AI {
#pragma nounroll
    for (int it = 0; it < (1 << 16) && !ALL(grn[1] == E + 1); ++it) {
      __builtin_amdgcn_s_sleep(SLEEPN);
      asm volatile("buffer_load_dwordx2 %0, %1, %2, %3 offen sc1\n\ts_waitcnt vmcnt(0)" : "=v"(grn) : "v"(xrow * 8), "s"(rsWS), "s"(WS_GR + rb * 1024) : "memory");
    }
    const float den = U2F(grn[0] & 0xffff0000u);
    nm = hh ? 0u : __builtin_amdgcn_perm(grn[0], grn[0], 0x01000100u);
    denb = grn[0] >> 16;
    rc.r = __builtin_amdgcn_rcpf(den);
    const float k1 = -2.f * 1.4426950408889634f * 0.7978845608028654f;
    rc.K1 = k1 * rc.r; rc.K2 = (k1 * 0.044715f) * rc.r * (rc.r * rc.r); rc.Ri = den * den; rc.Lr = 1.4426950408889634f * rc.r;
  };
  WAITV(7);
  BAR(); MEMFENCE();
  readX(0); readA(0, 0); readA(0, 1); readA(0, 2);
  SFOR(n, 28)
    SB();
    if constexpr (n == 27) {
#pragma unroll
      for (int j = 0; j < 3; j++) ldsw<unsigned>(aCSw, j * 512, hilo(csA[j][0]));
    }
    if constexpr ((n & 3) == 3) {
      if constexpr (n == 27) WAITV(0);
      else if constexpr (n == 23) asm volatile("s_waitcnt vmcnt(%c1)" : "+v"(grn) : "i"(10 + (CTN >= 15 && CTN < 23)) : "memory");
      else if constexpr (n == 19) waitv<11 + (CTN >= 15 && CTN < 19)>();
      else if constexpr (n == 3 || n == 15) WAITV(11); else WAITV(10);
      { WAITL0(); BAR(); MEMFENCE(); }
    }
    constexpr int bar = (n + 1) >> 2, ph = (n + 1) & 3;
#pragma unroll
    for (int j = 0; j < 3; j++) {
      if constexpr (n < 27) { if (j == 0) readX(n + 1); readA(n + 1, j); }
      else { wa[0][j] = ldsr<bf16x8>(aCSr, j * 512); if (AT && GG == 1 && j < 2) { readB(2 * j); readB(2 * j + 1); } }
      SB();
      accA[j] = MFMA(wa[n & 1][j], xf[n], accA[j]);
      const int slot = 3 * ph + j;
      if (bar == 0) issueG(0, slot - 2);
      else if (bar == 6) { if (slot < 5) issueG(bar, slot); }
      else if (bar <= 6) { if (slot == 0) issueG(bar, 0); if (slot == 2) issueG(bar, 1); if (slot == 5) issueG(bar, 2); if (slot == 7) issueG(bar, 3); if (slot == 10) issueG(bar, 4); }
      else if (j < 2) {
        if constexpr (AT) dma(lw2, L_FREE + j * 1024, lane * 16, mkrs(pphi, 16 * 1568), half * 8 * 1568 + w * 2048, j * 1024);
        else issueW2(0, j);
      }
      if ((n & 3) == 0) csA[j] = MFMA16(sel, wa[n & 1][j], csA[j]);
      if (n == CTN && j == 1) {
        const unsigned per = (w & 1) ? 13 : 49;
        dmat(LB + L_ST + w * 256, (w >> 1) == 3 && lane < per ? (b * per + lane) * 128 : 0x80000000u, mkrs((w & 1) ? pwo : pwd, ((w & 1) ? 448 : 1792) * XROW));
      }
      if (n == 25 && j == 0) rowconst();
      if (n == 14 && j == 2) asm volatile("buffer_load_dwordx2 %0, %1, %2, %3 offen sc1" : "=v"(grn) : "v"(xrow * 8), "s"(rsWS), "s"(WS_GR + rb * 1024) : "memory");
      SB();
    }
  SEND
  { const bf16x8 xe = mkxe();
#pragma unroll
  for (int j = 0; j < 3; j++) accA[j] = MFMA(wa[0][j], xe, accA[j]); }
  asm volatile("" : "+v"(accA[0]), "+v"(accA[1]), "+v"(accA[2]));
  SB();

  const unsigned sFown = WS_FH + (rt * 32 + (AT ? 28 + 2 * half : 2 * c) + GG) * FS;
  const unsigned aWP = LB + (4 * hu + q4) * 56 + 8 * pc;
  const unsigned T12 = (E + 1) & 0xfffu, T16 = (E + 1) & 0xffffu;
  const unsigned sF = WS_FH + rt2 * 32 * FS;
  const unsigned fc = l31 >> 1;
  const bool nd1 = GG ? (fc >> 1) == wr || (fc >= 8 && fc < 8 + XS / 2) : (fc >= 8 + XS / 2 && fc < 14) || fc == 14 + (wr >> 1);
  const bool nde = nd1 && fc < 14, ndl = nde;
  const unsigned aIW = LB + L_PHI + xrow * 64 + ((hu ^ ((xrow >> 1) & 7)) << 3);
  SmS sm;
  unsigned aWK = aWP + L_WPK, aWQ = aWP + L_FREE;
  asm volatile("" : "+v"(aWK)); if constexpr (AT) asm volatile("" : "+v"(aWQ));
  auto kstep = [&](auto s_) AI {
    constexpr int s = decltype(s_)::value;
    if constexpr (s == 0) { sm.w0 = tr2(aWK, 0, 8 * 56); sm.w1 = tr2(aWK, 16 * 56, 24 * 56); }
    if constexpr (s == 1) sm.p = featproj2(accA[0], sm.w0, sm.w1, hh);
    if constexpr (s == 2 || s == 3) {
#pragma unroll
      for (int k = 2 * s - 4; k < 2 * s - 2; k++) {
        u32x2 vv = {pk2(accA[1][4 * k], accA[1][4 * k + 1]), pk2(accA[1][4 * k + 2], accA[1][4 * k + 3])};
        if (k == 3) vv[0] = hh ? denb : vv[0];
        ldsw<u32x2>(aIW ^ (unsigned)(k << 4), L_V - L_PHI, vv);
      }
    }
    if constexpr (s == 4) sm.bx = -max16(sm.p) * rc.Lr;
    if constexpr (s == 5) sm.bn = min16(sm.p) * rc.Lr;
    if constexpr (s >= 6 && s < 22) {
      constexpr int i = s - 6;
      sm.e[i] = __builtin_amdgcn_exp2f(__builtin_fmaf(sm.p[i], rc.Lr, sm.bx)); sm.en[i] = __builtin_amdgcn_exp2f(__builtin_fmaf(sm.p[i], -rc.Lr, sm.bn));
      if constexpr (i == 0) { sm.sp = sm.e[0]; sm.sn = sm.en[0]; } else if constexpr (i < 12) { sm.sp += sm.e[i]; sm.sn += sm.en[i]; }
      else if constexpr (i == 12) { sm.tp = sm.e[12]; sm.tn = sm.en[12]; } else { sm.tp += sm.e[i]; sm.tn += sm.en[i]; }
    }
    if constexpr (s == 22) { sm.sp = __builtin_amdgcn_rcpf(xsum(sm.sp + (hh ? 0.f : sm.tp))) * rc.r; sm.sn = __builtin_amdgcn_rcpf(xsum(sm.sn + (hh ? 0.f : sm.tn))) * rc.r; }
    if constexpr (s >= 23) {
#pragma unroll
      for (int u = (s == 29 ? 6 : s - 23); u <= (s == 29 ? 7 : s - 23); u++) {
        const int k = u >> 1;
        if (u & 1) { u32x2 t = {pk2s(sm.en[4 * k] * sm.sn, sm.en[4 * k + 1] * sm.sn), pk2s(sm.en[4 * k + 2] * sm.sn, sm.en[4 * k + 3] * sm.sn)}; ldsw<u32x2>(aIW ^ (unsigned)(k << 4), 8192, t); }
        else { u32x2 t = {pk2s(sm.e[4 * k] * sm.sp, sm.e[4 * k + 1] * sm.sp), pk2s(sm.e[4 * k + 2] * sm.sp, sm.e[4 * k + 3] * sm.sp)}; ldsw<u32x2>(aIW ^ (unsigned)(k << 4), 0, t); }
      }
    }
  };
  auto qstep = [&](auto s_, f32x16& a, unsigned lh) AI {
    constexpr int s = decltype(s_)::value;
    if constexpr (s == 0) { sm.w0 = tr2(aWQ, lh * 1568, lh * 1568 + 8 * 56); sm.w1 = tr2(aWQ, lh * 1568 + 16 * 56, lh * 1568 + 24 * 56); }
    if constexpr (s == 1) sm.p = featproj2(a, sm.w0, sm.w1, hh);
    if constexpr (s == 3) sm.bx = -max16(sm.p) * rc.Lr;
    if constexpr (s == 4) sm.bn = min16(sm.p) * rc.Lr;
    if constexpr (s >= 5 && s < 21) {
      constexpr int i = (s - 5) >> 1;
      float t0, t1;
      if constexpr ((s - 5) & 1) { t0 = __builtin_fmaf(sm.p[2 * i], -rc.Lr, sm.bn); t1 = __builtin_fmaf(sm.p[2 * i + 1], -rc.Lr, sm.bn); }
      else { t0 = __builtin_fmaf(sm.p[2 * i], rc.Lr, sm.bx); t1 = __builtin_fmaf(sm.p[2 * i + 1], rc.Lr, sm.bx); }
      a[8 * ((s - 5) & 1) + i] = U2F(pk2(__builtin_amdgcn_exp2f(t0), __builtin_amdgcn_exp2f(t1)));
      if constexpr (s == 20) asm volatile("" : "+v"(a));
    }
  };
  bf16x8 ka_[4], kb_[2][4]; f32x4 kd[2];
  auto kvstep = [&](auto s_) AI {
    constexpr int s = decltype(s_)::value;
    if constexpr (s == 0) {
      const unsigned g4 = lane >> 4, r0 = 8 * g4 + q4, sw = 4 * blk + (q4 >> 1);
      unsigned b0 = LB + L_PHI + r0 * 64 + ((p4 ^ sw) << 3);
      unsigned a0 = (b0 ^ ((wr >> 1) << 5)) + (wr & 1) * 8192;
#pragma unroll
      for (int k = 0; k < 4; k++) {
        ka_[k] = __builtin_shufflevector(trd2(a0, k * 2048), trd2(a0 ^ 16u, k * 2048 + 256), 0, 1, 2, 3, 4, 5, 6, 7);
        kb_[0][k] = __builtin_shufflevector(trd2(b0, L_V - L_PHI + k * 2048), trd2(b0 ^ 16u, L_V - L_PHI + k * 2048 + 256), 0, 1, 2, 3, 4, 5, 6, 7);
        kb_[1][k] = __builtin_shufflevector(trd2(b0 ^ 32u, L_V - L_PHI + k * 2048), trd2(b0 ^ 48u, L_V - L_PHI + k * 2048 + 256), 0, 1, 2, 3, 4, 5, 6, 7);
      }
    }
    if constexpr (s == 1) {
      f32x4 z = {0.f, 0.f, 0.f, 0.f}; kd[0] = z; kd[1] = z;
#pragma unroll
      for (int k = 0; k < 4; k++) { kd[0] = MFMA16(ka_[k], kb_[0][k], kd[0]); kd[1] = MFMA16(ka_[k], kb_[1][k], kd[1]); }
    }
    if constexpr (s == 3) {
#pragma unroll
      for (int d = 0; d < 2; d++) {
        u32x4 t;
#pragma unroll
        for (int i = 0; i < 4; i++) t[i] = (F2U(kd[d][i]) & ~0xfffu) | T12;
        st16(t, rsWS, (lane >> 4) * 512 + (lane & 15) * 16, ((head * 16 + rb) * 2 + (wr & 1)) * 4096 + (wr >> 1) * 2048, WS_KVP + d * 256);
      }
    }
  };

  unsigned fl1 = 0;
  u32x4 pv[8];
  const unsigned vKP = l31 * 16 + hu * 8192, sKP = (rb * 32 + half) * 4096 + wr * 1024 + GG * 512, sKR = (8 * half + w) * 8192;
  auto hop1 = [&]() AI {
    waittag<16384>(pv, rsWS, vKP, sKP + WS_KVP, T12, 0xfffu);
    u32x4 o;
#pragma unroll
    for (int k = 0; k < 4; k++) {
      float sum = U2F(pv[0][k]);
#pragma unroll
      for (int i = 1; i < 8; i++) sum += U2F(pv[i][k]);
      sum = xsum(sum);
      sum = l31 == 29 ? 1.f : sum;
      if (GG == 1) sum = wr == 3 ? 0.f : sum;
      o[k] = (pk2s(sum, sum) & 0xffff0000u) | T16;
    }
    st16(o, rsWS, hh ? 0x80000000u : l31 * 16, (rb * 2 + half) * 4096 + wr * 1024 + GG * 512, WS_KVR);
    if (b == 0 && w == 4) stflag(E + 4, rsWS, WS_EPOCH);
    SB();
  };
  bf16x8 cb[NT];
  auto ldflag = [&]() AI { asm volatile("buffer_load_dword %0, %1, %2, %3 offen sc1" : "=v"(fl1) : "v"(nd1 ? lane * FS : 0x80000000u), "s"(rsWS), "s"(sF) : "memory"); };
  auto kpath = [&]() AI {
    const f32x16 p = featproj(accA[0], aWK, 0, hh);
#pragma unroll
    for (int k = 0; k < 4; k++) {
      u32x2 vv = {pk2(accA[1][4 * k], accA[1][4 * k + 1]), pk2(accA[1][4 * k + 2], accA[1][4 * k + 3])};
      if (k == 3) vv[0] = hh ? denb : vv[0];
      ldsw<u32x2>(aIW ^ (unsigned)(k << 4), L_V - L_PHI, vv);
    }
    const f32x2 bx = dup2(-max16(p) * rc.Lr), bn = dup2(min16(p) * rc.Lr), L2 = dup2(rc.Lr);
    f32x2 ep[8], en[8];
#pragma unroll
    for (int i = 0; i < 8; i++) { ep[i] = exp2v(fma2(sub2(p, i), L2, bx)); en[i] = exp2v(fma2(sub2(p, i), -L2, bn)); }
    f32x2 sp = ((ep[0] + ep[1]) + (ep[2] + ep[3])) + (ep[4] + ep[5]), sn = ((en[0] + en[1]) + (en[2] + en[3])) + (en[4] + en[5]);
    f32x2 tp = ep[6] + ep[7], tn = en[6] + en[7];
    float a0 = sp[0], a1 = sn[0], a2 = tp[0], a3 = tn[0];
    asm("" : "+v"(a0), "+v"(a1), "+v"(a2), "+v"(a3));
    a0 += sp[1]; a2 += tp[1]; asm("" : "+v"(a0), "+v"(a2)); a1 += sn[1]; a3 += tn[1];
    const f32x2 ip = dup2(__builtin_amdgcn_rcpf(xsum(a0 + (hh ? 0.f : a2))) * rc.r), in = dup2(__builtin_amdgcn_rcpf(xsum(a1 + (hh ? 0.f : a3))) * rc.r);
#pragma unroll
    for (int k = 0; k < 4; k++) {
      const f32x2 u0 = ep[2 * k] * ip, u1 = ep[2 * k + 1] * ip, v0 = en[2 * k] * in, v1 = en[2 * k + 1] * in;
      u32x2 t0 = {pk2(u0[0], u0[1]), pk2(u1[0], u1[1])}, t1 = {pk2(v0[0], v0[1]), pk2(v1[0], v1[1])};
      ldsw<u32x2>(aIW ^ (unsigned)(k << 4), 0, t0); ldsw<u32x2>(aIW ^ (unsigned)(k << 4), 8192, t1);
    }
  };
  const unsigned sA = WS_A2 + rt2 * 144 * 1024;
  u32x4 af[NS][2];
  auto loadA1 = [&](int i, int m) AI { af[i][m] = ld16(rsWS, vL16, sA + ksb(i) * 1024, (144 * m + ksi(i)) * 1024); };
  unsigned ln = vL16, aW = 0;
  auto mkaw = [&]() AI {
    asm volatile("" : "+v"(ln));
    aW = lw2 + (ln & 0x200) + ((ln & 0xc0) << 1) + ((((ln >> 3) & 0x20) | ((ln >> 1) & 0x10)) ^ ((ln >> 1) & 0x40)) + ((ln >> 1) & 8);
  };
  bf16x8 wf[NS][2];
  auto readW = [&](int i) AI {
    const unsigned ra = ring_area(i < EA ? 0 : (i - EA) % RING), la = aW + (ra & 0x30000u);
    wf[i][0] = tr2(la, ra & 0xffffu, (ra & 0xffffu) + 1024); wf[i][1] = tr2(la ^ 64u, ra & 0xffffu, (ra & 0xffffu) + 1024);
  };
  auto stream = [&]() AI {
    SFOR(q, 28 * NT)
      constexpr int n = q / NT, j = q % NT;
      if constexpr (j == 0) {
        if constexpr (n == 27) {
#pragma unroll
          for (int t = 0; t < NT; t++) ldsw<unsigned>(aCSw, ((3 + t) & 3) * 512, hilo(csB[t][0]));
          if constexpr (AT && GG) WAITV(8);
          WAITL0(); BAR(); MEMFENCE();
        }
        else if constexpr (!AT && (n & 3) == 3) { asm volatile("s_waitcnt lgkmcnt(4)" ::: "memory"); BAR(); MEMFENCE(); }
      }
      if constexpr (q + 4 < 28 * NT) readB(q + 4);
      if constexpr (n == 27) cb[j] = ldsr<bf16x8>(aCSr, ((3 + j) & 3) * 512);
      SB();
      accB[j] = MFMA(wq[q & 7], xf[n], accB[j]);
      if constexpr ((n & 3) == 1) csB[j] = MFMA16(sel, wq[q & 7], csB[j]);
      if constexpr (!AT) {
        if constexpr (j == 0 && n >= 3 && n < 7) issueW2(EA + 1 + ((n - 3) >> 1), (n - 3) & 1);
        if constexpr (j == 0 && n >= 7 && n < 27 && (n & 1)) issueW2(EA + 1 + ((n + 1) >> 2), ((n + 1) >> 1) & 1);
        if constexpr (n == 27) issueW2(EA + 8, j);
        if constexpr (j == 1 && n == F1N) { waitv<(GG ? (EA >= 2 ? 2 * (EA - 1) : 0) : (EA >= 3 ? 2 : 0)) + (F1N < 7 ? F1N - 2 : 4 + (F1N - 5) / 2)>(); stflag(E + 1, rsWS, sFown); }
        if constexpr (q == 2 * CK1 - 7) ldflag();
        if constexpr (q == 2 * CK1 - 1) {
          asm volatile("s_waitcnt vmcnt(%c5)" : "+v"(fl1), "+v"(accB[0]), "+v"(accB[NT - 1]), "+v"(csB[0]), "+v"(csB[NT - 1]) : "i"((CK1 & 1) ? 1 : 2) : "memory");
          if (!ALL(!nde || (fl1 - E - 1u) <= 1u)) poll(rsWS, lane * FS, sF, E, nde ? 1u : 0u);
        }
        if constexpr (q >= 2 * CK1 && q < 2 * CK1 + 2 * NE) loadA1((q - 2 * CK1) >> 1, q & 1);
      } else {
        if constexpr (GG == 1 && n < 16 && (n & 1) == 0) clri(n >> 1);
        if constexpr (GG == 0 && j < 2 && n >= 1 && 2 * (n - 1) + j < 21) qstep(IntC<2 * (n - 1) + j>{}, accA[2], 0);
        if constexpr (GG == 0 && q >= 3 * H0L && q < 3 * H0L + 8) pv[q - 3 * H0L] = ld16(rsWS, vKP, sKP, WS_KVP + (q - 3 * H0L) * 16384);
        if constexpr (GG == 0 && j == 2 && n == H0C) { asm volatile("" : "+v"(accB[0]), "+v"(accB[1]), "+v"(accB[NT - 1]), "+v"(csB[0]), "+v"(csB[1]), "+v"(csB[NT - 1])); hop1(); }
      }
      SB();
    SEND
    const bf16x8 xe = mkxe();
#pragma unroll
    for (int t = 0; t < NT; t++) { accB[t] = MFMA(cb[t], xe, accB[t]); asm volatile("" : "+v"(accB[t])); }
    SB();
  };
  constexpr int NL = 2 * CK1 + 2 * NE > 56 ? 2 * CK1 + 2 * NE - 56 : 0;
  auto mv = [&](auto i_, auto y_) AI {
    constexpr int i = decltype(i_)::value;
    if constexpr (i < EA) { if (i == 0) mkaw(); waitv<decltype(y_)::value>(); readW(i); WAITL0(); SB(); issueW2(i + 1, 0); issueW2(i + 1, 1); SB(); }
  };
  auto tile = [&](f32x16& a, int t) AI { asm volatile("" : "+v"(a)); st16(epiP(a, rc), rsWS, vL16, sA2, t * 1024); SB(); };
  if constexpr (!AT) {
    if constexpr (GG == 0) {
      kpath(); mv(IntC<0>{}, IntC<0>{}); WAITL0(); BARF();
      kvstep(IntC<0>{}); kvstep(IntC<1>{}); kvstep(IntC<3>{}); SB();
      mv(IntC<1>{}, IntC<2>{});
      tile(accA[2], 0);
    } else {
      tile(accA[0], 1); tile(accA[1], 2); mv(IntC<0>{}, IntC<2>{}); BARF();
      tile(accA[2], 3); mv(IntC<1>{}, IntC<1>{});
    }
    readB(0); readB(1); readB(2); readB(3);
    stream();
    tile(accB[0], 4 + 2 * GG); tile(accB[NT - 1], 5 + 2 * GG);
    SFOR(u, NL) loadA1((56 + u - 2 * CK1) >> 1, u & 1); SEND
  } else if constexpr (GG == 0) {
    SFOR(q, 8) clri(q); SEND
    SFOR(s, 30) kstep(s_); SEND
    WAITV(8);
    WAITL0(); BARF();
    kvstep(IntC<0>{});
    readB(0); readB(1); readB(2); readB(3);
    kvstep(IntC<1>{}); kvstep(IntC<3>{});
    WAITV(2); stflag(E + 1, rsWS, sFown);
    SB();
    stream();
  } else {
    stream();
    SFOR(s, 21) qstep(s_, accA[0], 1); SEND
    WAITV(0); stflag(E + 1, rsWS, sFown);
    SFOR(s, 21) qstep(s_, accA[1], 2); SEND
    SFOR(s, 21)
      if constexpr (s < 8) pv[s] = ld16(rsWS, vKP, sKP, WS_KVP + s * 16384);
      qstep(s_, accA[2], 3);
    SEND
    hop1();
    BARF();
  }

  if constexpr (AT) {
    auto qexp = [&](f32x16& a, unsigned lh) AI { a = dualexpP(featproj(a, aWQ, lh * 1568, hh), rc.Lr); asm volatile("" : "+v"(a)); SB(); };
    if constexpr (GG == 0) qexp(accB[0], 4);
#pragma unroll
    for (int i = 0; i < 8; i++) pv[i] = ld16(rsWS, vL16, sKR, WS_KVR + i * 1024);
    if constexpr (GG == 0) { qexp(accB[1 % NT], 5); qexp(accB[NT - 1], 6); } else qexp(accB[0], 7);
    waittag<1024>(pv, rsWS, vL16, sKR + WS_KVR, T16, 0xffffu);
#pragma unroll
    for (int i = 0; i < 4; i++) {
      u32x4 f = {__builtin_amdgcn_perm(pv[2 * i][1], pv[2 * i][0], 0x07060302u), __builtin_amdgcn_perm(pv[2 * i][3], pv[2 * i][2], 0x07060302u),
                 __builtin_amdgcn_perm(pv[2 * i + 1][1], pv[2 * i + 1][0], 0x07060302u), __builtin_amdgcn_perm(pv[2 * i + 1][3], pv[2 * i + 1][2], 0x07060302u)};
      ldsw<u32x4>(LB + w * 4096 + vL16, i * 1024, f);
    }
    WAITL0(); BAR(); MEMFENCE();
    f32x16 op[4], on[4];
    auto omm = [&](const f32x16& phi, unsigned lh, int z) AI {
#pragma unroll
      for (int i = 0; i < 16; i++) { op[z][i] = 0.f; on[z][i] = 0.f; }
      bf16x8 kf[4];
#pragma unroll
      for (int i = 0; i < 4; i++) kf[i] = ldsr<bf16x8>(LB + vL16, lh * 4096 + i * 1024);
#pragma unroll
      for (int i = 0; i < 2; i++) { op[z] = MFMA(kf[i], sub4(phi, i), op[z]); on[z] = MFMA(kf[2 + i], sub4(phi, 2 + i), on[z]); }
    };
    auto ofin = [&](unsigned lh, int z) AI {
      const float ia = __builtin_amdgcn_rcpf(op[z][13]), ib = __builtin_amdgcn_rcpf(on[z][13]);
      const float id = __builtin_amdgcn_rcpf(__builtin_fmaf(op[z][12], ia, __builtin_fmaf(on[z][12], ib, 1e-6f)));
      const float ca = xhi(ia * id), cb2 = xhi(ib * id);
      const f32x2 c2 = dup2(ca), d2 = dup2(cb2);
      u32x4 f0, f1;
#pragma unroll
      for (int i = 0; i < 4; i++) {
        const f32x2 u = fma2(sub2(op[z], i), c2, sub2(on[z], i) * d2), v = fma2(sub2(op[z], 4 + i), c2, sub2(on[z], 4 + i) * d2);
        f0[i] = pk2(u[0], u[1]); f1[i] = pk2(v[0], v[1]);
      }
      f1[2] = hh ? 0u : f1[2]; f1[3] = hh ? 0u : f1[3];
      st16(f0, rsWS, vL16, sA2, (112 + 2 * lh) * 1024); st16(f1, rsWS, vL16, sA2, (113 + 2 * lh) * 1024);
      SB();
    };
    if constexpr (GG) { omm(accA[0], 1, 0); omm(accA[1], 2, 1); SB(); ofin(1, 0); omm(accA[2], 3, 2); omm(accB[0], 7, 3); SB(); ofin(2, 1); ofin(3, 2); ofin(7, 3); }
    else { omm(accA[2], 0, 0); omm(accB[0], 4, 1); SB(); ofin(0, 0); omm(accB[1 % NT], 5, 2); omm(accB[NT - 1], 6, 3); SB(); ofin(4, 1); ofin(5, 2); ofin(6, 3); }
  } else {
    f32x16 acc[2][2];
#pragma unroll
    for (int i = 0; i < 4; i++)
#pragma unroll
      for (int k = 0; k < 16; k++) acc[i >> 1][i & 1][k] = 0.f;
    u32x4 xr;
    const unsigned vO = (ln >> 7) * XROW + (ln & 0x70), sO = (rt2 * 32 + 8 * w) * XROW + ci * 128;
    auto mm4 = [&](int i) AI {
#pragma unroll
      for (int nn = 0; nn < 2; nn++)
#pragma unroll
        for (int mm = 0; mm < 2; mm++) acc[nn][mm] = MFMA(wf[i][nn], __builtin_bit_cast(bf16x8, af[i][mm]), acc[nn][mm]);
      SB();
    };
    constexpr int NI = NE - NL / 2;
    waitv<2 + NL>();
#pragma unroll
    for (int i = 0; i < NI; i++) asm volatile("" : "+v"(af[i][0]), "+v"(af[i][1]));
    SFOR(i, 9 - EA) readW(EA + i); SEND
    SB();
    SFOR(i, EB) mm4(i); SEND
    waitv<NL>(); stflag(E + 2, rsWS, sFown);
    WAITV(1);
#pragma unroll
    for (int i = NI; i < NE; i++) asm volatile("" : "+v"(af[i][0]), "+v"(af[i][1]));
    WAITL0(); SB();
    BARF();
    SB();
    if constexpr (GG) __builtin_amdgcn_s_setprio(1);
    constexpr int NB = 9 - EA;
    SFOR(i, 9)
      if constexpr (i == NB - 2) ldflag();
      if constexpr (i < NB) { issueW2(EA + 9 + i, 0); issueW2(EA + 9 + i, 1); SB(); }
      if constexpr (EB + i < NE) mm4(EB + i);
    SEND
    asm volatile("s_waitcnt vmcnt(4)" : "+v"(fl1) :: "memory");
    { const unsigned need = ndl ? 2u : nd1 && fc >= 14 ? 1u : 0u;
      if (!ALL(!need || fl1 == E + need)) poll(rsWS, lane * FS, sF, E, need); }
#pragma unroll
    for (int j = NE; j < NE + PD; j++) { loadA1(j, 0); loadA1(j, 1); }
    asm volatile("buffer_load_dwordx4 %0, %1, %2, %3 offen" : "=v"(xr) : "v"(vO), "s"(rsX), "s"(sO) : "memory");
    SFOR(i1, NS - NE)
      constexpr int i = NE + i1;
      asm volatile("s_waitcnt vmcnt(%c2)" : "+v"(af[i][0]), "+v"(af[i][1]) : "i"(2 * ((i + PD <= NS ? i + PD : NS) - 1 - i) + (i1 < PD && i < NO + 8 * GG ? 1 : 0)) : "memory");
      if constexpr (i == 9 && i1 == 0) readW(9);
      if constexpr (i + 1 >= 9 && i + 1 < NS) readW(i + 1);
      if constexpr (GG == 0 && i >= NO) {
        auto stale = [&]() AI {
          unsigned m = UMAX(UMAX(UMAX(af[i][0][0], af[i][0][1]), UMAX(af[i][0][2], af[i][0][3])), UMAX(UMAX(af[i][1][0], af[i][1][1]), UMAX(af[i][1][2], af[i][1][3])));
          return ANY(m == ~0u);
        };
        if (__builtin_expect(stale(), 0)) {
          int it = 0;
#pragma nounroll
          do {
            __builtin_amdgcn_s_sleep(SLEEPR);
            { SFOR(u, PD - 1) if constexpr (i + 1 + u < NS) rel1<false>(af[i + 1 + u][0], af[i + 1 + u][1], rsWS, vL16, sA + (ksb(i + 1 + u) + ksi(i + 1 + u)) * 1024); SEND }
            rel1<true>(af[i][0], af[i][1], rsWS, vL16, sA + (ksb(i) + ksi(i)) * 1024);
          } while (++it < (1 << 14) && stale());
        }
      }
      SB();
      if constexpr (i + PD < NS) { loadA1(i + PD, 0); loadA1(i + PD, 1); }
      mm4(i);
    SEND
    __builtin_amdgcn_s_setprio(0);
#pragma unroll
    for (int qd = 0; qd < 4; qd++)
#pragma unroll
      for (int k = 0; k < 4; k++) {
        const unsigned ra = ring_area(2 * qd + (k >> 1)) + (k & 1) * 1024;
        ldsw<bf16x8>(((lw2 + vL16) ^ (unsigned)(32 * (4 * (qd >> 1) + k))) + (ra & 0x30000u), ra & 0xffffu, sub4(acc[qd >> 1][qd & 1], k));
      }
    WAITL0(); BAR(); MEMFENCE();
    const unsigned c8 = (ln >> 4) & 7, j2 = (c8 & 4) + 2 * GG + ((c8 >> 1) & 1);
    const unsigned rd = LB + ((0x51407362u >> (4 * j2)) & 7) * 16384 + (c8 & 1) * 1024 + (((8 * wr + (ln >> 7)) << 4) ^ (c8 << 5));
    asm volatile("" : "+v"(xr));
    f32x4 sum[2];
#pragma unroll
    for (int k = 0; k < 2; k++) {
      f32x4 t = {U2F(xr[2 * k] << 16), U2F(xr[2 * k] & 0xffff0000u), U2F(xr[2 * k + 1] << 16), U2F(xr[2 * k + 1] & 0xffff0000u)};
#pragma unroll
      for (int v = 0; v < 8; v++) t += ldsr<f32x4>(rd, v * 2048 + k * 512);
      sum[k] = t;
    }
    u32x4 o = {pk2(sum[0][0], sum[0][1]), pk2(sum[0][2], sum[0][3]), pk2(sum[1][0], sum[1][1]), pk2(sum[1][2], sum[1][3])};
    __builtin_amdgcn_raw_buffer_store_b128(o, mkrs(pout, 2048 * XROW), vO + sO, 0, 0);
  }
  };
  if (!attn && g) prog(BoolC<false>{}, IntC<1>{});
  else if (!attn) prog(BoolC<false>{}, IntC<0>{});
  else if (g) prog(BoolC<true>{}, IntC<1>{});
  else prog(BoolC<true>{}, IntC<0>{});
}

extern "C" void kernel_launch(void* const* d_in, const int* in_sizes, int n_in, void* d_out, int out_size, void* d_ws, size_t ws_size,
                              hipStream_t stream) {
  fused_kernel<<<256, 512, 0, stream>>>((const char*)d_in[0], (const char*)d_in[1], (const char*)d_in[5], (const char*)d_in[6], (char*)d_ws,
                                        (const char*)d_in[2], (const char*)d_in[7], (const char*)d_in[3], (char*)d_out);
}
```
